```python
import torch

def _softmax(x, axis=-1):
    return torch.softmax(x, dim=axis)

def _gelu(x):
    return torch.nn.functional.gelu(x, approximate='tanh')

def _silu(x):
    return torch.nn.functional.silu(x)

def _sigmoid(x):
    return torch.sigmoid(x)

def _layernorm(x, axis=-1, eps=1e-05):
    m = x.mean(dim=axis, keepdim=True)
    v = x.var(dim=axis, keepdim=True, unbiased=False)
    return (x - m) / torch.sqrt(v + eps)

def _scatter_add(tgt, idx, src, axis=0):
    return tgt.clone().index_add_(axis, idx, src)

def _segment_sum(src, seg, n):
    out = torch.zeros((n,) + src.shape[1:], dtype=src.dtype)
    return out.index_add_(0, seg, src)

def _pad(x, pw):
    flat = tuple((v for pair in reversed(pw) for v in pair))
    return torch.nn.functional.pad(x, flat)
kernel_name = 'adaLN_cond__sliding_block__mixer_channel_mlp__prompt'

def setup_inputs():

    _w = lambda a: (lambda t: t.to(torch.bfloat16) if t.is_floating_point() else t)(torch.as_tensor(a))

    def _A_setup():
        B, S, D = (1, 2048, 512)
        return {'x': _w((lambda *s: torch.randn(*s, dtype=torch.bfloat16) * 0.05)(B, S, D)), 'wqkv': _w((lambda *s: torch.randn(*s, dtype=torch.bfloat16) * 0.05)(D, 3 * D)), 'wo': _w((lambda *s: torch.randn(*s, dtype=torch.bfloat16) * 0.05)(D, D))}
    _A_in = _A_setup()

    def _B_setup():
        B, S, D, F = (1, 2048, 512, 1792)
        return {'x': _w((lambda *s: torch.randn(*s, dtype=torch.bfloat16) * 0.05)(B, S, D)), 'w1': _w((lambda *s: torch.randn(*s, dtype=torch.bfloat16) * 3.2)(D, F) * D ** (-0.5)), 'b1': _w((lambda *s: torch.randn(*s, dtype=torch.bfloat16) * 1.6)(F)), 'w2': _w((lambda *s: torch.randn(*s, dtype=torch.bfloat16) * 0.2)(F, D) * F ** (-0.5)), 'b2': _w((lambda *s: torch.randn(*s, dtype=torch.bfloat16) * 0.4)(D))}
    _B_in = _B_setup()
    return {'A_x': _A_in['x'], 'A_wqkv': _A_in['wqkv'], 'A_wo': _A_in['wo'], 'B_x': _B_in['x'], 'B_w1': _B_in['w1'], 'B_b1': _B_in['b1'], 'B_w2': _B_in['w2'], 'B_b2': _B_in['b2'], 'c': _w((lambda *s: torch.randn(*s, dtype=torch.bfloat16) * 0.1)(1, 512) * 8.0), 'w_mod': _w((lambda *s: torch.randn(*s, dtype=torch.bfloat16) * 0.1)(512, 6 * 512) * 512 ** (-0.5) * 8.0)}

def reference(A_x, A_wqkv, A_wo, B_x, B_w1, B_b1, B_w2, B_b2, c, w_mod):

    def _A_ref(x, wqkv, wo):
        B, S, D = x.shape
        H = 32
        Dh = D // H
        W = 256
        qkv = (lambda x, *d: x.permute(*d))((x @ wqkv).reshape(B, S, 3, H, Dh), 2, 0, 3, 1, 4)
        q, k, v = (qkv[0], qkv[1], qkv[2])
        scores = torch.einsum('bhqd,bhkd->bhqk', q, k) * Dh ** (-0.5)
        idx = torch.arange(S)
        mask = (idx[:, None] - idx[None, :] >= 0) & (idx[:, None] - idx[None, :] < W)
        scores = torch.where(mask[None, None], scores, -1000000000.0)
        p = _softmax(scores, axis=-1)
        o = torch.einsum('bhqk,bhkd->bhqd', p, v)
        return (lambda x, *d: x.permute(*d))(o, 0, 2, 1, 3).reshape(B, S, D) @ wo

    def _B_ref(x, w1, b1, w2, b2):
        y = _layernorm(x, axis=-1)
        h = _gelu(torch.einsum('bsd,df->bsf', y, w1) + b1)
        return torch.einsum('bsf,fd->bsd', h, w2) + b2
    x = A_x
    mod = (_silu(c) @ w_mod).reshape(c.shape[0], 6, 1, -1)
    sa, ca, ga, sb, cb, gb = (mod[:, 0].clone(), mod[:, 1].clone(), mod[:, 2].clone(), mod[:, 3].clone(), mod[:, 4].clone(), mod[:, 5].clone())
    h = _layernorm(x, axis=-1) * (1 + ca) + sa
    x = x + ga * _A_ref(h, A_wqkv, A_wo)
    h = _layernorm(x, axis=-1) * (1 + cb) + sb
    x = x + gb * _B_ref(h, B_w1, B_b1, B_w2, B_b2)
    return x
```

```cpp
#include <hip/hip_runtime.h>
#include <hip/hip_bf16.h>
typedef unsigned short u16;
__device__ __forceinline__ float bf2f(u16 v){ return __uint_as_float(((unsigned)v)<<16); }
__device__ __forceinline__ u16 f2bf(float f){ unsigned u=__float_as_uint(f); u = (u + 0x7FFF + ((u>>16)&1))>>16; return (u16)u; }
constexpr int S=2048,D=512,H=32,DH=16,W=256,F=1792;

__global__ void k_mod(const u16* c,const u16* wmod,float* mod){
  int j=blockIdx.x*blockDim.x+threadIdx.x;
  float acc=0;
  for(int k=0;k<D;++k){ float cv=bf2f(c[k]); float s=cv/(1.f+__expf(-cv)); acc+=s*bf2f(wmod[k*3072+j]); }
  mod[j]=acc;
}
template<bool INF32,bool DOUBLE>
__global__ void k_ln(const void* in,const float* shift,const float* scale,u16* out){
  int row=blockIdx.x; int t=threadIdx.x;
  __shared__ float red[256];
  float v0,v1;
  if(INF32){ v0=((const float*)in)[row*D+t]; v1=((const float*)in)[row*D+t+256]; }
  else { v0=bf2f(((const u16*)in)[row*D+t]); v1=bf2f(((const u16*)in)[row*D+t+256]); }
  auto rsum=[&](float x){ red[t]=x; __syncthreads(); for(int s=128;s>0;s>>=1){ if(t<s) red[t]+=red[t+s]; __syncthreads(); } float r=red[0]; __syncthreads(); return r; };
  float m=rsum(v0+v1)/D; float d0=v0-m,d1=v1-m; float var=rsum(d0*d0+d1*d1)/D; float rs=1.f/sqrtf(var+1e-5f);
  float h0=d0*rs*(1.f+scale[t])+shift[t], h1=d1*rs*(1.f+scale[t+256])+shift[t+256];
  if(DOUBLE){ float m2=rsum(h0+h1)/D; float e0=h0-m2,e1=h1-m2; float var2=rsum(e0*e0+e1*e1)/D; float rs2=1.f/sqrtf(var2+1e-5f); h0=e0*rs2; h1=e1*rs2; }
  out[row*D+t]=f2bf(h0); out[row*D+t+256]=f2bf(h1);
}
template<int EPI>
__global__ void k_gemm(const u16* A,const u16* B,int K,int N,const u16* bias,const float* gate,const void* res,void* out){
  __shared__ float As[16][17],Bs[16][17];
  int tx=threadIdx.x&15,ty=threadIdx.x>>4; int row=blockIdx.y*16+ty,col=blockIdx.x*16+tx; float acc=0;
  for(int k0=0;k0<K;k0+=16){ As[ty][tx]=bf2f(A[row*K+k0+tx]); Bs[ty][tx]=bf2f(B[(k0+ty)*N+col]); __syncthreads();
    for(int k=0;k<16;++k) acc+=As[ty][k]*Bs[k][tx]; __syncthreads(); }
  if(EPI==0){ ((u16*)out)[row*N+col]=f2bf(acc); }
  else if(EPI==1){ ((float*)out)[row*N+col]=bf2f(((const u16*)res)[row*N+col])+gate[col]*acc; }
  else if(EPI==2){ float x=acc+bf2f(bias[col]); float u=0.7978845608028654f*(x+0.044715f*x*x*x); float g=0.5f*x*(1.f+tanhf(u)); ((u16*)out)[row*N+col]=f2bf(g); }
  else { float x=acc+bf2f(bias[col]); ((u16*)out)[row*N+col]=f2bf(((const float*)res)[row*N+col]+gate[col]*x); }
}
__global__ void k_attn(const u16* qkv,u16* o){
  int q=blockIdx.x; int h=threadIdx.x;
  if(h>=H) return;
  float qv[DH]; for(int d=0;d<DH;++d) qv[d]=bf2f(qkv[q*1536+h*DH+d]);
  int lo=q-W+1; if(lo<0) lo=0; float m=-1e30f;
  for(int k=lo;k<=q;++k){ float s=0; for(int d=0;d<DH;++d) s+=qv[d]*bf2f(qkv[k*1536+512+h*DH+d]); s*=0.25f; m=fmaxf(m,s);}
  float l=0,acc[DH]; for(int d=0;d<DH;++d) acc[d]=0;
  for(int k=lo;k<=q;++k){ float s=0; for(int d=0;d<DH;++d) s+=qv[d]*bf2f(qkv[k*1536+512+h*DH+d]); s*=0.25f; float p=__expf(s-m); l+=p; for(int d=0;d<DH;++d) acc[d]+=p*bf2f(qkv[k*1536+1024+h*DH+d]); }
  for(int d=0;d<DH;++d) o[q*D+h*DH+d]=f2bf(acc[d]/l);
}
__global__ void k_dummy(uint4* p){ p[blockIdx.x*blockDim.x+threadIdx.x]=make_uint4(0,0,0,0); }
#define NDUMMY 0
extern "C" void kernel_launch(void* const* d_in, const int* in_sizes, int n_in, void* d_out, int out_size, void* d_ws, size_t ws_size, hipStream_t stream) {
  const u16* x=(const u16*)d_in[0]; const u16* wqkv=(const u16*)d_in[1]; const u16* wo=(const u16*)d_in[2];
  const u16* w1=(const u16*)d_in[4]; const u16* b1=(const u16*)d_in[5]; const u16* w2=(const u16*)d_in[6]; const u16* b2=(const u16*)d_in[7];
  const u16* c=(const u16*)d_in[8]; const u16* wmod=(const u16*)d_in[9];
  char* ws=(char*)d_ws;
  float* mod=(float*)ws; u16* h=(u16*)(ws+(1<<20)); u16* qkv=(u16*)(ws+(4<<20)); u16* o=(u16*)(ws+(12<<20)); float* x1=(float*)(ws+(16<<20)); u16* y=(u16*)(ws+(24<<20)); u16* h1=(u16*)(ws+(28<<20));
  k_mod<<<12,256,0,stream>>>(c,wmod,mod);
  k_ln<false,false><<<S,256,0,stream>>>(x,mod,mod+512,h);
  k_gemm<0><<<dim3(1536/16,S/16),256,0,stream>>>(h,wqkv,D,1536,nullptr,nullptr,nullptr,qkv);
  k_attn<<<S,64,0,stream>>>(qkv,o);
  k_gemm<1><<<dim3(512/16,S/16),256,0,stream>>>(o,wo,D,512,nullptr,mod+1024,x,x1);
  k_ln<true,true><<<S,256,0,stream>>>(x1,mod+1536,mod+2048,y);
  k_gemm<2><<<dim3(F/16,S/16),256,0,stream>>>(y,w1,D,F,b1,nullptr,nullptr,h1);
  k_gemm<3><<<dim3(512/16,S/16),256,0,stream>>>(h1,w2,F,512,b2,mod+2560,x1,d_out);
  for(int i=0;i<NDUMMY;++i) k_dummy<<<256,256,0,stream>>>((uint4*)(ws+(64<<20)));
}
```

```cpp
#include <hip/hip_runtime.h>
#include <hip/hip_bf16.h>
#include <stdint.h>

typedef unsigned short u16;
typedef short v4s __attribute__((ext_vector_type(4)));
typedef short v8s __attribute__((ext_vector_type(8)));
typedef float f2 __attribute__((ext_vector_type(2)));
typedef float f4 __attribute__((ext_vector_type(4)));
typedef float f16v __attribute__((ext_vector_type(16)));
typedef unsigned v2u __attribute__((ext_vector_type(2)));
typedef unsigned v4u __attribute__((ext_vector_type(4)));
typedef __bf16 b2 __attribute__((ext_vector_type(2)));
#define LAS __attribute__((address_space(3)))
#define GAS __attribute__((address_space(1)))
#define DI __device__ __forceinline__
typedef __amdgpu_buffer_rsrc_t srd_t;

constexpr int SEQ = 2048, DM = 512, FF = 1792;
constexpr unsigned WS_FLAG = 0;
constexpr unsigned WS_MFLAG = 5 * 8192;
constexpr unsigned WS_FLAG_BYTES = 6 * 8192;
constexpr unsigned WS_WV = 64 << 10;
constexpr unsigned WS_MOD = 128 << 10;
constexpr unsigned WS_ST1 = 192 << 10;
constexpr unsigned WS_ST2 = 512 << 10;
constexpr unsigned WS_HF = 2u << 20;
constexpr unsigned WS_KV = 4u << 20;
constexpr unsigned WS_OF = 8u << 20;
constexpr unsigned WS_X1F = 10u << 20;
constexpr unsigned WS_H1F = 12u << 20;
constexpr unsigned WS_STAMP = 20u << 20;
constexpr unsigned WS_BYTES = 21u << 20;
constexpr unsigned L_WQ = 0, L_WK = 32768, L_WV = 65536;
constexpr unsigned L_CA = 98304, L_M2 = 131072;
constexpr unsigned L_PARTU = 98304;
constexpr unsigned L_PARTV = 32768;
constexpr unsigned L_AK = 98304;
constexpr unsigned KPL = 6144;
constexpr unsigned L_AV = L_AK + 4 * KPL;
constexpr unsigned VPL = 6208, VHD = 12416;
constexpr unsigned L_ONE = L_AV + 2 * VHD;
constexpr unsigned L_AQ = L_ONE + 6400;
constexpr unsigned L_PERS = L_AQ + 8192;
static_assert(L_PERS + 1024 <= 163840, "lds");
static_assert((L_AV % 256) == 0 && (L_ONE % 256) == 0, "bank plan");
constexpr unsigned L_WO = 0;
constexpr unsigned L_W1P = 65536;
constexpr unsigned L_W1S = 0;
constexpr unsigned L_XCH = 16384;
constexpr unsigned L_XF = 16384 + 12288;
constexpr unsigned L_W2X = 98304;

struct Params { const u16* x; const u16* wqkv; const u16* wo; const u16* w1; const u16* b1; const u16* w2; const u16* b2; const u16* c; const u16* wmod; u16* out; char* ws; };

DI float bf2f(unsigned v) { return __uint_as_float(v << 16); }
DI float bflo(unsigned v) { return __uint_as_float(v << 16); }
DI float bfhi(unsigned v) { return __uint_as_float(v & 0xffff0000u); }
DI unsigned pk(float a, float b) { f2 v = {a, b}; b2 r = __builtin_convertvector(v, b2); return __builtin_bit_cast(unsigned, r); }
DI float rbf(float a) { return bflo(pk(a, 0.f)); }
DI v8s cat(v4s a, v4s b) { return __builtin_shufflevector(a, b, 0, 1, 2, 3, 4, 5, 6, 7); }
DI v4s trd(const LAS char* p) { return __builtin_amdgcn_ds_read_tr16_b64_v4i16((LAS v4s*)p); }
DI v8s asv8(v4u v) { return __builtin_bit_cast(v8s, v); }
#define MFMA16(a, b, c) __builtin_amdgcn_mfma_f32_16x16x32_bf16((a), (b), (c), 0, 0, 0)
#define MFMA32(a, b, c) __builtin_amdgcn_mfma_f32_32x32x16_bf16((a), (b), (c), 0, 0, 0)
#define BAR() asm volatile("s_waitcnt lgkmcnt(0)\n\ts_barrier" ::: "memory")
#define VM0() asm volatile("s_waitcnt vmcnt(0)" ::: "memory")
#define CFENCE() asm volatile("" ::: "memory")
DI void glds(srd_t r, unsigned voff, unsigned soff, unsigned ldsaddr) {
  asm volatile("s_mov_b32 m0, %0\n\ts_nop 0\n\tbuffer_load_dwordx4 %1, %2, %3 offen lds" ::"s"(ldsaddr), "v"(voff), "s"(r), "s"(soff) : "memory");
}
DI void glds_sc1(srd_t r, unsigned voff, unsigned soff, unsigned ldsaddr) {
  asm volatile("s_mov_b32 m0, %0\n\ts_nop 0\n\tbuffer_load_dwordx4 %1, %2, %3 offen sc1 lds" ::"s"(ldsaddr), "v"(voff), "s"(r), "s"(soff) : "memory");
}
DI v4u ld16(srd_t r, unsigned voff, unsigned soff) { return __builtin_amdgcn_raw_buffer_load_b128(r, voff, soff, 16); }
DI v2u ld8(srd_t r, unsigned voff, unsigned soff) { return __builtin_amdgcn_raw_buffer_load_b64(r, voff, soff, 16); }
DI void st16(srd_t r, v4u v, unsigned voff, unsigned soff) { __builtin_amdgcn_raw_buffer_store_b128(v, r, voff + soff, 0, 16); }
DI void st8(srd_t r, v2u v, unsigned voff, unsigned soff) { __builtin_amdgcn_raw_buffer_store_b64(v, r, voff, soff, 16); }
DI float sum16(float v) { auto r = __builtin_amdgcn_permlane16_swap(__float_as_uint(v), __float_as_uint(v), false, false); return __uint_as_float(r[0]) + __uint_as_float(r[1]); }
DI float sum32(float v) { auto r = __builtin_amdgcn_permlane32_swap(__float_as_uint(v), __float_as_uint(v), false, false); return __uint_as_float(r[0]) + __uint_as_float(r[1]); }
DI float max32(float v) { auto r = __builtin_amdgcn_permlane32_swap(__float_as_uint(v), __float_as_uint(v), false, false); return fmaxf(__uint_as_float(r[0]), __uint_as_float(r[1])); }
DI float sumg(float v) { return sum32(sum16(v)); }
#define RLX_AGENT __ATOMIC_RELAXED, __HIP_MEMORY_SCOPE_AGENT
DI void set_flag(char* ws, unsigned off, unsigned tag) { __hip_atomic_store((GAS unsigned*)(ws + off), tag, RLX_AGENT); }
DI void wait_words(char* ws, unsigned off, int n, int lane, unsigned tag) {
  GAS unsigned* p = (GAS unsigned*)(ws + off) + (lane < n ? lane : 0);
  for (unsigned spins = 0; spins < (1u << 17); ++spins) {
    const unsigned v = __hip_atomic_load(p, RLX_AGENT);
    if (__all(v == tag)) break;
  }
  CFENCE();
}
#define STAMP(k)
#define STAMPW(k, wv)

DI void mod_gemv(const LAS char* img, const v8s (&sf)[16], f4& a0, f4& a1, int lane) {
  const int g = lane >> 4, q = (lane & 15) >> 2, pp = lane & 3;
  const LAS char* b0 = img + (4 * g + q) * 64 + ((0 ^ (g & 1)) * 32) + pp * 8;
  const LAS char* b1 = img + (4 * g + q) * 64 + ((1 ^ (g & 1)) * 32) + pp * 8;
  a0 = f4{0, 0, 0, 0}; a1 = f4{0, 0, 0, 0};
#pragma unroll
  for (int s = 0; s < 16; ++s) {
    a0 = MFMA16(cat(trd(b0 + s * 2048), trd(b0 + s * 2048 + 1024)), sf[s], a0);
    a1 = MFMA16(cat(trd(b1 + s * 2048), trd(b1 + s * 2048 + 1024)), sf[s], a1);
  }
}
DI float silu(float v) { return v * __builtin_amdgcn_rcpf(1.f + __builtin_amdgcn_exp2f(-1.4426950408889634f * v)); }

template <bool FULL>
DI void attention(LAS char* sm, int t, int hd, int kt0, int lane, f16v& O, srd_t rWo, srd_t rW1, unsigned voffB, unsigned voffA1, unsigned wo_soff, unsigned w1_soff, int w) {
  const int qr = lane & 31, h = lane >> 5, blk = (lane >> 4) & 1, q = (lane & 15) >> 2, pp = lane & 3;
  const v8s Qf = *(const LAS v8s*)(sm + L_AQ + (hd * 2 + h) * 2048 + (32 * t + qr) * 16);
  const LAS char* kb = sm + L_AK + (hd * 2 + h) * KPL + (32 * t + qr) * 16;
  const unsigned vadr = blk == 0 ? L_AV + hd * VHD + (pp >> 1) * VPL + (pp & 1) * 8 + (4 * h + q) * 16
                                 : L_ONE + ((hd * 128 + (h ? 0 : 128)) & 255);
  const LAS char* vb = sm + vadr + t * 512;
  f16v mU, mL;
#pragma unroll
  for (int r = 0; r < 16; ++r) { const int kr = (r & 3) + 8 * (r >> 2) + 4 * h; mU[r] = kr > qr ? -1e30f : 0.f; mL[r] = kr > qr ? 0.f : -1e30f; }
  const f16v zero = {};
  float m = -3e38f;
#pragma unroll
  for (int kt = 0; kt < 9; ++kt) {
    if (FULL || kt >= kt0) {
      const v8s Kf = *(const LAS v8s*)(kb + kt * 512);
      const f16v S = MFMA32(Kf, Qf, kt == 0 ? mL : (kt == 8 ? mU : zero));
#pragma unroll
      for (int r = 0; r < 16; r += 2) m = fmaxf(fmaxf(m, S[r]), S[r + 1]);
    }
    if (kt < 8) glds(rWo, voffB, wo_soff + (w * 8 + kt) * 8 * 1024, L_WO + (w * 8 + kt) * 1024);
  }
  m = max32(m);
  f16v nm;
#pragma unroll
  for (int r = 0; r < 16; ++r) { nm[r] = -m; mU[r] -= m; mL[r] -= m; }
  O = zero;
#pragma unroll
  for (int kt = 0; kt < 9; ++kt) {
    if (FULL || kt >= kt0) {
      const v8s Kf = *(const LAS v8s*)(kb + kt * 512);
      const f16v S = MFMA32(Kf, Qf, kt == 0 ? mL : (kt == 8 ? mU : nm));
      v4u P0, P1;
#pragma unroll
      for (int r = 0; r < 4; ++r) {
        P0[r] = pk(__builtin_amdgcn_exp2f(S[2 * r]), __builtin_amdgcn_exp2f(S[2 * r + 1]));
        P1[r] = pk(__builtin_amdgcn_exp2f(S[8 + 2 * r]), __builtin_amdgcn_exp2f(S[8 + 2 * r + 1]));
      }
      const v8s V0 = cat(trd(vb + kt * 512), trd(vb + kt * 512 + 128));
      const v8s V1 = cat(trd(vb + kt * 512 + 256), trd(vb + kt * 512 + 384));
      O = MFMA32(V0, asv8(P0), O);
      O = MFMA32(V1, asv8(P1), O);
    }
    if (kt < 4) glds(rW1, voffA1, w1_soff + (w * 4 + kt) * 16 * (FF * 2), L_W1P + (w * 4 + kt) * 1024);
  }
}

__global__ void __launch_bounds__(512, 2) fused_block(Params p) {
  __shared__ __attribute__((aligned(16))) char smem[163840];
  LAS char* sm = (LAS char*)smem;
  const int tid = threadIdx.x, lane = tid & 63;
  const int w = __builtin_amdgcn_readfirstlane(tid >> 6);
  const int b = blockIdx.x, ci = (b & 7) * 2 + ((b >> 3) & 1), cj = b >> 4;
  const int tok = lane & 15, g = lane >> 4, q4 = (lane & 15) >> 2, pp = lane & 3;
  const unsigned TAG = 1u;
  const srd_t rWS = __builtin_amdgcn_make_buffer_rsrc(p.ws, 0, WS_BYTES, 0x00020000);
  const srd_t rWqkv = __builtin_amdgcn_make_buffer_rsrc((void*)p.wqkv, 0, DM * 1536 * 2, 0x00020000);
  const srd_t rWmod = __builtin_amdgcn_make_buffer_rsrc((void*)p.wmod, 0, DM * 3072 * 2, 0x00020000);
  const int rA = lane >> 2, pA = lane & 3;
  const unsigned colA = (((pA >> 1) ^ ((rA >> 2) & 1)) * 32) + (pA & 1) * 16;
  const int rB = lane >> 3, pB = lane & 7;
  const unsigned colB = (((pB >> 1) ^ ((rB >> 1) & 3)) * 32) + (pB & 1) * 16;
  STAMP(0);

  if (w < 4) {
    __builtin_amdgcn_s_setprio(3);
#pragma unroll
    for (int ii = 0; ii < 8; ++ii) glds(rWmod, rA * 6144 + colA, (w * 8 + ii) * 16 * 6144 + (512 + 32 * cj) * 2, L_CA + (w * 8 + ii) * 1024);
  }
  const bool m2 = (cj % 3 == 0) && cj < 15;
  const int m2id = ci * 5 + cj / 3;
  v8s sf[16];
  v2u xa[2][2];
  if (w <= 4) {
    const v4u cr = *(const v4u*)(p.c + 8 * lane);
    if (w < 4) {
#pragma unroll
      for (int u = 0; u < 2; ++u) { const u16* xr = p.x + (size_t)(128 * ci + 16 * (2 * w + u) + tok) * DM + 32 * cj + 4 * g; xa[u][0] = *(const v2u*)xr; xa[u][1] = *(const v2u*)(xr + 16); }
    }
    LAS char* sc = sm + L_M2 + w * 1024;
    *(LAS v4u*)(sc + lane * 16) = v4u{pk(silu(bflo(cr[0])), silu(bfhi(cr[0]))), pk(silu(bflo(cr[1])), silu(bfhi(cr[1]))), pk(silu(bflo(cr[2])), silu(bfhi(cr[2]))), pk(silu(bflo(cr[3])), silu(bfhi(cr[3])))};
    asm volatile("s_waitcnt lgkmcnt(0)" ::: "memory");
#pragma unroll
    for (int s = 0; s < 16; ++s) sf[s] = cat(*(const LAS v4s*)(sc + (32 * s + 4 * g) * 2), *(const LAS v4s*)(sc + (32 * s + 16 + 4 * g) * 2));
  }
  if (w >= 5) {
    __builtin_amdgcn_s_sleep(8);
#pragma unroll
    for (int ii = 0; ii < 8; ++ii) glds(rWqkv, rA * 3072 + colA, ii * 16 * 3072 + ((w - 5) * 512 + 32 * cj) * 2, (w - 5) * 32768 + ii * 1024);
  }
  if (w < 4) VM0();
  BAR();
  if (w >= 5) {
#pragma unroll 8
    for (int ii = 8; ii < 32; ++ii) glds(rWqkv, rA * 3072 + colA, ii * 16 * 3072 + ((w - 5) * 512 + 32 * cj) * 2, (w - 5) * 32768 + ii * 1024);
  }
  STAMP(1);
  if (w < 4) {
    f4 a0, a1;
    mod_gemv(sm + L_CA, sf, a0, a1, lane);
    float wv[8];
#pragma unroll
    for (int r = 0; r < 4; ++r) { wv[r] = rbf(1.f + rbf(a0[r])); wv[4 + r] = rbf(1.f + rbf(a1[r])); }
    if (w == 0 && tok == 0) {
      st16(rWS, v4u{__float_as_uint(wv[0]), __float_as_uint(wv[1]), __float_as_uint(wv[2]), __float_as_uint(wv[3])}, WS_WV + ci * 2048 + cj * 128 + g * 16, 0);
      st16(rWS, v4u{__float_as_uint(wv[4]), __float_as_uint(wv[5]), __float_as_uint(wv[6]), __float_as_uint(wv[7])}, WS_WV + ci * 2048 + cj * 128 + 64 + g * 16, 0);
    }
#pragma unroll
    for (int u = 0; u < 2; ++u) {
      const float x0 = bflo(xa[u][0][0]), x1 = bfhi(xa[u][0][0]), x2 = bflo(xa[u][0][1]), x3 = bfhi(xa[u][0][1]);
      const float x4 = bflo(xa[u][1][0]), x5 = bfhi(xa[u][1][0]), x6 = bflo(xa[u][1][1]), x7 = bfhi(xa[u][1][1]);
      const int T = 2 * w + u;
      st16(rWS, v4u{pk(x0 * wv[0], x1 * wv[1]), pk(x2 * wv[2], x3 * wv[3]), pk(x4 * wv[4], x5 * wv[5]), pk(x6 * wv[6], x7 * wv[7])},
           WS_HF + ((ci * 8 + T) * 16 + cj) * 1024 + lane * 16, 0);
      const float s1 = sumg(((x0 + x1) + (x2 + x3)) + ((x4 + x5) + (x6 + x7)));
      const float s2 = sumg(((x0 * x0 + x1 * x1) + (x2 * x2 + x3 * x3)) + ((x4 * x4 + x5 * x5) + (x6 * x6 + x7 * x7)));
      if (g == 0) st8(rWS, v2u{__float_as_uint(s1), __float_as_uint(s2)}, WS_ST1 + ((ci * 16 + cj) * 128 + 16 * T + tok) * 8, 0);
    }
    VM0();
    if (lane == 0) set_flag(p.ws, WS_FLAG + 0 * 8192 + ci * 512 + (cj * 4 + w) * 4, TAG);
    __builtin_amdgcn_s_setprio(0);
  } else if (w == 4) {
    if (m2) {
      const int mcol = m2id < 16 ? m2id * 32 : 1024 + (m2id - 16) * 32;
#pragma unroll 8
      for (int ii = 0; ii < 32; ++ii) glds(rWmod, rA * 6144 + colA, ii * 16 * 6144 + mcol * 2, L_M2 + ii * 1024);
      VM0();
      f4 a0, a1;
      mod_gemv(sm + L_M2, sf, a0, a1, lane);
      if (tok == 0) {
        st16(rWS, v4u{__float_as_uint(rbf(a0[0])), __float_as_uint(rbf(a0[1])), __float_as_uint(rbf(a0[2])), __float_as_uint(rbf(a0[3]))}, WS_MOD + mcol * 4 + g * 16, 0);
        st16(rWS, v4u{__float_as_uint(rbf(a1[0])), __float_as_uint(rbf(a1[1])), __float_as_uint(rbf(a1[2])), __float_as_uint(rbf(a1[3]))}, WS_MOD + mcol * 4 + 64 + g * 16, 0);
      }
      VM0();
      if (lane == 0) set_flag(p.ws, WS_MFLAG + m2id * 4, TAG);
    }
    wait_words(p.ws, WS_FLAG + 0 * 8192 + ci * 512, 64, lane, TAG);
  }
  STAMP(2);
  VM0();
  BAR();
  STAMP(3);

  f4 accQ[2], accKV[4];
  v8s act[16];
  float rstd, nmu;
  {
#pragma unroll
    for (int s = 0; s < 16; ++s) act[s] = asv8(ld16(rWS, lane * 16, WS_HF + ((ci * 8 + w) * 16 + s) * 1024));
    v2u st[4];
#pragma unroll
    for (int e = 0; e < 4; ++e) st[e] = ld8(rWS, ((4 * g + e) * 128 + 16 * w + tok) * 8, WS_ST1 + ci * 16 * 128 * 8);
    v8s auxf[2];
#pragma unroll
    for (int u = 0; u < 2; ++u) {
      const unsigned o = (32 * (2 * w + u) + 4 * g) * 4;
      const v4u lo = ld16(rWS, o, WS_WV + ci * 2048), hi = ld16(rWS, o + 64, WS_WV + ci * 2048);
      v4u t = {pk(__uint_as_float(lo[0]), __uint_as_float(lo[1])), pk(__uint_as_float(lo[2]), __uint_as_float(lo[3])),
               pk(__uint_as_float(hi[0]), __uint_as_float(hi[1])), pk(__uint_as_float(hi[2]), __uint_as_float(hi[3]))};
      auxf[u] = asv8(t);
    }
    const LAS char* bA0 = sm + (4 * g + q4) * 64 + ((0 ^ (g & 1)) * 32) + pp * 8;
    const LAS char* bA1 = sm + (4 * g + q4) * 64 + ((1 ^ (g & 1)) * 32) + pp * 8;
#define WFRAG(grp, hf, s) cat(trd(((hf) ? bA1 : bA0) + (grp) * 32768 + (s) * 2048), trd(((hf) ? bA1 : bA0) + (grp) * 32768 + (s) * 2048 + 1024))
    {
      f4 au[6];
#pragma unroll
      for (int nt = 0; nt < 6; ++nt) au[nt] = f4{0, 0, 0, 0};
#pragma unroll
      for (int u = 0; u < 2; ++u)
#pragma unroll
        for (int nt = 0; nt < 6; ++nt) {
          const LAS char* bb = ((nt & 1) ? bA1 : bA0) + (nt >> 1) * 32768 + (2 * w + u) * 2048;
          au[nt] = MFMA16(cat(trd(bb), trd(bb + 1024)), auxf[u], au[nt]);
        }
      if (tok == 0) {
#pragma unroll
        for (int nt = 0; nt < 6; ++nt) *(LAS f4*)(sm + L_PARTU + (w * 96 + nt * 16 + 4 * g) * 4) = au[nt];
      }
    }
    BAR();
    if (tid < 96) {
      float s = 0;
#pragma unroll
      for (int e = 0; e < 8; ++e) s += *(const LAS float*)(sm + L_PARTU + (e * 96 + tid) * 4);
      *(LAS float*)(sm + L_PERS + tid * 4) = s;
    }
#pragma unroll
    for (int nt = 0; nt < 4; ++nt) accKV[nt] = f4{0, 0, 0, 0};
#pragma unroll
    for (int s = 0; s < 16; ++s)
#pragma unroll
      for (int nt = 0; nt < 4; ++nt) accKV[nt] = MFMA16(WFRAG(1 + (nt >> 1), nt & 1, s), act[s], accKV[nt]);
    float s1 = 0, s2 = 0;
#pragma unroll
    for (int e = 0; e < 4; ++e) { s1 += __uint_as_float(st[e][0]); s2 += __uint_as_float(st[e][1]); }
    s1 = sumg(s1); s2 = sumg(s2);
    const float mu = s1 * (1.f / 512), var = fmaxf(s2 * (1.f / 512) - mu * mu, 0.f);
    rstd = __builtin_amdgcn_rsqf(var + 1e-5f); nmu = -rstd * mu;
    if (w == 4) {
      wait_words(p.ws, WS_MFLAG, 64, lane, TAG);
      wait_words(p.ws, WS_MFLAG + 256, 16, lane, TAG);
    }
    STAMP(4);
    BAR();
    v4u salo[2], sahi[2];
#pragma unroll
    for (int u = 0; u < 2; ++u) { const unsigned o = (32 * (2 * w + u) + 4 * g) * 4; salo[u] = ld16(rWS, o, WS_MOD); sahi[u] = ld16(rWS, o + 64, WS_MOD); }
    {
      v4u kk, vv;
      unsigned pkd[4][2];
#pragma unroll
      for (int nt = 0; nt < 4; ++nt) {
        const f4 u = *(const LAS f4*)(sm + L_PERS + (32 + nt * 16 + 4 * g) * 4);
        pkd[nt][0] = pk(rstd * accKV[nt][0] + nmu * u[0], rstd * accKV[nt][1] + nmu * u[1]);
        pkd[nt][1] = pk(rstd * accKV[nt][2] + nmu * u[2], rstd * accKV[nt][3] + nmu * u[3]);
      }
      { auto r0 = __builtin_amdgcn_permlane16_swap(pkd[0][0], pkd[1][0], false, false); auto r1 = __builtin_amdgcn_permlane16_swap(pkd[0][1], pkd[1][1], false, false);
        kk = v4u{r0[0], r1[0], r0[1], r1[1]}; }
      { auto r0 = __builtin_amdgcn_permlane16_swap(pkd[2][0], pkd[3][0], false, false); auto r1 = __builtin_amdgcn_permlane16_swap(pkd[2][1], pkd[3][1], false, false);
        vv = v4u{r0[0], r1[0], r0[1], r1[1]}; }
      const int hd = g & 1, pl = g >> 1;
      st16(rWS, kk, ((0 * 2 + hd) * 2 + pl) * 2048 + (16 * w + tok) * 16, WS_KV + (ci * 16 + cj) * 16384);
      st16(rWS, vv, ((1 * 2 + hd) * 2 + pl) * 2048 + (16 * w + tok) * 16, WS_KV + (ci * 16 + cj) * 16384);
      *(LAS v4u*)(sm + L_AK + (hd * 2 + pl) * KPL + (256 + 16 * w + tok) * 16) = kk;
      *(LAS v4u*)(sm + L_AV + hd * VHD + pl * VPL + (256 + 16 * w + tok) * 16) = vv;
    }
#pragma unroll
    for (int nt = 0; nt < 2; ++nt) accQ[nt] = f4{0, 0, 0, 0};
#pragma unroll
    for (int s = 0; s < 16; ++s)
#pragma unroll
      for (int nt = 0; nt < 2; ++nt) accQ[nt] = MFMA16(WFRAG(0, nt, s), act[s], accQ[nt]);
    {
      f4 av[4];
#pragma unroll
      for (int nt = 0; nt < 4; ++nt) av[nt] = f4{0, 0, 0, 0};
#pragma unroll
      for (int u = 0; u < 2; ++u) {
        v4u t = {pk(__uint_as_float(salo[u][0]), __uint_as_float(salo[u][1])), pk(__uint_as_float(salo[u][2]), __uint_as_float(salo[u][3])),
                 pk(__uint_as_float(sahi[u][0]), __uint_as_float(sahi[u][1])), pk(__uint_as_float(sahi[u][2]), __uint_as_float(sahi[u][3]))};
#pragma unroll
        for (int nt = 0; nt < 4; ++nt) {
          const LAS char* bb = ((nt & 1) ? bA1 : bA0) + (nt >> 1) * 65536 + (2 * w + u) * 2048;
          av[nt] = MFMA16(cat(trd(bb), trd(bb + 1024)), asv8(t), av[nt]);
        }
      }
      if (tok == 0) {
#pragma unroll
        for (int nt = 0; nt < 4; ++nt) *(LAS f4*)(sm + L_PARTV + (w * 64 + nt * 16 + 4 * g) * 4) = av[nt];
      }
    }
    VM0();
    BAR();
    if (tid == 256) set_flag(p.ws, WS_FLAG + 1 * 8192 + ci * 512 + cj * 4, TAG);
    STAMP(5);
    if (tid < 64) {
      float s = 0;
#pragma unroll
      for (int e = 0; e < 8; ++e) s += *(const LAS float*)(sm + L_PARTV + (e * 64 + tid) * 4);
      *(LAS float*)(sm + L_PERS + (96 + tid) * 4) = s;
    }
    if (tid < 400) *(LAS v4u*)(sm + L_ONE + tid * 16) = v4u{0x3f803f80u, 0x3f803f80u, 0x3f803f80u, 0x3f803f80u};
    BAR();
    {
      unsigned pq[2][2];
      const float qs = 0.25f * 1.4426950408889634f;
#pragma unroll
      for (int nt = 0; nt < 2; ++nt) {
        const f4 u = *(const LAS f4*)(sm + L_PERS + (nt * 16 + 4 * g) * 4), v = *(const LAS f4*)(sm + L_PERS + (96 + nt * 16 + 4 * g) * 4);
        pq[nt][0] = pk(qs * (rstd * accQ[nt][0] + nmu * u[0] + v[0]), qs * (rstd * accQ[nt][1] + nmu * u[1] + v[1]));
        pq[nt][1] = pk(qs * (rstd * accQ[nt][2] + nmu * u[2] + v[2]), qs * (rstd * accQ[nt][3] + nmu * u[3] + v[3]));
      }
      auto r0 = __builtin_amdgcn_permlane16_swap(pq[0][0], pq[1][0], false, false); auto r1 = __builtin_amdgcn_permlane16_swap(pq[0][1], pq[1][1], false, false);
      *(LAS v4u*)(sm + L_AQ + ((g & 1) * 2 + (g >> 1)) * 2048 + (16 * w + tok) * 16) = v4u{r0[0], r1[0], r0[1], r1[1]};
    }
    if (ci < 2) {
      for (int e = tid; e < 8 * 256; e += 512) {
        const int pln = e >> 8, key = e & 255;
        if (ci == 0 || key < 128) {
          const unsigned a = pln < 4 ? L_AK + pln * KPL : L_AV + ((pln - 4) >> 1) * VHD + (pln & 1) * VPL;
          *(LAS v4u*)(sm + a + key * 16) = v4u{0, 0, 0, 0};
        }
      }
    }
    if (w == 4) {
      if (ci >= 1) wait_words(p.ws, WS_FLAG + 1 * 8192 + (ci - 1) * 512 + cj * 4, 1, lane, TAG);
      if (ci >= 2) wait_words(p.ws, WS_FLAG + 1 * 8192 + (ci - 2) * 512 + cj * 4, 1, lane, TAG);
    }
    BAR();
    STAMP(6);
    {
      const unsigned dst = w < 4 ? L_AK + w * KPL : L_AV + ((w - 4) >> 1) * VHD + (w & 1) * VPL;
#pragma unroll
      for (int src = 0; src < 2; ++src)
        if (ci - 2 + src >= 0) {
#pragma unroll
          for (int hf = 0; hf < 2; ++hf) glds_sc1(rWS, lane * 16, WS_KV + ((ci - 2 + src) * 16 + cj) * 16384 + w * 2048 + hf * 1024, dst + (src * 128 + hf * 64) * 16);
        }
    }
    VM0();
    BAR();
    STAMP(7);
  }

  const srd_t rWo = __builtin_amdgcn_make_buffer_rsrc((void*)p.wo, 0, DM * DM * 2, 0x00020000);
  const srd_t rW1 = __builtin_amdgcn_make_buffer_rsrc((void*)p.w1, 0, DM * FF * 2, 0x00020000);
  const int hf = cj & 1, ct = cj >> 1, par = cj & 1;
  const unsigned w1_cb = (112 * cj + 16 * par) * 2;
  const int tt = w & 3, kh = w >> 2;
  const int orow = 128 * ci + 64 * hf + 16 * tt + tok, ocol = 64 * ct + 32 * kh + 4 * g;
  const v2u xr0 = *(const v2u*)(p.x + (size_t)orow * DM + ocol), xr1 = *(const v2u*)(p.x + (size_t)orow * DM + ocol + 16);
  {
    const int t = w & 3, hd = w >> 2;
    const int kt0 = ci >= 2 ? 0 : 8 - 4 * ci - t;
    f16v O;
    if (ci >= 2) attention<true>(sm, t, hd, 0, lane, O, rWo, rW1, rB * 1024 + colB, rA * (FF * 2) + colA, ct * 128, w1_cb, w);
    else attention<false>(sm, t, hd, kt0, lane, O, rWo, rW1, rB * 1024 + colB, rA * (FF * 2) + colA, ct * 128, w1_cb, w);
    const int h = lane >> 5;
    const float inv = __builtin_amdgcn_rcpf(O[8]);
    const f4 b0 = *(const LAS f4*)(sm + L_PERS + (96 + 32 + hd * 16 + 4 * h) * 4), b1 = *(const LAS f4*)(sm + L_PERS + (96 + 32 + hd * 16 + 8 + 4 * h) * 4);
    const v4u o = {pk(O[0] * inv + b0[0], O[1] * inv + b0[1]), pk(O[2] * inv + b0[2], O[3] * inv + b0[3]),
                   pk(O[4] * inv + b1[0], O[5] * inv + b1[1]), pk(O[6] * inv + b1[2], O[7] * inv + b1[3])};
    const int qr = lane & 31;
    st16(rWS, o, ((2 * t + (qr >> 4)) * 16) * 1024 + ((2 * hd + h) * 16 + (qr & 15)) * 16, WS_OF + (ci * 8 * 16 + cj) * 1024);
  }
  VM0();
  BAR();
  if (tid == 256) set_flag(p.ws, WS_FLAG + 2 * 8192 + ci * 512 + cj * 4, TAG);
  STAMP(8);
  if (w < 4) {
#pragma unroll 8
    for (int ii = 0; ii < 16; ++ii) { const int pc = w * 16 + ii; glds(rW1, rA * (FF * 2) + colA, (pc & 31) * 16 * (FF * 2) + w1_cb + (1 + (pc >> 5)) * 64, L_W1P + 32768 + pc * 1024); }
  }
  v4u gal = ld16(rWS, (1024 + ocol) * 4, WS_MOD), gah = ld16(rWS, (1024 + ocol + 16) * 4, WS_MOD);
  v4u cbl = ld16(rWS, (2048 + ocol) * 4, WS_MOD), cbh = ld16(rWS, (2048 + ocol + 16) * 4, WS_MOD);
  v4u sbl = ld16(rWS, (1536 + ocol) * 4, WS_MOD), sbh = ld16(rWS, (1536 + ocol + 16) * 4, WS_MOD);
  if (w == 4) wait_words(p.ws, WS_FLAG + 2 * 8192 + ci * 512, 16, lane, TAG);
  BAR();
  STAMP(9);

  float x1[8];
  {
    v8s oa[8];
#pragma unroll
    for (int s = 0; s < 8; ++s) oa[s] = asv8(ld16(rWS, lane * 16, WS_OF + ((ci * 8 + 4 * hf + tt) * 16 + 8 * kh + s) * 1024));
    const int rowo = 16 * (g >> 1) + 4 * (g & 1) + q4;
    const LAS char* bo[4];
#pragma unroll
    for (int nt = 0; nt < 4; ++nt) bo[nt] = sm + L_WO + kh * 8 * 4096 + rowo * 128 + ((nt ^ ((rowo >> 1) & 3)) * 32) + pp * 8;
    f4 acc[4];
#pragma unroll
    for (int nt = 0; nt < 4; ++nt) acc[nt] = f4{0, 0, 0, 0};
#pragma unroll
    for (int s = 0; s < 8; ++s)
#pragma unroll
      for (int nt = 0; nt < 4; ++nt) acc[nt] = MFMA16(cat(trd(bo[nt] + s * 4096), trd(bo[nt] + s * 4096 + 1024)), oa[s], acc[nt]);
    VM0();
    BAR();
    *(LAS f4*)(sm + L_XCH + w * 2048 + lane * 16) = kh ? acc[0] : acc[2];
    *(LAS f4*)(sm + L_XCH + w * 2048 + 1024 + lane * 16) = kh ? acc[1] : acc[3];
    BAR();
    const f4 o0 = *(const LAS f4*)(sm + L_XCH + (w ^ 4) * 2048 + lane * 16), o1 = *(const LAS f4*)(sm + L_XCH + (w ^ 4) * 2048 + 1024 + lane * 16);
    const f4 t0 = (kh ? acc[2] : acc[0]) + o0, t1 = (kh ? acc[3] : acc[1]) + o1;
    const float xin[8] = {bflo(xr0[0]), bfhi(xr0[0]), bflo(xr0[1]), bfhi(xr0[1]), bflo(xr1[0]), bfhi(xr1[0]), bflo(xr1[1]), bfhi(xr1[1])};
    float wv[8], sv[8];
#pragma unroll
    for (int r = 0; r < 4; ++r) {
      x1[r] = rbf(xin[r] + __uint_as_float(gal[r]) * t0[r]); x1[4 + r] = rbf(xin[4 + r] + __uint_as_float(gah[r]) * t1[r]);
      wv[r] = rbf(1.f + __uint_as_float(cbl[r])); wv[4 + r] = rbf(1.f + __uint_as_float(cbh[r]));
      sv[r] = __uint_as_float(sbl[r]); sv[4 + r] = __uint_as_float(sbh[r]);
    }
    st16(rWS, v4u{pk(x1[0] * wv[0], x1[1] * wv[1]), pk(x1[2] * wv[2], x1[3] * wv[3]), pk(x1[4] * wv[4], x1[5] * wv[5]), pk(x1[6] * wv[6], x1[7] * wv[7])},
         lane * 16, WS_X1F + ((ci * 8 + 4 * hf + tt) * 16 + 2 * ct + kh) * 1024);
    float s1 = 0, s2 = 0, s3 = 0, s4 = 0, s5 = 0, s6 = 0;
#pragma unroll
    for (int r = 0; r < 8; ++r) { const float xw = x1[r] * wv[r]; s1 += x1[r]; s2 += x1[r] * x1[r]; s3 += xw; s4 += xw * wv[r]; s5 += xw * xw; s6 += xw * sv[r]; }
    s1 = sumg(s1); s2 = sumg(s2); s3 = sumg(s3); s4 = sumg(s4); s5 = sumg(s5); s6 = sumg(s6);
    if (g < 2) st16(rWS, g == 0 ? v4u{__float_as_uint(s1), __float_as_uint(s2), __float_as_uint(s3), __float_as_uint(s4)} : v4u{__float_as_uint(s5), __float_as_uint(s6), 0u, 0u},
                    (64 * hf + 16 * tt + tok) * 32 + g * 16, WS_ST2 + (ci * 16 + 2 * ct + kh) * 4096);
  }
  VM0();
  BAR();
  if (tid == 256) set_flag(p.ws, WS_FLAG + 3 * 8192 + ci * 512 + cj * 4, TAG);
  STAMP(10);
  if (w < 4) {
    const srd_t rW2 = __builtin_amdgcn_make_buffer_rsrc((void*)p.w2, 0, FF * DM * 2, 0x00020000);
    const unsigned scol = (112 * cj + (par ? 0 : 96)) * 2;
#pragma unroll
    for (int ii = 0; ii < 4; ++ii) glds(rW1, (lane >> 1) * (FF * 2) + (lane & 1) * 16, (w * 4 + ii) * 32 * (FF * 2) + scol, L_W1S + (w * 4 + ii) * 1024);
#pragma unroll
    for (int ii = 0; ii < 8; ++ii) { const int pc = w * 8 + ii; glds(rW2, rB * 1024 + colB, ((pc >> 4) * 896 + (pc & 15) * 8) * 1024 + ct * 128, 32768 + pc * 1024); }
  }

  float Cw, Cw2, Cws, Cs, Cs2;
  v8s aux2[2];
  {
    const v4u c0 = ld16(rWS, lane * 32, WS_MOD + 2048 * 4), c1 = ld16(rWS, lane * 32 + 16, WS_MOD + 2048 * 4);
    const v4u d0 = ld16(rWS, lane * 32, WS_MOD + 1536 * 4), d1 = ld16(rWS, lane * 32 + 16, WS_MOD + 1536 * 4);
    Cw = Cw2 = Cws = Cs = Cs2 = 0;
#pragma unroll
    for (int r = 0; r < 4; ++r) {
      const float w0 = rbf(1.f + __uint_as_float(c0[r])), w1 = rbf(1.f + __uint_as_float(c1[r])), s0 = __uint_as_float(d0[r]), s1 = __uint_as_float(d1[r]);
      Cw += w0 + w1; Cw2 += w0 * w0 + w1 * w1; Cws += w0 * s0 + w1 * s1; Cs += s0 + s1; Cs2 += s0 * s0 + s1 * s1;
    }
#pragma unroll
    for (int o = 1; o < 16; o <<= 1) { Cw += __shfl_xor(Cw, o); Cw2 += __shfl_xor(Cw2, o); Cws += __shfl_xor(Cws, o); Cs += __shfl_xor(Cs, o); Cs2 += __shfl_xor(Cs2, o); }
    Cw = sumg(Cw); Cw2 = sumg(Cw2); Cws = sumg(Cws); Cs = sumg(Cs); Cs2 = sumg(Cs2);
#pragma unroll
    for (int u = 0; u < 2; ++u) {
      const unsigned o = (32 * (2 * w + u) + 4 * g) * 4;
      const unsigned vb = WS_MOD + (tok == 1 ? 1536 : 2048) * 4;
      const v4u lo = ld16(rWS, vb + o, 0), hi = ld16(rWS, vb + o + 64, 0);
      float e[8];
#pragma unroll
      for (int r = 0; r < 4; ++r) {
        e[r] = tok == 0 ? 1.f + __uint_as_float(lo[r]) : (tok == 1 ? __uint_as_float(lo[r]) : 1.f);
        e[4 + r] = tok == 0 ? 1.f + __uint_as_float(hi[r]) : (tok == 1 ? __uint_as_float(hi[r]) : 1.f);
      }
      aux2[u] = asv8(v4u{pk(e[0], e[1]), pk(e[2], e[3]), pk(e[4], e[5]), pk(e[6], e[7])});
    }
  }
  const LAS char* bP0 = sm + L_W1P + (4 * g + q4) * 64 + ((0 ^ (g & 1)) * 32) + pp * 8;
  const LAS char* bP1 = sm + L_W1P + (4 * g + q4) * 64 + ((1 ^ (g & 1)) * 32) + pp * 8;
  const LAS char* bS = sm + L_W1S + (4 * g + q4) * 32 + pp * 8;
#define W1FRAG(nt, s) ((nt) == 6 ? cat(trd(bS + (s) * 1024), trd(bS + (s) * 1024 + 512)) \
                                 : cat(trd((((nt) & 1) ? bP1 : bP0) + ((nt) >> 1) * 32768 + (s) * 2048), trd((((nt) & 1) ? bP1 : bP0) + ((nt) >> 1) * 32768 + (s) * 2048 + 1024)))
  v2u b1r[7];
#pragma unroll
  for (int nt = 0; nt < 7; ++nt) b1r[nt] = *(const v2u*)(p.b1 + (nt == 6 ? 112 * cj + (par ? 0 : 96) : 112 * cj + 16 * par + nt * 16) + 4 * g);
  if (w == 4) wait_words(p.ws, WS_FLAG + 3 * 8192 + ci * 512, 16, lane, TAG);
  VM0();
  BAR();
  STAMP(11);
  v4u stg[8];
  {
    v8s act2[16];
#pragma unroll
    for (int s = 0; s < 16; ++s) act2[s] = asv8(ld16(rWS, lane * 16, WS_X1F + ((ci * 8 + w) * 16 + s) * 1024));
    v4u sa[4], sb[4];
#pragma unroll
    for (int e = 0; e < 4; ++e) { const unsigned o = (4 * g + e) * 4096 + (16 * w + tok) * 32; sa[e] = ld16(rWS, o, WS_ST2 + ci * 16 * 4096); sb[e] = ld16(rWS, o + 16, WS_ST2 + ci * 16 * 4096); }
    {
      f4 au[7];
#pragma unroll
      for (int nt = 0; nt < 7; ++nt) au[nt] = f4{0, 0, 0, 0};
#pragma unroll
      for (int u = 0; u < 2; ++u) {
        const int so = 2 * w + u;
#pragma unroll
        for (int nt = 0; nt < 7; ++nt) {
          const LAS char* bb = nt == 6 ? bS + so * 1024 : ((nt & 1) ? bP1 : bP0) + (nt >> 1) * 32768 + so * 2048;
          au[nt] = MFMA16(cat(trd(bb), trd(bb + (nt == 6 ? 512 : 1024))), aux2[u], au[nt]);
        }
      }
      if (tok < 3) {
#pragma unroll
        for (int nt = 0; nt < 7; ++nt) *(LAS f4*)(sm + L_XCH + ((w * 3 + tok) * 112 + nt * 16 + 4 * g) * 4) = au[nt];
      }
    }
    BAR();
    if (tid < 336) {
      float s = 0;
#pragma unroll
      for (int e = 0; e < 8; ++e) s += *(const LAS float*)(sm + L_XCH + (e * 336 + tid) * 4);
      *(LAS float*)(sm + L_XF + tid * 4) = s;
    }
    {
      const char* w2b = (const char*)p.w2 + ct * 128 + (size_t)rB * 1024 + colB;
#pragma unroll
      for (int e = 0; e < 8; ++e) { const int pc = w * 8 + e; const int chunk = (pc >> 4) == 0 ? 5 : ((pc >> 4) == 1 ? 12 : ((pc >> 4) == 2 ? 6 : 13));
        stg[e] = *(const v4u*)(w2b + (size_t)(chunk * 128 + (pc & 15) * 8) * 1024); }
    }
    f4 acc[7];
#pragma unroll
    for (int nt = 0; nt < 7; ++nt) acc[nt] = f4{0, 0, 0, 0};
#pragma unroll
    for (int s = 0; s < 16; ++s)
#pragma unroll
      for (int nt = 0; nt < 7; ++nt) acc[nt] = MFMA16(W1FRAG(nt, s), act2[s], acc[nt]);
    float s1 = 0, s2 = 0, s3 = 0, s4 = 0, s5 = 0, s6 = 0;
#pragma unroll
    for (int e = 0; e < 4; ++e) { s1 += __uint_as_float(sa[e][0]); s2 += __uint_as_float(sa[e][1]); s3 += __uint_as_float(sa[e][2]); s4 += __uint_as_float(sa[e][3]); s5 += __uint_as_float(sb[e][0]); s6 += __uint_as_float(sb[e][1]); }
    s1 = sumg(s1); s2 = sumg(s2); s3 = sumg(s3); s4 = sumg(s4); s5 = sumg(s5); s6 = sumg(s6);
    const float mu1 = s1 * (1.f / 512), var1 = fmaxf(s2 * (1.f / 512) - mu1 * mu1, 0.f), r1 = __builtin_amdgcn_rsqf(var1 + 1e-5f);
    const float mu2 = (r1 * (s3 - mu1 * Cw) + Cs) * (1.f / 512);
    const float hh = (r1 * r1 * (s5 - 2.f * mu1 * s4 + mu1 * mu1 * Cw2) + 2.f * r1 * (s6 - mu1 * Cws) + Cs2) * (1.f / 512);
    const float r2 = __builtin_amdgcn_rsqf(fmaxf(hh - mu2 * mu2, 0.f) + 1e-5f);
    const float cA = r2 * r1, cB = -cA * mu1, cC = r2, cD = -r2 * mu2;
    BAR();
    STAMP(12);
    unsigned hp[7][2];
#pragma unroll
    for (int nt = 0; nt < 7; ++nt) {
      const f4 u = *(const LAS f4*)(sm + L_XF + (nt * 16 + 4 * g) * 4), v = *(const LAS f4*)(sm + L_XF + (112 + nt * 16 + 4 * g) * 4), t = *(const LAS f4*)(sm + L_XF + (224 + nt * 16 + 4 * g) * 4);
      const v2u bb = b1r[nt];
      const float bias[4] = {bflo(bb[0]), bfhi(bb[0]), bflo(bb[1]), bfhi(bb[1])};
      float o[4];
#pragma unroll
      for (int r = 0; r < 4; ++r) {
        const float z = cA * acc[nt][r] + (cB * u[r] + (cC * v[r] + (cD * t[r] + bias[r])));
        const float pz = z * (-2.3022081985f - 0.1029432397f * z * z);
        o[r] = z * __builtin_amdgcn_rcpf(1.f + __builtin_amdgcn_exp2f(pz));
      }
      hp[nt][0] = pk(o[0], o[1]); hp[nt][1] = pk(o[2], o[3]);
    }
    const unsigned S0 = (7 * cj + par) >> 1;
    const unsigned hb = WS_H1F + (ci * 8 + w) * 56 * 1024;
#pragma unroll
    for (int pr = 0; pr < 3; ++pr) st16(rWS, v4u{hp[2 * pr][0], hp[2 * pr][1], hp[2 * pr + 1][0], hp[2 * pr + 1][1]}, lane * 16, hb + (S0 + pr) * 1024);
    { const unsigned NT = 7 * cj + (par ? 0 : 6); st8(rWS, v2u{hp[6][0], hp[6][1]}, lane * 8, hb + (NT >> 1) * 1024 + (NT & 1) * 512); }
  }
  VM0();
  BAR();
  if (tid == 256) set_flag(p.ws, WS_FLAG + 4 * 8192 + ci * 512 + cj * 4, TAG);
  STAMP(13);
  if (w < 4) {
    const srd_t rW2 = __builtin_amdgcn_make_buffer_rsrc((void*)p.w2, 0, FF * DM * 2, 0x00020000);
#pragma unroll 8
    for (int ii = 0; ii < 32; ++ii) {
      const int it = 1 + w, khh = ii >> 4, pc = ii & 15;
      const unsigned slot = (w == 3 ? 0u : 65536u + w * 32768u) + khh * 16384u;
      glds(rW2, rB * 1024 + colB, ((khh * 7 + it) * 128 + pc * 8) * 1024 + ct * 128, slot + pc * 1024);
    }
  }
  const v4u gbl = ld16(rWS, (2560 + ocol) * 4, WS_MOD), gbh = ld16(rWS, (2560 + ocol + 16) * 4, WS_MOD);
  const v2u b2l = *(const v2u*)(p.b2 + ocol), b2h = *(const v2u*)(p.b2 + ocol + 16);
  if (w == 4) wait_words(p.ws, WS_FLAG + 4 * 8192 + ci * 512, 16, lane, TAG);
  VM0();
  BAR();
  STAMP(14);

  {
    v8s ha[28];
    const unsigned hb = WS_H1F + ((ci * 8 + 4 * hf + tt) * 56 + 28 * kh) * 1024;
#pragma unroll
    for (int s = 0; s < 28; ++s) {
      if (s % 7 == 3) { const v2u lo = ld8(rWS, lane * 8, hb + s * 1024), hi = ld8(rWS, lane * 8 + 512, hb + s * 1024); ha[s] = asv8(v4u{lo[0], lo[1], hi[0], hi[1]}); }
      else ha[s] = asv8(ld16(rWS, lane * 16, hb + s * 1024));
    }
    const int rown = 4 * g + q4;
    unsigned bw[4];
#pragma unroll
    for (int nt = 0; nt < 4; ++nt) bw[nt] = kh * 16384 + rown * 128 + ((nt ^ ((rown >> 1) & 3)) * 32) + pp * 8;
    f4 acc[4];
#pragma unroll
    for (int nt = 0; nt < 4; ++nt) acc[nt] = f4{0, 0, 0, 0};
#pragma unroll
    for (int it = 0; it < 7; ++it) {
      const unsigned slot = it == 0 ? 32768u : it == 1 ? 65536u : it == 2 ? 98304u : it == 3 ? 131072u : it == 4 ? 0u : it == 5 ? 32768u : 65536u;
      if (it == 2) {
        BAR();
#pragma unroll
        for (int e = 0; e < 8; ++e) { const int pc = w * 8 + e; *(LAS v4u*)(sm + 32768 + (pc >> 5) * 32768 + ((pc >> 4) & 1) * 16384 + (pc & 15) * 1024 + lane * 16) = stg[e]; }
      }
      if (it == 5) BAR();
#pragma unroll
      for (int s4 = 0; s4 < 4; ++s4)
#pragma unroll
        for (int nt = 0; nt < 4; ++nt)
          acc[nt] = MFMA16(cat(trd(sm + bw[nt] + slot + s4 * 4096), trd(sm + bw[nt] + slot + s4 * 4096 + 2048)), ha[it * 4 + s4], acc[nt]);
    }
    BAR();
    *(LAS f4*)(sm + L_W2X + w * 2048 + lane * 16) = kh ? acc[0] : acc[2];
    *(LAS f4*)(sm + L_W2X + w * 2048 + 1024 + lane * 16) = kh ? acc[1] : acc[3];
    BAR();
    const f4 o0 = *(const LAS f4*)(sm + L_W2X + (w ^ 4) * 2048 + lane * 16), o1 = *(const LAS f4*)(sm + L_W2X + (w ^ 4) * 2048 + 1024 + lane * 16);
    const f4 t0 = (kh ? acc[2] : acc[0]) + o0, t1 = (kh ? acc[3] : acc[1]) + o1;
    const float bl[4] = {bflo(b2l[0]), bfhi(b2l[0]), bflo(b2l[1]), bfhi(b2l[1])}, bh[4] = {bflo(b2h[0]), bfhi(b2h[0]), bflo(b2h[1]), bfhi(b2h[1])};
    float o[8];
#pragma unroll
    for (int r = 0; r < 4; ++r) { o[r] = x1[r] + __uint_as_float(gbl[r]) * (t0[r] + bl[r]); o[4 + r] = x1[4 + r] + __uint_as_float(gbh[r]) * (t1[r] + bh[r]); }
    auto r0 = __builtin_amdgcn_permlane16_swap(pk(o[0], o[1]), pk(o[4], o[5]), false, false);
    auto r1 = __builtin_amdgcn_permlane16_swap(pk(o[2], o[3]), pk(o[6], o[7]), false, false);
    *(v4u*)(p.out + (size_t)orow * DM + 64 * ct + 32 * kh + (g & 1) * 16 + (g >> 1) * 8) = v4u{r0[0], r1[0], r0[1], r1[1]};
  }
  STAMP(15);
}

extern "C" void kernel_launch(void* const* d_in, const int* in_sizes, int n_in, void* d_out, int out_size, void* d_ws, size_t ws_size, hipStream_t stream) {
  Params p;
  p.x = (const u16*)d_in[0]; p.wqkv = (const u16*)d_in[1]; p.wo = (const u16*)d_in[2];
  p.w1 = (const u16*)d_in[4]; p.b1 = (const u16*)d_in[5]; p.w2 = (const u16*)d_in[6]; p.b2 = (const u16*)d_in[7];
  p.c = (const u16*)d_in[8]; p.wmod = (const u16*)d_in[9]; p.out = (u16*)d_out; p.ws = (char*)d_ws;
  hipMemsetAsync(d_ws, 0, WS_FLAG_BYTES, stream);
  fused_block<<<256, 512, 0, stream>>>(p);
}
```

```cpp
#include <hip/hip_runtime.h>
#include <hip/hip_bf16.h>
#include <stdint.h>

typedef unsigned short u16;
typedef short v4s __attribute__((ext_vector_type(4)));
typedef short v8s __attribute__((ext_vector_type(8)));
typedef float f2 __attribute__((ext_vector_type(2)));
typedef float f4 __attribute__((ext_vector_type(4)));
typedef float f16v __attribute__((ext_vector_type(16)));
typedef unsigned v2u __attribute__((ext_vector_type(2)));
typedef unsigned v4u __attribute__((ext_vector_type(4)));
typedef __bf16 b2 __attribute__((ext_vector_type(2)));
#define LAS __attribute__((address_space(3)))
#define GAS __attribute__((address_space(1)))
#define DI __device__ __forceinline__
typedef __amdgpu_buffer_rsrc_t srd_t;

constexpr int SEQ = 2048, DM = 512, FF = 1792;
constexpr unsigned WS_FLAG = 0;
constexpr unsigned WS_FM = 5 * 8192;
constexpr unsigned WS_FQ = 5 * 8192 + 512;
constexpr unsigned WS_FW1 = WS_FQ + 1024;
constexpr unsigned WS_FWO = WS_FW1 + 1024;
constexpr unsigned WS_FW2 = WS_FWO + 512;
constexpr unsigned WS_FLAG_BYTES = 6 * 8192;
static_assert(WS_FW2 + 2048 <= WS_FLAG_BYTES, "flags");
constexpr unsigned WS_WV = 64 << 10;
constexpr unsigned WS_MOD = 128 << 10;
constexpr unsigned WS_ST1 = 192 << 10;
constexpr unsigned WS_ST2A = 512 << 10;
constexpr unsigned WS_ST2B = 1024 << 10;
constexpr unsigned WS_HF = 2u << 20;
constexpr unsigned WS_KV = 4u << 20;
constexpr unsigned WS_OF = 8u << 20;
constexpr unsigned WS_X1F = 10u << 20;
constexpr unsigned WS_H1F = 12u << 20;
constexpr unsigned WS_TQ = 19u << 20;
constexpr unsigned WS_TW1 = WS_TQ + (1536u << 10);
constexpr unsigned WS_TWO = WS_TW1 + (1792u << 10);
constexpr unsigned WS_TW2 = WS_TWO + (512u << 10);
constexpr unsigned WS_STAMP = 25u << 20;
constexpr unsigned WS_BYTES = 26u << 20;
static_assert(WS_TW2 + (1792u << 10) <= WS_STAMP, "ws");
constexpr unsigned L_XS = 0;
constexpr unsigned L_SQ = 8192, L_S1 = 16384, L_SO = 24576;
constexpr unsigned L_SC = 40960;
constexpr unsigned L_CAP = 49152;
constexpr unsigned L_WQKV = 0;
constexpr unsigned L_CA = 98304, L_M2 = 131072;
constexpr unsigned L_PARTU = 98304;
constexpr unsigned L_MODC = 32768;
constexpr unsigned L_B2C = L_MODC + 6144;
constexpr unsigned L_PARTV = 32768 + 8192;
constexpr unsigned L_AK = 98304;
constexpr unsigned KPL = 6144;
constexpr unsigned L_AV = L_AK + 4 * KPL;
constexpr unsigned VPL = 6208, VHD = 12416;
constexpr unsigned L_ONE = L_AV + 2 * VHD;
constexpr unsigned L_AQ = L_ONE + 6400;
constexpr unsigned L_PERS = L_AQ + 8192;
static_assert(L_PERS + 1024 <= 163840, "lds");
static_assert((L_AV % 256) == 0 && (L_ONE % 256) == 0, "bank plan");
constexpr unsigned L_WO = 0;
constexpr unsigned L_W1 = 65536;
constexpr unsigned L_W1S = 0;
constexpr unsigned L_XCH = 16384;
constexpr unsigned L_XF = 16384 + 12288;
constexpr unsigned L_W2X = 98304;

struct Params { const u16* x; const u16* wqkv; const u16* wo; const u16* w1; const u16* b1; const u16* w2; const u16* b2; const u16* c; const u16* wmod; u16* out; char* ws; };

DI float bflo(unsigned v) { return __uint_as_float(v << 16); }
DI float bfhi(unsigned v) { return __uint_as_float(v & 0xffff0000u); }
DI unsigned pk(float a, float b) { f2 v = {a, b}; b2 r = __builtin_convertvector(v, b2); return __builtin_bit_cast(unsigned, r); }
DI float rbf(float a) { return bflo(pk(a, 0.f)); }
DI v8s cat(v4s a, v4s b) { return __builtin_shufflevector(a, b, 0, 1, 2, 3, 4, 5, 6, 7); }
DI v4s trd(unsigned a) { return __builtin_amdgcn_ds_read_tr16_b64_v4i16((LAS v4s*)(uintptr_t)a); }
DI v8s asv8(v4u v) { return __builtin_bit_cast(v8s, v); }
DI v4u asv4u(v8s v) { return __builtin_bit_cast(v4u, v); }
DI v8s lds16(unsigned a) { return *(const LAS v8s*)(uintptr_t)a; }
DI f4 ldsf4(unsigned a) { return *(const LAS f4*)(uintptr_t)a; }
DI v2u lds8(unsigned a) { return *(const LAS v2u*)(uintptr_t)a; }
#define MFMA16(a, b, c) __builtin_amdgcn_mfma_f32_16x16x32_bf16((a), (b), (c), 0, 0, 0)
#define MFMA32(a, b, c) __builtin_amdgcn_mfma_f32_32x32x16_bf16((a), (b), (c), 0, 0, 0)
#define BAR() asm volatile("s_waitcnt lgkmcnt(0)\n\ts_barrier" ::: "memory")
#define VM0() asm volatile("s_waitcnt vmcnt(0)" ::: "memory")
#define LG0() asm volatile("s_waitcnt lgkmcnt(0)" ::: "memory")
#define CFENCE() asm volatile("" ::: "memory")
#define SB() __builtin_amdgcn_sched_barrier(0)
DI unsigned opq(unsigned v) { asm volatile("" : "+v"(v)); return v; }
DI void glds(srd_t r, unsigned voff, unsigned soff, unsigned ldsaddr) {
  asm volatile("s_mov_b32 m0, %0\n\ts_nop 0\n\tbuffer_load_dwordx4 %1, %2, %3 offen lds" ::"s"(ldsaddr), "v"(voff), "s"(r), "s"(soff) : "memory");
}
DI void glds1(srd_t r, unsigned voff, unsigned soff, unsigned ldsaddr) {
  asm volatile("s_mov_b32 m0, %0\n\ts_nop 0\n\tbuffer_load_dwordx4 %1, %2, %3 offen sc1 lds" ::"s"(ldsaddr), "v"(voff), "s"(r), "s"(soff) : "memory");
}
DI v4u ld16(srd_t r, unsigned voff, unsigned soff) { return __builtin_amdgcn_raw_buffer_load_b128(r, voff, soff, 16); }
DI v2u ld8(srd_t r, unsigned voff, unsigned soff) { return __builtin_amdgcn_raw_buffer_load_b64(r, voff, soff, 16); }
DI void st16(srd_t r, v4u v, unsigned off) { __builtin_amdgcn_raw_buffer_store_b128(v, r, off, 0, 16); }
DI void st8(srd_t r, v2u v, unsigned off) { __builtin_amdgcn_raw_buffer_store_b64(v, r, off, 0, 16); }
DI float sum16(float v) { auto r = __builtin_amdgcn_permlane16_swap(__float_as_uint(v), __float_as_uint(v), false, false); return __uint_as_float(r[0]) + __uint_as_float(r[1]); }
DI float sum32(float v) { auto r = __builtin_amdgcn_permlane32_swap(__float_as_uint(v), __float_as_uint(v), false, false); return __uint_as_float(r[0]) + __uint_as_float(r[1]); }
DI float max32(float v) { auto r = __builtin_amdgcn_permlane32_swap(__float_as_uint(v), __float_as_uint(v), false, false); return fmaxf(__uint_as_float(r[0]), __uint_as_float(r[1])); }
DI float sumg(float v) { return sum32(sum16(v)); }
#define RLX_AGENT __ATOMIC_RELAXED, __HIP_MEMORY_SCOPE_AGENT
DI void set_flag(char* ws, unsigned off, unsigned tag) { __hip_atomic_store((GAS unsigned*)(ws + off), tag, RLX_AGENT); }
DI void wait_words(char* ws, unsigned off, int n, int lane, unsigned tag) {
  GAS unsigned* p = (GAS unsigned*)(ws + off) + (lane < n ? lane : 0);
  for (unsigned spins = 0; spins < (1u << 17); ++spins) {
    const unsigned v = __hip_atomic_load(p, RLX_AGENT);
    if (__all(v == tag)) break;
  }
  CFENCE();
}
#define STAMP(k)
#define STAMP_FLUSH()
DI float silu(float v) { return v * __builtin_amdgcn_rcpf(1.f + __builtin_amdgcn_exp2f(-1.4426950408889634f * v)); }

DI v8s trfragA(unsigned img, int half, int lane) {
  const int g = lane >> 4, q = (lane & 15) >> 2, pp = lane & 3;
  const unsigned a = img + (4 * g + q) * 64 + ((half ^ (g & 1)) * 32) + pp * 8;
  return cat(trd(a), trd(a + 1024));
}

template <bool FULL>
DI void attention(int t, int hd, int kt0, int lane, f16v& O, srd_t rWS, unsigned wo_src, unsigned w1_src, int w, const u16* xres, v2u& xr0, v2u& xr1, unsigned& stampv) {
  const int qr = lane & 31, h = lane >> 5, blk = (lane >> 4) & 1, q = (lane & 15) >> 2, pp = lane & 3;
  const v8s Qf = lds16(L_AQ + (hd * 2 + h) * 2048 + (32 * t + qr) * 16);
  const unsigned kb = opq(L_AK + (hd * 2 + h) * KPL + (32 * t + qr) * 16);
  const unsigned vb = opq((blk == 0 ? L_AV + hd * VHD + (pp >> 1) * VPL + (pp & 1) * 8 + (4 * h + q) * 16 : L_ONE + ((hd * 128 + (h ? 0 : 128)) & 255)) + t * 512);
  f16v mU, mL;
#pragma unroll
  for (int r = 0; r < 16; ++r) { const int kr = (r & 3) + 8 * (r >> 2) + 4 * h; mU[r] = kr > qr ? -1e30f : 0.f; mL[r] = kr > qr ? 0.f : -1e30f; }
  const f16v zero = {};
#define RIDE(n) do { if ((n) < 8) glds1(rWS, lane * 16, wo_src + (w * 8 + (n)) * 1024, L_WO + (w * 8 + (n)) * 1024); \
                     else glds1(rWS, lane * 16, w1_src + (w * 4 + (n) - 8) * 1024, L_W1 + (w * 4 + (n) - 8) * 1024); } while (0)
  if (FULL) {
    v8s Kf[9];
#pragma unroll
    for (int kt = 0; kt < 9; ++kt) Kf[kt] = lds16(kb + kt * 512);
    f16v S[2];
    float m0 = -3e38f, m1 = -3e38f;
    S[0] = MFMA32(Kf[0], Qf, mL);
    SB();
#pragma unroll
    for (int kt = 0; kt < 9; ++kt) {
      if (kt < 8) S[(kt + 1) & 1] = MFMA32(Kf[kt + 1], Qf, kt + 1 == 8 ? mU : zero);
      if (kt == 2) RIDE(0);
      if (kt == 6) RIDE(1);
#pragma unroll
      for (int r = 0; r < 16; r += 4) { m0 = fmaxf(fmaxf(m0, S[kt & 1][r]), S[kt & 1][r + 1]); m1 = fmaxf(fmaxf(m1, S[kt & 1][r + 2]), S[kt & 1][r + 3]); }
      SB();
    }
    const float m = max32(fmaxf(m0, m1));
    STAMP(23);
    f16v nm;
#pragma unroll
    for (int r = 0; r < 16; ++r) { nm[r] = -m; mU[r] -= m; mL[r] -= m; }
    O = zero;
    v8s V0 = cat(trd(vb), trd(vb + 128)), V1 = cat(trd(vb + 256), trd(vb + 384));
    S[0] = MFMA32(Kf[0], Qf, mL);
    SB();
#pragma unroll
    for (int kt = 0; kt < 9; ++kt) {
      if (kt < 8) S[(kt + 1) & 1] = MFMA32(Kf[kt + 1], Qf, kt + 1 == 8 ? mU : nm);
      RIDE(2 + kt);
      SB();
      v4u P0, P1;
#pragma unroll
      for (int r = 0; r < 4; ++r) P0[r] = pk(__builtin_amdgcn_exp2f(S[kt & 1][2 * r]), __builtin_amdgcn_exp2f(S[kt & 1][2 * r + 1]));
      O = MFMA32(V0, asv8(P0), O);
      if (kt < 8) V0 = cat(trd(vb + (kt + 1) * 512), trd(vb + (kt + 1) * 512 + 128));
      if (kt == 1) RIDE(11);
      if (kt == 3) xr0 = *(const v2u*)xres;
      if (kt == 5) xr1 = *(const v2u*)(xres + 16);
      SB();
#pragma unroll
      for (int r = 0; r < 4; ++r) P1[r] = pk(__builtin_amdgcn_exp2f(S[kt & 1][8 + 2 * r]), __builtin_amdgcn_exp2f(S[kt & 1][8 + 2 * r + 1]));
      O = MFMA32(V1, asv8(P1), O);
      if (kt < 8) V1 = cat(trd(vb + (kt + 1) * 512 + 256), trd(vb + (kt + 1) * 512 + 384));
      SB();
    }
  } else {
#pragma unroll
    for (int n = 0; n < 12; ++n) RIDE(n);
    xr0 = *(const v2u*)xres; xr1 = *(const v2u*)(xres + 16);
    float m = -3e38f;
#pragma unroll
    for (int kt = 0; kt < 9; ++kt) {
      if (kt >= kt0) {
        const f16v S = MFMA32(lds16(kb + kt * 512), Qf, kt == 8 ? mU : zero);
#pragma unroll
        for (int r = 0; r < 16; r += 2) m = fmaxf(fmaxf(m, S[r]), S[r + 1]);
      }
    }
    m = max32(m);
    f16v nm;
#pragma unroll
    for (int r = 0; r < 16; ++r) { nm[r] = -m; mU[r] -= m; }
    O = zero;
#pragma unroll
    for (int kt = 0; kt < 9; ++kt) {
      if (kt >= kt0) {
        const f16v S = MFMA32(lds16(kb + kt * 512), Qf, kt == 8 ? mU : nm);
        v4u P0, P1;
#pragma unroll
        for (int r = 0; r < 4; ++r) {
          P0[r] = pk(__builtin_amdgcn_exp2f(S[2 * r]), __builtin_amdgcn_exp2f(S[2 * r + 1]));
          P1[r] = pk(__builtin_amdgcn_exp2f(S[8 + 2 * r]), __builtin_amdgcn_exp2f(S[8 + 2 * r + 1]));
        }
        const v8s V0 = cat(trd(vb + kt * 512), trd(vb + kt * 512 + 128));
        const v8s V1 = cat(trd(vb + kt * 512 + 256), trd(vb + kt * 512 + 384));
        O = MFMA32(V0, asv8(P0), O);
        O = MFMA32(V1, asv8(P1), O);
      }
    }
  }
#undef RIDE
}

__global__ void __launch_bounds__(512, 2) fused_block(Params p) {
  __shared__ __attribute__((aligned(16))) char smem[163840];
  LAS char* sm = (LAS char*)smem;
  const int tid = threadIdx.x, lane = tid & 63;
  const int w = __builtin_amdgcn_readfirstlane(tid >> 6);
  const int b = blockIdx.x, ci = (b & 7) * 2 + ((b >> 3) & 1), cj = b >> 4;
  const int tok = lane & 15, g = lane >> 4, q4 = (lane & 15) >> 2, pp = lane & 3;
  const int hf = cj & 1, ct = cj >> 1, par = cj & 1;
  const unsigned TAG = 1u;
  unsigned stampv = 0;
  const srd_t rWS = __builtin_amdgcn_make_buffer_rsrc(p.ws, 0, WS_BYTES, 0x00020000);
  const int rA = lane >> 2, pA = lane & 3;
  const unsigned colA = (((pA >> 1) ^ ((rA >> 2) & 1)) * 32) + (pA & 1) * 16;
  const int rB = lane >> 3, pB = lane & 7;
  const unsigned colB = (((pB >> 1) ^ ((rB >> 1) & 3)) * 32) + (pB & 1) * 16;
  STAMP(0);

  const bool m2 = (cj % 3 == 0) && cj < 15;
  const int m2id = ci * 5 + cj / 3;
  v8s sf[16];
  v2u xa[2][2];
  if (w < 4) {
    __builtin_amdgcn_s_setprio(3);
    const srd_t rWmod = __builtin_amdgcn_make_buffer_rsrc((void*)p.wmod, 0, DM * 3072 * 2, 0x00020000);
    const srd_t rX = __builtin_amdgcn_make_buffer_rsrc((void*)p.x, 0, SEQ * DM * 2, 0x00020000);
#pragma unroll
    for (int ii = 0; ii < 8; ++ii) glds(rWmod, rA * 6144 + colA, (w * 8 + ii) * 16 * 6144 + (512 + 32 * cj) * 2, L_CA + (w * 8 + ii) * 1024);
#pragma unroll
    for (int u = 0; u < 2; ++u) glds(rX, rA * 1024 + pA * 16, (128 * ci + 16 * (2 * w + u)) * 1024 + 64 * cj, L_XS + (2 * w + u) * 1024);
  } else if (w == 5) {
    const srd_t r = __builtin_amdgcn_make_buffer_rsrc((void*)p.wqkv, 0, DM * 1536 * 2, 0x00020000);
#pragma unroll
    for (int e = 0; e < 6; ++e) glds(r, rA * 3072 + colA, (32 * ci + 16 * (e & 1)) * 3072 + ((e >> 1) * 512 + 32 * cj) * 2, L_SQ + e * 1024);
  } else if (w == 6) {
    const srd_t r = __builtin_amdgcn_make_buffer_rsrc((void*)p.w1, 0, DM * FF * 2, 0x00020000);
#pragma unroll
    for (int e = 0; e < 6; ++e) glds(r, rA * (FF * 2) + colA, (32 * ci + 16 * (e & 1)) * (FF * 2) + (112 * cj + 16 * par + (e >> 1) * 32) * 2, L_S1 + e * 1024);
    glds(r, (lane >> 1) * (FF * 2) + (lane & 1) * 16, 32 * ci * (FF * 2) + (112 * cj + (par ? 0 : 96)) * 2, L_S1 + 6144);
  } else if (w == 7) {
    if (b < 128) {
      const srd_t r = __builtin_amdgcn_make_buffer_rsrc((void*)p.wo, 0, DM * DM * 2, 0x00020000);
#pragma unroll
      for (int e = 0; e < 4; ++e) glds(r, rB * 1024 + colB, (32 * (b >> 3) + 8 * e) * 1024 + (b & 7) * 128, L_SO + e * 1024);
    }
    if (b < 224) {
      const srd_t r = __builtin_amdgcn_make_buffer_rsrc((void*)p.w2, 0, FF * DM * 2, 0x00020000);
#pragma unroll
      for (int e = 0; e < 8; ++e) { const int id = 2 * b + (e >> 2); glds(r, rB * 1024 + colB, (32 * (id >> 3) + 8 * (e & 3)) * 1024 + (id & 7) * 128, L_SO + 4096 + e * 1024); }
    }
  }
  if (w <= 4) {
    const v4u cr = *(const v4u*)(p.c + 8 * lane);
    const unsigned sc = L_SC + w * 1024;
    *(LAS v4u*)(uintptr_t)(sc + lane * 16) = v4u{pk(silu(bflo(cr[0])), silu(bfhi(cr[0]))), pk(silu(bflo(cr[1])), silu(bfhi(cr[1]))), pk(silu(bflo(cr[2])), silu(bfhi(cr[2]))), pk(silu(bflo(cr[3])), silu(bfhi(cr[3])))};
    LG0();
    if (w < 4) {
#pragma unroll
      for (int s = 0; s < 4; ++s) { const v2u lo = lds8(sc + (32 * (4 * w + s) + 4 * g) * 2), hi = lds8(sc + (32 * (4 * w + s) + 16 + 4 * g) * 2); sf[s] = asv8(v4u{lo[0], lo[1], hi[0], hi[1]}); }
    } else {
#pragma unroll
      for (int s = 0; s < 16; ++s) { const v2u lo = lds8(sc + (32 * s + 4 * g) * 2), hi = lds8(sc + (32 * s + 16 + 4 * g) * 2); sf[s] = asv8(v4u{lo[0], lo[1], hi[0], hi[1]}); }
    }
  }
  v8s rt[12];
  if (w >= 5) {
    VM0();
    if (w == 5) {
#pragma unroll
      for (int nt = 0; nt < 6; ++nt) st16(rWS, asv4u(trfragA(L_SQ + (nt >> 1) * 2048, nt & 1, lane)), WS_TQ + ((cj * 6 + nt) * 16 + ci) * 1024 + lane * 16);
    } else if (w == 6) {
#pragma unroll
      for (int nt = 0; nt < 6; ++nt) rt[nt] = trfragA(L_S1 + (nt >> 1) * 2048, nt & 1, lane);
      { const unsigned a = L_S1 + 6144 + (4 * g + q4) * 32 + pp * 8; rt[6] = cat(trd(a), trd(a + 512)); }
    } else {
      {
        const int row = 16 * (g >> 1) + 4 * (g & 1) + q4;
#pragma unroll
        for (int nt = 0; nt < 4; ++nt) { const unsigned a = L_SO + row * 128 + ((nt ^ ((row >> 1) & 3)) * 32) + pp * 8; rt[nt] = cat(trd(a), trd(a + 1024)); }
      }
      const int row = 4 * g + q4;
#pragma unroll
      for (int e = 0; e < 8; ++e) { const int nt = e & 3; const unsigned a = L_SO + 4096 + (e >> 2) * 4096 + row * 128 + ((nt ^ ((row >> 1) & 3)) * 32) + pp * 8; rt[4 + e] = cat(trd(a), trd(a + 2048)); }
    }
  } else if (w < 4) VM0();
  STAMP(1);
  BAR();
  if (w < 4) {
#pragma unroll
    for (int u = 0; u < 2; ++u) { const unsigned a = L_XS + (2 * w + u) * 1024 + tok * 64 + g * 8; xa[u][0] = lds8(a); xa[u][1] = lds8(a + 32); }
    f4 a0 = {0, 0, 0, 0}, a1 = {0, 0, 0, 0};
    v8s W[4][2];
#pragma unroll
    for (int s = 0; s < 4; ++s) { W[s][0] = trfragA(L_CA + (4 * w + s) * 2048, 0, lane); W[s][1] = trfragA(L_CA + (4 * w + s) * 2048, 1, lane); }
#pragma unroll
    for (int s = 0; s < 4; ++s) { a0 = MFMA16(W[s][0], sf[s], a0); a1 = MFMA16(W[s][1], sf[s], a1); }
    if (tok == 0) { *(LAS f4*)(uintptr_t)(L_CAP + (w * 32 + 4 * g) * 4) = a0; *(LAS f4*)(uintptr_t)(L_CAP + (w * 32 + 16 + 4 * g) * 4) = a1; }
  } else if (w == 5) {
    VM0();
    if (lane == 0) set_flag(p.ws, WS_FQ + (cj * 16 + ci) * 4, TAG);
  }
  STAMP(17);
  BAR();
  float wv[8];
  if (w < 4) {
    f4 a0 = ldsf4(L_CAP + 4 * g * 4), a1 = ldsf4(L_CAP + (16 + 4 * g) * 4);
#pragma unroll
    for (int e = 1; e < 4; ++e) { a0 += ldsf4(L_CAP + (e * 32 + 4 * g) * 4); a1 += ldsf4(L_CAP + (e * 32 + 16 + 4 * g) * 4); }
#pragma unroll
    for (int r = 0; r < 4; ++r) { wv[r] = rbf(1.f + rbf(a0[r])); wv[4 + r] = rbf(1.f + rbf(a1[r])); }
  }
  BAR();
  STAMP(19);
  if (w < 4) {
#pragma unroll
    for (int u = 0; u < 2; ++u) {
      const float x0 = bflo(xa[u][0][0]), x1 = bfhi(xa[u][0][0]), x2 = bflo(xa[u][0][1]), x3 = bfhi(xa[u][0][1]);
      const float x4 = bflo(xa[u][1][0]), x5 = bfhi(xa[u][1][0]), x6 = bflo(xa[u][1][1]), x7 = bfhi(xa[u][1][1]);
      const int T = 2 * w + u;
      st16(rWS, v4u{pk(x0 * wv[0], x1 * wv[1]), pk(x2 * wv[2], x3 * wv[3]), pk(x4 * wv[4], x5 * wv[5]), pk(x6 * wv[6], x7 * wv[7])},
           WS_HF + ((ci * 8 + T) * 16 + cj) * 1024 + lane * 16);
      const float s1 = sumg(((x0 + x1) + (x2 + x3)) + ((x4 + x5) + (x6 + x7)));
      const float s2 = sumg(((x0 * x0 + x1 * x1) + (x2 * x2 + x3 * x3)) + ((x4 * x4 + x5 * x5) + (x6 * x6 + x7 * x7)));
      if (g == 0) st8(rWS, v2u{__float_as_uint(s1), __float_as_uint(s2)}, WS_ST1 + ((ci * 16 + cj) * 128 + 16 * T + tok) * 8);
    }
    if (w == 0 && tok == 0) {
      st16(rWS, v4u{__float_as_uint(wv[0]), __float_as_uint(wv[1]), __float_as_uint(wv[2]), __float_as_uint(wv[3])}, WS_WV + ci * 2048 + cj * 128 + g * 16);
      st16(rWS, v4u{__float_as_uint(wv[4]), __float_as_uint(wv[5]), __float_as_uint(wv[6]), __float_as_uint(wv[7])}, WS_WV + ci * 2048 + cj * 128 + 64 + g * 16);
    }
    STAMP(21);
    VM0();
    if (lane == 0) set_flag(p.ws, WS_FLAG + 0 * 8192 + ci * 512 + (cj * 4 + w) * 4, TAG);
    __builtin_amdgcn_s_setprio(0);
  } else if (w == 4) {
    if (m2) {
      const srd_t rWmod = __builtin_amdgcn_make_buffer_rsrc((void*)p.wmod, 0, DM * 3072 * 2, 0x00020000);
      const int mcol = m2id < 16 ? m2id * 32 : 1024 + (m2id - 16) * 32;
#pragma unroll 8
      for (int ii = 0; ii < 32; ++ii) glds(rWmod, rA * 6144 + colA, ii * 16 * 6144 + mcol * 2, L_M2 + ii * 1024);
      VM0();
      f4 a0 = {0, 0, 0, 0}, a1 = {0, 0, 0, 0};
#pragma unroll
      for (int s0 = 0; s0 < 16; s0 += 4) {
        v8s W[4][2];
#pragma unroll
        for (int s = 0; s < 4; ++s) { W[s][0] = trfragA(L_M2 + (s0 + s) * 2048, 0, lane); W[s][1] = trfragA(L_M2 + (s0 + s) * 2048, 1, lane); }
#pragma unroll
        for (int s = 0; s < 4; ++s) { a0 = MFMA16(W[s][0], sf[s0 + s], a0); a1 = MFMA16(W[s][1], sf[s0 + s], a1); }
      }
      if (tok == 0) {
        st8(rWS, v2u{pk(a0[0], a0[1]), pk(a0[2], a0[3])}, WS_MOD + mcol * 2 + g * 8);
        st8(rWS, v2u{pk(a1[0], a1[1]), pk(a1[2], a1[3])}, WS_MOD + mcol * 2 + 32 + g * 8);
      }
      VM0();
      if (lane == 0) set_flag(p.ws, WS_FM + m2id * 4, TAG);
    }
    wait_words(p.ws, WS_FLAG + 0 * 8192 + ci * 512, 64, lane, TAG);
  } else {
    wait_words(p.ws, WS_FQ + cj * 64, 16, lane, TAG);
#pragma unroll
    for (int s = 0; s < 16; ++s)
#pragma unroll
      for (int e = 0; e < 2; ++e) { const int nt = (w - 5) * 2 + e; glds1(rWS, lane * 16, WS_TQ + ((cj * 6 + nt) * 16 + s) * 1024, L_WQKV + (nt * 16 + s) * 1024); }
    if (w == 6) {
#pragma unroll
      for (int nt = 0; nt < 7; ++nt) st16(rWS, asv4u(rt[nt]), WS_TW1 + ((cj * 7 + nt) * 16 + ci) * 1024 + lane * 16);
    } else if (w == 7) {
      if (b < 128) {
#pragma unroll
        for (int nt = 0; nt < 4; ++nt) st16(rWS, asv4u(rt[nt]), WS_TWO + (((b & 7) * 16 + (b >> 3)) * 4 + nt) * 1024 + lane * 16);
      }
      if (b < 224) {
#pragma unroll
        for (int e = 0; e < 8; ++e) { const int id = 2 * b + (e >> 2); st16(rWS, asv4u(rt[4 + e]), WS_TW2 + (((id & 7) * 56 + (id >> 3)) * 4 + (e & 3)) * 1024 + lane * 16); }
      }
    }
  }
  STAMP(2);
  VM0();
  if (lane == 0) {
    if (w == 6) set_flag(p.ws, WS_FW1 + (cj * 16 + ci) * 4, TAG);
    if (w == 7) {
      if (b < 128) set_flag(p.ws, WS_FWO + ((b & 7) * 16 + (b >> 3)) * 4, TAG);
      if (b < 224) { set_flag(p.ws, WS_FW2 + (((2 * b) & 7) * 64 + ((2 * b) >> 3)) * 4, TAG); set_flag(p.ws, WS_FW2 + (((2 * b + 1) & 7) * 64 + ((2 * b + 1) >> 3)) * 4, TAG); }
    }
  }
  BAR();
  STAMP(3);

  f4 accQ[2], accKV[4];
  float rstd, nmu;
  v2u gaR[2], cbR[2], sbR[2], gbR[2], b2R[2];
  v8s aux2[2];
  float Cw, Cw2, Cws, Cs, Cs2;
  const int tt = w & 3, kh = w >> 2;
  const int ocol = 64 * ct + 32 * kh + 4 * g;
  {
    const unsigned bq = opq(L_WQKV + lane * 16), bv = opq(L_WQKV + 65536 + lane * 16);
#define QF(nt, s) lds16(((nt) < 4 ? bq : bv) + ((nt) & 3) * 16384 + (s) * 1024)
    v8s act[16];
    const unsigned hsrc = WS_HF + (ci * 8 + w) * 16 * 1024;
    v4u wl[2], wh[2];
#pragma unroll
    for (int u = 0; u < 2; ++u) { const unsigned o = (32 * (2 * w + u) + 4 * g) * 4; wl[u] = ld16(rWS, o, WS_WV + ci * 2048); wh[u] = ld16(rWS, o + 64, WS_WV + ci * 2048); }
#pragma unroll
    for (int s = 0; s < 6; ++s) act[s] = asv8(ld16(rWS, lane * 16, hsrc + s * 1024));
    {
      f4 au[6];
      v8s W[2][6];
      const unsigned c0 = opq(bq + w * 2048), d0 = opq(bv + w * 2048);
#pragma unroll
      for (int u = 0; u < 2; ++u)
#pragma unroll
        for (int nt = 0; nt < 6; ++nt) W[u][nt] = lds16((nt < 4 ? c0 : d0) + (nt & 3) * 16384 + u * 1024);
#pragma unroll
      for (int nt = 0; nt < 6; ++nt) au[nt] = f4{0, 0, 0, 0};
#pragma unroll
      for (int u = 0; u < 2; ++u) {
        const v8s a = asv8(v4u{pk(__uint_as_float(wl[u][0]), __uint_as_float(wl[u][1])), pk(__uint_as_float(wl[u][2]), __uint_as_float(wl[u][3])),
                               pk(__uint_as_float(wh[u][0]), __uint_as_float(wh[u][1])), pk(__uint_as_float(wh[u][2]), __uint_as_float(wh[u][3]))});
#pragma unroll
        for (int nt = 0; nt < 6; ++nt) au[nt] = MFMA16(W[u][nt], a, au[nt]);
      }
      if (tok == 0) {
#pragma unroll
        for (int nt = 0; nt < 6; ++nt) *(LAS f4*)(sm + L_PARTU + (w * 96 + nt * 16 + 4 * g) * 4) = au[nt];
      }
    }
    BAR();
    if (tid < 96) {
      float s = 0;
#pragma unroll
      for (int e = 0; e < 8; ++e) s += *(const LAS float*)(sm + L_PARTU + (e * 96 + tid) * 4);
      *(LAS float*)(sm + L_PERS + tid * 4) = s;
    }
    STAMP(16);
    v2u st[4];
#pragma unroll
    for (int nt = 0; nt < 4; ++nt) accKV[nt] = f4{0, 0, 0, 0};
    {
      v8s W[2][4];
#pragma unroll
      for (int s = 0; s < 2; ++s)
#pragma unroll
        for (int nt = 0; nt < 4; ++nt) W[s][nt] = QF(2 + nt, s);
      SB();
#pragma unroll
      for (int s = 0; s < 16; ++s) {
        if (s + 6 < 16) act[s + 6] = asv8(ld16(rWS, lane * 16, hsrc + (s + 6) * 1024));
        else if (s < 14) st[s - 10] = ld8(rWS, ((4 * g + s - 10) * 128 + 16 * w + tok) * 8, WS_ST1 + ci * 16 * 128 * 8);
#pragma unroll
        for (int nt = 0; nt < 4; ++nt) {
          accKV[nt] = MFMA16(W[s & 1][nt], act[s], accKV[nt]);
          if (s + 2 < 16) W[s & 1][nt] = QF(2 + nt, s + 2);
          SB();
        }
      }
    }
    float s1 = 0, s2 = 0;
#pragma unroll
    for (int e = 0; e < 4; ++e) { s1 += __uint_as_float(st[e][0]); s2 += __uint_as_float(st[e][1]); }
    s1 = sumg(s1); s2 = sumg(s2);
    const float mu = s1 * (1.f / 512), var = fmaxf(s2 * (1.f / 512) - mu * mu, 0.f);
    rstd = __builtin_amdgcn_rsqf(var + 1e-5f); nmu = -rstd * mu;
    if (w == 4) {
      GAS unsigned* f0 = (GAS unsigned*)(p.ws + WS_FM) + lane;
      GAS unsigned* f1 = (GAS unsigned*)(p.ws + (lane < 16 ? WS_FM + 256 + lane * 4 : lane < 32 ? WS_FW1 + cj * 64 + (lane - 16) * 4 : WS_FWO + ct * 64 + (lane & 15) * 4));
      GAS unsigned* f2 = (GAS unsigned*)(p.ws + WS_FW2 + ct * 256) + (lane < 56 ? lane : 0);
      for (unsigned spins = 0; spins < (1u << 17); ++spins) {
        const unsigned a = __hip_atomic_load(f0, RLX_AGENT), c = __hip_atomic_load(f1, RLX_AGENT), d = __hip_atomic_load(f2, RLX_AGENT);
        if (__all(a == TAG && c == TAG && d == TAG)) break;
      }
      CFENCE();
    }
    STAMP(4);
    BAR();
    {
      v4u kk, vv;
      unsigned pkd[4][2];
#pragma unroll
      for (int nt = 0; nt < 4; ++nt) {
        const f4 u = ldsf4(L_PERS + (32 + nt * 16 + 4 * g) * 4);
        pkd[nt][0] = pk(rstd * accKV[nt][0] + nmu * u[0], rstd * accKV[nt][1] + nmu * u[1]);
        pkd[nt][1] = pk(rstd * accKV[nt][2] + nmu * u[2], rstd * accKV[nt][3] + nmu * u[3]);
      }
      { auto r0 = __builtin_amdgcn_permlane16_swap(pkd[0][0], pkd[1][0], false, false); auto r1 = __builtin_amdgcn_permlane16_swap(pkd[0][1], pkd[1][1], false, false);
        kk = v4u{r0[0], r1[0], r0[1], r1[1]}; }
      { auto r0 = __builtin_amdgcn_permlane16_swap(pkd[2][0], pkd[3][0], false, false); auto r1 = __builtin_amdgcn_permlane16_swap(pkd[2][1], pkd[3][1], false, false);
        vv = v4u{r0[0], r1[0], r0[1], r1[1]}; }
      const int hd = g & 1, pl = g >> 1;
      st16(rWS, kk, WS_KV + (ci * 16 + cj) * 16384 + ((0 * 2 + hd) * 2 + pl) * 2048 + (16 * w + tok) * 16);
      st16(rWS, vv, WS_KV + (ci * 16 + cj) * 16384 + ((1 * 2 + hd) * 2 + pl) * 2048 + (16 * w + tok) * 16);
      *(LAS v4u*)(sm + L_AK + (hd * 2 + pl) * KPL + (256 + 16 * w + tok) * 16) = kk;
      *(LAS v4u*)(sm + L_AV + hd * VHD + pl * VPL + (256 + 16 * w + tok) * 16) = vv;
    }
    if (w < 6) glds1(rWS, lane * 16, WS_MOD + w * 1024, L_MODC + w * 1024);
    if (w == 6 && lane < 8) *(LAS v4u*)(sm + L_B2C + lane * 16) = *(const v4u*)(p.b2 + 64 * ct + lane * 8);
#pragma unroll
    for (int nt = 0; nt < 2; ++nt) accQ[nt] = f4{0, 0, 0, 0};
    {
      v8s W[4][2];
#pragma unroll
      for (int s = 0; s < 4; ++s) { W[s][0] = QF(0, s); W[s][1] = QF(1, s); }
      SB();
#pragma unroll
      for (int s = 0; s < 16; ++s)
#pragma unroll
        for (int nt = 0; nt < 2; ++nt) {
          accQ[nt] = MFMA16(W[s & 3][nt], act[s], accQ[nt]);
          if (s + 4 < 16) W[s & 3][nt] = QF(nt, s + 4);
          SB();
        }
    }
    VM0();
    BAR();
    if (tid == 256) set_flag(p.ws, WS_FLAG + 1 * 8192 + ci * 512 + cj * 4, TAG);
    STAMP(5);
    {
      f4 av[4];
      v8s W[2][4], a[2];
      const unsigned c0 = opq(bq + w * 2048), d0 = opq(bv + w * 2048);
#pragma unroll
      for (int u = 0; u < 2; ++u) {
        const v2u lo = lds8(L_MODC + (32 * (2 * w + u) + 4 * g) * 2), hi = lds8(L_MODC + (32 * (2 * w + u) + 16 + 4 * g) * 2);
        a[u] = asv8(v4u{lo[0], lo[1], hi[0], hi[1]});
#pragma unroll
        for (int nt = 0; nt < 4; ++nt) W[u][nt] = lds16((nt < 2 ? c0 : d0) + (nt & 1) * 16384 + u * 1024);
      }
#pragma unroll
      for (int nt = 0; nt < 4; ++nt) av[nt] = f4{0, 0, 0, 0};
#pragma unroll
      for (int u = 0; u < 2; ++u)
#pragma unroll
        for (int nt = 0; nt < 4; ++nt) av[nt] = MFMA16(W[u][nt], a[u], av[nt]);
      if (tok == 0) {
#pragma unroll
        for (int nt = 0; nt < 4; ++nt) *(LAS f4*)(sm + L_PARTV + (w * 64 + nt * 16 + 4 * g) * 4) = av[nt];
      }
    }
    {
#pragma unroll
      for (int e = 0; e < 2; ++e) {
        gaR[e] = lds8(L_MODC + (1024 + ocol + 16 * e) * 2); sbR[e] = lds8(L_MODC + (1536 + ocol + 16 * e) * 2);
        cbR[e] = lds8(L_MODC + (2048 + ocol + 16 * e) * 2); gbR[e] = lds8(L_MODC + (2560 + ocol + 16 * e) * 2);
        b2R[e] = lds8(L_B2C + (32 * kh + 4 * g + 16 * e) * 2);
      }
      const v4u c0 = asv4u(lds16(L_MODC + (2048 + 8 * lane) * 2)), d0 = asv4u(lds16(L_MODC + (1536 + 8 * lane) * 2));
      Cw = Cw2 = Cws = Cs = Cs2 = 0;
#pragma unroll
      for (int r = 0; r < 4; ++r) {
        const float w0 = rbf(1.f + bflo(c0[r])), w1 = rbf(1.f + bfhi(c0[r])), s0 = bflo(d0[r]), s1_ = bfhi(d0[r]);
        Cw += w0 + w1; Cw2 += w0 * w0 + w1 * w1; Cws += w0 * s0 + w1 * s1_; Cs += s0 + s1_; Cs2 += s0 * s0 + s1_ * s1_;
      }
#pragma unroll
      for (int o = 1; o < 16; o <<= 1) { Cw += __shfl_xor(Cw, o); Cw2 += __shfl_xor(Cw2, o); Cws += __shfl_xor(Cws, o); Cs += __shfl_xor(Cs, o); Cs2 += __shfl_xor(Cs2, o); }
      Cw = sumg(Cw); Cw2 = sumg(Cw2); Cws = sumg(Cws); Cs = sumg(Cs); Cs2 = sumg(Cs2);
#pragma unroll
      for (int u = 0; u < 2; ++u) {
        const unsigned o = (32 * (2 * w + u) + 4 * g) * 2 + L_MODC + (tok == 1 ? 1536 : 2048) * 2;
        const v2u lo = lds8(o), hi = lds8(o + 32);
        v4u t;
        t[0] = tok == 0 ? pk(1.f + bflo(lo[0]), 1.f + bfhi(lo[0])) : (tok == 1 ? lo[0] : 0x3f803f80u);
        t[1] = tok == 0 ? pk(1.f + bflo(lo[1]), 1.f + bfhi(lo[1])) : (tok == 1 ? lo[1] : 0x3f803f80u);
        t[2] = tok == 0 ? pk(1.f + bflo(hi[0]), 1.f + bfhi(hi[0])) : (tok == 1 ? hi[0] : 0x3f803f80u);
        t[3] = tok == 0 ? pk(1.f + bflo(hi[1]), 1.f + bfhi(hi[1])) : (tok == 1 ? hi[1] : 0x3f803f80u);
        aux2[u] = asv8(t);
      }
    }
    if (tid < 400) *(LAS v4u*)(sm + L_ONE + tid * 16) = v4u{0x3f803f80u, 0x3f803f80u, 0x3f803f80u, 0x3f803f80u};
    BAR();
    if (tid < 64) {
      float s = 0;
#pragma unroll
      for (int e = 0; e < 8; ++e) s += *(const LAS float*)(sm + L_PARTV + (e * 64 + tid) * 4);
      *(LAS float*)(sm + L_PERS + (96 + tid) * 4) = s;
    }
    if (ci < 2) {
      for (int e = tid; e < 8 * 256; e += 512) {
        const int pln = e >> 8, key = e & 255;
        if (ci == 0 || key < 128) {
          const unsigned a = pln < 4 ? L_AK + pln * KPL : L_AV + ((pln - 4) >> 1) * VHD + (pln & 1) * VPL;
          *(LAS v4u*)(sm + a + key * 16) = v4u{0, 0, 0, 0};
        }
      }
    }
    if (w == 4) {
      if (ci >= 1) wait_words(p.ws, WS_FLAG + 1 * 8192 + (ci - 1) * 512 + cj * 4, 1, lane, TAG);
      if (ci >= 2) wait_words(p.ws, WS_FLAG + 1 * 8192 + (ci - 2) * 512 + cj * 4, 1, lane, TAG);
    }
    BAR();
    STAMP(6);
    {
      const unsigned dst = w < 4 ? L_AK + w * KPL : L_AV + ((w - 4) >> 1) * VHD + (w & 1) * VPL;
#pragma unroll
      for (int src = 0; src < 2; ++src)
        if (ci - 2 + src >= 0) {
#pragma unroll
          for (int e = 0; e < 2; ++e) glds1(rWS, lane * 16, WS_KV + ((ci - 2 + src) * 16 + cj) * 16384 + w * 2048 + e * 1024, dst + (src * 128 + e * 64) * 16);
        }
    }
    {
      unsigned pq[2][2];
      const float qs = 0.25f * 1.4426950408889634f;
#pragma unroll
      for (int nt = 0; nt < 2; ++nt) {
        const f4 u = ldsf4(L_PERS + (nt * 16 + 4 * g) * 4), v = ldsf4(L_PERS + (96 + nt * 16 + 4 * g) * 4);
        pq[nt][0] = pk(qs * (rstd * accQ[nt][0] + nmu * u[0] + v[0]), qs * (rstd * accQ[nt][1] + nmu * u[1] + v[1]));
        pq[nt][1] = pk(qs * (rstd * accQ[nt][2] + nmu * u[2] + v[2]), qs * (rstd * accQ[nt][3] + nmu * u[3] + v[3]));
      }
      auto r0 = __builtin_amdgcn_permlane16_swap(pq[0][0], pq[1][0], false, false); auto r1 = __builtin_amdgcn_permlane16_swap(pq[0][1], pq[1][1], false, false);
      *(LAS v4u*)(sm + L_AQ + ((g & 1) * 2 + (g >> 1)) * 2048 + (16 * w + tok) * 16) = v4u{r0[0], r1[0], r0[1], r1[1]};
    }
    VM0();
    BAR();
    STAMP(7);
  }

  const int orow = 128 * ci + 64 * hf + 16 * tt + tok;
  v2u xr0, xr1;
  {
    const int t = w & 3, hd = w >> 2;
    const int kt0 = ci >= 2 ? 0 : 8 - 4 * ci - t;
    f16v O;
    const u16* xres = p.x + (size_t)orow * DM + ocol;
    if (ci >= 2) attention<true>(t, hd, 0, lane, O, rWS, WS_TWO + ct * 65536, WS_TW1 + cj * 7 * 16384, w, xres, xr0, xr1, stampv);
    else attention<false>(t, hd, kt0, lane, O, rWS, WS_TWO + ct * 65536, WS_TW1 + cj * 7 * 16384, w, xres, xr0, xr1, stampv);
    const int h = lane >> 5;
    const float inv = __builtin_amdgcn_rcpf(O[8]);
    const f4 b0 = ldsf4(L_PERS + (96 + 32 + hd * 16 + 4 * h) * 4), b1 = ldsf4(L_PERS + (96 + 32 + hd * 16 + 8 + 4 * h) * 4);
    const v4u o = {pk(O[0] * inv + b0[0], O[1] * inv + b0[1]), pk(O[2] * inv + b0[2], O[3] * inv + b0[3]),
                   pk(O[4] * inv + b1[0], O[5] * inv + b1[1]), pk(O[6] * inv + b1[2], O[7] * inv + b1[3])};
    const int qr = lane & 31;
    st16(rWS, o, WS_OF + (ci * 8 * 16 + cj) * 1024 + ((2 * t + (qr >> 4)) * 16) * 1024 + ((2 * hd + h) * 16 + (qr & 15)) * 16);
  }
  STAMP(24);
  VM0();
  BAR();
  if (tid == 256) set_flag(p.ws, WS_FLAG + 2 * 8192 + ci * 512 + cj * 4, TAG);
  STAMP(8);
  if (w < 4) {
#pragma unroll
    for (int ii = 0; ii < 16; ++ii) glds1(rWS, lane * 16, WS_TW1 + (cj * 7 + 2 + w) * 16384 + ii * 1024, L_W1 + (2 + w) * 16384 + ii * 1024);
  }
  if (w == 4) wait_words(p.ws, WS_FLAG + 2 * 8192 + ci * 512, 16, lane, TAG);
  BAR();
  STAMP(9);

  float x1[8];
  {
    v8s oa[8];
    const unsigned osrc = WS_OF + ((ci * 8 + 4 * hf + tt) * 16 + 8 * kh) * 1024;
#pragma unroll
    for (int s = 0; s < 8; ++s) oa[s] = asv8(ld16(rWS, lane * 16, osrc + s * 1024));
    const unsigned bo = opq(L_WO + kh * 32768 + lane * 16);
    f4 acc[4];
#pragma unroll
    for (int nt = 0; nt < 4; ++nt) acc[nt] = f4{0, 0, 0, 0};
    {
      v8s W[2][4];
#pragma unroll
      for (int s = 0; s < 2; ++s)
#pragma unroll
        for (int nt = 0; nt < 4; ++nt) W[s][nt] = lds16(bo + (s * 4 + nt) * 1024);
      SB();
#pragma unroll
      for (int s = 0; s < 8; ++s)
#pragma unroll
        for (int nt = 0; nt < 4; ++nt) {
          acc[nt] = MFMA16(W[s & 1][nt], oa[s], acc[nt]);
          if (s + 2 < 8) W[s & 1][nt] = lds16(bo + ((s + 2) * 4 + nt) * 1024);
          SB();
        }
    }
    STAMP(26);
    VM0();
    BAR();
    *(LAS f4*)(sm + L_XCH + w * 2048 + lane * 16) = kh ? acc[0] : acc[2];
    *(LAS f4*)(sm + L_XCH + w * 2048 + 1024 + lane * 16) = kh ? acc[1] : acc[3];
    BAR();
    const f4 o0 = ldsf4(L_XCH + (w ^ 4) * 2048 + lane * 16), o1 = ldsf4(L_XCH + (w ^ 4) * 2048 + 1024 + lane * 16);
    const f4 t0 = (kh ? acc[2] : acc[0]) + o0, t1 = (kh ? acc[3] : acc[1]) + o1;
    const float xin[8] = {bflo(xr0[0]), bfhi(xr0[0]), bflo(xr0[1]), bfhi(xr0[1]), bflo(xr1[0]), bfhi(xr1[0]), bflo(xr1[1]), bfhi(xr1[1])};
    const float ga[8] = {bflo(gaR[0][0]), bfhi(gaR[0][0]), bflo(gaR[0][1]), bfhi(gaR[0][1]), bflo(gaR[1][0]), bfhi(gaR[1][0]), bflo(gaR[1][1]), bfhi(gaR[1][1])};
    const float cb[8] = {bflo(cbR[0][0]), bfhi(cbR[0][0]), bflo(cbR[0][1]), bfhi(cbR[0][1]), bflo(cbR[1][0]), bfhi(cbR[1][0]), bflo(cbR[1][1]), bfhi(cbR[1][1])};
    const float sv[8] = {bflo(sbR[0][0]), bfhi(sbR[0][0]), bflo(sbR[0][1]), bfhi(sbR[0][1]), bflo(sbR[1][0]), bfhi(sbR[1][0]), bflo(sbR[1][1]), bfhi(sbR[1][1])};
    float wv[8];
#pragma unroll
    for (int r = 0; r < 8; ++r) { x1[r] = rbf(xin[r] + ga[r] * (r < 4 ? t0[r & 3] : t1[r & 3])); wv[r] = rbf(1.f + cb[r]); }
    st16(rWS, v4u{pk(x1[0] * wv[0], x1[1] * wv[1]), pk(x1[2] * wv[2], x1[3] * wv[3]), pk(x1[4] * wv[4], x1[5] * wv[5]), pk(x1[6] * wv[6], x1[7] * wv[7])},
         WS_X1F + ((ci * 8 + 4 * hf + tt) * 16 + 2 * ct + kh) * 1024 + lane * 16);
    float s1 = 0, s2 = 0, s3 = 0, s4 = 0, s5 = 0, s6 = 0;
#pragma unroll
    for (int r = 0; r < 8; ++r) { const float xw = x1[r] * wv[r]; s1 += x1[r]; s2 += x1[r] * x1[r]; s3 += xw; s4 += xw * wv[r]; s5 += xw * xw; s6 += xw * sv[r]; }
    s1 = sumg(s1); s2 = sumg(s2); s3 = sumg(s3); s4 = sumg(s4); s5 = sumg(s5); s6 = sumg(s6);
    const unsigned rec = (ci * 16 + 2 * ct + kh) * 128 + 64 * hf + 16 * tt + tok;
    if (g == 0) st16(rWS, v4u{__float_as_uint(s1), __float_as_uint(s2), __float_as_uint(s3), __float_as_uint(s4)}, WS_ST2A + rec * 16);
    if (g == 1) st8(rWS, v2u{__float_as_uint(s5), __float_as_uint(s6)}, WS_ST2B + rec * 8);
  }
  VM0();
  BAR();
  if (tid == 256) set_flag(p.ws, WS_FLAG + 3 * 8192 + ci * 512 + cj * 4, TAG);
  STAMP(10);
  if (w < 4) {
#pragma unroll
    for (int ii = 0; ii < 4; ++ii) glds1(rWS, lane * 16, WS_TW1 + (cj * 7 + 6) * 16384 + (w * 4 + ii) * 1024, L_W1S + (w * 4 + ii) * 1024);
#pragma unroll
    for (int ii = 0; ii < 8; ++ii) { const int pc = w * 8 + ii; glds1(rWS, lane * 16, WS_TW2 + ct * 229376 + (pc >> 4) * 7 * 16384 + (pc & 15) * 1024, 32768 + pc * 1024); }
  }
  float b1v = 0.f;
  if (tid < 112) { const int nt = tid >> 4; b1v = bflo(p.b1[(nt == 6 ? 112 * cj + (par ? 0 : 96) : 112 * cj + 16 * par + nt * 16) + (tid & 15)]); }
  if (w == 4) wait_words(p.ws, WS_FLAG + 3 * 8192 + ci * 512, 16, lane, TAG);
  VM0();
  BAR();
  STAMP(11);

  v4u stg[8];
  {
    const unsigned bP = opq(L_W1 + lane * 16), bR = opq(L_W1 + 65536 + lane * 16), bS = opq(L_W1S + lane * 16);
#define W1F(nt, s) lds16((nt) == 6 ? bS + (s) * 1024 : (nt) < 4 ? bP + (nt) * 16384 + (s) * 1024 : bR + ((nt) - 4) * 16384 + (s) * 1024)
    v8s act2[16];
    const unsigned asrc = WS_X1F + (ci * 8 + w) * 16 * 1024;
#pragma unroll
    for (int s = 0; s < 6; ++s) act2[s] = asv8(ld16(rWS, lane * 16, asrc + s * 1024));
    {
      f4 au[7];
#pragma unroll
      for (int nt = 0; nt < 7; ++nt) au[nt] = f4{0, 0, 0, 0};
      const unsigned c0 = opq(bP + w * 2048), d0 = opq(bR + w * 2048), e0 = opq(bS + w * 2048);
#pragma unroll
      for (int u = 0; u < 2; ++u) {
        v8s W[7];
#pragma unroll
        for (int nt = 0; nt < 7; ++nt) W[nt] = lds16(nt == 6 ? e0 + u * 1024 : nt < 4 ? c0 + nt * 16384 + u * 1024 : d0 + (nt - 4) * 16384 + u * 1024);
#pragma unroll
        for (int nt = 0; nt < 7; ++nt) au[nt] = MFMA16(W[nt], aux2[u], au[nt]);
      }
      if (tok < 3) {
#pragma unroll
        for (int nt = 0; nt < 7; ++nt) *(LAS f4*)(sm + L_XCH + ((w * 3 + tok) * 112 + nt * 16 + 4 * g) * 4) = au[nt];
      }
    }
    BAR();
    if (tid < 336) {
      float s = 0;
#pragma unroll
      for (int e = 0; e < 8; ++e) s += *(const LAS float*)(sm + L_XCH + (e * 336 + tid) * 4);
      *(LAS float*)(sm + L_XF + tid * 4) = s;
    }
    if (tid < 112) *(LAS float*)(sm + L_XF + (336 + tid) * 4) = b1v;
    STAMP(18);
    v4u sa[4]; v2u sb[4];
    f4 acc[7];
#pragma unroll
    for (int nt = 0; nt < 7; ++nt) acc[nt] = f4{0, 0, 0, 0};
    {
      v8s W[7];
#pragma unroll
      for (int nt = 0; nt < 7; ++nt) W[nt] = W1F(nt, 0);
      SB();
#pragma unroll
      for (int s = 0; s < 16; ++s) {
        if (s + 6 < 16) act2[s + 6] = asv8(ld16(rWS, lane * 16, asrc + (s + 6) * 1024));
        if (s >= 4 && s < 8) sa[s - 4] = ld16(rWS, ((4 * g + s - 4) * 128 + 16 * w + tok) * 16, WS_ST2A + ci * 16 * 128 * 16);
        if (s >= 10 && s < 14) sb[s - 10] = ld8(rWS, ((4 * g + s - 10) * 128 + 16 * w + tok) * 8, WS_ST2B + ci * 16 * 128 * 8);
        if (s >= 8) { const int e = s - 8, pc = w * 8 + e; const int chunk = (pc >> 4) == 0 ? 5 : ((pc >> 4) == 1 ? 12 : ((pc >> 4) == 2 ? 6 : 13));
          stg[e] = ld16(rWS, lane * 16, WS_TW2 + ct * 229376 + chunk * 16384 + (pc & 15) * 1024); }
#pragma unroll
        for (int nt = 0; nt < 7; ++nt) {
          acc[nt] = MFMA16(W[nt], act2[s], acc[nt]);
          if (s + 1 < 16) W[nt] = W1F(nt, s + 1);
          SB();
        }
      }
    }
    STAMP(20);
    float s1 = 0, s2 = 0, s3 = 0, s4 = 0, s5 = 0, s6 = 0;
#pragma unroll
    for (int e = 0; e < 4; ++e) { s1 += __uint_as_float(sa[e][0]); s2 += __uint_as_float(sa[e][1]); s3 += __uint_as_float(sa[e][2]); s4 += __uint_as_float(sa[e][3]); s5 += __uint_as_float(sb[e][0]); s6 += __uint_as_float(sb[e][1]); }
    s1 = sumg(s1); s2 = sumg(s2); s3 = sumg(s3); s4 = sumg(s4); s5 = sumg(s5); s6 = sumg(s6);
    const float mu1 = s1 * (1.f / 512), var1 = fmaxf(s2 * (1.f / 512) - mu1 * mu1, 0.f), r1 = __builtin_amdgcn_rsqf(var1 + 1e-5f);
    const float mu2 = (r1 * (s3 - mu1 * Cw) + Cs) * (1.f / 512);
    const float hh = (r1 * r1 * (s5 - 2.f * mu1 * s4 + mu1 * mu1 * Cw2) + 2.f * r1 * (s6 - mu1 * Cws) + Cs2) * (1.f / 512);
    const float r2 = __builtin_amdgcn_rsqf(fmaxf(hh - mu2 * mu2, 0.f) + 1e-5f);
    const float cA = r2 * r1, cB = -cA * mu1, cC = r2, cD = -r2 * mu2;
    BAR();
    STAMP(12);
    unsigned hp[7][2];
#pragma unroll
    for (int nt = 0; nt < 7; ++nt) {
      const f4 u = ldsf4(L_XF + (nt * 16 + 4 * g) * 4), v = ldsf4(L_XF + (112 + nt * 16 + 4 * g) * 4), t = ldsf4(L_XF + (224 + nt * 16 + 4 * g) * 4), bias = ldsf4(L_XF + (336 + nt * 16 + 4 * g) * 4);
      float o[4];
#pragma unroll
      for (int r = 0; r < 4; ++r) {
        const float z = cA * acc[nt][r] + (cB * u[r] + (cC * v[r] + (cD * t[r] + bias[r])));
        const float pz = z * (-2.3022081985f - 0.1029432397f * z * z);
        o[r] = z * __builtin_amdgcn_rcpf(1.f + __builtin_amdgcn_exp2f(pz));
      }
      hp[nt][0] = pk(o[0], o[1]); hp[nt][1] = pk(o[2], o[3]);
    }
    const unsigned S0 = (7 * cj + par) >> 1;
    const unsigned hb = WS_H1F + (ci * 8 + w) * 56 * 1024;
#pragma unroll
    for (int pr = 0; pr < 3; ++pr) st16(rWS, v4u{hp[2 * pr][0], hp[2 * pr][1], hp[2 * pr + 1][0], hp[2 * pr + 1][1]}, hb + (S0 + pr) * 1024 + lane * 16);
    { const unsigned NT = 7 * cj + (par ? 0 : 6); st8(rWS, v2u{hp[6][0], hp[6][1]}, hb + (NT >> 1) * 1024 + (NT & 1) * 512 + lane * 8); }
  }
  STAMP(25);
  VM0();
  BAR();
  if (tid == 256) set_flag(p.ws, WS_FLAG + 4 * 8192 + ci * 512 + cj * 4, TAG);
  STAMP(13);
  if (w < 4) {
    const unsigned slot = w == 3 ? 0u : 65536u + w * 32768u;
#pragma unroll 8
    for (int ii = 0; ii < 32; ++ii) glds1(rWS, lane * 16, WS_TW2 + ct * 229376 + ((ii >> 4) * 7 + 1 + w) * 16384 + (ii & 15) * 1024, slot + ii * 1024);
  }
  if (w == 4) wait_words(p.ws, WS_FLAG + 4 * 8192 + ci * 512, 16, lane, TAG);
  VM0();
  BAR();
  STAMP(14);

  {
    v8s ha[28];
    const unsigned hb = WS_H1F + ((ci * 8 + 4 * hf + tt) * 56 + 28 * kh) * 1024;
#define HLOAD(s) do { if ((s) % 7 == 3) { const v2u lo_ = ld8(rWS, lane * 8, hb + (s) * 1024), hi_ = ld8(rWS, lane * 8 + 512, hb + (s) * 1024); ha[s] = asv8(v4u{lo_[0], lo_[1], hi_[0], hi_[1]}); } \
                      else ha[s] = asv8(ld16(rWS, lane * 16, hb + (s) * 1024)); } while (0)
#pragma unroll
    for (int s = 0; s < 8; ++s) HLOAD(s);
    const unsigned bw = opq(kh * 16384 + lane * 16), bw2 = opq(bw + 65536), bw3 = opq(bw + 131072);
    f4 acc[4];
#pragma unroll
    for (int nt = 0; nt < 4; ++nt) acc[nt] = f4{0, 0, 0, 0};
#define W2F(nt, S) lds16((((S) >> 2) == 0 || ((S) >> 2) == 5 ? bw + 32768 : ((S) >> 2) == 1 || ((S) >> 2) == 6 ? bw2 : ((S) >> 2) == 2 ? bw2 + 32768 : ((S) >> 2) == 3 ? bw3 : bw) + (((S) & 3) * 4 + (nt)) * 1024)
    {
      v8s W[2][4];
#pragma unroll
      for (int S = 0; S < 2; ++S)
#pragma unroll
        for (int nt = 0; nt < 4; ++nt) W[S][nt] = W2F(nt, S);
      SB();
#pragma unroll
      for (int S = 0; S < 28; ++S) {
        if (S + 8 < 28) HLOAD(S + 8);
        if (S == 8) {
          BAR();
#pragma unroll
          for (int e = 0; e < 8; ++e) { const int pc = w * 8 + e; *(LAS v4u*)(sm + 32768 + (pc >> 5) * 32768 + ((pc >> 4) & 1) * 16384 + (pc & 15) * 1024 + lane * 16) = stg[e]; }
        }
        if (S == 18) BAR();
#pragma unroll
        for (int nt = 0; nt < 4; ++nt) {
          acc[nt] = MFMA16(W[S & 1][nt], ha[S], acc[nt]);
          if (S + 2 < 28) W[S & 1][nt] = W2F(nt, S + 2);
          SB();
        }
      }
    }
    STAMP(22);
    BAR();
    *(LAS f4*)(sm + L_W2X + w * 2048 + lane * 16) = kh ? acc[0] : acc[2];
    *(LAS f4*)(sm + L_W2X + w * 2048 + 1024 + lane * 16) = kh ? acc[1] : acc[3];
    BAR();
    const f4 o0 = ldsf4(L_W2X + (w ^ 4) * 2048 + lane * 16), o1 = ldsf4(L_W2X + (w ^ 4) * 2048 + 1024 + lane * 16);
    const f4 t0 = (kh ? acc[2] : acc[0]) + o0, t1 = (kh ? acc[3] : acc[1]) + o1;
    const float gb[8] = {bflo(gbR[0][0]), bfhi(gbR[0][0]), bflo(gbR[0][1]), bfhi(gbR[0][1]), bflo(gbR[1][0]), bfhi(gbR[1][0]), bflo(gbR[1][1]), bfhi(gbR[1][1])};
    const float bb[8] = {bflo(b2R[0][0]), bfhi(b2R[0][0]), bflo(b2R[0][1]), bfhi(b2R[0][1]), bflo(b2R[1][0]), bfhi(b2R[1][0]), bflo(b2R[1][1]), bfhi(b2R[1][1])};
    float o[8];
#pragma unroll
    for (int r = 0; r < 8; ++r) o[r] = x1[r] + gb[r] * ((r < 4 ? t0[r & 3] : t1[r & 3]) + bb[r]);
    auto r0 = __builtin_amdgcn_permlane16_swap(pk(o[0], o[1]), pk(o[4], o[5]), false, false);
    auto r1 = __builtin_amdgcn_permlane16_swap(pk(o[2], o[3]), pk(o[6], o[7]), false, false);
    *(v4u*)(p.out + (size_t)orow * DM + 64 * ct + 32 * kh + (g & 1) * 16 + (g >> 1) * 8) = v4u{r0[0], r1[0], r0[1], r1[1]};
  }
  STAMP(15);
  STAMP_FLUSH();
}

__global__ void zero_flags(uint4* p) { p[blockIdx.x * 256 + threadIdx.x] = make_uint4(0, 0, 0, 0); }

extern "C" void kernel_launch(void* const* d_in, const int* in_sizes, int n_in, void* d_out, int out_size, void* d_ws, size_t ws_size, hipStream_t stream) {
  Params p;
  p.x = (const u16*)d_in[0]; p.wqkv = (const u16*)d_in[1]; p.wo = (const u16*)d_in[2];
  p.w1 = (const u16*)d_in[4]; p.b1 = (const u16*)d_in[5]; p.w2 = (const u16*)d_in[6]; p.b2 = (const u16*)d_in[7];
  p.c = (const u16*)d_in[8]; p.wmod = (const u16*)d_in[9]; p.out = (u16*)d_out; p.ws = (char*)d_ws;
  zero_flags<<<12, 256, 0, stream>>>((uint4*)d_ws);
  fused_block<<<256, 512, 0, stream>>>(p);
}
```

```cpp
#include <hip/hip_runtime.h>
#include <hip/hip_bf16.h>
#include <stdint.h>
#include <stdio.h>

typedef unsigned short u16;
typedef short v4s __attribute__((ext_vector_type(4)));
typedef short v8s __attribute__((ext_vector_type(8)));
typedef float f2 __attribute__((ext_vector_type(2)));
typedef float f4 __attribute__((ext_vector_type(4)));
typedef float f16v __attribute__((ext_vector_type(16)));
typedef unsigned v2u __attribute__((ext_vector_type(2)));
typedef unsigned v4u __attribute__((ext_vector_type(4)));
typedef __bf16 b2 __attribute__((ext_vector_type(2)));
#define LAS __attribute__((address_space(3)))
#define GAS __attribute__((address_space(1)))
#define DI __device__ __forceinline__
typedef __amdgpu_buffer_rsrc_t srd_t;

constexpr int SEQ = 2048, DM = 512, FF = 1792;
constexpr unsigned WS_FLAG = 0;
constexpr unsigned WS_FM = 5 * 8192;
constexpr unsigned WS_FQ = 5 * 8192 + 512;
constexpr unsigned WS_FW1 = WS_FQ + 1024;
constexpr unsigned WS_FWO = WS_FW1 + 1024;
constexpr unsigned WS_FW2 = WS_FWO + 512;
constexpr unsigned WS_FLAG_BYTES = 6 * 8192;
static_assert(WS_FW2 + 2048 <= WS_FLAG_BYTES, "flags");
constexpr unsigned WS_WV = 64 << 10;
constexpr unsigned WS_MOD = 128 << 10;
constexpr unsigned WS_ST1 = 192 << 10;
constexpr unsigned WS_ST2A = 512 << 10;
constexpr unsigned WS_ST2B = 1024 << 10;
constexpr unsigned WS_HF = 2u << 20;
constexpr unsigned WS_KV = 4u << 20;
constexpr unsigned WS_OF = 8u << 20;
constexpr unsigned WS_X1F = 10u << 20;
constexpr unsigned WS_H1F = 12u << 20;
constexpr unsigned WS_TQ = 19u << 20;
constexpr unsigned WS_TW1 = WS_TQ + (1536u << 10);
constexpr unsigned WS_TWO = WS_TW1 + (1792u << 10);
constexpr unsigned WS_TW2 = WS_TWO + (512u << 10);
constexpr unsigned WS_STAMP = 25u << 20;
constexpr unsigned WS_BYTES = 26u << 20;
static_assert(WS_TW2 + (1792u << 10) <= WS_STAMP, "ws");
constexpr unsigned L_XS = 0;
constexpr unsigned L_SQ = 8192, L_S1 = 16384, L_SO = 24576;
constexpr unsigned L_SC = 40960;
constexpr unsigned L_CAP = 49152;
constexpr unsigned L_WQKV = 0;
constexpr unsigned L_CA = 98304, L_M2 = 131072;
constexpr unsigned L_PARTU = 98304;
constexpr unsigned L_MODC = 32768;
constexpr unsigned L_B2C = L_MODC + 6144;
constexpr unsigned L_PARTV = 32768 + 8192;
constexpr unsigned L_AK = 98304;
constexpr unsigned KPL = 6144;
constexpr unsigned L_AV = L_AK + 4 * KPL;
constexpr unsigned VPL = 6208, VHD = 12416;
constexpr unsigned L_ONE = L_AV + 2 * VHD;
constexpr unsigned L_AQ = L_ONE + 6400;
constexpr unsigned L_PERS = L_AQ + 8192;
static_assert(L_PERS + 1024 <= 163840, "lds");
static_assert((L_AV % 256) == 0 && (L_ONE % 256) == 0, "bank plan");
constexpr unsigned L_WO = 0;
constexpr unsigned L_W1 = 65536;
constexpr unsigned L_W1S = 0;
constexpr unsigned L_XCH = 16384;
constexpr unsigned L_XF = 16384 + 12288;
constexpr unsigned L_W2X = 98304;

struct Params { const u16* x; const u16* wqkv; const u16* wo; const u16* w1; const u16* b1; const u16* w2; const u16* b2; const u16* c; const u16* wmod; u16* out; char* ws; };

DI float bflo(unsigned v) { return __uint_as_float(v << 16); }
DI float bfhi(unsigned v) { return __uint_as_float(v & 0xffff0000u); }
DI unsigned pk(float a, float b) { f2 v = {a, b}; b2 r = __builtin_convertvector(v, b2); return __builtin_bit_cast(unsigned, r); }
DI float rbf(float a) { return bflo(pk(a, 0.f)); }
DI v8s cat(v4s a, v4s b) { return __builtin_shufflevector(a, b, 0, 1, 2, 3, 4, 5, 6, 7); }
DI v4s trd(unsigned a) { return __builtin_amdgcn_ds_read_tr16_b64_v4i16((LAS v4s*)(uintptr_t)a); }
DI v8s asv8(v4u v) { return __builtin_bit_cast(v8s, v); }
DI v4u asv4u(v8s v) { return __builtin_bit_cast(v4u, v); }
DI v8s lds16(unsigned a) { return *(const LAS v8s*)(uintptr_t)a; }
DI f4 ldsf4(unsigned a) { return *(const LAS f4*)(uintptr_t)a; }
DI v2u lds8(unsigned a) { return *(const LAS v2u*)(uintptr_t)a; }
#define MFMA16(a, b, c) __builtin_amdgcn_mfma_f32_16x16x32_bf16((a), (b), (c), 0, 0, 0)
#define MFMA32(a, b, c) __builtin_amdgcn_mfma_f32_32x32x16_bf16((a), (b), (c), 0, 0, 0)
#define BAR() asm volatile("s_waitcnt lgkmcnt(0)\n\ts_barrier" ::: "memory")
#define VM0() asm volatile("s_waitcnt vmcnt(0)" ::: "memory")
#define LG0() asm volatile("s_waitcnt lgkmcnt(0)" ::: "memory")
#define CFENCE() asm volatile("" ::: "memory")
#define SB() __builtin_amdgcn_sched_barrier(0)
DI unsigned opq(unsigned v) { asm volatile("" : "+v"(v)); return v; }
DI void glds(srd_t r, unsigned voff, unsigned soff, unsigned ldsaddr) {
  asm volatile("s_mov_b32 m0, %0\n\ts_nop 0\n\tbuffer_load_dwordx4 %1, %2, %3 offen lds" ::"s"(ldsaddr), "v"(voff), "s"(r), "s"(soff) : "memory");
}
DI void glds1(srd_t r, unsigned voff, unsigned soff, unsigned ldsaddr) {
  asm volatile("s_mov_b32 m0, %0\n\ts_nop 0\n\tbuffer_load_dwordx4 %1, %2, %3 offen sc1 lds" ::"s"(ldsaddr), "v"(voff), "s"(r), "s"(soff) : "memory");
}
DI v4u ld16(srd_t r, unsigned voff, unsigned soff) { return __builtin_amdgcn_raw_buffer_load_b128(r, voff, soff, 16); }
DI v2u ld8(srd_t r, unsigned voff, unsigned soff) { return __builtin_amdgcn_raw_buffer_load_b64(r, voff, soff, 16); }
DI void st16(srd_t r, v4u v, unsigned off) { __builtin_amdgcn_raw_buffer_store_b128(v, r, off, 0, 16); }
DI void st8(srd_t r, v2u v, unsigned off) { __builtin_amdgcn_raw_buffer_store_b64(v, r, off, 0, 16); }
DI float sum16(float v) { auto r = __builtin_amdgcn_permlane16_swap(__float_as_uint(v), __float_as_uint(v), false, false); return __uint_as_float(r[0]) + __uint_as_float(r[1]); }
DI float sum32(float v) { auto r = __builtin_amdgcn_permlane32_swap(__float_as_uint(v), __float_as_uint(v), false, false); return __uint_as_float(r[0]) + __uint_as_float(r[1]); }
DI float max32(float v) { auto r = __builtin_amdgcn_permlane32_swap(__float_as_uint(v), __float_as_uint(v), false, false); return fmaxf(__uint_as_float(r[0]), __uint_as_float(r[1])); }
DI float sumg(float v) { return sum32(sum16(v)); }
#define RLX_AGENT __ATOMIC_RELAXED, __HIP_MEMORY_SCOPE_AGENT
DI void set_flag(char* ws, unsigned off, unsigned tag) { __hip_atomic_store((GAS unsigned*)(ws + off), tag, RLX_AGENT); }
DI void wait_words(char* ws, unsigned off, int n, int lane, unsigned tag) {
  GAS unsigned* p = (GAS unsigned*)(ws + off) + (lane < n ? lane : 0);
  for (unsigned spins = 0; spins < (1u << 17); ++spins) {
    const unsigned v = __hip_atomic_load(p, RLX_AGENT);
    if (__all(v == tag)) break;
  }
  CFENCE();
}
#define STAMP(k)
#define STAMP_FLUSH()
DI float silu(float v) { return v * __builtin_amdgcn_rcpf(1.f + __builtin_amdgcn_exp2f(-1.4426950408889634f * v)); }

DI v8s trfragA(unsigned img, int half, int lane) {
  const int g = lane >> 4, q = (lane & 15) >> 2, pp = lane & 3;
  const unsigned a = img + (4 * g + q) * 64 + ((half ^ (g & 1)) * 32) + pp * 8;
  return cat(trd(a), trd(a + 1024));
}

template <bool FULL>
DI void attention(int t, int hd, int kt0, int lane, f16v& O, srd_t rWS, unsigned wo_src, unsigned w1_src, int w, const u16* xres, v2u& xr0, v2u& xr1, unsigned& stampv) {
  const int qr = lane & 31, h = lane >> 5, blk = (lane >> 4) & 1, q = (lane & 15) >> 2, pp = lane & 3;
  const v8s Qf = lds16(L_AQ + (hd * 2 + h) * 2048 + (32 * t + qr) * 16);
  const unsigned kb = opq(L_AK + (hd * 2 + h) * KPL + (32 * t + qr) * 16);
  const unsigned vb = opq((blk == 0 ? L_AV + hd * VHD + (pp >> 1) * VPL + (pp & 1) * 8 + (4 * h + q) * 16 : L_ONE + ((hd * 128 + (h ? 0 : 128)) & 255)) + t * 512);
  f16v mU, mL;
#pragma unroll
  for (int r = 0; r < 16; ++r) { const int kr = (r & 3) + 8 * (r >> 2) + 4 * h; mU[r] = kr > qr ? -1e30f : 0.f; mL[r] = kr > qr ? 0.f : -1e30f; }
  const f16v zero = {};
#define RIDE(n) do { if ((n) < 8) glds1(rWS, lane * 16, wo_src + (w * 8 + (n)) * 1024, L_WO + (w * 8 + (n)) * 1024); \
                     else glds1(rWS, lane * 16, w1_src + (w * 4 + (n) - 8) * 1024, L_W1 + (w * 4 + (n) - 8) * 1024); } while (0)
  if (FULL) {
    v8s Kf[9];
#pragma unroll
    for (int kt = 0; kt < 9; ++kt) Kf[kt] = lds16(kb + kt * 512);
    f16v S[2];
    float m0 = -3e38f, m1 = -3e38f;
    S[0] = MFMA32(Kf[0], Qf, mL);
    SB();
#pragma unroll
    for (int kt = 0; kt < 9; ++kt) {
      if (kt < 8) S[(kt + 1) & 1] = MFMA32(Kf[kt + 1], Qf, kt + 1 == 8 ? mU : zero);
      if (kt == 2) RIDE(0);
      if (kt == 6) RIDE(1);
#pragma unroll
      for (int r = 0; r < 16; r += 4) { m0 = fmaxf(fmaxf(m0, S[kt & 1][r]), S[kt & 1][r + 1]); m1 = fmaxf(fmaxf(m1, S[kt & 1][r + 2]), S[kt & 1][r + 3]); }
      SB();
    }
    const float m = max32(fmaxf(m0, m1));
    STAMP(23);
    f16v nm;
#pragma unroll
    for (int r = 0; r < 16; ++r) { nm[r] = -m; mU[r] -= m; mL[r] -= m; }
    O = zero;
    v8s V0 = cat(trd(vb), trd(vb + 128)), V1 = cat(trd(vb + 256), trd(vb + 384));
    S[0] = MFMA32(Kf[0], Qf, mL);
    SB();
#pragma unroll
    for (int kt = 0; kt < 9; ++kt) {
      if (kt < 8) S[(kt + 1) & 1] = MFMA32(Kf[kt + 1], Qf, kt + 1 == 8 ? mU : nm);
      RIDE(2 + kt);
      SB();
      v4u P0, P1;
#pragma unroll
      for (int r = 0; r < 4; ++r) P0[r] = pk(__builtin_amdgcn_exp2f(S[kt & 1][2 * r]), __builtin_amdgcn_exp2f(S[kt & 1][2 * r + 1]));
      O = MFMA32(V0, asv8(P0), O);
      if (kt < 8) V0 = cat(trd(vb + (kt + 1) * 512), trd(vb + (kt + 1) * 512 + 128));
      if (kt == 1) RIDE(11);
      if (kt == 3) xr0 = *(const v2u*)xres;
      if (kt == 5) xr1 = *(const v2u*)(xres + 16);
      SB();
#pragma unroll
      for (int r = 0; r < 4; ++r) P1[r] = pk(__builtin_amdgcn_exp2f(S[kt & 1][8 + 2 * r]), __builtin_amdgcn_exp2f(S[kt & 1][8 + 2 * r + 1]));
      O = MFMA32(V1, asv8(P1), O);
      if (kt < 8) V1 = cat(trd(vb + (kt + 1) * 512 + 256), trd(vb + (kt + 1) * 512 + 384));
      SB();
    }
  } else {
#pragma unroll
    for (int n = 0; n < 12; ++n) RIDE(n);
    xr0 = *(const v2u*)xres; xr1 = *(const v2u*)(xres + 16);
    float m = -3e38f;
#pragma unroll
    for (int kt = 0; kt < 9; ++kt) {
      if (kt >= kt0) {
        const f16v S = MFMA32(lds16(kb + kt * 512), Qf, kt == 8 ? mU : zero);
#pragma unroll
        for (int r = 0; r < 16; r += 2) m = fmaxf(fmaxf(m, S[r]), S[r + 1]);
      }
    }
    m = max32(m);
    f16v nm;
#pragma unroll
    for (int r = 0; r < 16; ++r) { nm[r] = -m; mU[r] -= m; }
    O = zero;
#pragma unroll
    for (int kt = 0; kt < 9; ++kt) {
      if (kt >= kt0) {
        const f16v S = MFMA32(lds16(kb + kt * 512), Qf, kt == 8 ? mU : nm);
        v4u P0, P1;
#pragma unroll
        for (int r = 0; r < 4; ++r) {
          P0[r] = pk(__builtin_amdgcn_exp2f(S[2 * r]), __builtin_amdgcn_exp2f(S[2 * r + 1]));
          P1[r] = pk(__builtin_amdgcn_exp2f(S[8 + 2 * r]), __builtin_amdgcn_exp2f(S[8 + 2 * r + 1]));
        }
        const v8s V0 = cat(trd(vb + kt * 512), trd(vb + kt * 512 + 128));
        const v8s V1 = cat(trd(vb + kt * 512 + 256), trd(vb + kt * 512 + 384));
        O = MFMA32(V0, asv8(P0), O);
        O = MFMA32(V1, asv8(P1), O);
      }
    }
  }
#undef RIDE
}

__global__ void __launch_bounds__(512, 2) fused_block(Params p) {
  __shared__ __attribute__((aligned(16))) char smem[163840];
  LAS char* sm = (LAS char*)smem;
  const int tid = threadIdx.x, lane = tid & 63;
  const int w = __builtin_amdgcn_readfirstlane(tid >> 6);
  const int b = blockIdx.x, ci = (b & 7) * 2 + ((b >> 3) & 1), cj = b >> 4;
  const int tok = lane & 15, g = lane >> 4, q4 = (lane & 15) >> 2, pp = lane & 3;
  const int hf = cj & 1, ct = cj >> 1, par = cj & 1;
  const unsigned TAG = 1u;
  unsigned stampv = 0;
  const srd_t rWS = __builtin_amdgcn_make_buffer_rsrc(p.ws, 0, WS_BYTES, 0x00020000);
  const int rA = lane >> 2, pA = lane & 3;
  const unsigned colA = (((pA >> 1) ^ ((rA >> 2) & 1)) * 32) + (pA & 1) * 16;
  const int rB = lane >> 3, pB = lane & 7;
  const unsigned colB = (((pB >> 1) ^ ((rB >> 1) & 3)) * 32) + (pB & 1) * 16;
  STAMP(0);
  const unsigned long long hp_t0 = __builtin_amdgcn_s_memrealtime();

  const bool m2 = (cj % 3 == 0) && cj < 15;
  const int m2id = ci * 5 + cj / 3;
  v8s sf[16];
  v2u xa[2][2];
  if (w < 4) {
    __builtin_amdgcn_s_setprio(3);
    const srd_t rWmod = __builtin_amdgcn_make_buffer_rsrc((void*)p.wmod, 0, DM * 3072 * 2, 0x00020000);
    const srd_t rX = __builtin_amdgcn_make_buffer_rsrc((void*)p.x, 0, SEQ * DM * 2, 0x00020000);
#pragma unroll
    for (int ii = 0; ii < 8; ++ii) glds(rWmod, rA * 6144 + colA, (w * 8 + ii) * 16 * 6144 + (512 + 32 * cj) * 2, L_CA + (w * 8 + ii) * 1024);
#pragma unroll
    for (int u = 0; u < 2; ++u) glds(rX, rA * 1024 + pA * 16, (128 * ci + 16 * (2 * w + u)) * 1024 + 64 * cj, L_XS + (2 * w + u) * 1024);
  } else if (w == 5) {
    const srd_t r = __builtin_amdgcn_make_buffer_rsrc((void*)p.wqkv, 0, DM * 1536 * 2, 0x00020000);
#pragma unroll
    for (int e = 0; e < 6; ++e) glds(r, rA * 3072 + colA, (32 * ci + 16 * (e & 1)) * 3072 + ((e >> 1) * 512 + 32 * cj) * 2, L_SQ + e * 1024);
  } else if (w == 6) {
    const srd_t r = __builtin_amdgcn_make_buffer_rsrc((void*)p.w1, 0, DM * FF * 2, 0x00020000);
#pragma unroll
    for (int e = 0; e < 6; ++e) glds(r, rA * (FF * 2) + colA, (32 * ci + 16 * (e & 1)) * (FF * 2) + (112 * cj + 16 * par + (e >> 1) * 32) * 2, L_S1 + e * 1024);
    glds(r, (lane >> 1) * (FF * 2) + (lane & 1) * 16, 32 * ci * (FF * 2) + (112 * cj + (par ? 0 : 96)) * 2, L_S1 + 6144);
  } else if (w == 7) {
    if (b < 128) {
      const srd_t r = __builtin_amdgcn_make_buffer_rsrc((void*)p.wo, 0, DM * DM * 2, 0x00020000);
#pragma unroll
      for (int e = 0; e < 4; ++e) glds(r, rB * 1024 + colB, (32 * (b >> 3) + 8 * e) * 1024 + (b & 7) * 128, L_SO + e * 1024);
    }
    if (b < 224) {
      const srd_t r = __builtin_amdgcn_make_buffer_rsrc((void*)p.w2, 0, FF * DM * 2, 0x00020000);
#pragma unroll
      for (int e = 0; e < 8; ++e) { const int id = 2 * b + (e >> 2); glds(r, rB * 1024 + colB, (32 * (id >> 3) + 8 * (e & 3)) * 1024 + (id & 7) * 128, L_SO + 4096 + e * 1024); }
    }
  }
  if (w <= 4) {
    const v4u cr = *(const v4u*)(p.c + 8 * lane);
    const unsigned sc = L_SC + w * 1024;
    *(LAS v4u*)(uintptr_t)(sc + lane * 16) = v4u{pk(silu(bflo(cr[0])), silu(bfhi(cr[0]))), pk(silu(bflo(cr[1])), silu(bfhi(cr[1]))), pk(silu(bflo(cr[2])), silu(bfhi(cr[2]))), pk(silu(bflo(cr[3])), silu(bfhi(cr[3])))};
    LG0();
    if (w < 4) {
#pragma unroll
      for (int s = 0; s < 4; ++s) { const v2u lo = lds8(sc + (32 * (4 * w + s) + 4 * g) * 2), hi = lds8(sc + (32 * (4 * w + s) + 16 + 4 * g) * 2); sf[s] = asv8(v4u{lo[0], lo[1], hi[0], hi[1]}); }
    } else {
#pragma unroll
      for (int s = 0; s < 16; ++s) { const v2u lo = lds8(sc + (32 * s + 4 * g) * 2), hi = lds8(sc + (32 * s + 16 + 4 * g) * 2); sf[s] = asv8(v4u{lo[0], lo[1], hi[0], hi[1]}); }
    }
  }
  v8s rt[12];
  if (w >= 5) {
    VM0();
    if (w == 5) {
#pragma unroll
      for (int nt = 0; nt < 6; ++nt) st16(rWS, asv4u(trfragA(L_SQ + (nt >> 1) * 2048, nt & 1, lane)), WS_TQ + ((cj * 6 + nt) * 16 + ci) * 1024 + lane * 16);
    } else if (w == 6) {
#pragma unroll
      for (int nt = 0; nt < 6; ++nt) rt[nt] = trfragA(L_S1 + (nt >> 1) * 2048, nt & 1, lane);
      { const unsigned a = L_S1 + 6144 + (4 * g + q4) * 32 + pp * 8; rt[6] = cat(trd(a), trd(a + 512)); }
    } else {
      {
        const int row = 16 * (g >> 1) + 4 * (g & 1) + q4;
#pragma unroll
        for (int nt = 0; nt < 4; ++nt) { const unsigned a = L_SO + row * 128 + ((nt ^ ((row >> 1) & 3)) * 32) + pp * 8; rt[nt] = cat(trd(a), trd(a + 1024)); }
      }
      const int row = 4 * g + q4;
#pragma unroll
      for (int e = 0; e < 8; ++e) { const int nt = e & 3; const unsigned a = L_SO + 4096 + (e >> 2) * 4096 + row * 128 + ((nt ^ ((row >> 1) & 3)) * 32) + pp * 8; rt[4 + e] = cat(trd(a), trd(a + 2048)); }
    }
  } else if (w < 4) VM0();
  STAMP(1);
  BAR();
  if (w < 4) {
#pragma unroll
    for (int u = 0; u < 2; ++u) { const unsigned a = L_XS + (2 * w + u) * 1024 + tok * 64 + g * 8; xa[u][0] = lds8(a); xa[u][1] = lds8(a + 32); }
    f4 a0 = {0, 0, 0, 0}, a1 = {0, 0, 0, 0};
    v8s W[4][2];
#pragma unroll
    for (int s = 0; s < 4; ++s) { W[s][0] = trfragA(L_CA + (4 * w + s) * 2048, 0, lane); W[s][1] = trfragA(L_CA + (4 * w + s) * 2048, 1, lane); }
#pragma unroll
    for (int s = 0; s < 4; ++s) { a0 = MFMA16(W[s][0], sf[s], a0); a1 = MFMA16(W[s][1], sf[s], a1); }
    if (tok == 0) { *(LAS f4*)(uintptr_t)(L_CAP + (w * 32 + 4 * g) * 4) = a0; *(LAS f4*)(uintptr_t)(L_CAP + (w * 32 + 16 + 4 * g) * 4) = a1; }
  } else if (w == 5) {
    VM0();
    if (lane == 0) set_flag(p.ws, WS_FQ + (cj * 16 + ci) * 4, TAG);
  }
  STAMP(17);
  BAR();
  float wv[8];
  if (w < 4) {
    f4 a0 = ldsf4(L_CAP + 4 * g * 4), a1 = ldsf4(L_CAP + (16 + 4 * g) * 4);
#pragma unroll
    for (int e = 1; e < 4; ++e) { a0 += ldsf4(L_CAP + (e * 32 + 4 * g) * 4); a1 += ldsf4(L_CAP + (e * 32 + 16 + 4 * g) * 4); }
#pragma unroll
    for (int r = 0; r < 4; ++r) { wv[r] = rbf(1.f + rbf(a0[r])); wv[4 + r] = rbf(1.f + rbf(a1[r])); }
  }
  BAR();
  STAMP(19);
  if (w < 4) {
#pragma unroll
    for (int u = 0; u < 2; ++u) {
      const float x0 = bflo(xa[u][0][0]), x1 = bfhi(xa[u][0][0]), x2 = bflo(xa[u][0][1]), x3 = bfhi(xa[u][0][1]);
      const float x4 = bflo(xa[u][1][0]), x5 = bfhi(xa[u][1][0]), x6 = bflo(xa[u][1][1]), x7 = bfhi(xa[u][1][1]);
      const int T = 2 * w + u;
      st16(rWS, v4u{pk(x0 * wv[0], x1 * wv[1]), pk(x2 * wv[2], x3 * wv[3]), pk(x4 * wv[4], x5 * wv[5]), pk(x6 * wv[6], x7 * wv[7])},
           WS_HF + ((ci * 8 + T) * 16 + cj) * 1024 + lane * 16);
      const float s1 = sumg(((x0 + x1) + (x2 + x3)) + ((x4 + x5) + (x6 + x7)));
      const float s2 = sumg(((x0 * x0 + x1 * x1) + (x2 * x2 + x3 * x3)) + ((x4 * x4 + x5 * x5) + (x6 * x6 + x7 * x7)));
      if (g == 0) st8(rWS, v2u{__float_as_uint(s1), __float_as_uint(s2)}, WS_ST1 + ((ci * 16 + cj) * 128 + 16 * T + tok) * 8);
    }
    if (w == 0 && tok == 0) {
      st16(rWS, v4u{__float_as_uint(wv[0]), __float_as_uint(wv[1]), __float_as_uint(wv[2]), __float_as_uint(wv[3])}, WS_WV + ci * 2048 + cj * 128 + g * 16);
      st16(rWS, v4u{__float_as_uint(wv[4]), __float_as_uint(wv[5]), __float_as_uint(wv[6]), __float_as_uint(wv[7])}, WS_WV + ci * 2048 + cj * 128 + 64 + g * 16);
    }
    STAMP(21);
    VM0();
    if (lane == 0) set_flag(p.ws, WS_FLAG + 0 * 8192 + ci * 512 + (cj * 4 + w) * 4, TAG);
    __builtin_amdgcn_s_setprio(0);
  } else if (w == 4) {
    if (m2) {
      const srd_t rWmod = __builtin_amdgcn_make_buffer_rsrc((void*)p.wmod, 0, DM * 3072 * 2, 0x00020000);
      const int mcol = m2id < 16 ? m2id * 32 : 1024 + (m2id - 16) * 32;
#pragma unroll 8
      for (int ii = 0; ii < 32; ++ii) glds(rWmod, rA * 6144 + colA, ii * 16 * 6144 + mcol * 2, L_M2 + ii * 1024);
      VM0();
      f4 a0 = {0, 0, 0, 0}, a1 = {0, 0, 0, 0};
#pragma unroll
      for (int s0 = 0; s0 < 16; s0 += 4) {
        v8s W[4][2];
#pragma unroll
        for (int s = 0; s < 4; ++s) { W[s][0] = trfragA(L_M2 + (s0 + s) * 2048, 0, lane); W[s][1] = trfragA(L_M2 + (s0 + s) * 2048, 1, lane); }
#pragma unroll
        for (int s = 0; s < 4; ++s) { a0 = MFMA16(W[s][0], sf[s0 + s], a0); a1 = MFMA16(W[s][1], sf[s0 + s], a1); }
      }
      if (tok == 0) {
        st8(rWS, v2u{pk(a0[0], a0[1]), pk(a0[2], a0[3])}, WS_MOD + mcol * 2 + g * 8);
        st8(rWS, v2u{pk(a1[0], a1[1]), pk(a1[2], a1[3])}, WS_MOD + mcol * 2 + 32 + g * 8);
      }
      VM0();
      if (lane == 0) set_flag(p.ws, WS_FM + m2id * 4, TAG);
    }
    wait_words(p.ws, WS_FLAG + 0 * 8192 + ci * 512, 64, lane, TAG);
  } else {
    wait_words(p.ws, WS_FQ + cj * 64, 16, lane, TAG);
#pragma unroll
    for (int s = 0; s < 16; ++s)
#pragma unroll
      for (int e = 0; e < 2; ++e) { const int nt = (w - 5) * 2 + e; glds1(rWS, lane * 16, WS_TQ + ((cj * 6 + nt) * 16 + s) * 1024, L_WQKV + (nt * 16 + s) * 1024); }
    if (w == 6) {
#pragma unroll
      for (int nt = 0; nt < 7; ++nt) st16(rWS, asv4u(rt[nt]), WS_TW1 + ((cj * 7 + nt) * 16 + ci) * 1024 + lane * 16);
    } else if (w == 7) {
      if (b < 128) {
#pragma unroll
        for (int nt = 0; nt < 4; ++nt) st16(rWS, asv4u(rt[nt]), WS_TWO + (((b & 7) * 16 + (b >> 3)) * 4 + nt) * 1024 + lane * 16);
      }
      if (b < 224) {
#pragma unroll
        for (int e = 0; e < 8; ++e) { const int id = 2 * b + (e >> 2); st16(rWS, asv4u(rt[4 + e]), WS_TW2 + (((id & 7) * 56 + (id >> 3)) * 4 + (e & 3)) * 1024 + lane * 16); }
      }
    }
  }
  STAMP(2);
  VM0();
  if (lane == 0) {
    if (w == 6) set_flag(p.ws, WS_FW1 + (cj * 16 + ci) * 4, TAG);
    if (w == 7) {
      if (b < 128) set_flag(p.ws, WS_FWO + ((b & 7) * 16 + (b >> 3)) * 4, TAG);
      if (b < 224) { set_flag(p.ws, WS_FW2 + (((2 * b) & 7) * 64 + ((2 * b) >> 3)) * 4, TAG); set_flag(p.ws, WS_FW2 + (((2 * b + 1) & 7) * 64 + ((2 * b + 1) >> 3)) * 4, TAG); }
    }
  }
  BAR();
  STAMP(3);

  f4 accQ[2], accKV[4];
  float rstd, nmu;
  v2u gaR[2], cbR[2], sbR[2], gbR[2], b2R[2];
  v8s aux2[2];
  float Cw, Cw2, Cws, Cs, Cs2;
  const int tt = w & 3, kh = w >> 2;
  const int ocol = 64 * ct + 32 * kh + 4 * g;
  {
    const unsigned bq = opq(L_WQKV + lane * 16), bv = opq(L_WQKV + 65536 + lane * 16);
#define QF(nt, s) lds16(((nt) < 4 ? bq : bv) + ((nt) & 3) * 16384 + (s) * 1024)
    v8s act[16];
    const unsigned hsrc = WS_HF + (ci * 8 + w) * 16 * 1024;
    v4u wl[2], wh[2];
#pragma unroll
    for (int u = 0; u < 2; ++u) { const unsigned o = (32 * (2 * w + u) + 4 * g) * 4; wl[u] = ld16(rWS, o, WS_WV + ci * 2048); wh[u] = ld16(rWS, o + 64, WS_WV + ci * 2048); }
#pragma unroll
    for (int s = 0; s < 6; ++s) act[s] = asv8(ld16(rWS, lane * 16, hsrc + s * 1024));
    {
      f4 au[6];
      v8s W[2][6];
      const unsigned c0 = opq(bq + w * 2048), d0 = opq(bv + w * 2048);
#pragma unroll
      for (int u = 0; u < 2; ++u)
#pragma unroll
        for (int nt = 0; nt < 6; ++nt) W[u][nt] = lds16((nt < 4 ? c0 : d0) + (nt & 3) * 16384 + u * 1024);
#pragma unroll
      for (int nt = 0; nt < 6; ++nt) au[nt] = f4{0, 0, 0, 0};
#pragma unroll
      for (int u = 0; u < 2; ++u) {
        const v8s a = asv8(v4u{pk(__uint_as_float(wl[u][0]), __uint_as_float(wl[u][1])), pk(__uint_as_float(wl[u][2]), __uint_as_float(wl[u][3])),
                               pk(__uint_as_float(wh[u][0]), __uint_as_float(wh[u][1])), pk(__uint_as_float(wh[u][2]), __uint_as_float(wh[u][3]))});
#pragma unroll
        for (int nt = 0; nt < 6; ++nt) au[nt] = MFMA16(W[u][nt], a, au[nt]);
      }
      if (tok == 0) {
#pragma unroll
        for (int nt = 0; nt < 6; ++nt) *(LAS f4*)(sm + L_PARTU + (w * 96 + nt * 16 + 4 * g) * 4) = au[nt];
      }
    }
    BAR();
    if (tid < 96) {
      float s = 0;
#pragma unroll
      for (int e = 0; e < 8; ++e) s += *(const LAS float*)(sm + L_PARTU + (e * 96 + tid) * 4);
      *(LAS float*)(sm + L_PERS + tid * 4) = s;
    }
    STAMP(16);
    v2u st[4];
#pragma unroll
    for (int nt = 0; nt < 4; ++nt) accKV[nt] = f4{0, 0, 0, 0};
    {
      v8s W[2][4];
#pragma unroll
      for (int s = 0; s < 2; ++s)
#pragma unroll
        for (int nt = 0; nt < 4; ++nt) W[s][nt] = QF(2 + nt, s);
      SB();
#pragma unroll
      for (int s = 0; s < 16; ++s) {
        if (s + 6 < 16) act[s + 6] = asv8(ld16(rWS, lane * 16, hsrc + (s + 6) * 1024));
        else if (s < 14) st[s - 10] = ld8(rWS, ((4 * g + s - 10) * 128 + 16 * w + tok) * 8, WS_ST1 + ci * 16 * 128 * 8);
#pragma unroll
        for (int nt = 0; nt < 4; ++nt) {
          accKV[nt] = MFMA16(W[s & 1][nt], act[s], accKV[nt]);
          if (s + 2 < 16) W[s & 1][nt] = QF(2 + nt, s + 2);
          SB();
        }
      }
    }
    float s1 = 0, s2 = 0;
#pragma unroll
    for (int e = 0; e < 4; ++e) { s1 += __uint_as_float(st[e][0]); s2 += __uint_as_float(st[e][1]); }
    s1 = sumg(s1); s2 = sumg(s2);
    const float mu = s1 * (1.f / 512), var = fmaxf(s2 * (1.f / 512) - mu * mu, 0.f);
    rstd = __builtin_amdgcn_rsqf(var + 1e-5f); nmu = -rstd * mu;
    if (w == 4) {
      GAS unsigned* f0 = (GAS unsigned*)(p.ws + WS_FM) + lane;
      GAS unsigned* f1 = (GAS unsigned*)(p.ws + (lane < 16 ? WS_FM + 256 + lane * 4 : lane < 32 ? WS_FW1 + cj * 64 + (lane - 16) * 4 : WS_FWO + ct * 64 + (lane & 15) * 4));
      GAS unsigned* f2 = (GAS unsigned*)(p.ws + WS_FW2 + ct * 256) + (lane < 56 ? lane : 0);
      for (unsigned spins = 0; spins < (1u << 17); ++spins) {
        const unsigned a = __hip_atomic_load(f0, RLX_AGENT), c = __hip_atomic_load(f1, RLX_AGENT), d = __hip_atomic_load(f2, RLX_AGENT);
        if (__all(a == TAG && c == TAG && d == TAG)) break;
      }
      CFENCE();
    }
    STAMP(4);
    BAR();
    {
      v4u kk, vv;
      unsigned pkd[4][2];
#pragma unroll
      for (int nt = 0; nt < 4; ++nt) {
        const f4 u = ldsf4(L_PERS + (32 + nt * 16 + 4 * g) * 4);
        pkd[nt][0] = pk(rstd * accKV[nt][0] + nmu * u[0], rstd * accKV[nt][1] + nmu * u[1]);
        pkd[nt][1] = pk(rstd * accKV[nt][2] + nmu * u[2], rstd * accKV[nt][3] + nmu * u[3]);
      }
      { auto r0 = __builtin_amdgcn_permlane16_swap(pkd[0][0], pkd[1][0], false, false); auto r1 = __builtin_amdgcn_permlane16_swap(pkd[0][1], pkd[1][1], false, false);
        kk = v4u{r0[0], r1[0], r0[1], r1[1]}; }
      { auto r0 = __builtin_amdgcn_permlane16_swap(pkd[2][0], pkd[3][0], false, false); auto r1 = __builtin_amdgcn_permlane16_swap(pkd[2][1], pkd[3][1], false, false);
        vv = v4u{r0[0], r1[0], r0[1], r1[1]}; }
      const int hd = g & 1, pl = g >> 1;
      st16(rWS, kk, WS_KV + (ci * 16 + cj) * 16384 + ((0 * 2 + hd) * 2 + pl) * 2048 + (16 * w + tok) * 16);
      st16(rWS, vv, WS_KV + (ci * 16 + cj) * 16384 + ((1 * 2 + hd) * 2 + pl) * 2048 + (16 * w + tok) * 16);
      *(LAS v4u*)(sm + L_AK + (hd * 2 + pl) * KPL + (256 + 16 * w + tok) * 16) = kk;
      *(LAS v4u*)(sm + L_AV + hd * VHD + pl * VPL + (256 + 16 * w + tok) * 16) = vv;
    }
    if (w < 6) glds1(rWS, lane * 16, WS_MOD + w * 1024, L_MODC + w * 1024);
    if (w == 6 && lane < 8) *(LAS v4u*)(sm + L_B2C + lane * 16) = *(const v4u*)(p.b2 + 64 * ct + lane * 8);
#pragma unroll
    for (int nt = 0; nt < 2; ++nt) accQ[nt] = f4{0, 0, 0, 0};
    {
      v8s W[4][2];
#pragma unroll
      for (int s = 0; s < 4; ++s) { W[s][0] = QF(0, s); W[s][1] = QF(1, s); }
      SB();
#pragma unroll
      for (int s = 0; s < 16; ++s)
#pragma unroll
        for (int nt = 0; nt < 2; ++nt) {
          accQ[nt] = MFMA16(W[s & 3][nt], act[s], accQ[nt]);
          if (s + 4 < 16) W[s & 3][nt] = QF(nt, s + 4);
          SB();
        }
    }
    VM0();
    BAR();
    if (tid == 256) set_flag(p.ws, WS_FLAG + 1 * 8192 + ci * 512 + cj * 4, TAG);
    STAMP(5);
    {
      f4 av[4];
      v8s W[2][4], a[2];
      const unsigned c0 = opq(bq + w * 2048), d0 = opq(bv + w * 2048);
#pragma unroll
      for (int u = 0; u < 2; ++u) {
        const v2u lo = lds8(L_MODC + (32 * (2 * w + u) + 4 * g) * 2), hi = lds8(L_MODC + (32 * (2 * w + u) + 16 + 4 * g) * 2);
        a[u] = asv8(v4u{lo[0], lo[1], hi[0], hi[1]});
#pragma unroll
        for (int nt = 0; nt < 4; ++nt) W[u][nt] = lds16((nt < 2 ? c0 : d0) + (nt & 1) * 16384 + u * 1024);
      }
#pragma unroll
      for (int nt = 0; nt < 4; ++nt) av[nt] = f4{0, 0, 0, 0};
#pragma unroll
      for (int u = 0; u < 2; ++u)
#pragma unroll
        for (int nt = 0; nt < 4; ++nt) av[nt] = MFMA16(W[u][nt], a[u], av[nt]);
      if (tok == 0) {
#pragma unroll
        for (int nt = 0; nt < 4; ++nt) *(LAS f4*)(sm + L_PARTV + (w * 64 + nt * 16 + 4 * g) * 4) = av[nt];
      }
    }
    {
#pragma unroll
      for (int e = 0; e < 2; ++e) {
        gaR[e] = lds8(L_MODC + (1024 + ocol + 16 * e) * 2); sbR[e] = lds8(L_MODC + (1536 + ocol + 16 * e) * 2);
        cbR[e] = lds8(L_MODC + (2048 + ocol + 16 * e) * 2); gbR[e] = lds8(L_MODC + (2560 + ocol + 16 * e) * 2);
        b2R[e] = lds8(L_B2C + (32 * kh + 4 * g + 16 * e) * 2);
      }
      const v4u c0 = asv4u(lds16(L_MODC + (2048 + 8 * lane) * 2)), d0 = asv4u(lds16(L_MODC + (1536 + 8 * lane) * 2));
      Cw = Cw2 = Cws = Cs = Cs2 = 0;
#pragma unroll
      for (int r = 0; r < 4; ++r) {
        const float w0 = rbf(1.f + bflo(c0[r])), w1 = rbf(1.f + bfhi(c0[r])), s0 = bflo(d0[r]), s1_ = bfhi(d0[r]);
        Cw += w0 + w1; Cw2 += w0 * w0 + w1 * w1; Cws += w0 * s0 + w1 * s1_; Cs += s0 + s1_; Cs2 += s0 * s0 + s1_ * s1_;
      }
#pragma unroll
      for (int o = 1; o < 16; o <<= 1) { Cw += __shfl_xor(Cw, o); Cw2 += __shfl_xor(Cw2, o); Cws += __shfl_xor(Cws, o); Cs += __shfl_xor(Cs, o); Cs2 += __shfl_xor(Cs2, o); }
      Cw = sumg(Cw); Cw2 = sumg(Cw2); Cws = sumg(Cws); Cs = sumg(Cs); Cs2 = sumg(Cs2);
#pragma unroll
      for (int u = 0; u < 2; ++u) {
        const unsigned o = (32 * (2 * w + u) + 4 * g) * 2 + L_MODC + (tok == 1 ? 1536 : 2048) * 2;
        const v2u lo = lds8(o), hi = lds8(o + 32);
        v4u t;
        t[0] = tok == 0 ? pk(1.f + bflo(lo[0]), 1.f + bfhi(lo[0])) : (tok == 1 ? lo[0] : 0x3f803f80u);
        t[1] = tok == 0 ? pk(1.f + bflo(lo[1]), 1.f + bfhi(lo[1])) : (tok == 1 ? lo[1] : 0x3f803f80u);
        t[2] = tok == 0 ? pk(1.f + bflo(hi[0]), 1.f + bfhi(hi[0])) : (tok == 1 ? hi[0] : 0x3f803f80u);
        t[3] = tok == 0 ? pk(1.f + bflo(hi[1]), 1.f + bfhi(hi[1])) : (tok == 1 ? hi[1] : 0x3f803f80u);
        aux2[u] = asv8(t);
      }
    }
    if (tid < 400) *(LAS v4u*)(sm + L_ONE + tid * 16) = v4u{0x3f803f80u, 0x3f803f80u, 0x3f803f80u, 0x3f803f80u};
    BAR();
    if (tid < 64) {
      float s = 0;
#pragma unroll
      for (int e = 0; e < 8; ++e) s += *(const LAS float*)(sm + L_PARTV + (e * 64 + tid) * 4);
      *(LAS float*)(sm + L_PERS + (96 + tid) * 4) = s;
    }
    if (ci < 2) {
      for (int e = tid; e < 8 * 256; e += 512) {
        const int pln = e >> 8, key = e & 255;
        if (ci == 0 || key < 128) {
          const unsigned a = pln < 4 ? L_AK + pln * KPL : L_AV + ((pln - 4) >> 1) * VHD + (pln & 1) * VPL;
          *(LAS v4u*)(sm + a + key * 16) = v4u{0, 0, 0, 0};
        }
      }
    }
    if (w == 4) {
      if (ci >= 1) wait_words(p.ws, WS_FLAG + 1 * 8192 + (ci - 1) * 512 + cj * 4, 1, lane, TAG);
      if (ci >= 2) wait_words(p.ws, WS_FLAG + 1 * 8192 + (ci - 2) * 512 + cj * 4, 1, lane, TAG);
    }
    BAR();
    STAMP(6);
    {
      const unsigned dst = w < 4 ? L_AK + w * KPL : L_AV + ((w - 4) >> 1) * VHD + (w & 1) * VPL;
#pragma unroll
      for (int src = 0; src < 2; ++src)
        if (ci - 2 + src >= 0) {
#pragma unroll
          for (int e = 0; e < 2; ++e) glds1(rWS, lane * 16, WS_KV + ((ci - 2 + src) * 16 + cj) * 16384 + w * 2048 + e * 1024, dst + (src * 128 + e * 64) * 16);
        }
    }
    {
      unsigned pq[2][2];
      const float qs = 0.25f * 1.4426950408889634f;
#pragma unroll
      for (int nt = 0; nt < 2; ++nt) {
        const f4 u = ldsf4(L_PERS + (nt * 16 + 4 * g) * 4), v = ldsf4(L_PERS + (96 + nt * 16 + 4 * g) * 4);
        pq[nt][0] = pk(qs * (rstd * accQ[nt][0] + nmu * u[0] + v[0]), qs * (rstd * accQ[nt][1] + nmu * u[1] + v[1]));
        pq[nt][1] = pk(qs * (rstd * accQ[nt][2] + nmu * u[2] + v[2]), qs * (rstd * accQ[nt][3] + nmu * u[3] + v[3]));
      }
      auto r0 = __builtin_amdgcn_permlane16_swap(pq[0][0], pq[1][0], false, false); auto r1 = __builtin_amdgcn_permlane16_swap(pq[0][1], pq[1][1], false, false);
      *(LAS v4u*)(sm + L_AQ + ((g & 1) * 2 + (g >> 1)) * 2048 + (16 * w + tok) * 16) = v4u{r0[0], r1[0], r0[1], r1[1]};
    }
    VM0();
    BAR();
    STAMP(7);
  }

  const int orow = 128 * ci + 64 * hf + 16 * tt + tok;
  v2u xr0, xr1;
  {
    const int t = w & 3, hd = w >> 2;
    const int kt0 = ci >= 2 ? 0 : 8 - 4 * ci - t;
    f16v O;
    const u16* xres = p.x + (size_t)orow * DM + ocol;
    if (ci >= 2) attention<true>(t, hd, 0, lane, O, rWS, WS_TWO + ct * 65536, WS_TW1 + cj * 7 * 16384, w, xres, xr0, xr1, stampv);
    else attention<false>(t, hd, kt0, lane, O, rWS, WS_TWO + ct * 65536, WS_TW1 + cj * 7 * 16384, w, xres, xr0, xr1, stampv);
    const int h = lane >> 5;
    const float inv = __builtin_amdgcn_rcpf(O[8]);
    const f4 b0 = ldsf4(L_PERS + (96 + 32 + hd * 16 + 4 * h) * 4), b1 = ldsf4(L_PERS + (96 + 32 + hd * 16 + 8 + 4 * h) * 4);
    const v4u o = {pk(O[0] * inv + b0[0], O[1] * inv + b0[1]), pk(O[2] * inv + b0[2], O[3] * inv + b0[3]),
                   pk(O[4] * inv + b1[0], O[5] * inv + b1[1]), pk(O[6] * inv + b1[2], O[7] * inv + b1[3])};
    const int qr = lane & 31;
    st16(rWS, o, WS_OF + (ci * 8 * 16 + cj) * 1024 + ((2 * t + (qr >> 4)) * 16) * 1024 + ((2 * hd + h) * 16 + (qr & 15)) * 16);
  }
  STAMP(24);
  VM0();
  BAR();
  if (tid == 256) set_flag(p.ws, WS_FLAG + 2 * 8192 + ci * 512 + cj * 4, TAG);
  STAMP(8);
  if (w < 4) {
#pragma unroll
    for (int ii = 0; ii < 16; ++ii) glds1(rWS, lane * 16, WS_TW1 + (cj * 7 + 2 + w) * 16384 + ii * 1024, L_W1 + (2 + w) * 16384 + ii * 1024);
  }
  if (w == 4) wait_words(p.ws, WS_FLAG + 2 * 8192 + ci * 512, 16, lane, TAG);
  BAR();
  STAMP(9);

  float x1[8];
  {
    v8s oa[8];
    const unsigned osrc = WS_OF + ((ci * 8 + 4 * hf + tt) * 16 + 8 * kh) * 1024;
#pragma unroll
    for (int s = 0; s < 8; ++s) oa[s] = asv8(ld16(rWS, lane * 16, osrc + s * 1024));
    const unsigned bo = opq(L_WO + kh * 32768 + lane * 16);
    f4 acc[4];
#pragma unroll
    for (int nt = 0; nt < 4; ++nt) acc[nt] = f4{0, 0, 0, 0};
    {
      v8s W[2][4];
#pragma unroll
      for (int s = 0; s < 2; ++s)
#pragma unroll
        for (int nt = 0; nt < 4; ++nt) W[s][nt] = lds16(bo + (s * 4 + nt) * 1024);
      SB();
#pragma unroll
      for (int s = 0; s < 8; ++s)
#pragma unroll
        for (int nt = 0; nt < 4; ++nt) {
          acc[nt] = MFMA16(W[s & 1][nt], oa[s], acc[nt]);
          if (s + 2 < 8) W[s & 1][nt] = lds16(bo + ((s + 2) * 4 + nt) * 1024);
          SB();
        }
    }
    STAMP(26);
    VM0();
    BAR();
    *(LAS f4*)(sm + L_XCH + w * 2048 + lane * 16) = kh ? acc[0] : acc[2];
    *(LAS f4*)(sm + L_XCH + w * 2048 + 1024 + lane * 16) = kh ? acc[1] : acc[3];
    BAR();
    const f4 o0 = ldsf4(L_XCH + (w ^ 4) * 2048 + lane * 16), o1 = ldsf4(L_XCH + (w ^ 4) * 2048 + 1024 + lane * 16);
    const f4 t0 = (kh ? acc[2] : acc[0]) + o0, t1 = (kh ? acc[3] : acc[1]) + o1;
    const float xin[8] = {bflo(xr0[0]), bfhi(xr0[0]), bflo(xr0[1]), bfhi(xr0[1]), bflo(xr1[0]), bfhi(xr1[0]), bflo(xr1[1]), bfhi(xr1[1])};
    const float ga[8] = {bflo(gaR[0][0]), bfhi(gaR[0][0]), bflo(gaR[0][1]), bfhi(gaR[0][1]), bflo(gaR[1][0]), bfhi(gaR[1][0]), bflo(gaR[1][1]), bfhi(gaR[1][1])};
    const float cb[8] = {bflo(cbR[0][0]), bfhi(cbR[0][0]), bflo(cbR[0][1]), bfhi(cbR[0][1]), bflo(cbR[1][0]), bfhi(cbR[1][0]), bflo(cbR[1][1]), bfhi(cbR[1][1])};
    const float sv[8] = {bflo(sbR[0][0]), bfhi(sbR[0][0]), bflo(sbR[0][1]), bfhi(sbR[0][1]), bflo(sbR[1][0]), bfhi(sbR[1][0]), bflo(sbR[1][1]), bfhi(sbR[1][1])};
    float wv[8];
#pragma unroll
    for (int r = 0; r < 8; ++r) { x1[r] = rbf(xin[r] + ga[r] * (r < 4 ? t0[r & 3] : t1[r & 3])); wv[r] = rbf(1.f + cb[r]); }
    st16(rWS, v4u{pk(x1[0] * wv[0], x1[1] * wv[1]), pk(x1[2] * wv[2], x1[3] * wv[3]), pk(x1[4] * wv[4], x1[5] * wv[5]), pk(x1[6] * wv[6], x1[7] * wv[7])},
         WS_X1F + ((ci * 8 + 4 * hf + tt) * 16 + 2 * ct + kh) * 1024 + lane * 16);
    float s1 = 0, s2 = 0, s3 = 0, s4 = 0, s5 = 0, s6 = 0;
#pragma unroll
    for (int r = 0; r < 8; ++r) { const float xw = x1[r] * wv[r]; s1 += x1[r]; s2 += x1[r] * x1[r]; s3 += xw; s4 += xw * wv[r]; s5 += xw * xw; s6 += xw * sv[r]; }
    s1 = sumg(s1); s2 = sumg(s2); s3 = sumg(s3); s4 = sumg(s4); s5 = sumg(s5); s6 = sumg(s6);
    const unsigned rec = (ci * 16 + 2 * ct + kh) * 128 + 64 * hf + 16 * tt + tok;
    if (g == 0) st16(rWS, v4u{__float_as_uint(s1), __float_as_uint(s2), __float_as_uint(s3), __float_as_uint(s4)}, WS_ST2A + rec * 16);
    if (g == 1) st8(rWS, v2u{__float_as_uint(s5), __float_as_uint(s6)}, WS_ST2B + rec * 8);
  }
  VM0();
  BAR();
  if (tid == 256) set_flag(p.ws, WS_FLAG + 3 * 8192 + ci * 512 + cj * 4, TAG);
  STAMP(10);
  if (w < 4) {
#pragma unroll
    for (int ii = 0; ii < 4; ++ii) glds1(rWS, lane * 16, WS_TW1 + (cj * 7 + 6) * 16384 + (w * 4 + ii) * 1024, L_W1S + (w * 4 + ii) * 1024);
#pragma unroll
    for (int ii = 0; ii < 8; ++ii) { const int pc = w * 8 + ii; glds1(rWS, lane * 16, WS_TW2 + ct * 229376 + (pc >> 4) * 7 * 16384 + (pc & 15) * 1024, 32768 + pc * 1024); }
  }
  float b1v = 0.f;
  if (tid < 112) { const int nt = tid >> 4; b1v = bflo(p.b1[(nt == 6 ? 112 * cj + (par ? 0 : 96) : 112 * cj + 16 * par + nt * 16) + (tid & 15)]); }
  if (w == 4) wait_words(p.ws, WS_FLAG + 3 * 8192 + ci * 512, 16, lane, TAG);
  VM0();
  BAR();
  STAMP(11);

  v4u stg[8];
  {
    const unsigned bP = opq(L_W1 + lane * 16), bR = opq(L_W1 + 65536 + lane * 16), bS = opq(L_W1S + lane * 16);
#define W1F(nt, s) lds16((nt) == 6 ? bS + (s) * 1024 : (nt) < 4 ? bP + (nt) * 16384 + (s) * 1024 : bR + ((nt) - 4) * 16384 + (s) * 1024)
    v8s act2[16];
    const unsigned asrc = WS_X1F + (ci * 8 + w) * 16 * 1024;
#pragma unroll
    for (int s = 0; s < 6; ++s) act2[s] = asv8(ld16(rWS, lane * 16, asrc + s * 1024));
    {
      f4 au[7];
#pragma unroll
      for (int nt = 0; nt < 7; ++nt) au[nt] = f4{0, 0, 0, 0};
      const unsigned c0 = opq(bP + w * 2048), d0 = opq(bR + w * 2048), e0 = opq(bS + w * 2048);
#pragma unroll
      for (int u = 0; u < 2; ++u) {
        v8s W[7];
#pragma unroll
        for (int nt = 0; nt < 7; ++nt) W[nt] = lds16(nt == 6 ? e0 + u * 1024 : nt < 4 ? c0 + nt * 16384 + u * 1024 : d0 + (nt - 4) * 16384 + u * 1024);
#pragma unroll
        for (int nt = 0; nt < 7; ++nt) au[nt] = MFMA16(W[nt], aux2[u], au[nt]);
      }
      if (tok < 3) {
#pragma unroll
        for (int nt = 0; nt < 7; ++nt) *(LAS f4*)(sm + L_XCH + ((w * 3 + tok) * 112 + nt * 16 + 4 * g) * 4) = au[nt];
      }
    }
    BAR();
    if (tid < 336) {
      float s = 0;
#pragma unroll
      for (int e = 0; e < 8; ++e) s += *(const LAS float*)(sm + L_XCH + (e * 336 + tid) * 4);
      *(LAS float*)(sm + L_XF + tid * 4) = s;
    }
    if (tid < 112) *(LAS float*)(sm + L_XF + (336 + tid) * 4) = b1v;
    STAMP(18);
    v4u sa[4]; v2u sb[4];
    f4 acc[7];
#pragma unroll
    for (int nt = 0; nt < 7; ++nt) acc[nt] = f4{0, 0, 0, 0};
    {
      v8s W[7];
#pragma unroll
      for (int nt = 0; nt < 7; ++nt) W[nt] = W1F(nt, 0);
      SB();
#pragma unroll
      for (int s = 0; s < 16; ++s) {
        if (s + 6 < 16) act2[s + 6] = asv8(ld16(rWS, lane * 16, asrc + (s + 6) * 1024));
        if (s >= 4 && s < 8) sa[s - 4] = ld16(rWS, ((4 * g + s - 4) * 128 + 16 * w + tok) * 16, WS_ST2A + ci * 16 * 128 * 16);
        if (s >= 10 && s < 14) sb[s - 10] = ld8(rWS, ((4 * g + s - 10) * 128 + 16 * w + tok) * 8, WS_ST2B + ci * 16 * 128 * 8);
        if (s >= 8) { const int e = s - 8, pc = w * 8 + e; const int chunk = (pc >> 4) == 0 ? 5 : ((pc >> 4) == 1 ? 12 : ((pc >> 4) == 2 ? 6 : 13));
          stg[e] = ld16(rWS, lane * 16, WS_TW2 + ct * 229376 + chunk * 16384 + (pc & 15) * 1024); }
#pragma unroll
        for (int nt = 0; nt < 7; ++nt) {
          acc[nt] = MFMA16(W[nt], act2[s], acc[nt]);
          if (s + 1 < 16) W[nt] = W1F(nt, s + 1);
          SB();
        }
      }
    }
    STAMP(20);
    float s1 = 0, s2 = 0, s3 = 0, s4 = 0, s5 = 0, s6 = 0;
#pragma unroll
    for (int e = 0; e < 4; ++e) { s1 += __uint_as_float(sa[e][0]); s2 += __uint_as_float(sa[e][1]); s3 += __uint_as_float(sa[e][2]); s4 += __uint_as_float(sa[e][3]); s5 += __uint_as_float(sb[e][0]); s6 += __uint_as_float(sb[e][1]); }
    s1 = sumg(s1); s2 = sumg(s2); s3 = sumg(s3); s4 = sumg(s4); s5 = sumg(s5); s6 = sumg(s6);
    const float mu1 = s1 * (1.f / 512), var1 = fmaxf(s2 * (1.f / 512) - mu1 * mu1, 0.f), r1 = __builtin_amdgcn_rsqf(var1 + 1e-5f);
    const float mu2 = (r1 * (s3 - mu1 * Cw) + Cs) * (1.f / 512);
    const float hh = (r1 * r1 * (s5 - 2.f * mu1 * s4 + mu1 * mu1 * Cw2) + 2.f * r1 * (s6 - mu1 * Cws) + Cs2) * (1.f / 512);
    const float r2 = __builtin_amdgcn_rsqf(fmaxf(hh - mu2 * mu2, 0.f) + 1e-5f);
    const float cA = r2 * r1, cB = -cA * mu1, cC = r2, cD = -r2 * mu2;
    BAR();
    STAMP(12);
    unsigned hp[7][2];
#pragma unroll
    for (int nt = 0; nt < 7; ++nt) {
      const f4 u = ldsf4(L_XF + (nt * 16 + 4 * g) * 4), v = ldsf4(L_XF + (112 + nt * 16 + 4 * g) * 4), t = ldsf4(L_XF + (224 + nt * 16 + 4 * g) * 4), bias = ldsf4(L_XF + (336 + nt * 16 + 4 * g) * 4);
      float o[4];
#pragma unroll
      for (int r = 0; r < 4; ++r) {
        const float z = cA * acc[nt][r] + (cB * u[r] + (cC * v[r] + (cD * t[r] + bias[r])));
        const float pz = z * (-2.3022081985f - 0.1029432397f * z * z);
        o[r] = z * __builtin_amdgcn_rcpf(1.f + __builtin_amdgcn_exp2f(pz));
      }
      hp[nt][0] = pk(o[0], o[1]); hp[nt][1] = pk(o[2], o[3]);
    }
    const unsigned S0 = (7 * cj + par) >> 1;
    const unsigned hb = WS_H1F + (ci * 8 + w) * 56 * 1024;
#pragma unroll
    for (int pr = 0; pr < 3; ++pr) st16(rWS, v4u{hp[2 * pr][0], hp[2 * pr][1], hp[2 * pr + 1][0], hp[2 * pr + 1][1]}, hb + (S0 + pr) * 1024 + lane * 16);
    { const unsigned NT = 7 * cj + (par ? 0 : 6); st8(rWS, v2u{hp[6][0], hp[6][1]}, hb + (NT >> 1) * 1024 + (NT & 1) * 512 + lane * 8); }
  }
  STAMP(25);
  VM0();
  BAR();
  if (tid == 256) set_flag(p.ws, WS_FLAG + 4 * 8192 + ci * 512 + cj * 4, TAG);
  STAMP(13);
  if (w < 4) {
    const unsigned slot = w == 3 ? 0u : 65536u + w * 32768u;
#pragma unroll 8
    for (int ii = 0; ii < 32; ++ii) glds1(rWS, lane * 16, WS_TW2 + ct * 229376 + ((ii >> 4) * 7 + 1 + w) * 16384 + (ii & 15) * 1024, slot + ii * 1024);
  }
  if (w == 4) wait_words(p.ws, WS_FLAG + 4 * 8192 + ci * 512, 16, lane, TAG);
  VM0();
  BAR();
  STAMP(14);

  {
    v8s ha[28];
    const unsigned hb = WS_H1F + ((ci * 8 + 4 * hf + tt) * 56 + 28 * kh) * 1024;
#define HLOAD(s) do { if ((s) % 7 == 3) { const v2u lo_ = ld8(rWS, lane * 8, hb + (s) * 1024), hi_ = ld8(rWS, lane * 8 + 512, hb + (s) * 1024); ha[s] = asv8(v4u{lo_[0], lo_[1], hi_[0], hi_[1]}); } \
                      else ha[s] = asv8(ld16(rWS, lane * 16, hb + (s) * 1024)); } while (0)
#pragma unroll
    for (int s = 0; s < 8; ++s) HLOAD(s);
    const unsigned bw = opq(kh * 16384 + lane * 16), bw2 = opq(bw + 65536), bw3 = opq(bw + 131072);
    f4 acc[4];
#pragma unroll
    for (int nt = 0; nt < 4; ++nt) acc[nt] = f4{0, 0, 0, 0};
#define W2F(nt, S) lds16((((S) >> 2) == 0 || ((S) >> 2) == 5 ? bw + 32768 : ((S) >> 2) == 1 || ((S) >> 2) == 6 ? bw2 : ((S) >> 2) == 2 ? bw2 + 32768 : ((S) >> 2) == 3 ? bw3 : bw) + (((S) & 3) * 4 + (nt)) * 1024)
    {
      v8s W[2][4];
#pragma unroll
      for (int S = 0; S < 2; ++S)
#pragma unroll
        for (int nt = 0; nt < 4; ++nt) W[S][nt] = W2F(nt, S);
      SB();
#pragma unroll
      for (int S = 0; S < 28; ++S) {
        if (S + 8 < 28) HLOAD(S + 8);
        if (S == 8) {
          BAR();
#pragma unroll
          for (int e = 0; e < 8; ++e) { const int pc = w * 8 + e; *(LAS v4u*)(sm + 32768 + (pc >> 5) * 32768 + ((pc >> 4) & 1) * 16384 + (pc & 15) * 1024 + lane * 16) = stg[e]; }
        }
        if (S == 18) BAR();
#pragma unroll
        for (int nt = 0; nt < 4; ++nt) {
          acc[nt] = MFMA16(W[S & 1][nt], ha[S], acc[nt]);
          if (S + 2 < 28) W[S & 1][nt] = W2F(nt, S + 2);
          SB();
        }
      }
    }
    STAMP(22);
    BAR();
    *(LAS f4*)(sm + L_W2X + w * 2048 + lane * 16) = kh ? acc[0] : acc[2];
    *(LAS f4*)(sm + L_W2X + w * 2048 + 1024 + lane * 16) = kh ? acc[1] : acc[3];
    BAR();
    const f4 o0 = ldsf4(L_W2X + (w ^ 4) * 2048 + lane * 16), o1 = ldsf4(L_W2X + (w ^ 4) * 2048 + 1024 + lane * 16);
    const f4 t0 = (kh ? acc[2] : acc[0]) + o0, t1 = (kh ? acc[3] : acc[1]) + o1;
    const float gb[8] = {bflo(gbR[0][0]), bfhi(gbR[0][0]), bflo(gbR[0][1]), bfhi(gbR[0][1]), bflo(gbR[1][0]), bfhi(gbR[1][0]), bflo(gbR[1][1]), bfhi(gbR[1][1])};
    const float bb[8] = {bflo(b2R[0][0]), bfhi(b2R[0][0]), bflo(b2R[0][1]), bfhi(b2R[0][1]), bflo(b2R[1][0]), bfhi(b2R[1][0]), bflo(b2R[1][1]), bfhi(b2R[1][1])};
    float o[8];
#pragma unroll
    for (int r = 0; r < 8; ++r) o[r] = x1[r] + gb[r] * ((r < 4 ? t0[r & 3] : t1[r & 3]) + bb[r]);
    auto r0 = __builtin_amdgcn_permlane16_swap(pk(o[0], o[1]), pk(o[4], o[5]), false, false);
    auto r1 = __builtin_amdgcn_permlane16_swap(pk(o[2], o[3]), pk(o[6], o[7]), false, false);
    *(v4u*)(p.out + (size_t)orow * DM + 64 * ct + 32 * kh + (g & 1) * 16 + (g >> 1) * 8) = v4u{r0[0], r1[0], r0[1], r1[1]};
  }
  STAMP(15);
  STAMP_FLUSH();
  __syncthreads();
  if (tid == 0) {
    unsigned long long t1 = __builtin_amdgcn_s_memrealtime();
    unsigned* hw = (unsigned*)(p.ws + 47 * 1024);
    atomicMax(hw, ~(unsigned)hp_t0); atomicMax(hw + 1, (unsigned)t1);
    __threadfence();
    unsigned n = atomicAdd(hw + 2, 1u);
    if (n == 255) { __threadfence(); unsigned a = ~atomicMax(hw, 0u), e = atomicMax(hw + 1, 0u); printf("HPRINT in-kernel duration %.2f us\n", (e - a) * 0.01); }
  }
}

__global__ void zero_flags(uint4* p) { p[blockIdx.x * 256 + threadIdx.x] = make_uint4(0, 0, 0, 0); }

extern "C" void kernel_launch(void* const* d_in, const int* in_sizes, int n_in, void* d_out, int out_size, void* d_ws, size_t ws_size, hipStream_t stream) {
  Params p;
  p.x = (const u16*)d_in[0]; p.wqkv = (const u16*)d_in[1]; p.wo = (const u16*)d_in[2];
  p.w1 = (const u16*)d_in[4]; p.b1 = (const u16*)d_in[5]; p.w2 = (const u16*)d_in[6]; p.b2 = (const u16*)d_in[7];
  p.c = (const u16*)d_in[8]; p.wmod = (const u16*)d_in[9]; p.out = (u16*)d_out; p.ws = (char*)d_ws;
  zero_flags<<<12, 256, 0, stream>>>((uint4*)d_ws);
  fused_block<<<256, 512, 0, stream>>>(p);
}
```

```cpp
#include <hip/hip_runtime.h>
#include <hip/hip_bf16.h>
#include <stdint.h>

typedef unsigned short u16;
typedef short v4s __attribute__((ext_vector_type(4)));
typedef short v8s __attribute__((ext_vector_type(8)));
typedef float f2 __attribute__((ext_vector_type(2)));
typedef float f4 __attribute__((ext_vector_type(4)));
typedef float f16v __attribute__((ext_vector_type(16)));
typedef unsigned v2u __attribute__((ext_vector_type(2)));
typedef unsigned v4u __attribute__((ext_vector_type(4)));
typedef __bf16 b2 __attribute__((ext_vector_type(2)));
#define LAS __attribute__((address_space(3)))
#define GAS __attribute__((address_space(1)))
#define DI __device__ __forceinline__
typedef __amdgpu_buffer_rsrc_t srd_t;

constexpr int SEQ = 2048, DM = 512, FF = 1792;
constexpr unsigned WS_FLAG = 0;
constexpr unsigned WS_MOD = 128 << 10;
constexpr unsigned WS_ST1 = 192 << 10;
constexpr unsigned WS_ST2A = 512 << 10;
constexpr unsigned WS_ST2B = 1024 << 10;
constexpr unsigned WS_HF = 2u << 20;
constexpr unsigned WS_KV = 4u << 20;
constexpr unsigned WS_OF = 8u << 20;
constexpr unsigned WS_X1F = 10u << 20;
constexpr unsigned WS_H1F = 12u << 20;
constexpr unsigned WS_TQ = 19u << 20;
constexpr unsigned WS_TW1 = WS_TQ + (1536u << 10);
constexpr unsigned WS_TWO = WS_TW1 + (1792u << 10);
constexpr unsigned WS_TW2 = WS_TWO + (512u << 10);
constexpr unsigned WS_STAMP = 25u << 20;
constexpr unsigned WS_BYTES = 27u << 20;
static_assert(WS_TW2 + (1792u << 10) <= WS_STAMP, "ws");
constexpr unsigned K0_CA = 0, K0_M2 = 32768;
constexpr unsigned K0_XS = 65536;
constexpr unsigned K0_SQ = 73728, K0_S1 = 79872;
constexpr unsigned K0_CAP = 87040, K0_SC = 88064;
constexpr unsigned K0_SO = 98304;
constexpr unsigned L_WQKV = 0;
constexpr unsigned L_PARTU = 98304;
constexpr unsigned L_MODC = 154112;
constexpr unsigned L_B2C = L_MODC + 6144;
constexpr unsigned L_AK = 98304;
constexpr unsigned KPL = 6144;
constexpr unsigned L_AV = L_AK + 4 * KPL;
constexpr unsigned VPL = 6208, VHD = 12416;
constexpr unsigned L_ONE = L_AV + 2 * VHD;
constexpr unsigned L_AQ = L_ONE + 6400;
constexpr unsigned L_PERS = L_AQ + 8192;
static_assert(L_MODC == L_AQ && L_B2C + 128 <= L_PERS, "mod copy");
static_assert(L_PERS + 1024 <= 163840, "lds");
static_assert((L_AV % 256) == 0 && (L_ONE % 256) == 0, "bank plan");
constexpr unsigned L_WO = 0;
constexpr unsigned L_W1 = 65536;
constexpr unsigned L_W1S = 0;
constexpr unsigned L_XCH = 16384;
constexpr unsigned L_XF = 16384 + 12288;
constexpr unsigned L_W2X = 98304;

struct Params { const u16* x; const u16* wqkv; const u16* wo; const u16* w1; const u16* b1; const u16* w2; const u16* b2; const u16* c; const u16* wmod; u16* out; char* ws; };

DI float bflo(unsigned v) { return __uint_as_float(v << 16); }
DI float bfhi(unsigned v) { return __uint_as_float(v & 0xffff0000u); }
DI unsigned pk(float a, float b) { f2 v = {a, b}; b2 r = __builtin_convertvector(v, b2); return __builtin_bit_cast(unsigned, r); }
DI float rbf(float a) { return bflo(pk(a, 0.f)); }
DI v8s cat(v4s a, v4s b) { return __builtin_shufflevector(a, b, 0, 1, 2, 3, 4, 5, 6, 7); }
DI v4s trd(unsigned a) { return __builtin_amdgcn_ds_read_tr16_b64_v4i16((LAS v4s*)(uintptr_t)a); }
DI v8s asv8(v4u v) { return __builtin_bit_cast(v8s, v); }
DI v4u asv4u(v8s v) { return __builtin_bit_cast(v4u, v); }
DI v8s lds16(unsigned a) { return *(const LAS v8s*)(uintptr_t)a; }
DI f4 ldsf4(unsigned a) { return *(const LAS f4*)(uintptr_t)a; }
DI v2u lds8(unsigned a) { return *(const LAS v2u*)(uintptr_t)a; }
#define MFMA16(a, b, c) __builtin_amdgcn_mfma_f32_16x16x32_bf16((a), (b), (c), 0, 0, 0)
#define MFMA32(a, b, c) __builtin_amdgcn_mfma_f32_32x32x16_bf16((a), (b), (c), 0, 0, 0)
#define BAR() asm volatile("s_waitcnt lgkmcnt(0)\n\ts_barrier" ::: "memory")
#define VM0() asm volatile("s_waitcnt vmcnt(0)" ::: "memory")
#define LG0() asm volatile("s_waitcnt lgkmcnt(0)" ::: "memory")
#define CFENCE() asm volatile("" ::: "memory")
#define SB() __builtin_amdgcn_sched_barrier(0)
DI unsigned opq(unsigned v) { asm volatile("" : "+v"(v)); return v; }
DI void glds(srd_t r, unsigned voff, unsigned soff, unsigned ldsaddr) {
  asm volatile("s_mov_b32 m0, %0\n\ts_nop 0\n\tbuffer_load_dwordx4 %1, %2, %3 offen lds" ::"s"(ldsaddr), "v"(voff), "s"(r), "s"(soff) : "memory");
}
DI void glds1(srd_t r, unsigned voff, unsigned soff, unsigned ldsaddr) {
  asm volatile("s_mov_b32 m0, %0\n\ts_nop 0\n\tbuffer_load_dwordx4 %1, %2, %3 offen sc1 lds" ::"s"(ldsaddr), "v"(voff), "s"(r), "s"(soff) : "memory");
}
DI void glds4(srd_t r, unsigned voff, unsigned soff, unsigned ldsaddr) {
  asm volatile("s_mov_b32 m0, %0\n\ts_nop 0\n\tbuffer_load_dwordx4 %1, %2, %3 offen lds\n\tbuffer_load_dwordx4 %1, %2, %3 offen offset:1024 lds\n\t"
               "buffer_load_dwordx4 %1, %2, %3 offen offset:2048 lds\n\tbuffer_load_dwordx4 %1, %2, %3 offen offset:3072 lds" ::"s"(ldsaddr), "v"(voff), "s"(r), "s"(soff) : "memory");
}
DI void glds2x1(srd_t r, unsigned voff, unsigned soff, unsigned ldsaddr) {
  asm volatile("s_mov_b32 m0, %0\n\ts_nop 0\n\tbuffer_load_dwordx4 %1, %2, %3 offen sc1 lds\n\tbuffer_load_dwordx4 %1, %2, %3 offen offset:1024 sc1 lds" ::"s"(ldsaddr), "v"(voff), "s"(r), "s"(soff) : "memory");
}
DI v4u ld16(srd_t r, unsigned voff, unsigned soff) { return __builtin_amdgcn_raw_buffer_load_b128(r, voff, soff, 16); }
DI v2u ld8(srd_t r, unsigned voff, unsigned soff) { return __builtin_amdgcn_raw_buffer_load_b64(r, voff, soff, 16); }
DI v4u ldp16(srd_t r, unsigned voff, unsigned soff) { return __builtin_amdgcn_raw_buffer_load_b128(r, voff, soff, 0); }
DI void stp16(srd_t r, v4u v, unsigned off) { __builtin_amdgcn_raw_buffer_store_b128(v, r, off, 0, 0); }
DI void st16(srd_t r, v4u v, unsigned off) { __builtin_amdgcn_raw_buffer_store_b128(v, r, off, 0, 16); }
DI void st8(srd_t r, v2u v, unsigned off) { __builtin_amdgcn_raw_buffer_store_b64(v, r, off, 0, 16); }
DI float sum16(float v) { auto r = __builtin_amdgcn_permlane16_swap(__float_as_uint(v), __float_as_uint(v), false, false); return __uint_as_float(r[0]) + __uint_as_float(r[1]); }
DI float sum32(float v) { auto r = __builtin_amdgcn_permlane32_swap(__float_as_uint(v), __float_as_uint(v), false, false); return __uint_as_float(r[0]) + __uint_as_float(r[1]); }
DI float max32(float v) { auto r = __builtin_amdgcn_permlane32_swap(__float_as_uint(v), __float_as_uint(v), false, false); return fmaxf(__uint_as_float(r[0]), __uint_as_float(r[1])); }
DI float sumg(float v) { return sum32(sum16(v)); }
#define RLX_AGENT __ATOMIC_RELAXED, __HIP_MEMORY_SCOPE_AGENT
DI void arrive(char* ws, unsigned off) { (void)__hip_atomic_fetch_add((GAS unsigned*)(ws + off), 1u, RLX_AGENT); }
DI void wait_count(char* ws, unsigned off, unsigned n) {
  GAS unsigned* p = (GAS unsigned*)(ws + off);
  for (unsigned spins = 0; spins < (1u << 17); ++spins)
    if (__builtin_amdgcn_readfirstlane(__hip_atomic_load(p, RLX_AGENT)) >= n) break;
  CFENCE();
}
#define STAMP(k)
#define STAMP_FLUSH(o)
#define CSTAMP(k)
DI float silu(float v) { return v * __builtin_amdgcn_rcpf(1.f + __builtin_amdgcn_exp2f(-1.4426950408889634f * v)); }

DI v8s trfragA(unsigned img, int half, int lane) {
  const int g = lane >> 4, q = (lane & 15) >> 2, pp = lane & 3;
  const unsigned a = img + (4 * g + q) * 64 + ((half ^ (g & 1)) * 32) + pp * 8;
  return cat(trd(a), trd(a + 1024));
}

template <bool FULL>
DI void attention(int t, int hd, int kt0, int lane, f16v mU, f16v mL, f16v& O, srd_t rWS, unsigned wo_src, unsigned w1_src, int w, const u16* xres, v2u& xr0, v2u& xr1, unsigned& stampv) {
  const int qr = lane & 31, h = lane >> 5, blk = (lane >> 4) & 1, q = (lane & 15) >> 2, pp = lane & 3;
  const v8s Qf = lds16(L_AQ + (hd * 2 + h) * 2048 + (32 * t + qr) * 16);
  const unsigned kb = opq(L_AK + (hd * 2 + h) * KPL + (32 * t + qr) * 16);
  const unsigned vb = opq((blk == 0 ? L_AV + hd * VHD + (pp >> 1) * VPL + (pp & 1) * 8 + (4 * h + q) * 16 : L_ONE + ((hd * 128 + (h ? 0 : 128)) & 255)) + t * 512);
  const f16v zero = {};
#define RIDE(n) do { if ((n) < 2) glds4(rWS, lane * 16, wo_src + (w * 2 + (n)) * 4096, L_WO + (w * 2 + (n)) * 4096); \
                     else glds4(rWS, lane * 16, w1_src + w * 4096, L_W1 + w * 4096); } while (0)
  if (FULL) {
    v8s Kf[9];
#pragma unroll
    for (int kt = 0; kt < 9; ++kt) Kf[kt] = lds16(kb + kt * 512);
    v8s V0 = cat(trd(vb), trd(vb + 128)), V1 = cat(trd(vb + 256), trd(vb + 384));
    f16v S[2];
    float m0 = -3e38f, m1 = -3e38f;
    S[0] = MFMA32(Kf[0], Qf, mL);
    SB();
#pragma unroll
    for (int kt = 0; kt < 9; ++kt) {
      if (kt < 8) S[(kt + 1) & 1] = MFMA32(Kf[kt + 1], Qf, kt + 1 == 8 ? mU : zero);
      SB();
#pragma unroll
      for (int r = 0; r < 16; r += 4) { m0 = fmaxf(fmaxf(m0, S[kt & 1][r]), S[kt & 1][r + 1]); m1 = fmaxf(fmaxf(m1, S[kt & 1][r + 2]), S[kt & 1][r + 3]); }
      SB();
    }
    const float m = max32(fmaxf(m0, m1));
    STAMP(23);
    f16v nm;
#pragma unroll
    for (int r = 0; r < 16; ++r) { nm[r] = -m; mU[r] -= m; mL[r] -= m; }
    O = zero;
    S[0] = MFMA32(Kf[0], Qf, mL);
    SB();
#pragma unroll
    for (int kt = 0; kt < 9; ++kt) {
      if (kt < 8) S[(kt + 1) & 1] = MFMA32(Kf[kt + 1], Qf, kt + 1 == 8 ? mU : nm);
      if (kt == 0) RIDE(0);
      if (kt == 3) RIDE(1);
      if (kt == 6) RIDE(2);
      SB();
      v4u P0, P1;
#pragma unroll
      for (int r = 0; r < 4; ++r) P0[r] = pk(__builtin_amdgcn_exp2f(S[kt & 1][2 * r]), __builtin_amdgcn_exp2f(S[kt & 1][2 * r + 1]));
      O = MFMA32(V0, asv8(P0), O);
      if (kt < 8) V0 = cat(trd(vb + (kt + 1) * 512), trd(vb + (kt + 1) * 512 + 128));
      if (kt == 2) xr0 = *(const v2u*)xres;
      if (kt == 5) xr1 = *(const v2u*)(xres + 16);
      SB();
#pragma unroll
      for (int r = 0; r < 4; ++r) P1[r] = pk(__builtin_amdgcn_exp2f(S[kt & 1][8 + 2 * r]), __builtin_amdgcn_exp2f(S[kt & 1][8 + 2 * r + 1]));
      O = MFMA32(V1, asv8(P1), O);
      if (kt < 8) V1 = cat(trd(vb + (kt + 1) * 512 + 256), trd(vb + (kt + 1) * 512 + 384));
      SB();
    }
  } else {
#pragma unroll
    for (int n = 0; n < 3; ++n) RIDE(n);
    xr0 = *(const v2u*)xres; xr1 = *(const v2u*)(xres + 16);
    float m = -3e38f;
#pragma unroll
    for (int kt = 0; kt < 9; ++kt) {
      if (kt >= kt0) {
        const f16v S = MFMA32(lds16(kb + kt * 512), Qf, kt == 8 ? mU : zero);
#pragma unroll
        for (int r = 0; r < 16; r += 2) m = fmaxf(fmaxf(m, S[r]), S[r + 1]);
      }
    }
    m = max32(m);
    f16v nm;
#pragma unroll
    for (int r = 0; r < 16; ++r) { nm[r] = -m; mU[r] -= m; }
    O = zero;
#pragma unroll
    for (int kt = 0; kt < 9; ++kt) {
      if (kt >= kt0) {
        const f16v S = MFMA32(lds16(kb + kt * 512), Qf, kt == 8 ? mU : nm);
        v4u P0, P1;
#pragma unroll
        for (int r = 0; r < 4; ++r) {
          P0[r] = pk(__builtin_amdgcn_exp2f(S[2 * r]), __builtin_amdgcn_exp2f(S[2 * r + 1]));
          P1[r] = pk(__builtin_amdgcn_exp2f(S[8 + 2 * r]), __builtin_amdgcn_exp2f(S[8 + 2 * r + 1]));
        }
        const v8s V0 = cat(trd(vb + kt * 512), trd(vb + kt * 512 + 128));
        const v8s V1 = cat(trd(vb + kt * 512 + 256), trd(vb + kt * 512 + 384));
        O = MFMA32(V0, asv8(P0), O);
        O = MFMA32(V1, asv8(P1), O);
      }
    }
  }
#undef RIDE
}

__global__ void __launch_bounds__(512, 2) prep_block(Params p) {
  __shared__ __attribute__((aligned(16))) char smem[98304 + 12288];
  asm volatile("" ::"s"((unsigned)(uintptr_t)(LAS char*)smem));
  const int tid = threadIdx.x, lane = tid & 63;
  const int w = __builtin_amdgcn_readfirstlane(tid >> 6);
  const int b = blockIdx.x, ci = (b & 7) * 2 + ((b >> 3) & 1), cj = b >> 4;
  const int tok = lane & 15, g = lane >> 4, q4 = (lane & 15) >> 2, pp = lane & 3;
  const int par = cj & 1;
  unsigned stampv = 0;
  const srd_t rWS = __builtin_amdgcn_make_buffer_rsrc(p.ws, 0, WS_BYTES, 0x00020000);
  const int rA = lane >> 2, pA = lane & 3;
  const unsigned colA = (((pA >> 1) ^ ((rA >> 2) & 1)) * 32) + (pA & 1) * 16;
  const int rB = lane >> 3, pB = lane & 7;
  const unsigned colB = (((pB >> 1) ^ ((rB >> 1) & 3)) * 32) + (pB & 1) * 16;
  STAMP(0);
  const bool m2 = (cj % 3 == 0) && cj < 15;
  const int m2id = ci * 5 + cj / 3;
  const srd_t rWmod = __builtin_amdgcn_make_buffer_rsrc((void*)p.wmod, 0, DM * 3072 * 2, 0x00020000);
  if (w < 4) {
    const srd_t rX = __builtin_amdgcn_make_buffer_rsrc((void*)p.x, 0, SEQ * DM * 2, 0x00020000);
#pragma unroll
    for (int ii = 0; ii < 8; ++ii) glds(rWmod, rA * 6144 + colA, (w * 8 + ii) * 16 * 6144 + (512 + 32 * cj) * 2, K0_CA + (w * 8 + ii) * 1024);
#pragma unroll
    for (int u = 0; u < 2; ++u) glds(rX, rA * 1024 + pA * 16, (128 * ci + 16 * (2 * w + u)) * 1024 + 64 * cj, K0_XS + (2 * w + u) * 1024);
  } else if (w == 4) {
    if (m2) {
      const int mcol = m2id < 16 ? m2id * 32 : 1024 + (m2id - 16) * 32;
#pragma unroll 8
      for (int ii = 0; ii < 32; ++ii) glds(rWmod, rA * 6144 + colA, ii * 16 * 6144 + mcol * 2, K0_M2 + ii * 1024);
    }
  } else if (w == 5) {
    const srd_t r = __builtin_amdgcn_make_buffer_rsrc((void*)p.wqkv, 0, DM * 1536 * 2, 0x00020000);
#pragma unroll
    for (int e = 0; e < 6; ++e) glds(r, rA * 3072 + colA, (32 * ci + 16 * (e & 1)) * 3072 + ((e >> 1) * 512 + 32 * cj) * 2, K0_SQ + e * 1024);
  } else if (w == 6) {
    const srd_t r = __builtin_amdgcn_make_buffer_rsrc((void*)p.w1, 0, DM * FF * 2, 0x00020000);
#pragma unroll
    for (int e = 0; e < 6; ++e) glds(r, rA * (FF * 2) + colA, (32 * ci + 16 * (e & 1)) * (FF * 2) + (112 * cj + 16 * par + (e >> 1) * 32) * 2, K0_S1 + e * 1024);
    glds(r, (lane >> 1) * (FF * 2) + (lane & 1) * 16, 32 * ci * (FF * 2) + (112 * cj + (par ? 0 : 96)) * 2, K0_S1 + 6144);
  } else {
    if (b < 128) {
      const srd_t r = __builtin_amdgcn_make_buffer_rsrc((void*)p.wo, 0, DM * DM * 2, 0x00020000);
#pragma unroll
      for (int e = 0; e < 4; ++e) glds(r, rB * 1024 + colB, (32 * (b >> 3) + 8 * e) * 1024 + (b & 7) * 128, K0_SO + e * 1024);
    }
    if (b < 224) {
      const srd_t r = __builtin_amdgcn_make_buffer_rsrc((void*)p.w2, 0, FF * DM * 2, 0x00020000);
#pragma unroll
      for (int e = 0; e < 8; ++e) { const int id = 2 * b + (e >> 2); glds(r, rB * 1024 + colB, (32 * (id >> 3) + 8 * (e & 3)) * 1024 + (id & 7) * 128, K0_SO + 4096 + e * 1024); }
    }
  }
  v8s sf[16];
  if (w <= 4) {
    const v4u cr = *(const v4u*)(p.c + 8 * lane);
    const unsigned sc = K0_SC + w * 1024;
    *(LAS v4u*)(uintptr_t)(sc + lane * 16) = v4u{pk(silu(bflo(cr[0])), silu(bfhi(cr[0]))), pk(silu(bflo(cr[1])), silu(bfhi(cr[1]))), pk(silu(bflo(cr[2])), silu(bfhi(cr[2]))), pk(silu(bflo(cr[3])), silu(bfhi(cr[3])))};
    LG0();
    if (w < 4) {
#pragma unroll
      for (int s = 0; s < 4; ++s) { const v2u lo = lds8(sc + (32 * (4 * w + s) + 4 * g) * 2), hi = lds8(sc + (32 * (4 * w + s) + 16 + 4 * g) * 2); sf[s] = asv8(v4u{lo[0], lo[1], hi[0], hi[1]}); }
    } else {
#pragma unroll
      for (int s = 0; s < 16; ++s) { const v2u lo = lds8(sc + (32 * s + 4 * g) * 2), hi = lds8(sc + (32 * s + 16 + 4 * g) * 2); sf[s] = asv8(v4u{lo[0], lo[1], hi[0], hi[1]}); }
    }
  }
  VM0();
  STAMP(1);
  if (w >= 5) {
    if (w == 5) {
#pragma unroll
      for (int nt = 0; nt < 6; ++nt) stp16(rWS, asv4u(trfragA(K0_SQ + (nt >> 1) * 2048, nt & 1, lane)), WS_TQ + ((cj * 6 + nt) * 16 + ci) * 1024 + lane * 16);
    } else if (w == 6) {
#pragma unroll
      for (int nt = 0; nt < 6; ++nt) stp16(rWS, asv4u(trfragA(K0_S1 + (nt >> 1) * 2048, nt & 1, lane)), WS_TW1 + ((cj * 7 + nt) * 16 + ci) * 1024 + lane * 16);
      { const unsigned a = K0_S1 + 6144 + (4 * g + q4) * 32 + pp * 8; stp16(rWS, asv4u(cat(trd(a), trd(a + 512))), WS_TW1 + ((cj * 7 + 6) * 16 + ci) * 1024 + lane * 16); }
    } else {
      if (b < 128) {
        const int row = 16 * (g >> 1) + 4 * (g & 1) + q4;
#pragma unroll
        for (int nt = 0; nt < 4; ++nt) { const unsigned a = K0_SO + row * 128 + ((nt ^ ((row >> 1) & 3)) * 32) + pp * 8;
          stp16(rWS, asv4u(cat(trd(a), trd(a + 1024))), WS_TWO + (((b & 7) * 16 + (b >> 3)) * 4 + nt) * 1024 + lane * 16); }
      }
      if (b < 224) {
        const int row = 4 * g + q4;
#pragma unroll
        for (int e = 0; e < 8; ++e) { const int id = 2 * b + (e >> 2), nt = e & 3; const unsigned a = K0_SO + 4096 + (e >> 2) * 4096 + row * 128 + ((nt ^ ((row >> 1) & 3)) * 32) + pp * 8;
          stp16(rWS, asv4u(cat(trd(a), trd(a + 2048))), WS_TW2 + (((id & 7) * 56 + (id >> 3)) * 4 + nt) * 1024 + lane * 16); }
      }
    }
  } else if (w == 4) {
    if (m2) {
      const int mcol = m2id < 16 ? m2id * 32 : 1024 + (m2id - 16) * 32;
      f4 a0 = {0, 0, 0, 0}, a1 = {0, 0, 0, 0};
#pragma unroll
      for (int s0 = 0; s0 < 16; s0 += 4) {
        v8s W[4][2];
#pragma unroll
        for (int s = 0; s < 4; ++s) { W[s][0] = trfragA(K0_M2 + (s0 + s) * 2048, 0, lane); W[s][1] = trfragA(K0_M2 + (s0 + s) * 2048, 1, lane); }
#pragma unroll
        for (int s = 0; s < 4; ++s) { a0 = MFMA16(W[s][0], sf[s0 + s], a0); a1 = MFMA16(W[s][1], sf[s0 + s], a1); }
      }
      if (tok == 0) {
        *(v2u*)(p.ws + WS_MOD + mcol * 2 + g * 8) = v2u{pk(a0[0], a0[1]), pk(a0[2], a0[3])};
        *(v2u*)(p.ws + WS_MOD + mcol * 2 + 32 + g * 8) = v2u{pk(a1[0], a1[1]), pk(a1[2], a1[3])};
      }
    }
  } else {
    f4 a0 = {0, 0, 0, 0}, a1 = {0, 0, 0, 0};
    v8s W[4][2];
#pragma unroll
    for (int s = 0; s < 4; ++s) { W[s][0] = trfragA(K0_CA + (4 * w + s) * 2048, 0, lane); W[s][1] = trfragA(K0_CA + (4 * w + s) * 2048, 1, lane); }
#pragma unroll
    for (int s = 0; s < 4; ++s) { a0 = MFMA16(W[s][0], sf[s], a0); a1 = MFMA16(W[s][1], sf[s], a1); }
    if (tok == 0) { *(LAS f4*)(uintptr_t)(K0_CAP + (w * 32 + 4 * g) * 4) = a0; *(LAS f4*)(uintptr_t)(K0_CAP + (w * 32 + 16 + 4 * g) * 4) = a1; }
  }
  if (tid < 16 && b < 160) ((GAS v4u*)(p.ws + WS_FLAG))[b * 16 + tid] = v4u{0, 0, 0, 0};
  BAR();
  STAMP(2);
  if (w < 4) {
    f4 a0 = ldsf4(K0_CAP + 4 * g * 4), a1 = ldsf4(K0_CAP + (16 + 4 * g) * 4);
#pragma unroll
    for (int e = 1; e < 4; ++e) { a0 += ldsf4(K0_CAP + (e * 32 + 4 * g) * 4); a1 += ldsf4(K0_CAP + (e * 32 + 16 + 4 * g) * 4); }
    float wv[8];
#pragma unroll
    for (int r = 0; r < 4; ++r) { wv[r] = rbf(1.f + rbf(a0[r])); wv[4 + r] = rbf(1.f + rbf(a1[r])); }
#pragma unroll
    for (int u = 0; u < 2; ++u) {
      const unsigned a = K0_XS + (2 * w + u) * 1024 + tok * 64 + g * 8;
      const v2u xl = lds8(a), xh = lds8(a + 32);
      const float x0 = bflo(xl[0]), x1 = bfhi(xl[0]), x2 = bflo(xl[1]), x3 = bfhi(xl[1]);
      const float x4 = bflo(xh[0]), x5 = bfhi(xh[0]), x6 = bflo(xh[1]), x7 = bfhi(xh[1]);
      const int T = 2 * w + u;
      stp16(rWS, v4u{pk(x0 * wv[0], x1 * wv[1]), pk(x2 * wv[2], x3 * wv[3]), pk(x4 * wv[4], x5 * wv[5]), pk(x6 * wv[6], x7 * wv[7])},
            WS_HF + ((ci * 8 + T) * 16 + cj) * 1024 + lane * 16);
      const float s1 = sumg(((x0 + x1) + (x2 + x3)) + ((x4 + x5) + (x6 + x7)));
      const float s2 = sumg(((x0 * x0 + x1 * x1) + (x2 * x2 + x3 * x3)) + ((x4 * x4 + x5 * x5) + (x6 * x6 + x7 * x7)));
      if (g == 0) *(v2u*)(p.ws + WS_ST1 + ((ci * 16 + cj) * 128 + 16 * T + tok) * 8) = v2u{__float_as_uint(s1), __float_as_uint(s2)};
    }
    if (ci == 0 && w == 0 && tok == 0) {
      *(v2u*)(p.ws + WS_MOD + (512 + 32 * cj) * 2 + g * 8) = v2u{pk(wv[0], wv[1]), pk(wv[2], wv[3])};
      *(v2u*)(p.ws + WS_MOD + (512 + 32 * cj) * 2 + 32 + g * 8) = v2u{pk(wv[4], wv[5]), pk(wv[6], wv[7])};
    }
  }
  STAMP(3);
  STAMP_FLUSH(524288);
}

__global__ void __launch_bounds__(512, 2) fused_block(Params p) {
  __shared__ __attribute__((aligned(16))) char smem[163840];
  LAS char* sm = (LAS char*)smem;
  const int tid = threadIdx.x, lane = tid & 63;
  const int w = __builtin_amdgcn_readfirstlane(tid >> 6);
  const int b = blockIdx.x, ci = (b & 7) * 2 + ((b >> 3) & 1), cj = b >> 4;
  const int tok = lane & 15, g = lane >> 4;
  const int hf = cj & 1, ct = cj >> 1, par = cj & 1;
  const int wq = w < 4 ? w : w - 1;
  unsigned stampv = 0;
  const srd_t rWS = __builtin_amdgcn_make_buffer_rsrc(p.ws, 0, WS_BYTES, 0x00020000);
  STAMP(0);
  CSTAMP(40);

  f4 accQ[2], accKV[4];
  float rstd, nmu;
  v2u gaR[2], cbR[2], sbR[2], gbR[2], b2R[2];
  v8s aux2[2];
  float Cw, Cw2, Cws, Cs, Cs2;
  f16v mU, mL;
  const int tt = w & 3, kh = w >> 2;
  const int ocol = 64 * ct + 32 * kh + 4 * g;
  {
    v4u b2v = {0, 0, 0, 0};
    if (w == 6 && lane < 8) b2v = *(const v4u*)(p.b2 + 64 * ct + lane * 8);
#pragma unroll
    for (int e = 0; e < 3; ++e) glds4(rWS, lane * 16, WS_TQ + cj * 98304 + (w * 3 + e) * 4096, L_WQKV + (w * 3 + e) * 4096);
    if (w == 0) glds4(rWS, lane * 16, WS_MOD, L_MODC);
    if (w == 1) { glds(rWS, lane * 16, WS_MOD + 4096, L_MODC + 4096); glds(rWS, lane * 16, WS_MOD + 5120, L_MODC + 5120); }
    const unsigned bq = opq(L_WQKV + lane * 16), bv = opq(L_WQKV + 65536 + lane * 16);
#define QF(nt, s) lds16(((nt) < 4 ? bq : bv) + ((nt) & 3) * 16384 + (s) * 1024)
    v8s act[16];
    const unsigned hsrc = WS_HF + (ci * 8 + w) * 16 * 1024;
#pragma unroll
    for (int s = 0; s < 4; ++s) act[s] = asv8(ldp16(rWS, lane * 16, hsrc + s * 1024));
    if (tid < 400) *(LAS v4u*)(sm + L_ONE + tid * 16) = v4u{0x3f803f80u, 0x3f803f80u, 0x3f803f80u, 0x3f803f80u};
    if (w == 6 && lane < 8) *(LAS v4u*)(sm + L_B2C + lane * 16) = b2v;
    asm volatile("s_waitcnt vmcnt(4)" ::: "memory");
    BAR();
    STAMP(3);
    {
      f4 au[6];
      v8s W[2][6], a[2];
      const unsigned c0 = opq(bq + w * 2048), d0 = opq(bv + w * 2048);
#pragma unroll
      for (int u = 0; u < 2; ++u) {
        const unsigned o = L_MODC + ((tok == 0 ? 512 : 0) + 32 * (2 * w + u) + 4 * g) * 2;
        const v2u lo = lds8(o), hi = lds8(o + 32);
        a[u] = asv8(v4u{lo[0], lo[1], hi[0], hi[1]});
#pragma unroll
        for (int nt = 0; nt < 6; ++nt) W[u][nt] = lds16((nt < 4 ? c0 : d0) + (nt & 3) * 16384 + u * 1024);
      }
#pragma unroll
      for (int nt = 0; nt < 6; ++nt) au[nt] = f4{0, 0, 0, 0};
#pragma unroll
      for (int u = 0; u < 2; ++u)
#pragma unroll
        for (int nt = 0; nt < 6; ++nt) au[nt] = MFMA16(W[u][nt], a[u], au[nt]);
      if (tok < 2) {
#pragma unroll
        for (int nt = 0; nt < 6; ++nt) *(LAS f4*)(sm + L_PARTU + ((w * 2 + tok) * 96 + nt * 16 + 4 * g) * 4) = au[nt];
      }
    }
    BAR();
    if (tid >= 320) {
      const int e0 = tid - 320;
      float s = 0;
#pragma unroll
      for (int e = 0; e < 8; ++e) s += *(const LAS float*)(sm + L_PARTU + (e * 192 + e0) * 4);
      *(LAS float*)(sm + L_PERS + e0 * 4) = s;
    }
    STAMP(16);
    v2u st[4];
#pragma unroll
    for (int nt = 0; nt < 4; ++nt) accKV[nt] = f4{0, 0, 0, 0};
    {
      v8s W[2][4];
#pragma unroll
      for (int s = 0; s < 2; ++s)
#pragma unroll
        for (int nt = 0; nt < 4; ++nt) W[s][nt] = QF(2 + nt, s);
      SB();
#pragma unroll
      for (int s = 0; s < 16; ++s) {
        if (s < 4) { act[4 + 2 * s] = asv8(ldp16(rWS, lane * 16, hsrc + (4 + 2 * s) * 1024)); act[5 + 2 * s] = asv8(ldp16(rWS, lane * 16, hsrc + (5 + 2 * s) * 1024)); }
        else if (s < 8) act[8 + s] = asv8(ldp16(rWS, lane * 16, hsrc + (8 + s) * 1024));
        else if (s < 12) st[s - 8] = __builtin_amdgcn_raw_buffer_load_b64(rWS, ((4 * g + s - 8) * 128 + 16 * w + tok) * 8, WS_ST1 + ci * 16 * 128 * 8, 0);
#pragma unroll
        for (int nt = 0; nt < 4; ++nt) {
          accKV[nt] = MFMA16(W[s & 1][nt], act[s], accKV[nt]);
          if (s + 2 < 16) W[s & 1][nt] = QF(2 + nt, s + 2);
          SB();
        }
      }
    }
    float s1 = 0, s2 = 0;
#pragma unroll
    for (int e = 0; e < 4; ++e) { s1 += __uint_as_float(st[e][0]); s2 += __uint_as_float(st[e][1]); }
    s1 = sumg(s1); s2 = sumg(s2);
    const float mu = s1 * (1.f / 512), var = fmaxf(s2 * (1.f / 512) - mu * mu, 0.f);
    rstd = __builtin_amdgcn_rsqf(var + 1e-5f); nmu = -rstd * mu;
    STAMP(4);
    BAR();
    {
      v4u kk, vv;
      unsigned pkd[4][2];
#pragma unroll
      for (int nt = 0; nt < 4; ++nt) {
        const f4 u = ldsf4(L_PERS + (32 + nt * 16 + 4 * g) * 4), v = ldsf4(L_PERS + (96 + 32 + nt * 16 + 4 * g) * 4);
        pkd[nt][0] = pk(rstd * accKV[nt][0] + (nmu * u[0] + v[0]), rstd * accKV[nt][1] + (nmu * u[1] + v[1]));
        pkd[nt][1] = pk(rstd * accKV[nt][2] + (nmu * u[2] + v[2]), rstd * accKV[nt][3] + (nmu * u[3] + v[3]));
      }
      { auto r0 = __builtin_amdgcn_permlane16_swap(pkd[0][0], pkd[1][0], false, false); auto r1 = __builtin_amdgcn_permlane16_swap(pkd[0][1], pkd[1][1], false, false);
        kk = v4u{r0[0], r1[0], r0[1], r1[1]}; }
      { auto r0 = __builtin_amdgcn_permlane16_swap(pkd[2][0], pkd[3][0], false, false); auto r1 = __builtin_amdgcn_permlane16_swap(pkd[2][1], pkd[3][1], false, false);
        vv = v4u{r0[0], r1[0], r0[1], r1[1]}; }
      const int hd = g & 1, pl = g >> 1;
      st16(rWS, kk, WS_KV + (ci * 16 + cj) * 16384 + ((0 * 2 + hd) * 2 + pl) * 2048 + (16 * w + tok) * 16);
      st16(rWS, vv, WS_KV + (ci * 16 + cj) * 16384 + ((1 * 2 + hd) * 2 + pl) * 2048 + (16 * w + tok) * 16);
      *(LAS v4u*)(sm + L_AK + (hd * 2 + pl) * KPL + (256 + 16 * w + tok) * 16) = kk;
      *(LAS v4u*)(sm + L_AV + hd * VHD + pl * VPL + (256 + 16 * w + tok) * 16) = vv;
    }
#pragma unroll
    for (int nt = 0; nt < 2; ++nt) accQ[nt] = f4{0, 0, 0, 0};
    {
      v8s W[4][2];
#pragma unroll
      for (int s = 0; s < 4; ++s) { W[s][0] = QF(0, s); W[s][1] = QF(1, s); }
      SB();
#pragma unroll
      for (int s = 0; s < 16; ++s)
#pragma unroll
        for (int nt = 0; nt < 2; ++nt) {
          accQ[nt] = MFMA16(W[s & 3][nt], act[s], accQ[nt]);
          if (s + 4 < 16) W[s & 3][nt] = QF(nt, s + 4);
          SB();
        }
    }
    VM0();
    BAR();
    if (tid == 256) arrive(p.ws, WS_FLAG + 1 * 8192 + ci * 512 + cj * 4);
    STAMP(5);
    {
#pragma unroll
      for (int e = 0; e < 2; ++e) {
        gaR[e] = lds8(L_MODC + (1024 + ocol + 16 * e) * 2); sbR[e] = lds8(L_MODC + (1536 + ocol + 16 * e) * 2);
        cbR[e] = lds8(L_MODC + (2048 + ocol + 16 * e) * 2); gbR[e] = lds8(L_MODC + (2560 + ocol + 16 * e) * 2);
        b2R[e] = lds8(L_B2C + (32 * kh + 4 * g + 16 * e) * 2);
      }
      const v4u c0 = asv4u(lds16(L_MODC + (2048 + 8 * lane) * 2)), d0 = asv4u(lds16(L_MODC + (1536 + 8 * lane) * 2));
      v2u lo[2], hi[2];
#pragma unroll
      for (int u = 0; u < 2; ++u) { const unsigned o = (32 * (2 * w + u) + 4 * g) * 2 + L_MODC + (tok == 1 ? 1536 : 2048) * 2; lo[u] = lds8(o); hi[u] = lds8(o + 32); }
      BAR();
      const float qs = 0.25f * 1.4426950408889634f;
      unsigned pq[2][2];
#pragma unroll
      for (int nt = 0; nt < 2; ++nt) {
        const f4 u = ldsf4(L_PERS + (nt * 16 + 4 * g) * 4), v = ldsf4(L_PERS + (96 + nt * 16 + 4 * g) * 4);
        pq[nt][0] = pk(qs * (rstd * accQ[nt][0] + nmu * u[0] + v[0]), qs * (rstd * accQ[nt][1] + nmu * u[1] + v[1]));
        pq[nt][1] = pk(qs * (rstd * accQ[nt][2] + nmu * u[2] + v[2]), qs * (rstd * accQ[nt][3] + nmu * u[3] + v[3]));
      }
      auto r0 = __builtin_amdgcn_permlane16_swap(pq[0][0], pq[1][0], false, false); auto r1 = __builtin_amdgcn_permlane16_swap(pq[0][1], pq[1][1], false, false);
      *(LAS v4u*)(sm + L_AQ + ((g & 1) * 2 + (g >> 1)) * 2048 + (16 * w + tok) * 16) = v4u{r0[0], r1[0], r0[1], r1[1]};
      Cw = Cw2 = Cws = Cs = Cs2 = 0;
#pragma unroll
      for (int r = 0; r < 4; ++r) {
        const float w0 = rbf(1.f + bflo(c0[r])), w1 = rbf(1.f + bfhi(c0[r])), s0 = bflo(d0[r]), s1_ = bfhi(d0[r]);
        Cw += w0 + w1; Cw2 += w0 * w0 + w1 * w1; Cws += w0 * s0 + w1 * s1_; Cs += s0 + s1_; Cs2 += s0 * s0 + s1_ * s1_;
      }
#pragma unroll
      for (int o = 1; o < 16; o <<= 1) { Cw += __shfl_xor(Cw, o); Cw2 += __shfl_xor(Cw2, o); Cws += __shfl_xor(Cws, o); Cs += __shfl_xor(Cs, o); Cs2 += __shfl_xor(Cs2, o); }
      Cw = sumg(Cw); Cw2 = sumg(Cw2); Cws = sumg(Cws); Cs = sumg(Cs); Cs2 = sumg(Cs2);
#pragma unroll
      for (int u = 0; u < 2; ++u) {
        v4u t;
        t[0] = tok == 0 ? pk(1.f + bflo(lo[u][0]), 1.f + bfhi(lo[u][0])) : (tok == 1 ? lo[u][0] : 0x3f803f80u);
        t[1] = tok == 0 ? pk(1.f + bflo(lo[u][1]), 1.f + bfhi(lo[u][1])) : (tok == 1 ? lo[u][1] : 0x3f803f80u);
        t[2] = tok == 0 ? pk(1.f + bflo(hi[u][0]), 1.f + bfhi(hi[u][0])) : (tok == 1 ? hi[u][0] : 0x3f803f80u);
        t[3] = tok == 0 ? pk(1.f + bflo(hi[u][1]), 1.f + bfhi(hi[u][1])) : (tok == 1 ? hi[u][1] : 0x3f803f80u);
        aux2[u] = asv8(t);
      }
      const int qr = lane & 31, h = lane >> 5;
#pragma unroll
      for (int r = 0; r < 16; ++r) { const int kr = (r & 3) + 8 * (r >> 2) + 4 * h; mU[r] = kr > qr ? -1e30f : 0.f; mL[r] = kr > qr ? 0.f : -1e30f; }
      asm volatile("" : "+v"(mU), "+v"(mL), "+v"(Cw), "+v"(Cw2), "+v"(Cws), "+v"(Cs), "+v"(Cs2), "+v"(aux2[0]), "+v"(aux2[1]));
    }
    if (ci < 2) {
      for (int e = tid; e < 8 * 256; e += 512) {
        const int pln = e >> 8, key = e & 255;
        if (ci == 0 || key < 128) {
          const unsigned a = pln < 4 ? L_AK + pln * KPL : L_AV + ((pln - 4) >> 1) * VHD + (pln & 1) * VPL;
          *(LAS v4u*)(sm + a + key * 16) = v4u{0, 0, 0, 0};
        }
      }
    }
    if (w == 4) {
      if (ci >= 1) wait_count(p.ws, WS_FLAG + 1 * 8192 + (ci - 1) * 512 + cj * 4, 1);
      if (ci >= 2) wait_count(p.ws, WS_FLAG + 1 * 8192 + (ci - 2) * 512 + cj * 4, 1);
    }
    BAR();
    STAMP(6);
    {
      const unsigned dst = w < 4 ? L_AK + w * KPL : L_AV + ((w - 4) >> 1) * VHD + (w & 1) * VPL;
#pragma unroll
      for (int src = 0; src < 2; ++src)
        if (ci - 2 + src >= 0) glds2x1(rWS, lane * 16, WS_KV + ((ci - 2 + src) * 16 + cj) * 16384 + w * 2048, dst + src * 2048);
    }
    VM0();
    BAR();
    STAMP(7);
  }

  const int orow = 128 * ci + 64 * hf + 16 * tt + tok;
  v2u xr0, xr1;
  {
    const int t = w & 3, hd = w >> 2;
    const int kt0 = ci >= 2 ? 0 : 8 - 4 * ci - t;
    f16v O;
    const u16* xres = p.x + (size_t)orow * DM + ocol;
    if (ci >= 2) attention<true>(t, hd, 0, lane, mU, mL, O, rWS, WS_TWO + ct * 65536, WS_TW1 + cj * 7 * 16384, w, xres, xr0, xr1, stampv);
    else attention<false>(t, hd, kt0, lane, mU, mL, O, rWS, WS_TWO + ct * 65536, WS_TW1 + cj * 7 * 16384, w, xres, xr0, xr1, stampv);
    const int h = lane >> 5;
    const float inv = __builtin_amdgcn_rcpf(O[8]);
    const v4u o = {pk(O[0] * inv, O[1] * inv), pk(O[2] * inv, O[3] * inv), pk(O[4] * inv, O[5] * inv), pk(O[6] * inv, O[7] * inv)};
    const int qr = lane & 31;
    st16(rWS, o, WS_OF + (ci * 8 * 16 + cj) * 1024 + ((2 * t + (qr >> 4)) * 16) * 1024 + ((2 * hd + h) * 16 + (qr & 15)) * 16);
  }
  STAMP(24);
  VM0();
  BAR();
  if (tid == 256) arrive(p.ws, WS_FLAG + 2 * 8192 + ci * 512);
  STAMP(8);
  float b1v = 0.f;
  if (w != 4) {
#pragma unroll
    for (int e = 0; e < 3; ++e) { const int gi = wq + 7 * e; if (gi < 16) glds4(rWS, lane * 16, WS_TW1 + (cj * 7 + 2) * 16384 + gi * 4096, L_W1 + 32768 + gi * 4096); }
  } else wait_count(p.ws, WS_FLAG + 2 * 8192 + ci * 512, 16);
  BAR();
  STAMP(9);

  float x1[8];
  {
    v8s oa[8];
    const unsigned osrc = WS_OF + ((ci * 8 + 4 * hf + tt) * 16 + 8 * kh) * 1024;
#pragma unroll
    for (int s = 0; s < 3; ++s) oa[s] = asv8(ld16(rWS, lane * 16, osrc + s * 1024));
    const unsigned bo = opq(L_WO + kh * 32768 + lane * 16);
    f4 acc[4];
#pragma unroll
    for (int nt = 0; nt < 4; ++nt) acc[nt] = f4{0, 0, 0, 0};
    {
      v8s W[2][4];
#pragma unroll
      for (int s = 0; s < 2; ++s)
#pragma unroll
        for (int nt = 0; nt < 4; ++nt) W[s][nt] = lds16(bo + (s * 4 + nt) * 1024);
      SB();
#pragma unroll
      for (int s = 3; s < 8; ++s) oa[s] = asv8(ld16(rWS, lane * 16, osrc + s * 1024));
      if (tid < 112) { const int nt = tid >> 4; b1v = bflo(p.b1[(nt == 6 ? 112 * cj + (par ? 0 : 96) : 112 * cj + 16 * par + nt * 16) + (tid & 15)]); }
      SB();
#pragma unroll
      for (int s = 0; s < 8; ++s)
#pragma unroll
        for (int nt = 0; nt < 4; ++nt) {
          acc[nt] = MFMA16(W[s & 1][nt], oa[s], acc[nt]);
          if (s + 2 < 8) W[s & 1][nt] = lds16(bo + ((s + 2) * 4 + nt) * 1024);
          SB();
        }
    }
    STAMP(26);
    VM0();
    BAR();
    { const int gi = w;
      if (gi < 4) glds4(rWS, lane * 16, WS_TW1 + (cj * 7 + 6) * 16384 + gi * 4096, L_W1S + gi * 4096);
      else glds4(rWS, lane * 16, WS_TW2 + ct * 229376 + (gi - 4) * 4096, 32768 + (gi - 4) * 4096);
      if (w < 4) glds4(rWS, lane * 16, WS_TW2 + ct * 229376 + 7 * 16384 + w * 4096, 49152 + w * 4096); }
    *(LAS f4*)(sm + L_XCH + w * 2048 + lane * 16) = kh ? acc[0] : acc[2];
    *(LAS f4*)(sm + L_XCH + w * 2048 + 1024 + lane * 16) = kh ? acc[1] : acc[3];
    BAR();
    const f4 o0 = ldsf4(L_XCH + (w ^ 4) * 2048 + lane * 16), o1 = ldsf4(L_XCH + (w ^ 4) * 2048 + 1024 + lane * 16);
    const f4 t0 = (kh ? acc[2] : acc[0]) + o0, t1 = (kh ? acc[3] : acc[1]) + o1;
    const float xin[8] = {bflo(xr0[0]), bfhi(xr0[0]), bflo(xr0[1]), bfhi(xr0[1]), bflo(xr1[0]), bfhi(xr1[0]), bflo(xr1[1]), bfhi(xr1[1])};
    const float ga[8] = {bflo(gaR[0][0]), bfhi(gaR[0][0]), bflo(gaR[0][1]), bfhi(gaR[0][1]), bflo(gaR[1][0]), bfhi(gaR[1][0]), bflo(gaR[1][1]), bfhi(gaR[1][1])};
    const float cb[8] = {bflo(cbR[0][0]), bfhi(cbR[0][0]), bflo(cbR[0][1]), bfhi(cbR[0][1]), bflo(cbR[1][0]), bfhi(cbR[1][0]), bflo(cbR[1][1]), bfhi(cbR[1][1])};
    const float sv[8] = {bflo(sbR[0][0]), bfhi(sbR[0][0]), bflo(sbR[0][1]), bfhi(sbR[0][1]), bflo(sbR[1][0]), bfhi(sbR[1][0]), bflo(sbR[1][1]), bfhi(sbR[1][1])};
    float wv[8], xw[8];
#pragma unroll
    for (int r = 0; r < 8; ++r) { x1[r] = rbf(xin[r] + ga[r] * (r < 4 ? t0[r & 3] : t1[r & 3])); wv[r] = rbf(1.f + cb[r]); xw[r] = x1[r] * wv[r]; }
    st16(rWS, v4u{pk(xw[0], xw[1]), pk(xw[2], xw[3]), pk(xw[4], xw[5]), pk(xw[6], xw[7])}, WS_X1F + ((ci * 8 + 4 * hf + tt) * 16 + 2 * ct + kh) * 1024 + lane * 16);
    SB();
    float s1 = 0, s2 = 0, s3 = 0, s4 = 0, s5 = 0, s6 = 0;
#pragma unroll
    for (int r = 0; r < 8; ++r) { s1 += x1[r]; s2 += x1[r] * x1[r]; s3 += xw[r]; s4 += xw[r] * wv[r]; s5 += xw[r] * xw[r]; s6 += xw[r] * sv[r]; }
    s1 = sumg(s1); s2 = sumg(s2); s3 = sumg(s3); s4 = sumg(s4); s5 = sumg(s5); s6 = sumg(s6);
    const unsigned rec = (ci * 16 + 2 * ct + kh) * 128 + 64 * hf + 16 * tt + tok;
    if (g == 0) st16(rWS, v4u{__float_as_uint(s1), __float_as_uint(s2), __float_as_uint(s3), __float_as_uint(s4)}, WS_ST2A + rec * 16);
    if (g == 1) st8(rWS, v2u{__float_as_uint(s5), __float_as_uint(s6)}, WS_ST2B + rec * 8);
  }
  VM0();
  BAR();
  if (tid == 256) arrive(p.ws, WS_FLAG + 3 * 8192 + ci * 512);
  STAMP(10);

  v4u stg[8];
  {
    const unsigned bP = opq(L_W1 + lane * 16), bR = opq(L_W1 + 65536 + lane * 16), bS = opq(L_W1S + lane * 16);
#define W1F(nt, s) lds16((nt) == 6 ? bS + (s) * 1024 : (nt) < 4 ? bP + (nt) * 16384 + (s) * 1024 : bR + ((nt) - 4) * 16384 + (s) * 1024)
    {
      f4 au[7];
#pragma unroll
      for (int nt = 0; nt < 7; ++nt) au[nt] = f4{0, 0, 0, 0};
      const unsigned c0 = opq(bP + w * 2048), d0 = opq(bR + w * 2048), e0 = opq(bS + w * 2048);
#pragma unroll
      for (int u = 0; u < 2; ++u) {
        v8s W[7];
#pragma unroll
        for (int nt = 0; nt < 7; ++nt) W[nt] = lds16(nt == 6 ? e0 + u * 1024 : nt < 4 ? c0 + nt * 16384 + u * 1024 : d0 + (nt - 4) * 16384 + u * 1024);
#pragma unroll
        for (int nt = 0; nt < 7; ++nt) au[nt] = MFMA16(W[nt], aux2[u], au[nt]);
      }
      if (tok < 3) {
#pragma unroll
        for (int nt = 0; nt < 7; ++nt) *(LAS f4*)(sm + L_XCH + ((w * 3 + tok) * 112 + nt * 16 + 4 * g) * 4) = au[nt];
      }
    }
    BAR();
    if (tid < 112) *(LAS float*)(sm + L_XF + (336 + tid) * 4) = b1v;
    {
      const int e0 = w < 4 ? tid : tid - 64;
      if (w != 4 && e0 < 336) {
        float s = 0;
#pragma unroll
        for (int e = 0; e < 8; ++e) s += *(const LAS float*)(sm + L_XCH + (e * 336 + e0) * 4);
        *(LAS float*)(sm + L_XF + e0 * 4) = s;
      }
    }
    if (w == 4) wait_count(p.ws, WS_FLAG + 3 * 8192 + ci * 512, 16);
    BAR();
    STAMP(11);
    v8s act2[16];
    const unsigned asrc = WS_X1F + (ci * 8 + w) * 16 * 1024;
#pragma unroll
    for (int s = 0; s < 4; ++s) act2[s] = asv8(ld16(rWS, lane * 16, asrc + s * 1024));
    v4u sa[4]; v2u sb[4];
    f4 acc[7];
#pragma unroll
    for (int nt = 0; nt < 7; ++nt) acc[nt] = f4{0, 0, 0, 0};
    {
      v8s W[2][4];
#pragma unroll
      for (int s = 0; s < 2; ++s)
#pragma unroll
        for (int nt = 0; nt < 4; ++nt) W[s][nt] = W1F(nt, s);
      SB();
#pragma unroll
      for (int s = 0; s < 16; ++s) {
        if (s < 4) { act2[4 + 2 * s] = asv8(ld16(rWS, lane * 16, asrc + (4 + 2 * s) * 1024)); act2[5 + 2 * s] = asv8(ld16(rWS, lane * 16, asrc + (5 + 2 * s) * 1024)); }
        else if (s < 8) act2[8 + s] = asv8(ld16(rWS, lane * 16, asrc + (8 + s) * 1024));
        else if (s < 12) { sa[s - 8] = ld16(rWS, ((4 * g + s - 8) * 128 + 16 * w + tok) * 16, WS_ST2A + ci * 16 * 128 * 16);
                           sb[s - 8] = ld8(rWS, ((4 * g + s - 8) * 128 + 16 * w + tok) * 8, WS_ST2B + ci * 16 * 128 * 8); }
#pragma unroll
        for (int nt = 0; nt < 4; ++nt) {
          acc[nt] = MFMA16(W[s & 1][nt], act2[s], acc[nt]);
          if (s + 2 < 16) W[s & 1][nt] = W1F(nt, s + 2);
          SB();
        }
      }
    }
    STAMP(18);
    CSTAMP(42);
    v8s WB[2][3];
#pragma unroll
    for (int s = 0; s < 2; ++s)
#pragma unroll
      for (int nt = 0; nt < 3; ++nt) WB[s][nt] = W1F(4 + nt, s);
    float s1 = 0, s2 = 0, s3 = 0, s4 = 0, s5 = 0, s6 = 0;
#pragma unroll
    for (int e = 0; e < 4; ++e) { s1 += __uint_as_float(sa[e][0]); s2 += __uint_as_float(sa[e][1]); s3 += __uint_as_float(sa[e][2]); s4 += __uint_as_float(sa[e][3]); s5 += __uint_as_float(sb[e][0]); s6 += __uint_as_float(sb[e][1]); }
    s1 = sumg(s1); s2 = sumg(s2); s3 = sumg(s3); s4 = sumg(s4); s5 = sumg(s5); s6 = sumg(s6);
    const float mu1 = s1 * (1.f / 512), var1 = fmaxf(s2 * (1.f / 512) - mu1 * mu1, 0.f), r1 = __builtin_amdgcn_rsqf(var1 + 1e-5f);
    const float mu2 = (r1 * (s3 - mu1 * Cw) + Cs) * (1.f / 512);
    const float hh = (r1 * r1 * (s5 - 2.f * mu1 * s4 + mu1 * mu1 * Cw2) + 2.f * r1 * (s6 - mu1 * Cws) + Cs2) * (1.f / 512);
    const float r2 = __builtin_amdgcn_rsqf(fmaxf(hh - mu2 * mu2, 0.f) + 1e-5f);
    const float cA = r2 * r1, cB = -cA * mu1, cC = r2, cD = -r2 * mu2;
    const unsigned S0 = (7 * cj + par) >> 1;
    const unsigned hb = WS_H1F + (ci * 8 + w) * 56 * 1024;
    const unsigned xf = opq(L_XF + 16 * g);
#define BIAS4(nt) (cB * ldsf4(xf + (nt) * 64) + (cC * ldsf4(xf + 448 + (nt) * 64) + (cD * ldsf4(xf + 896 + (nt) * 64) + ldsf4(xf + 1344 + (nt) * 64))))
#define GELU1(z) ((z) * __builtin_amdgcn_rcpf(1.f + __builtin_amdgcn_exp2f((z) * (-2.3022081985f - 0.1029432397f * (z) * (z)))))
    {
      f4 bias = BIAS4(0);
      float o[4];
      unsigned hp[4][2];
      SB();
#pragma unroll
      for (int s = 0; s < 16; ++s) {
        const int nt0 = s >> 2, r = s & 3;
        if (s >= 4 && s < 12) { const int e = s - 4, pc = w * 8 + e; const int chunk = (pc >> 4) == 0 ? 5 : ((pc >> 4) == 1 ? 12 : ((pc >> 4) == 2 ? 6 : 13));
          stg[e] = ldp16(rWS, lane * 16, WS_TW2 + ct * 229376 + chunk * 16384 + (pc & 15) * 1024); }
        acc[4] = MFMA16(WB[s & 1][0], act2[s], acc[4]);
        if (s + 2 < 16) WB[s & 1][0] = W1F(4, s + 2);
        const float z = cA * acc[nt0][r] + bias[r];
        const float pz = z * (-2.3022081985f - 0.1029432397f * z * z);
        SB();
        acc[5] = MFMA16(WB[s & 1][1], act2[s], acc[5]);
        if (s + 2 < 16) WB[s & 1][1] = W1F(5, s + 2);
        const float ex = __builtin_amdgcn_exp2f(pz);
        if (r == 3 && nt0 < 3) bias = BIAS4(nt0 + 1);
        SB();
        acc[6] = MFMA16(WB[s & 1][2], act2[s], acc[6]);
        if (s + 2 < 16) WB[s & 1][2] = W1F(6, s + 2);
        o[r] = z * __builtin_amdgcn_rcpf(1.f + ex);
        if (r == 3) {
          hp[nt0][0] = pk(o[0], o[1]); hp[nt0][1] = pk(o[2], o[3]);
          if (nt0 & 1) st16(rWS, v4u{hp[nt0 - 1][0], hp[nt0 - 1][1], hp[nt0][0], hp[nt0][1]}, hb + (S0 + (nt0 >> 1)) * 1024 + lane * 16);
        }
        SB();
      }
    }
    STAMP(20);
    CSTAMP(43);
    {
      unsigned hp[3][2];
#pragma unroll
      for (int nt = 4; nt < 7; ++nt) {
        const f4 bias = BIAS4(nt);
        float o[4];
#pragma unroll
        for (int r = 0; r < 4; ++r) { const float z = cA * acc[nt][r] + bias[r]; o[r] = GELU1(z); }
        hp[nt - 4][0] = pk(o[0], o[1]); hp[nt - 4][1] = pk(o[2], o[3]);
        if (nt == 5) st16(rWS, v4u{hp[0][0], hp[0][1], hp[1][0], hp[1][1]}, hb + (S0 + 2) * 1024 + lane * 16);
      }
      const unsigned NT = 7 * cj + (par ? 0 : 6);
      st8(rWS, v2u{hp[2][0], hp[2][1]}, hb + (NT >> 1) * 1024 + (NT & 1) * 512 + lane * 8);
    }
  }
  STAMP(25);
  VM0();
  BAR();
  if (tid == 256) arrive(p.ws, WS_FLAG + 4 * 8192 + ci * 512);
  STAMP(13);
  if (w != 4) {
#pragma unroll
    for (int e = 0; e < 5; ++e) {
      const int gi = wq + 7 * e;
      if (gi < 32) { const int it = gi >> 3, khh = (gi >> 2) & 1, pc = gi & 3;
        glds4(rWS, lane * 16, WS_TW2 + ct * 229376 + (khh * 7 + 1 + it) * 16384 + pc * 4096, (it == 3 ? 0u : 65536u + it * 32768u) + khh * 16384u + pc * 4096u); }
    }
  } else wait_count(p.ws, WS_FLAG + 4 * 8192 + ci * 512, 16);
  VM0();
  BAR();
  STAMP(14);

  {
    v8s ha[28];
    const unsigned hb = WS_H1F + ((ci * 8 + 4 * hf + tt) * 56 + 28 * kh) * 1024;
#define HLOAD(s) do { if ((s) % 7 == 3) { const v2u lo_ = ld8(rWS, lane * 8, hb + (s) * 1024), hi_ = ld8(rWS, lane * 8 + 512, hb + (s) * 1024); ha[s] = asv8(v4u{lo_[0], lo_[1], hi_[0], hi_[1]}); } \
                      else ha[s] = asv8(ld16(rWS, lane * 16, hb + (s) * 1024)); } while (0)
#pragma unroll
    for (int s = 0; s < 3; ++s) HLOAD(s);
    const unsigned bw = opq(kh * 16384 + lane * 16), bw2 = opq(bw + 65536), bw3 = opq(bw + 131072);
    f4 acc[4];
#pragma unroll
    for (int nt = 0; nt < 4; ++nt) acc[nt] = f4{0, 0, 0, 0};
#define W2F(nt, S) lds16((((S) >> 2) == 0 || ((S) >> 2) == 5 ? bw + 32768 : ((S) >> 2) == 1 || ((S) >> 2) == 6 ? bw2 : ((S) >> 2) == 2 ? bw2 + 32768 : ((S) >> 2) == 3 ? bw3 : bw) + (((S) & 3) * 4 + (nt)) * 1024)
    {
      v8s W[2][4];
#pragma unroll
      for (int S = 0; S < 2; ++S)
#pragma unroll
        for (int nt = 0; nt < 4; ++nt) W[S][nt] = W2F(nt, S);
      SB();
#pragma unroll
      for (int s = 3; s < 8; ++s) HLOAD(s);
      SB();
#pragma unroll
      for (int S = 0; S < 28; ++S) {
        if (S + 8 < 28) HLOAD(S + 8);
        if (S == 8) BAR();
        if (S >= 8 && S < 16) { const int e = S - 8, pc = w * 8 + e; *(LAS v4u*)(sm + 32768 + (pc >> 5) * 32768 + ((pc >> 4) & 1) * 16384 + (pc & 15) * 1024 + lane * 16) = stg[e]; }
        if (S == 18) BAR();
#pragma unroll
        for (int nt = 0; nt < 4; ++nt) {
          acc[nt] = MFMA16(W[S & 1][nt], ha[S], acc[nt]);
          if (S + 2 < 28) W[S & 1][nt] = W2F(nt, S + 2);
          SB();
        }
      }
    }
    STAMP(22);
    BAR();
    *(LAS f4*)(sm + L_W2X + w * 2048 + lane * 16) = kh ? acc[0] : acc[2];
    *(LAS f4*)(sm + L_W2X + w * 2048 + 1024 + lane * 16) = kh ? acc[1] : acc[3];
    BAR();
    const f4 o0 = ldsf4(L_W2X + (w ^ 4) * 2048 + lane * 16), o1 = ldsf4(L_W2X + (w ^ 4) * 2048 + 1024 + lane * 16);
    const f4 t0 = (kh ? acc[2] : acc[0]) + o0, t1 = (kh ? acc[3] : acc[1]) + o1;
    const float gb[8] = {bflo(gbR[0][0]), bfhi(gbR[0][0]), bflo(gbR[0][1]), bfhi(gbR[0][1]), bflo(gbR[1][0]), bfhi(gbR[1][0]), bflo(gbR[1][1]), bfhi(gbR[1][1])};
    const float bb[8] = {bflo(b2R[0][0]), bfhi(b2R[0][0]), bflo(b2R[0][1]), bfhi(b2R[0][1]), bflo(b2R[1][0]), bfhi(b2R[1][0]), bflo(b2R[1][1]), bfhi(b2R[1][1])};
    float o[8];
#pragma unroll
    for (int r = 0; r < 8; ++r) o[r] = x1[r] + gb[r] * ((r < 4 ? t0[r & 3] : t1[r & 3]) + bb[r]);
    auto r0 = __builtin_amdgcn_permlane16_swap(pk(o[0], o[1]), pk(o[4], o[5]), false, false);
    auto r1 = __builtin_amdgcn_permlane16_swap(pk(o[2], o[3]), pk(o[6], o[7]), false, false);
    *(v4u*)(p.out + (size_t)orow * DM + 64 * ct + 32 * kh + (g & 1) * 16 + (g >> 1) * 8) = v4u{r0[0], r1[0], r0[1], r1[1]};
  }
  STAMP(15);
  CSTAMP(41);
  STAMP_FLUSH(0);
}

extern "C" void kernel_launch(void* const* d_in, const int* in_sizes, int n_in, void* d_out, int out_size, void* d_ws, size_t ws_size, hipStream_t stream) {
  Params p;
  p.x = (const u16*)d_in[0]; p.wqkv = (const u16*)d_in[1]; p.wo = (const u16*)d_in[2];
  p.w1 = (const u16*)d_in[4]; p.b1 = (const u16*)d_in[5]; p.w2 = (const u16*)d_in[6]; p.b2 = (const u16*)d_in[7];
  p.c = (const u16*)d_in[8]; p.wmod = (const u16*)d_in[9]; p.out = (u16*)d_out; p.ws = (char*)d_ws;
  prep_block<<<256, 512, 0, stream>>>(p);
  fused_block<<<256, 512, 0, stream>>>(p);
}
```

```cpp
#include <hip/hip_runtime.h>
#include <hip/hip_bf16.h>
#include <stdint.h>

typedef unsigned short u16;
typedef short v4s __attribute__((ext_vector_type(4)));
typedef short v8s __attribute__((ext_vector_type(8)));
typedef float f2 __attribute__((ext_vector_type(2)));
typedef float f4 __attribute__((ext_vector_type(4)));
typedef float f16v __attribute__((ext_vector_type(16)));
typedef unsigned v2u __attribute__((ext_vector_type(2)));
typedef unsigned v4u __attribute__((ext_vector_type(4)));
typedef __bf16 b2 __attribute__((ext_vector_type(2)));
#define LAS __attribute__((address_space(3)))
#define GAS __attribute__((address_space(1)))
#define DI __device__ __forceinline__
typedef __amdgpu_buffer_rsrc_t srd_t;

constexpr int SEQ = 2048, DM = 512, FF = 1792;
constexpr unsigned WS_EPOCH = 0;
constexpr unsigned WS_F0 = 4096;
constexpr unsigned WS_FP = WS_F0 + 8192;
constexpr unsigned WS_FM = WS_FP + 4 * 16 * 128;
constexpr unsigned WS_FQ = WS_FM + 1024;
constexpr unsigned WS_FW1 = WS_FQ + 2048;
constexpr unsigned WS_FWO = WS_FW1 + 2048;
constexpr unsigned WS_FW2 = WS_FWO + 1024;
static_assert(WS_FW2 + 4096 <= (64 << 10), "flags");
constexpr unsigned WS_WV = 64 << 10;
constexpr unsigned WS_MOD = 128 << 10;
constexpr unsigned WS_ST1 = 192 << 10;
constexpr unsigned WS_ST2A = 512 << 10;
constexpr unsigned WS_ST2B = 1024 << 10;
constexpr unsigned WS_HF = 2u << 20;
constexpr unsigned WS_KV = 4u << 20;
constexpr unsigned WS_OF = 8u << 20;
constexpr unsigned WS_X1F = 10u << 20;
constexpr unsigned WS_H1F = 12u << 20;
constexpr unsigned WS_TQ = 19u << 20;
constexpr unsigned WS_TW1 = WS_TQ + (1536u << 10);
constexpr unsigned WS_TWO = WS_TW1 + (1792u << 10);
constexpr unsigned WS_TW2 = WS_TWO + (512u << 10);
constexpr unsigned WS_STAMP = 25u << 20;
constexpr unsigned WS_BYTES = 27u << 20;
static_assert(WS_TW2 + (1792u << 10) <= WS_STAMP, "ws");
constexpr unsigned L_CA = 98304;
constexpr unsigned L_XS = 131072;
constexpr unsigned L_S1 = L_XS + 8192;
constexpr unsigned L_SO = L_S1 + 7168;
constexpr unsigned L_SC = L_SO + 12288;
constexpr unsigned L_CAP = L_SC + 2048;
constexpr unsigned L_CNT = L_CAP + 512;
static_assert(L_CNT + 16 <= 163840, "lds");
constexpr unsigned L_SQ = 0;
constexpr unsigned L_M2 = 8192;
constexpr unsigned L_WQKV = 0;
constexpr unsigned L_PARTU = 98304;
constexpr unsigned L_PARTV = 32768;
constexpr unsigned L_AK = 98304;
constexpr unsigned KPL = 6144;
constexpr unsigned L_AV = L_AK + 4 * KPL;
constexpr unsigned VPL = 6208, VHD = 12416;
constexpr unsigned L_MODA = L_AV + VPL;
constexpr unsigned L_MODB = L_AV + VHD;
constexpr unsigned L_B2C = L_MODB + 2048;
constexpr unsigned L_ONE = L_AV + 2 * VHD;
constexpr unsigned L_AQ = L_ONE + 6400;
constexpr unsigned L_PERS = L_AQ + 8192;
static_assert(L_PERS + 1024 <= 163840, "lds");
static_assert((L_AV % 256) == 0 && (L_ONE % 256) == 0, "bank plan");
constexpr unsigned L_WO = 0;
constexpr unsigned L_W1 = 65536;
constexpr unsigned L_W1S = 0;
constexpr unsigned L_XCH = 16384;
constexpr unsigned L_XF = 16384 + 12288;
constexpr unsigned L_W2X = 98304;

struct Params { const u16* x; const u16* wqkv; const u16* wo; const u16* w1; const u16* b1; const u16* w2; const u16* b2; const u16* c; const u16* wmod; u16* out; char* ws; };

DI float bflo(unsigned v) { return __uint_as_float(v << 16); }
DI float bfhi(unsigned v) { return __uint_as_float(v & 0xffff0000u); }
DI unsigned pk(float a, float b) { f2 v = {a, b}; b2 r = __builtin_convertvector(v, b2); return __builtin_bit_cast(unsigned, r); }
DI float rbf(float a) { return bflo(pk(a, 0.f)); }
DI v8s cat(v4s a, v4s b) { return __builtin_shufflevector(a, b, 0, 1, 2, 3, 4, 5, 6, 7); }
DI v4s trd(unsigned a) { return __builtin_amdgcn_ds_read_tr16_b64_v4i16((LAS v4s*)(uintptr_t)a); }
DI v8s asv8(v4u v) { return __builtin_bit_cast(v8s, v); }
DI v4u asv4u(v8s v) { return __builtin_bit_cast(v4u, v); }
DI v8s lds16(unsigned a) { return *(const LAS v8s*)(uintptr_t)a; }
DI f4 ldsf4(unsigned a) { return *(const LAS f4*)(uintptr_t)a; }
DI v2u lds8(unsigned a) { return *(const LAS v2u*)(uintptr_t)a; }
#define MFMA16(a, b, c) __builtin_amdgcn_mfma_f32_16x16x32_bf16((a), (b), (c), 0, 0, 0)
#define MFMA32(a, b, c) __builtin_amdgcn_mfma_f32_32x32x16_bf16((a), (b), (c), 0, 0, 0)
#define BAR() asm volatile("s_waitcnt lgkmcnt(0)\n\ts_barrier" ::: "memory")
#define VM0() asm volatile("s_waitcnt vmcnt(0)" ::: "memory")
#define LG0() asm volatile("s_waitcnt lgkmcnt(0)" ::: "memory")
#define CFENCE() asm volatile("" ::: "memory")
#define SB() __builtin_amdgcn_sched_barrier(0)
DI unsigned opq(unsigned v) { asm volatile("" : "+v"(v)); return v; }
DI void glds(srd_t r, unsigned voff, unsigned soff, unsigned ldsaddr) {
  asm volatile("s_mov_b32 m0, %0\n\ts_nop 0\n\tbuffer_load_dwordx4 %1, %2, %3 offen lds" ::"s"(ldsaddr), "v"(voff), "s"(r), "s"(soff) : "memory");
}
DI void glds1(srd_t r, unsigned voff, unsigned soff, unsigned ldsaddr) {
  asm volatile("s_mov_b32 m0, %0\n\ts_nop 0\n\tbuffer_load_dwordx4 %1, %2, %3 offen sc1 lds" ::"s"(ldsaddr), "v"(voff), "s"(r), "s"(soff) : "memory");
}
DI void glds4(srd_t r, unsigned voff, unsigned soff, unsigned ldsaddr) {
  asm volatile("s_mov_b32 m0, %0\n\ts_nop 0\n\tbuffer_load_dwordx4 %1, %2, %3 offen lds\n\tbuffer_load_dwordx4 %1, %2, %3 offen offset:1024 lds\n\t"
               "buffer_load_dwordx4 %1, %2, %3 offen offset:2048 lds\n\tbuffer_load_dwordx4 %1, %2, %3 offen offset:3072 lds" ::"s"(ldsaddr), "v"(voff), "s"(r), "s"(soff) : "memory");
}
DI void glds4x1(srd_t r, unsigned voff, unsigned soff, unsigned ldsaddr) {
  asm volatile("s_mov_b32 m0, %0\n\ts_nop 0\n\tbuffer_load_dwordx4 %1, %2, %3 offen sc1 lds\n\tbuffer_load_dwordx4 %1, %2, %3 offen offset:1024 sc1 lds\n\t"
               "buffer_load_dwordx4 %1, %2, %3 offen offset:2048 sc1 lds\n\tbuffer_load_dwordx4 %1, %2, %3 offen offset:3072 sc1 lds" ::"s"(ldsaddr), "v"(voff), "s"(r), "s"(soff) : "memory");
}
DI void glds2x1(srd_t r, unsigned voff, unsigned soff, unsigned ldsaddr) {
  asm volatile("s_mov_b32 m0, %0\n\ts_nop 0\n\tbuffer_load_dwordx4 %1, %2, %3 offen sc1 lds\n\tbuffer_load_dwordx4 %1, %2, %3 offen offset:1024 sc1 lds" ::"s"(ldsaddr), "v"(voff), "s"(r), "s"(soff) : "memory");
}
DI v4u ld16(srd_t r, unsigned voff, unsigned soff) { return __builtin_amdgcn_raw_buffer_load_b128(r, voff, soff, 16); }
DI v2u ld8(srd_t r, unsigned voff, unsigned soff) { return __builtin_amdgcn_raw_buffer_load_b64(r, voff, soff, 16); }
DI v4u ldp16(srd_t r, unsigned voff, unsigned soff) { return __builtin_amdgcn_raw_buffer_load_b128(r, voff, soff, 0); }
DI void stp16(srd_t r, v4u v, unsigned off) { __builtin_amdgcn_raw_buffer_store_b128(v, r, off, 0, 0); }
DI void st16(srd_t r, v4u v, unsigned off) { __builtin_amdgcn_raw_buffer_store_b128(v, r, off, 0, 16); }
DI void st8(srd_t r, v2u v, unsigned off) { __builtin_amdgcn_raw_buffer_store_b64(v, r, off, 0, 16); }
DI float sum16(float v) { auto r = __builtin_amdgcn_permlane16_swap(__float_as_uint(v), __float_as_uint(v), false, false); return __uint_as_float(r[0]) + __uint_as_float(r[1]); }
DI float sum32(float v) { auto r = __builtin_amdgcn_permlane32_swap(__float_as_uint(v), __float_as_uint(v), false, false); return __uint_as_float(r[0]) + __uint_as_float(r[1]); }
DI float max32(float v) { auto r = __builtin_amdgcn_permlane32_swap(__float_as_uint(v), __float_as_uint(v), false, false); return fmaxf(__uint_as_float(r[0]), __uint_as_float(r[1])); }
DI float sumg(float v) { return sum32(sum16(v)); }
#define RLX_AGENT __ATOMIC_RELAXED, __HIP_MEMORY_SCOPE_AGENT
typedef unsigned long long u64;
DI void arrive(char* ws, unsigned off, u64 tag) { (void)__hip_atomic_exchange((GAS u64*)(ws + off), tag, RLX_AGENT); }
DI void wait_words(char* ws, unsigned off, int n, int lane, u64 tag) {
  GAS u64* p = (GAS u64*)(ws + off) + (lane < n ? lane : 0);
  for (unsigned spins = 0; spins < (1u << 17); ++spins)
    if (__all(__hip_atomic_load(p, RLX_AGENT) == tag)) break;
  CFENCE();
}
template <int C> DI float dpp(float v) { return __uint_as_float(__builtin_amdgcn_update_dpp(0u, __float_as_uint(v), C, 0xf, 0xf, false)); }
DI float rowsum16(float v) { v += dpp<0x128>(v); v += dpp<0x124>(v); v += dpp<0x122>(v); v += dpp<0x121>(v); return v; }
#define PIN(x) asm volatile("" : "+v"(x))
#define STAMP(k)
#define STAMP_FLUSH(o)
#define CSTAMP(k)
DI float silu(float v) { return v * __builtin_amdgcn_rcpf(1.f + __builtin_amdgcn_exp2f(-1.4426950408889634f * v)); }

DI v8s trfragA(unsigned img, int half, int lane) {
  const int g = lane >> 4, q = (lane & 15) >> 2, pp = lane & 3;
  const unsigned a = img + (4 * g + q) * 64 + ((half ^ (g & 1)) * 32) + pp * 8;
  return cat(trd(a), trd(a + 1024));
}

template <bool FULL>
DI void attention(int t, int hd, int kt0, int lane, f16v mU, f16v mL, f16v& O, srd_t rWS, unsigned wo_src, unsigned w1_src, int w, const u16* xres, v2u& xr0, v2u& xr1, unsigned& stampv) {
  const int qr = lane & 31, h = lane >> 5, blk = (lane >> 4) & 1, q = (lane & 15) >> 2, pp = lane & 3;
  const v8s Qf = lds16(L_AQ + (hd * 2 + h) * 2048 + (32 * t + qr) * 16);
  const unsigned kb = opq(L_AK + (hd * 2 + h) * KPL + (32 * t + qr) * 16);
  const unsigned vb = opq((blk == 0 ? L_AV + hd * VHD + (pp >> 1) * VPL + (pp & 1) * 8 + (4 * h + q) * 16 : L_ONE + ((hd * 128 + (h ? 0 : 128)) & 255)) + t * 512);
  const f16v zero = {};
#define RIDE(n) do { if ((n) < 2) glds4x1(rWS, lane * 16, wo_src + (w * 2 + (n)) * 4096, L_WO + (w * 2 + (n)) * 4096); \
                     else glds4x1(rWS, lane * 16, w1_src + w * 4096, L_W1 + w * 4096); } while (0)
  if (FULL) {
    v8s Kf[9];
#pragma unroll
    for (int kt = 0; kt < 9; ++kt) Kf[kt] = lds16(kb + kt * 512);
    v8s V0 = cat(trd(vb), trd(vb + 128)), V1 = cat(trd(vb + 256), trd(vb + 384));
    f16v S[2];
    float m0 = -3e38f, m1 = -3e38f;
    S[0] = MFMA32(Kf[0], Qf, mL);
    SB();
#pragma unroll
    for (int kt = 0; kt < 9; ++kt) {
      if (kt < 8) S[(kt + 1) & 1] = MFMA32(Kf[kt + 1], Qf, kt + 1 == 8 ? mU : zero);
      SB();
#pragma unroll
      for (int r = 0; r < 16; r += 4) { m0 = fmaxf(fmaxf(m0, S[kt & 1][r]), S[kt & 1][r + 1]); m1 = fmaxf(fmaxf(m1, S[kt & 1][r + 2]), S[kt & 1][r + 3]); }
      SB();
    }
    const float m = max32(fmaxf(m0, m1));
    STAMP(23);
    f16v nm;
#pragma unroll
    for (int r = 0; r < 16; ++r) { nm[r] = -m; mU[r] -= m; mL[r] -= m; }
    O = zero;
    S[0] = MFMA32(Kf[0], Qf, mL);
    SB();
#pragma unroll
    for (int kt = 0; kt < 9; ++kt) {
      if (kt < 8) S[(kt + 1) & 1] = MFMA32(Kf[kt + 1], Qf, kt + 1 == 8 ? mU : nm);
      if (kt == 0) RIDE(0);
      if (kt == 3) RIDE(1);
      if (kt == 6) RIDE(2);
      SB();
      v4u P0, P1;
#pragma unroll
      for (int r = 0; r < 4; ++r) P0[r] = pk(__builtin_amdgcn_exp2f(S[kt & 1][2 * r]), __builtin_amdgcn_exp2f(S[kt & 1][2 * r + 1]));
      O = MFMA32(V0, asv8(P0), O);
      if (kt < 8) V0 = cat(trd(vb + (kt + 1) * 512), trd(vb + (kt + 1) * 512 + 128));
      if (kt == 2) xr0 = *(const v2u*)xres;
      if (kt == 5) xr1 = *(const v2u*)(xres + 16);
      SB();
#pragma unroll
      for (int r = 0; r < 4; ++r) P1[r] = pk(__builtin_amdgcn_exp2f(S[kt & 1][8 + 2 * r]), __builtin_amdgcn_exp2f(S[kt & 1][8 + 2 * r + 1]));
      O = MFMA32(V1, asv8(P1), O);
      if (kt < 8) V1 = cat(trd(vb + (kt + 1) * 512 + 256), trd(vb + (kt + 1) * 512 + 384));
      SB();
    }
  } else {
#pragma unroll
    for (int n = 0; n < 3; ++n) RIDE(n);
    xr0 = *(const v2u*)xres; xr1 = *(const v2u*)(xres + 16);
    float m = -3e38f;
#pragma unroll
    for (int kt = 0; kt < 9; ++kt) {
      if (kt >= kt0) {
        const f16v S = MFMA32(lds16(kb + kt * 512), Qf, kt == 8 ? mU : zero);
#pragma unroll
        for (int r = 0; r < 16; r += 2) m = fmaxf(fmaxf(m, S[r]), S[r + 1]);
      }
    }
    m = max32(m);
    f16v nm;
#pragma unroll
    for (int r = 0; r < 16; ++r) { nm[r] = -m; mU[r] -= m; }
    O = zero;
#pragma unroll
    for (int kt = 0; kt < 9; ++kt) {
      if (kt >= kt0) {
        const f16v S = MFMA32(lds16(kb + kt * 512), Qf, kt == 8 ? mU : nm);
        v4u P0, P1;
#pragma unroll
        for (int r = 0; r < 4; ++r) {
          P0[r] = pk(__builtin_amdgcn_exp2f(S[2 * r]), __builtin_amdgcn_exp2f(S[2 * r + 1]));
          P1[r] = pk(__builtin_amdgcn_exp2f(S[8 + 2 * r]), __builtin_amdgcn_exp2f(S[8 + 2 * r + 1]));
        }
        const v8s V0 = cat(trd(vb + kt * 512), trd(vb + kt * 512 + 128));
        const v8s V1 = cat(trd(vb + kt * 512 + 256), trd(vb + kt * 512 + 384));
        O = MFMA32(V0, asv8(P0), O);
        O = MFMA32(V1, asv8(P1), O);
      }
    }
  }
#undef RIDE
}

__global__ void __launch_bounds__(512, 2) fused_block(Params p) {
  __shared__ __attribute__((aligned(16))) char smem[163840];
  LAS char* sm = (LAS char*)smem;
  const int tid = threadIdx.x, lane = tid & 63;
  const int w = __builtin_amdgcn_readfirstlane(tid >> 6);
  const int b = blockIdx.x, ci = ((b & 7) >> 1) * 4 + ((b >> 3) & 3), cj = (b & 1) * 8 + (b >> 5);
  const int tok = lane & 15, g = lane >> 4, q4 = (lane & 15) >> 2, pp = lane & 3;
  const int hf = cj & 1, ct = cj >> 1, par = cj & 1;
  const int wq = w < 4 ? w : w - 1;
  unsigned stampv = 0;
  const srd_t rWS = __builtin_amdgcn_make_buffer_rsrc(p.ws, 0, WS_BYTES, 0x00020000);
  const int rA = lane >> 2, pA = lane & 3;
  const unsigned colA = (((pA >> 1) ^ ((rA >> 2) & 1)) * 32) + (pA & 1) * 16;
  const int rB = lane >> 3, pB = lane & 7;
  const unsigned colB = (((pB >> 1) ^ ((rB >> 1) & 3)) * 32) + (pB & 1) * 16;
  STAMP(0);
  CSTAMP(40);

  const bool m2 = (cj % 3 == 0) && cj < 15;
  const int m2id = cj == 0 ? ci : 16 + ci * 4 + cj / 3 - 1;
  const srd_t rWmod = __builtin_amdgcn_make_buffer_rsrc((void*)p.wmod, 0, DM * 3072 * 2, 0x00020000);
  if (w < 4) {
    __builtin_amdgcn_s_setprio(2);
    const srd_t rX = __builtin_amdgcn_make_buffer_rsrc((void*)p.x, 0, SEQ * DM * 2, 0x00020000);
#pragma unroll
    for (int i0 = 0; i0 < 8; ++i0) { const int ii = (i0 + (ci & 3) * 2 + (cj & 1)) & 7; glds(rWmod, rA * 6144 + colA, (w * 8 + ii) * 16 * 6144 + (512 + 32 * cj) * 2, L_CA + (w * 8 + ii) * 1024); }
#pragma unroll
    for (int u = 0; u < 2; ++u) glds(rX, rA * 1024 + pA * 16, (128 * ci + 16 * (2 * w + u)) * 1024 + 64 * cj, L_XS + (2 * w + u) * 1024);
  }
  if (tid == 256) { *(LAS unsigned*)(uintptr_t)L_CNT = 0u; *(LAS unsigned*)(uintptr_t)(L_CNT + 4) = m2 ? 0u : 1u; *(LAS unsigned*)(uintptr_t)(L_CNT + 8) = 0u; }
  BAR();
#define READ_TAG() do { const v2u ep_ = ld8(rWS, WS_EPOCH, 0); TAG = (((u64)__builtin_amdgcn_readfirstlane(ep_[1]) << 32) | __builtin_amdgcn_readfirstlane(ep_[0])) + 1; } while (0)
  v8s rt[12];
  f16v mU, mL;
  u64 TAG;
  if (w < 4) {
    const v2u ep = ld8(rWS, WS_EPOCH, 0);
    const v4u cr = *(const v4u*)(p.c + 128 * w + 8 * (lane & 15));
    const unsigned sc = L_SC + w * 256;
    if (lane < 16) *(LAS v4u*)(uintptr_t)(sc + lane * 16) = v4u{pk(silu(bflo(cr[0])), silu(bfhi(cr[0]))), pk(silu(bflo(cr[1])), silu(bfhi(cr[1]))), pk(silu(bflo(cr[2])), silu(bfhi(cr[2]))), pk(silu(bflo(cr[3])), silu(bfhi(cr[3])))};
    LG0();
    v8s sf[4];
#pragma unroll
    for (int s = 0; s < 4; ++s) { const v2u lo = lds8(sc + (32 * s + 4 * g) * 2), hi = lds8(sc + (32 * s + 16 + 4 * g) * 2); sf[s] = asv8(v4u{lo[0], lo[1], hi[0], hi[1]}); }
    TAG = (((u64)__builtin_amdgcn_readfirstlane(ep[1]) << 32) | __builtin_amdgcn_readfirstlane(ep[0])) + 1;
    VM0();
    STAMP(1);
    v2u xa[2][2];
#pragma unroll
    for (int u = 0; u < 2; ++u) { const unsigned a = L_XS + (2 * w + u) * 1024 + tok * 64 + g * 8; xa[u][0] = lds8(a); xa[u][1] = lds8(a + 32); }
    f4 a0 = {0, 0, 0, 0}, a1 = {0, 0, 0, 0};
    {
      v8s W[4][2];
#pragma unroll
      for (int s = 0; s < 4; ++s) { W[s][0] = trfragA(L_CA + (4 * w + s) * 2048, 0, lane); W[s][1] = trfragA(L_CA + (4 * w + s) * 2048, 1, lane); }
#pragma unroll
      for (int s = 0; s < 4; ++s) { a0 = MFMA16(W[s][0], sf[s], a0); a1 = MFMA16(W[s][1], sf[s], a1); }
    }
    if (tok == 0) { *(LAS f4*)(uintptr_t)(L_CAP + (w * 32 + 4 * g) * 4) = a0; *(LAS f4*)(uintptr_t)(L_CAP + (w * 32 + 16 + 4 * g) * 4) = a1; }
    LG0();
    if (lane == 0) __hip_atomic_fetch_add((LAS unsigned*)(uintptr_t)L_CNT, 1u, __ATOMIC_RELAXED, __HIP_MEMORY_SCOPE_WORKGROUP);
    while (*(volatile LAS unsigned*)(uintptr_t)L_CNT < 4u) {}
    CFENCE();
    STAMP(19);
    a0 = ldsf4(L_CAP + 4 * g * 4); a1 = ldsf4(L_CAP + (16 + 4 * g) * 4);
#pragma unroll
    for (int e = 1; e < 4; ++e) { a0 += ldsf4(L_CAP + (e * 32 + 4 * g) * 4); a1 += ldsf4(L_CAP + (e * 32 + 16 + 4 * g) * 4); }
    float wv[8];
#pragma unroll
    for (int r = 0; r < 4; ++r) { wv[r] = rbf(1.f + rbf(a0[r])); wv[4 + r] = rbf(1.f + rbf(a1[r])); }
#pragma unroll
    for (int u = 0; u < 2; ++u) {
      const float x0 = bflo(xa[u][0][0]), x1 = bfhi(xa[u][0][0]), x2 = bflo(xa[u][0][1]), x3 = bfhi(xa[u][0][1]);
      const float x4 = bflo(xa[u][1][0]), x5 = bfhi(xa[u][1][0]), x6 = bflo(xa[u][1][1]), x7 = bfhi(xa[u][1][1]);
      const int T = 2 * w + u;
      st16(rWS, v4u{pk(x0 * wv[0], x1 * wv[1]), pk(x2 * wv[2], x3 * wv[3]), pk(x4 * wv[4], x5 * wv[5]), pk(x6 * wv[6], x7 * wv[7])},
           WS_HF + ((ci * 8 + T) * 16 + cj) * 1024 + lane * 16);
      if (u == 0 && w == 0 && tok == 0) {
        st8(rWS, v2u{pk(wv[0], wv[1]), pk(wv[2], wv[3])}, WS_WV + ci * 1024 + cj * 64 + g * 8);
        st8(rWS, v2u{pk(wv[4], wv[5]), pk(wv[6], wv[7])}, WS_WV + ci * 1024 + cj * 64 + 32 + g * 8);
      }
      const float s1 = sumg(((x0 + x1) + (x2 + x3)) + ((x4 + x5) + (x6 + x7)));
      const float s2 = sumg(((x0 * x0 + x1 * x1) + (x2 * x2 + x3 * x3)) + ((x4 * x4 + x5 * x5) + (x6 * x6 + x7 * x7)));
      if (g == 0) st8(rWS, v2u{__float_as_uint(s1), __float_as_uint(s2)}, WS_ST1 + ((ci * 16 + cj) * 128 + 16 * T + tok) * 8);
    }
    STAMP(21);
    VM0();
    if (lane == 0) { arrive(p.ws, WS_F0 + (ci * 64 + cj * 4 + w) * 8, TAG); __hip_atomic_fetch_add((LAS unsigned*)(uintptr_t)(L_CNT + 8), 1u, __ATOMIC_RELAXED, __HIP_MEMORY_SCOPE_WORKGROUP); }
    __builtin_amdgcn_s_setprio(0);
  } else if (w == 4) {
    if (m2) {
      const int mcol = m2id < 16 ? m2id * 32 : 1024 + (m2id - 16) * 32;
      __builtin_amdgcn_s_sleep(4);
#pragma unroll
      for (int ii = 0; ii < 32; ++ii) glds(rWmod, rA * 6144 + colA, ii * 16 * 6144 + mcol * 2, L_M2 + ii * 1024);
      const v4u cr = *(const v4u*)(p.c + 8 * lane);
      const unsigned sc = L_SC + 1024;
      *(LAS v4u*)(uintptr_t)(sc + lane * 16) = v4u{pk(silu(bflo(cr[0])), silu(bfhi(cr[0]))), pk(silu(bflo(cr[1])), silu(bfhi(cr[1]))), pk(silu(bflo(cr[2])), silu(bfhi(cr[2]))), pk(silu(bflo(cr[3])), silu(bfhi(cr[3])))};
      LG0();
      v8s sf[16];
#pragma unroll
      for (int s = 0; s < 16; ++s) { const v2u lo = lds8(sc + (32 * s + 4 * g) * 2), hi = lds8(sc + (32 * s + 16 + 4 * g) * 2); sf[s] = asv8(v4u{lo[0], lo[1], hi[0], hi[1]}); }
      VM0();
      f4 a0 = {0, 0, 0, 0}, a1 = {0, 0, 0, 0};
#pragma unroll
      for (int s0 = 0; s0 < 16; s0 += 4) {
        v8s W[4][2];
#pragma unroll
        for (int s = 0; s < 4; ++s) { W[s][0] = trfragA(L_M2 + (s0 + s) * 2048, 0, lane); W[s][1] = trfragA(L_M2 + (s0 + s) * 2048, 1, lane); }
#pragma unroll
        for (int s = 0; s < 4; ++s) { a0 = MFMA16(W[s][0], sf[s0 + s], a0); a1 = MFMA16(W[s][1], sf[s0 + s], a1); }
      }
      if (tok == 0) {
        st8(rWS, v2u{pk(a0[0], a0[1]), pk(a0[2], a0[3])}, WS_MOD + mcol * 2 + g * 8);
        st8(rWS, v2u{pk(a1[0], a1[1]), pk(a1[2], a1[3])}, WS_MOD + mcol * 2 + 32 + g * 8);
      }
      LG0();
      if (lane == 0) *(volatile LAS unsigned*)(uintptr_t)(L_CNT + 4) = 1u;
      READ_TAG();
      VM0();
      if (lane == 0) arrive(p.ws, WS_FM + m2id * 8, TAG);
    } else READ_TAG();
    STAMP(1);
  } else {
    if (w == 5) {
      const srd_t r = __builtin_amdgcn_make_buffer_rsrc((void*)p.wqkv, 0, DM * 1536 * 2, 0x00020000);
#pragma unroll
      for (int e = 0; e < 6; ++e) glds(r, rA * 3072 + colA, (32 * ci + 16 * (e & 1)) * 3072 + ((e >> 1) * 512 + 32 * cj) * 2, L_SQ + e * 1024);
      READ_TAG();
      VM0();
#pragma unroll
      for (int nt = 0; nt < 6; ++nt) st16(rWS, asv4u(trfragA(L_SQ + (nt >> 1) * 2048, nt & 1, lane)), WS_TQ + ((cj * 6 + nt) * 16 + ci) * 1024 + lane * 16);
      VM0();
      if (lane == 0) arrive(p.ws, WS_FQ + (cj * 16 + ci) * 8, TAG);
    } else {
      READ_TAG();
      __builtin_amdgcn_s_sleep(16);
      if (w == 6) {
        const srd_t r1 = __builtin_amdgcn_make_buffer_rsrc((void*)p.w1, 0, DM * FF * 2, 0x00020000);
#pragma unroll
        for (int e = 0; e < 6; ++e) glds(r1, rA * (FF * 2) + colA, (32 * ci + 16 * (e & 1)) * (FF * 2) + (112 * cj + 16 * par + (e >> 1) * 32) * 2, L_S1 + e * 1024);
        glds(r1, (lane >> 1) * (FF * 2) + (lane & 1) * 16, 32 * ci * (FF * 2) + (112 * cj + (par ? 0 : 96)) * 2, L_S1 + 6144);
        VM0();
#pragma unroll
        for (int nt = 0; nt < 6; ++nt) rt[nt] = trfragA(L_S1 + (nt >> 1) * 2048, nt & 1, lane);
        { const unsigned a = L_S1 + 6144 + (4 * g + q4) * 32 + pp * 8; rt[6] = cat(trd(a), trd(a + 512)); }
      } else {
        const srd_t ro = __builtin_amdgcn_make_buffer_rsrc((void*)p.wo, 0, DM * DM * 2, 0x00020000);
#pragma unroll
        for (int e = 0; e < 4; ++e) glds(ro, rB * 1024 + colB, (32 * ((b & 127) >> 3) + 8 * e) * 1024 + (b & 7) * 128, L_SO + e * 1024);
        const srd_t r2 = __builtin_amdgcn_make_buffer_rsrc((void*)p.w2, 0, FF * DM * 2, 0x00020000);
        const int b2 = b < 224 ? b : 0;
#pragma unroll
        for (int e = 0; e < 8; ++e) { const int id = 2 * b2 + (e >> 2); glds(r2, rB * 1024 + colB, (32 * (id >> 3) + 8 * (e & 3)) * 1024 + (id & 7) * 128, L_SO + 4096 + e * 1024); }
        VM0();
        {
          const int row = 16 * (g >> 1) + 4 * (g & 1) + q4;
#pragma unroll
          for (int nt = 0; nt < 4; ++nt) { const unsigned a = L_SO + row * 128 + ((nt ^ ((row >> 1) & 3)) * 32) + pp * 8; rt[8 + nt] = cat(trd(a), trd(a + 1024)); }
        }
        const int row = 4 * g + q4;
#pragma unroll
        for (int e = 0; e < 8; ++e) { const int nt = e & 3; const unsigned a = L_SO + 4096 + (e >> 2) * 4096 + row * 128 + ((nt ^ ((row >> 1) & 3)) * 32) + pp * 8; rt[e] = cat(trd(a), trd(a + 2048)); }
      }
    }
    STAMP(1);
    if (w >= 6) {
      while (*(volatile LAS unsigned*)(uintptr_t)(L_CNT + 8) < 4u) __builtin_amdgcn_s_sleep(1);
      CFENCE();
      if (w == 6) {
#pragma unroll
        for (int e = 0; e < 7; ++e) st16(rWS, asv4u(rt[e]), WS_TW1 + ((cj * 7 + e) * 16 + ci) * 1024 + lane * 16);
      } else {
        if (b < 224) {
#pragma unroll
          for (int e = 0; e < 8; ++e) { const int id = 2 * b + (e >> 2); st16(rWS, asv4u(rt[e]), WS_TW2 + (((id & 7) * 56 + (id >> 3)) * 4 + (e & 3)) * 1024 + lane * 16); }
        }
        if (b < 128) {
#pragma unroll
          for (int e = 0; e < 4; ++e) st16(rWS, asv4u(rt[8 + e]), WS_TWO + (((b & 7) * 16 + (b >> 3)) * 4 + e) * 1024 + lane * 16);
        }
      }
    }
    wait_words(p.ws, WS_FQ + cj * 128, 16, lane, TAG);
    while (*(volatile LAS unsigned*)(uintptr_t)(L_CNT + 4) == 0u) __builtin_amdgcn_s_sleep(1);
    CFENCE();
    STAMP(19);
#pragma unroll
    for (int e = 0; e < 8; ++e) glds4x1(rWS, lane * 16, WS_TQ + cj * 98304 + ((w - 5) * 8 + ((e + 2 * (ci & 3)) & 7)) * 4096, L_WQKV + ((w - 5) * 8 + ((e + 2 * (ci & 3)) & 7)) * 4096);
  }
  {
    const int qr = lane & 31, h = lane >> 5;
#pragma unroll
    for (int r = 0; r < 16; ++r) { const int kr = (r & 3) + 8 * (r >> 2) + 4 * h; mU[r] = kr > qr ? -1e30f : 0.f; mL[r] = kr > qr ? 0.f : -1e30f; }
    asm volatile("" : "+v"(mU), "+v"(mL));
  }
  if (w == 0) wait_words(p.ws, WS_F0 + ci * 512, 64, lane, TAG);
  STAMP(2);
  if (w >= 5) VM0();
  BAR();
  STAMP(3);
  if (lane == 0) {
    if (w == 6) arrive(p.ws, WS_FW1 + (cj * 16 + ci) * 8, TAG);
    if (w == 7 && b < 128) arrive(p.ws, WS_FWO + ((b & 7) * 16 + (b >> 3)) * 8, TAG);
    if (w == 7 && b < 224) { arrive(p.ws, WS_FW2 + (((2 * b) & 7) * 64 + ((2 * b) >> 3)) * 8, TAG); arrive(p.ws, WS_FW2 + (((2 * b + 1) & 7) * 64 + ((2 * b + 1) >> 3)) * 8, TAG); }
  }

  f4 accQ[2], accKV[4];
  float rstd, nmu;
  v2u gaR[2], cbR[2], sbR[2], gbR[2], b2R[2];
  v4u mc0, md0;
  v2u mlo[2], mhi[2];
  const int tt = w & 3, kh = w >> 2;
  const int ocol = 64 * ct + 32 * kh + 4 * g;
  {
    const unsigned bq = opq(L_WQKV + lane * 16), bv = opq(L_WQKV + 65536 + lane * 16);
#define QF(nt, s) lds16(((nt) < 4 ? bq : bv) + ((nt) & 3) * 16384 + (s) * 1024)
    v8s act[16];
    const unsigned hsrc = WS_HF + (ci * 8 + w) * 16 * 1024;
    v2u wl[2], wh[2];
#pragma unroll
    for (int u = 0; u < 2; ++u) { const unsigned o = (32 * (2 * w + u) + 4 * g) * 2; wl[u] = ld8(rWS, o, WS_WV + ci * 1024); wh[u] = ld8(rWS, o + 32, WS_WV + ci * 1024); }
#pragma unroll
    for (int s = 0; s < 4; ++s) act[s] = asv8(ld16(rWS, lane * 16, hsrc + s * 1024));
    {
      f4 au[6];
      v8s W[2][6];
      const unsigned c0 = opq(bq + w * 2048), d0 = opq(bv + w * 2048);
#pragma unroll
      for (int u = 0; u < 2; ++u)
#pragma unroll
        for (int nt = 0; nt < 6; ++nt) W[u][nt] = lds16((nt < 4 ? c0 : d0) + (nt & 3) * 16384 + u * 1024);
#pragma unroll
      for (int nt = 0; nt < 6; ++nt) au[nt] = f4{0, 0, 0, 0};
#pragma unroll
      for (int u = 0; u < 2; ++u) {
        const v8s a = asv8(v4u{wl[u][0], wl[u][1], wh[u][0], wh[u][1]});
#pragma unroll
        for (int nt = 0; nt < 6; ++nt) au[nt] = MFMA16(W[u][nt], a, au[nt]);
      }
      if (tok == 0) {
#pragma unroll
        for (int nt = 0; nt < 6; ++nt) *(LAS f4*)(sm + L_PARTU + (w * 96 + nt * 16 + 4 * g) * 4) = au[nt];
      }
    }
    BAR();
    if (tid >= 384 && tid < 480) {
      const int e0 = tid - 384;
      float s = 0;
#pragma unroll
      for (int e = 0; e < 8; ++e) s += *(const LAS float*)(sm + L_PARTU + (e * 96 + e0) * 4);
      *(LAS float*)(sm + L_PERS + e0 * 4) = s;
    }
    STAMP(16);
    v2u st[4];
    u64 fm0 = 0, fm1 = 0;
#pragma unroll
    for (int nt = 0; nt < 4; ++nt) accKV[nt] = f4{0, 0, 0, 0};
#pragma unroll
    for (int nt = 0; nt < 2; ++nt) accQ[nt] = f4{0, 0, 0, 0};
    {
      v8s W[6];
#pragma unroll
      for (int nt = 0; nt < 6; ++nt) W[nt] = QF(nt, 0);
      SB();
#pragma unroll
      for (int s = 0; s < 16; ++s) {
        if (s == 0) act[4] = asv8(ld16(rWS, lane * 16, hsrc + 4 * 1024));
        if (s + 5 < 16) act[s + 5] = asv8(ld16(rWS, lane * 16, hsrc + (s + 5) * 1024));
        else if (s < 15) st[s - 11] = ld8(rWS, ((4 * g + s - 11) * 128 + 16 * w + tok) * 8, WS_ST1 + ci * 16 * 128 * 8);
        if (s == 12 && w == 4) fm0 = __hip_atomic_load((GAS u64*)(p.ws + WS_FM) + lane, RLX_AGENT), fm1 = __hip_atomic_load((GAS u64*)(p.ws + WS_FM + 512) + (lane & 15), RLX_AGENT);
#pragma unroll
        for (int nt = 0; nt < 6; ++nt) {
          if (nt < 2) accQ[nt] = MFMA16(W[nt], act[s], accQ[nt]); else accKV[nt - 2] = MFMA16(W[nt], act[s], accKV[nt - 2]);
          if (s + 1 < 16) W[nt] = QF(nt, s + 1);
          SB();
        }
      }
    }
    float s1 = 0, s2 = 0;
#pragma unroll
    for (int e = 0; e < 4; ++e) { s1 += __uint_as_float(st[e][0]); s2 += __uint_as_float(st[e][1]); }
    s1 = sumg(s1); s2 = sumg(s2);
    const float mu = s1 * (1.f / 512), var = fmaxf(s2 * (1.f / 512) - mu * mu, 0.f);
    rstd = __builtin_amdgcn_rsqf(var + 1e-5f); nmu = -rstd * mu;
    if (w == 4) {
      GAS u64* f0 = (GAS u64*)(p.ws + WS_FM) + lane;
      GAS u64* f1 = (GAS u64*)(p.ws + WS_FM + 512) + (lane & 15);
      for (unsigned spins = 0; spins < (1u << 17) && !__all(fm0 == TAG && fm1 == TAG); ++spins) { fm0 = __hip_atomic_load(f0, RLX_AGENT); fm1 = __hip_atomic_load(f1, RLX_AGENT); }
      CFENCE();
      glds4x1(rWS, lane * 16, WS_MOD, L_MODA);
      glds2x1(rWS, lane * 16, WS_MOD + 4096, L_MODB);
    }
    if (w == 2 && lane < 8) *(LAS v4u*)(sm + L_B2C + lane * 16) = *(const v4u*)(p.b2 + 64 * ct + lane * 8);
    STAMP(4);
    BAR();
    {
      v4u kk, vv;
      unsigned pkd[4][2];
#pragma unroll
      for (int nt = 0; nt < 4; ++nt) {
        const f4 u = ldsf4(L_PERS + (32 + nt * 16 + 4 * g) * 4);
        pkd[nt][0] = pk(rstd * accKV[nt][0] + nmu * u[0], rstd * accKV[nt][1] + nmu * u[1]);
        pkd[nt][1] = pk(rstd * accKV[nt][2] + nmu * u[2], rstd * accKV[nt][3] + nmu * u[3]);
      }
      { auto r0 = __builtin_amdgcn_permlane16_swap(pkd[0][0], pkd[1][0], false, false); auto r1 = __builtin_amdgcn_permlane16_swap(pkd[0][1], pkd[1][1], false, false);
        kk = v4u{r0[0], r1[0], r0[1], r1[1]}; }
      { auto r0 = __builtin_amdgcn_permlane16_swap(pkd[2][0], pkd[3][0], false, false); auto r1 = __builtin_amdgcn_permlane16_swap(pkd[2][1], pkd[3][1], false, false);
        vv = v4u{r0[0], r1[0], r0[1], r1[1]}; }
      const int hd = g & 1, pl = g >> 1;
      st16(rWS, kk, WS_KV + (ci * 16 + cj) * 16384 + ((0 * 2 + hd) * 2 + pl) * 2048 + (16 * w + tok) * 16);
      st16(rWS, vv, WS_KV + (ci * 16 + cj) * 16384 + ((1 * 2 + hd) * 2 + pl) * 2048 + (16 * w + tok) * 16);
      *(LAS v4u*)(sm + L_AK + (hd * 2 + pl) * KPL + (256 + 16 * w + tok) * 16) = kk;
      *(LAS v4u*)(sm + L_AV + hd * VHD + pl * VPL + (256 + 16 * w + tok) * 16) = vv;
    }
    VM0();
    BAR();
    if (tid == 256) arrive(p.ws, WS_FP + (0 * 16 + ci) * 128 + cj * 8, TAG);
    STAMP(5);
    if (tid < 400) *(LAS v4u*)(sm + L_ONE + tid * 16) = v4u{0x3f803f80u, 0x3f803f80u, 0x3f803f80u, 0x3f803f80u};
    {
      f4 av[4];
      v8s W[2][4], a[2];
      const unsigned c0 = opq(bq + w * 2048), d0 = opq(bv + w * 2048);
#pragma unroll
      for (int u = 0; u < 2; ++u) {
        const v2u lo = lds8(L_MODA + (32 * (2 * w + u) + 4 * g) * 2), hi = lds8(L_MODA + (32 * (2 * w + u) + 16 + 4 * g) * 2);
        a[u] = asv8(v4u{lo[0], lo[1], hi[0], hi[1]});
#pragma unroll
        for (int nt = 0; nt < 4; ++nt) W[u][nt] = lds16((nt < 2 ? c0 : d0) + (nt & 1) * 16384 + u * 1024);
      }
#pragma unroll
      for (int nt = 0; nt < 4; ++nt) av[nt] = f4{0, 0, 0, 0};
#pragma unroll
      for (int u = 0; u < 2; ++u)
#pragma unroll
        for (int nt = 0; nt < 4; ++nt) av[nt] = MFMA16(W[u][nt], a[u], av[nt]);
      if (tok == 0) {
#pragma unroll
        for (int nt = 0; nt < 4; ++nt) *(LAS f4*)(sm + L_PARTV + (w * 64 + nt * 16 + 4 * g) * 4) = av[nt];
      }
    }
    {
#pragma unroll
      for (int e = 0; e < 2; ++e) {
        gaR[e] = lds8(L_MODA + (1024 + ocol + 16 * e) * 2); sbR[e] = lds8(L_MODA + (1536 + ocol + 16 * e) * 2);
        cbR[e] = lds8(L_MODB + (ocol + 16 * e) * 2); gbR[e] = lds8(L_MODB + (512 + ocol + 16 * e) * 2);
        b2R[e] = lds8(L_B2C + (32 * kh + 4 * g + 16 * e) * 2);
      }
      mc0 = asv4u(lds16(L_MODB + 16 * lane)); md0 = asv4u(lds16(L_MODA + 3072 + 16 * lane));
#pragma unroll
      for (int u = 0; u < 2; ++u) { const unsigned o = (32 * (2 * w + u) + 4 * g) * 2 + (tok == 1 ? L_MODA + 3072 : L_MODB); mlo[u] = lds8(o); mhi[u] = lds8(o + 32); }
      BAR();
      if (tid < 64) {
        float s = 0;
#pragma unroll
        for (int e = 0; e < 8; ++e) s += *(const LAS float*)(sm + L_PARTV + (e * 64 + tid) * 4);
        *(LAS float*)(sm + L_PERS + (96 + tid) * 4) = s;
      }
    }
    if (ci < 2) {
      for (int e = tid; e < 8 * 256; e += 512) {
        const int pln = e >> 8, key = e & 255;
        if (ci == 0 || key < 128) {
          const unsigned a = pln < 4 ? L_AK + pln * KPL : L_AV + ((pln - 4) >> 1) * VHD + (pln & 1) * VPL;
          *(LAS v4u*)(sm + a + key * 16) = v4u{0, 0, 0, 0};
        }
      }
    }
    if (w == 4) {
      GAS u64* f0 = (GAS u64*)(p.ws + (lane == 0 && ci >= 1 ? WS_FP + (ci - 1) * 128 + cj * 8 : lane == 1 && ci >= 2 ? WS_FP + (ci - 2) * 128 + cj * 8
                                       : lane >= 16 && lane < 32 ? WS_FW1 + (cj * 16 + lane - 16) * 8 : WS_FWO + (ct * 16 + (lane & 15)) * 8));
      GAS u64* f1 = (GAS u64*)(p.ws + WS_FW2 + ct * 512) + (lane < 56 ? lane : 0);
      for (unsigned spins = 0; spins < (1u << 17); ++spins) {
        const u64 a = __hip_atomic_load(f0, RLX_AGENT), c = __hip_atomic_load(f1, RLX_AGENT);
        if (__all(a == TAG && c == TAG)) break;
      }
      CFENCE();
    }
    BAR();
    STAMP(6);
    {
      const unsigned dst = w < 4 ? L_AK + w * KPL : L_AV + ((w - 4) >> 1) * VHD + (w & 1) * VPL;
#pragma unroll
      for (int src = 0; src < 2; ++src)
        if (ci - 2 + src >= 0) glds2x1(rWS, lane * 16, WS_KV + ((ci - 2 + src) * 16 + cj) * 16384 + w * 2048, dst + src * 2048);
    }
    {
      const float qs = 0.25f * 1.4426950408889634f;
      unsigned pq[2][2];
#pragma unroll
      for (int nt = 0; nt < 2; ++nt) {
        const f4 u = ldsf4(L_PERS + (nt * 16 + 4 * g) * 4), v = ldsf4(L_PERS + (96 + nt * 16 + 4 * g) * 4);
        pq[nt][0] = pk(qs * (rstd * accQ[nt][0] + nmu * u[0] + v[0]), qs * (rstd * accQ[nt][1] + nmu * u[1] + v[1]));
        pq[nt][1] = pk(qs * (rstd * accQ[nt][2] + nmu * u[2] + v[2]), qs * (rstd * accQ[nt][3] + nmu * u[3] + v[3]));
      }
      auto r0 = __builtin_amdgcn_permlane16_swap(pq[0][0], pq[1][0], false, false); auto r1 = __builtin_amdgcn_permlane16_swap(pq[0][1], pq[1][1], false, false);
      *(LAS v4u*)(sm + L_AQ + ((g & 1) * 2 + (g >> 1)) * 2048 + (16 * w + tok) * 16) = v4u{r0[0], r1[0], r0[1], r1[1]};
    }
    VM0();
    BAR();
    STAMP(7);
  }

  const int orow = 128 * ci + 64 * hf + 16 * tt + tok;
  v2u xr0, xr1;
  {
    const int t = w & 3, hd = w >> 2;
    const int kt0 = ci >= 2 ? 0 : 8 - 4 * ci - t;
    f16v O;
    const u16* xres = p.x + (size_t)orow * DM + ocol;
    if (ci >= 2) attention<true>(t, hd, 0, lane, mU, mL, O, rWS, WS_TWO + ct * 65536, WS_TW1 + cj * 7 * 16384, w, xres, xr0, xr1, stampv);
    else attention<false>(t, hd, kt0, lane, mU, mL, O, rWS, WS_TWO + ct * 65536, WS_TW1 + cj * 7 * 16384, w, xres, xr0, xr1, stampv);
    const int h = lane >> 5;
    const float inv = __builtin_amdgcn_rcpf(O[8]);
    const f4 b0 = ldsf4(L_PERS + (96 + 32 + hd * 16 + 4 * h) * 4), b1 = ldsf4(L_PERS + (96 + 32 + hd * 16 + 8 + 4 * h) * 4);
    const v4u o = {pk(O[0] * inv + b0[0], O[1] * inv + b0[1]), pk(O[2] * inv + b0[2], O[3] * inv + b0[3]),
                   pk(O[4] * inv + b1[0], O[5] * inv + b1[1]), pk(O[6] * inv + b1[2], O[7] * inv + b1[3])};
    const int qr = lane & 31;
    st16(rWS, o, WS_OF + (ci * 8 * 16 + cj) * 1024 + ((2 * t + (qr >> 4)) * 16) * 1024 + ((2 * hd + h) * 16 + (qr & 15)) * 16);
  }
  STAMP(24);
  VM0();
  BAR();
  if (tid == 256) arrive(p.ws, WS_FP + (1 * 16 + ci) * 128 + cj * 8, TAG);
  STAMP(8);
  float b1v = 0.f;
  v8s aux2[2];
  float Cw = 0, Cw2 = 0, Cws = 0, Cs = 0, Cs2 = 0;
#define MODSUMS() do { \
      _Pragma("unroll") for (int r = 0; r < 4; ++r) { \
        const float w0 = rbf(1.f + bflo(mc0[r])), w1 = rbf(1.f + bfhi(mc0[r])), s0 = bflo(md0[r]), s1_ = bfhi(md0[r]); \
        Cw += w0 + w1; Cw2 += w0 * w0 + w1 * w1; Cws += w0 * s0 + w1 * s1_; Cs += s0 + s1_; Cs2 += s0 * s0 + s1_ * s1_; } \
      Cw = sumg(rowsum16(Cw)); Cw2 = sumg(rowsum16(Cw2)); Cws = sumg(rowsum16(Cws)); Cs = sumg(rowsum16(Cs)); Cs2 = sumg(rowsum16(Cs2)); \
      _Pragma("unroll") for (int u = 0; u < 2; ++u) { v4u t; \
        t[0] = tok == 0 ? pk(1.f + bflo(mlo[u][0]), 1.f + bfhi(mlo[u][0])) : (tok == 1 ? mlo[u][0] : 0x3f803f80u); \
        t[1] = tok == 0 ? pk(1.f + bflo(mlo[u][1]), 1.f + bfhi(mlo[u][1])) : (tok == 1 ? mlo[u][1] : 0x3f803f80u); \
        t[2] = tok == 0 ? pk(1.f + bflo(mhi[u][0]), 1.f + bfhi(mhi[u][0])) : (tok == 1 ? mhi[u][0] : 0x3f803f80u); \
        t[3] = tok == 0 ? pk(1.f + bflo(mhi[u][1]), 1.f + bfhi(mhi[u][1])) : (tok == 1 ? mhi[u][1] : 0x3f803f80u); \
        aux2[u] = asv8(t); } \
      asm volatile("" : "+v"(Cw), "+v"(Cw2), "+v"(Cws), "+v"(Cs), "+v"(Cs2), "+v"(aux2[0]), "+v"(aux2[1])); } while (0)
  if (w != 4) {
#pragma unroll
    for (int e = 0; e < 3; ++e) { const int gi = wq + 7 * e; if (gi < 16) glds4x1(rWS, lane * 16, WS_TW1 + (cj * 7 + 2) * 16384 + gi * 4096, L_W1 + 32768 + gi * 4096); }
  }
  MODSUMS();
  if (w == 4) wait_words(p.ws, WS_FP + (1 * 16 + ci) * 128, 16, lane, TAG);
  BAR();
  STAMP(9);

  float x1[8];
  {
    v8s oa[8];
    const unsigned osrc = WS_OF + ((ci * 8 + 4 * hf + tt) * 16 + 8 * kh) * 1024;
#pragma unroll
    for (int s = 0; s < 3; ++s) oa[s] = asv8(ld16(rWS, lane * 16, osrc + s * 1024));
    const unsigned bo = opq(L_WO + kh * 32768 + lane * 16);
    f4 acc[4];
#pragma unroll
    for (int nt = 0; nt < 4; ++nt) acc[nt] = f4{0, 0, 0, 0};
    {
      v8s W[2][4];
#pragma unroll
      for (int s = 0; s < 2; ++s)
#pragma unroll
        for (int nt = 0; nt < 4; ++nt) W[s][nt] = lds16(bo + (s * 4 + nt) * 1024);
      SB();
#pragma unroll
      for (int s = 3; s < 8; ++s) oa[s] = asv8(ld16(rWS, lane * 16, osrc + s * 1024));
      if (tid < 112) { const int nt = tid >> 4; b1v = bflo(p.b1[(nt == 6 ? 112 * cj + (par ? 0 : 96) : 112 * cj + 16 * par + nt * 16) + (tid & 15)]); }
      SB();
#pragma unroll
      for (int s = 0; s < 8; ++s)
#pragma unroll
        for (int nt = 0; nt < 4; ++nt) {
          acc[nt] = MFMA16(W[s & 1][nt], oa[s], acc[nt]);
          if (s + 2 < 8) W[s & 1][nt] = lds16(bo + ((s + 2) * 4 + nt) * 1024);
          SB();
        }
    }
    STAMP(26);
    VM0();
    BAR();
    { const int gi = w;
      if (gi < 4) glds4x1(rWS, lane * 16, WS_TW1 + (cj * 7 + 6) * 16384 + gi * 4096, L_W1S + gi * 4096);
      else glds4x1(rWS, lane * 16, WS_TW2 + ct * 229376 + (gi - 4) * 4096, 32768 + (gi - 4) * 4096);
      if (w < 4) glds4x1(rWS, lane * 16, WS_TW2 + ct * 229376 + 7 * 16384 + w * 4096, 49152 + w * 4096); }
    *(LAS f4*)(sm + L_XCH + w * 2048 + lane * 16) = kh ? acc[0] : acc[2];
    *(LAS f4*)(sm + L_XCH + w * 2048 + 1024 + lane * 16) = kh ? acc[1] : acc[3];
    BAR();
    const f4 o0 = ldsf4(L_XCH + (w ^ 4) * 2048 + lane * 16), o1 = ldsf4(L_XCH + (w ^ 4) * 2048 + 1024 + lane * 16);
    const f4 t0 = (kh ? acc[2] : acc[0]) + o0, t1 = (kh ? acc[3] : acc[1]) + o1;
    const float xin[8] = {bflo(xr0[0]), bfhi(xr0[0]), bflo(xr0[1]), bfhi(xr0[1]), bflo(xr1[0]), bfhi(xr1[0]), bflo(xr1[1]), bfhi(xr1[1])};
    const float ga[8] = {bflo(gaR[0][0]), bfhi(gaR[0][0]), bflo(gaR[0][1]), bfhi(gaR[0][1]), bflo(gaR[1][0]), bfhi(gaR[1][0]), bflo(gaR[1][1]), bfhi(gaR[1][1])};
    const float cb[8] = {bflo(cbR[0][0]), bfhi(cbR[0][0]), bflo(cbR[0][1]), bfhi(cbR[0][1]), bflo(cbR[1][0]), bfhi(cbR[1][0]), bflo(cbR[1][1]), bfhi(cbR[1][1])};
    const float sv[8] = {bflo(sbR[0][0]), bfhi(sbR[0][0]), bflo(sbR[0][1]), bfhi(sbR[0][1]), bflo(sbR[1][0]), bfhi(sbR[1][0]), bflo(sbR[1][1]), bfhi(sbR[1][1])};
    float wv[8], xw[8];
#pragma unroll
    for (int r = 0; r < 8; ++r) { x1[r] = rbf(xin[r] + ga[r] * (r < 4 ? t0[r & 3] : t1[r & 3])); wv[r] = rbf(1.f + cb[r]); xw[r] = x1[r] * wv[r]; }
    st16(rWS, v4u{pk(xw[0], xw[1]), pk(xw[2], xw[3]), pk(xw[4], xw[5]), pk(xw[6], xw[7])}, WS_X1F + ((ci * 8 + 4 * hf + tt) * 16 + 2 * ct + kh) * 1024 + lane * 16);
    SB();
    float s1 = 0, s2 = 0, s3 = 0, s4 = 0, s5 = 0, s6 = 0;
#pragma unroll
    for (int r = 0; r < 8; ++r) { s1 += x1[r]; s2 += x1[r] * x1[r]; s3 += xw[r]; s4 += xw[r] * wv[r]; s5 += xw[r] * xw[r]; s6 += xw[r] * sv[r]; }
    s1 = sumg(s1); s2 = sumg(s2); s3 = sumg(s3); s4 = sumg(s4); s5 = sumg(s5); s6 = sumg(s6);
    const unsigned rec = (ci * 16 + 2 * ct + kh) * 128 + 64 * hf + 16 * tt + tok;
    if (g == 0) st16(rWS, v4u{__float_as_uint(s1), __float_as_uint(s2), __float_as_uint(s3), __float_as_uint(s4)}, WS_ST2A + rec * 16);
    if (g == 1) st8(rWS, v2u{__float_as_uint(s5), __float_as_uint(s6)}, WS_ST2B + rec * 8);
  }
  VM0();
  BAR();
  if (tid == 256) arrive(p.ws, WS_FP + (2 * 16 + ci) * 128 + cj * 8, TAG);
  STAMP(10);

  v4u stg[8];
  {
    const unsigned bP = opq(L_W1 + lane * 16), bR = opq(L_W1 + 65536 + lane * 16), bS = opq(L_W1S + lane * 16);
#define W1F(nt, s) lds16((nt) == 6 ? bS + (s) * 1024 : (nt) < 4 ? bP + (nt) * 16384 + (s) * 1024 : bR + ((nt) - 4) * 16384 + (s) * 1024)
    {
      f4 au[7];
#pragma unroll
      for (int nt = 0; nt < 7; ++nt) au[nt] = f4{0, 0, 0, 0};
      const unsigned c0 = opq(bP + w * 2048), d0 = opq(bR + w * 2048), e0 = opq(bS + w * 2048);
#pragma unroll
      for (int u = 0; u < 2; ++u) {
        v8s W[7];
#pragma unroll
        for (int nt = 0; nt < 7; ++nt) W[nt] = lds16(nt == 6 ? e0 + u * 1024 : nt < 4 ? c0 + nt * 16384 + u * 1024 : d0 + (nt - 4) * 16384 + u * 1024);
#pragma unroll
        for (int nt = 0; nt < 7; ++nt) au[nt] = MFMA16(W[nt], aux2[u], au[nt]);
      }
      if (tok < 3) {
#pragma unroll
        for (int nt = 0; nt < 7; ++nt) *(LAS f4*)(sm + L_XCH + ((w * 3 + tok) * 112 + nt * 16 + 4 * g) * 4) = au[nt];
      }
    }
    BAR();
    if (tid < 112) *(LAS float*)(sm + L_XF + (336 + tid) * 4) = b1v;
    {
      const int e0 = w < 4 ? tid : tid - 64;
      if (w != 4 && e0 < 336) {
        float s = 0;
#pragma unroll
        for (int e = 0; e < 8; ++e) s += *(const LAS float*)(sm + L_XCH + (e * 336 + e0) * 4);
        *(LAS float*)(sm + L_XF + e0 * 4) = s;
      }
    }
    if (w == 4) wait_words(p.ws, WS_FP + (2 * 16 + ci) * 128, 16, lane, TAG);
    BAR();
    STAMP(11);
    v8s act2[16];
    const unsigned asrc = WS_X1F + (ci * 8 + w) * 16 * 1024;
#pragma unroll
    for (int s = 0; s < 4; ++s) act2[s] = asv8(ld16(rWS, lane * 16, asrc + s * 1024));
    v4u sa[4]; v2u sb[4];
    f4 acc[7];
#pragma unroll
    for (int nt = 0; nt < 7; ++nt) acc[nt] = f4{0, 0, 0, 0};
    {
      v8s W[6];
#pragma unroll
      for (int nt = 0; nt < 6; ++nt) W[nt] = W1F(nt, 0);
      SB();
#pragma unroll
      for (int s = 0; s < 16; ++s) {
        if (s == 0) act2[4] = asv8(ld16(rWS, lane * 16, asrc + 4 * 1024));
        if (s + 5 < 16) act2[s + 5] = asv8(ld16(rWS, lane * 16, asrc + (s + 5) * 1024));
        else if (s < 15) { sa[s - 11] = ld16(rWS, ((4 * g + s - 11) * 128 + 16 * w + tok) * 16, WS_ST2A + ci * 16 * 128 * 16);
                           sb[s - 11] = ld8(rWS, ((4 * g + s - 11) * 128 + 16 * w + tok) * 8, WS_ST2B + ci * 16 * 128 * 8); }
#pragma unroll
        for (int nt = 0; nt < 6; ++nt) {
          acc[nt] = MFMA16(W[nt], act2[s], acc[nt]);
          if (s + 1 < 16) W[nt] = W1F(nt, s + 1);
          SB();
        }
      }
    }
    STAMP(18);
    CSTAMP(42);
    v8s WB[4];
#pragma unroll
    for (int s = 0; s < 4; ++s) WB[s] = W1F(6, s);
    float s1 = 0, s2 = 0, s3 = 0, s4 = 0, s5 = 0, s6 = 0;
#pragma unroll
    for (int e = 0; e < 4; ++e) { s1 += __uint_as_float(sa[e][0]); s2 += __uint_as_float(sa[e][1]); s3 += __uint_as_float(sa[e][2]); s4 += __uint_as_float(sa[e][3]); s5 += __uint_as_float(sb[e][0]); s6 += __uint_as_float(sb[e][1]); }
    s1 = sumg(s1); s2 = sumg(s2); s3 = sumg(s3); s4 = sumg(s4); s5 = sumg(s5); s6 = sumg(s6);
    const float mu1 = s1 * (1.f / 512), var1 = fmaxf(s2 * (1.f / 512) - mu1 * mu1, 0.f), r1 = __builtin_amdgcn_rsqf(var1 + 1e-5f);
    const float mu2 = (r1 * (s3 - mu1 * Cw) + Cs) * (1.f / 512);
    const float hh = (r1 * r1 * (s5 - 2.f * mu1 * s4 + mu1 * mu1 * Cw2) + 2.f * r1 * (s6 - mu1 * Cws) + Cs2) * (1.f / 512);
    const float r2 = __builtin_amdgcn_rsqf(fmaxf(hh - mu2 * mu2, 0.f) + 1e-5f);
    const float cA = r2 * r1, cB = -cA * mu1, cC = r2, cD = -r2 * mu2;
    const unsigned S0 = (7 * cj + par) >> 1;
    const unsigned hb = WS_H1F + (ci * 8 + w) * 56 * 1024;
    const unsigned xf = opq(L_XF + 16 * g);
#define BIAS4(nt) __builtin_elementwise_fma(f4{cB, cB, cB, cB}, ldsf4(xf + (nt) * 64), __builtin_elementwise_fma(f4{cC, cC, cC, cC}, ldsf4(xf + 448 + (nt) * 64), \
                    __builtin_elementwise_fma(f4{cD, cD, cD, cD}, ldsf4(xf + 896 + (nt) * 64), ldsf4(xf + 1344 + (nt) * 64))))
#define GELU4(o, a, bias) do { const f4 z_ = __builtin_elementwise_fma(f4{cA, cA, cA, cA}, a, bias); const f4 q_ = __builtin_elementwise_fma(z_ * z_, f4{-0.1029432397f, -0.1029432397f, -0.1029432397f, -0.1029432397f}, \
        f4{-2.3022081985f, -2.3022081985f, -2.3022081985f, -2.3022081985f}) * z_; f4 e_; \
        _Pragma("unroll") for (int r = 0; r < 4; ++r) e_[r] = __builtin_amdgcn_exp2f(q_[r]); \
        e_ += f4{1.f, 1.f, 1.f, 1.f}; \
        _Pragma("unroll") for (int r = 0; r < 4; ++r) e_[r] = __builtin_amdgcn_rcpf(e_[r]); \
        o = z_ * e_; } while (0)
    {
      unsigned hp[2][2];
      f4 bias = BIAS4(0);
      PIN(bias);
      SB();
#pragma unroll
      for (int nt = 0; nt < 6; ++nt) {
#pragma unroll
        for (int k = (nt * 16) / 6; k < ((nt + 1) * 16) / 6; ++k) { acc[6] = MFMA16(WB[k & 3], act2[k], acc[6]); if (k + 4 < 16) WB[k & 3] = W1F(6, k + 4); }
        if (nt >= 1 && nt < 5) {
#pragma unroll
          for (int e = 2 * (nt - 1); e < 2 * nt; ++e) { const int pc = w * 8 + e; const int chunk = (pc >> 4) == 0 ? 5 : ((pc >> 4) == 1 ? 12 : ((pc >> 4) == 2 ? 6 : 13));
            stg[e] = ld16(rWS, lane * 16, WS_TW2 + ct * 229376 + chunk * 16384 + (pc & 15) * 1024); }
        }
        f4 nb = bias;
        if (nt < 5) nb = BIAS4(nt + 1); else nb = BIAS4(6);
        f4 o;
        GELU4(o, acc[nt], bias);
        hp[nt & 1][0] = pk(o[0], o[1]); hp[nt & 1][1] = pk(o[2], o[3]);
        PIN(hp[nt & 1][0]); PIN(hp[nt & 1][1]); PIN(nb);
        bias = nb;
        if (nt & 1) st16(rWS, v4u{hp[0][0], hp[0][1], hp[1][0], hp[1][1]}, hb + (S0 + (nt >> 1)) * 1024 + lane * 16);
        SB();
      }
      STAMP(20);
      CSTAMP(43);
      f4 o;
      GELU4(o, acc[6], bias);
      const unsigned NT = 7 * cj + (par ? 0 : 6);
      st8(rWS, v2u{pk(o[0], o[1]), pk(o[2], o[3])}, hb + (NT >> 1) * 1024 + (NT & 1) * 512 + lane * 8);
    }
  }
  STAMP(25);
  VM0();
  BAR();
  if (tid == 256) arrive(p.ws, WS_FP + (3 * 16 + ci) * 128 + cj * 8, TAG);
  STAMP(13);
  if (w != 4) {
#pragma unroll
    for (int e = 0; e < 5; ++e) {
      const int gi = wq + 7 * e;
      if (gi < 32) { const int it = gi >> 3, khh = (gi >> 2) & 1, pc = gi & 3;
        glds4x1(rWS, lane * 16, WS_TW2 + ct * 229376 + (khh * 7 + 1 + it) * 16384 + pc * 4096, (it == 3 ? 0u : 65536u + it * 32768u) + khh * 16384u + pc * 4096u); }
    }
  } else {
    if (b == 0) {
      GAS u64* f = (GAS u64*)(p.ws + WS_F0) + lane;
      for (unsigned spins = 0; spins < (1u << 17); ++spins) {
        bool ok = true;
#pragma unroll
        for (int i = 0; i < 16; ++i) ok &= __hip_atomic_load(f + i * 64, RLX_AGENT) == TAG;
        if (__all(ok)) break;
      }
      if (lane == 0) __hip_atomic_store((GAS u64*)(p.ws + WS_EPOCH), TAG, RLX_AGENT);
    }
    wait_words(p.ws, WS_FP + (3 * 16 + ci) * 128, 16, lane, TAG);
  }
  VM0();
  BAR();
  STAMP(14);

  {
    v8s ha[28];
    const unsigned hb = WS_H1F + ((ci * 8 + 4 * hf + tt) * 56 + 28 * kh) * 1024;
#define HLOAD(s) do { if ((s) % 7 == 3) { const v2u lo_ = ld8(rWS, lane * 8, hb + (s) * 1024), hi_ = ld8(rWS, lane * 8 + 512, hb + (s) * 1024); ha[s] = asv8(v4u{lo_[0], lo_[1], hi_[0], hi_[1]}); } \
                      else ha[s] = asv8(ld16(rWS, lane * 16, hb + (s) * 1024)); } while (0)
#pragma unroll
    for (int s = 0; s < 3; ++s) HLOAD(s);
    const unsigned bw = opq(kh * 16384 + lane * 16), bw2 = opq(bw + 65536), bw3 = opq(bw + 131072);
    f4 acc[4];
#pragma unroll
    for (int nt = 0; nt < 4; ++nt) acc[nt] = f4{0, 0, 0, 0};
#define W2F(nt, S) lds16((((S) >> 2) == 0 || ((S) >> 2) == 5 ? bw + 32768 : ((S) >> 2) == 1 || ((S) >> 2) == 6 ? bw2 : ((S) >> 2) == 2 ? bw2 + 32768 : ((S) >> 2) == 3 ? bw3 : bw) + (((S) & 3) * 4 + (nt)) * 1024)
    {
      v8s W[2][4];
#pragma unroll
      for (int S = 0; S < 2; ++S)
#pragma unroll
        for (int nt = 0; nt < 4; ++nt) W[S][nt] = W2F(nt, S);
      SB();
#pragma unroll
      for (int s = 3; s < 8; ++s) HLOAD(s);
      SB();
#pragma unroll
      for (int S = 0; S < 28; ++S) {
        if (S + 8 < 28) HLOAD(S + 8);
        if (S == 8) BAR();
        if (S >= 8 && S < 16) { const int e = S - 8, pc = w * 8 + e; *(LAS v4u*)(sm + 32768 + (pc >> 5) * 32768 + ((pc >> 4) & 1) * 16384 + (pc & 15) * 1024 + lane * 16) = stg[e]; }
        if (S == 18) BAR();
#pragma unroll
        for (int nt = 0; nt < 4; ++nt) {
          acc[nt] = MFMA16(W[S & 1][nt], ha[S], acc[nt]);
          if (S + 2 < 28) W[S & 1][nt] = W2F(nt, S + 2);
          SB();
        }
      }
    }
    STAMP(22);
    BAR();
    *(LAS f4*)(sm + L_W2X + w * 2048 + lane * 16) = kh ? acc[0] : acc[2];
    *(LAS f4*)(sm + L_W2X + w * 2048 + 1024 + lane * 16) = kh ? acc[1] : acc[3];
    BAR();
    const f4 o0 = ldsf4(L_W2X + (w ^ 4) * 2048 + lane * 16), o1 = ldsf4(L_W2X + (w ^ 4) * 2048 + 1024 + lane * 16);
    const f4 t0 = (kh ? acc[2] : acc[0]) + o0, t1 = (kh ? acc[3] : acc[1]) + o1;
    const float gb[8] = {bflo(gbR[0][0]), bfhi(gbR[0][0]), bflo(gbR[0][1]), bfhi(gbR[0][1]), bflo(gbR[1][0]), bfhi(gbR[1][0]), bflo(gbR[1][1]), bfhi(gbR[1][1])};
    const float bb[8] = {bflo(b2R[0][0]), bfhi(b2R[0][0]), bflo(b2R[0][1]), bfhi(b2R[0][1]), bflo(b2R[1][0]), bfhi(b2R[1][0]), bflo(b2R[1][1]), bfhi(b2R[1][1])};
    float o[8];
#pragma unroll
    for (int r = 0; r < 8; ++r) o[r] = x1[r] + gb[r] * ((r < 4 ? t0[r & 3] : t1[r & 3]) + bb[r]);
    auto r0 = __builtin_amdgcn_permlane16_swap(pk(o[0], o[1]), pk(o[4], o[5]), false, false);
    auto r1 = __builtin_amdgcn_permlane16_swap(pk(o[2], o[3]), pk(o[6], o[7]), false, false);
    *(v4u*)(p.out + (size_t)orow * DM + 64 * ct + 32 * kh + (g & 1) * 16 + (g >> 1) * 8) = v4u{r0[0], r1[0], r0[1], r1[1]};
  }
  STAMP(15);
  CSTAMP(41);
  STAMP_FLUSH(0);
}

extern "C" void kernel_launch(void* const* d_in, const int* in_sizes, int n_in, void* d_out, int out_size, void* d_ws, size_t ws_size, hipStream_t stream) {
  Params p;
  p.x = (const u16*)d_in[0]; p.wqkv = (const u16*)d_in[1]; p.wo = (const u16*)d_in[2];
  p.w1 = (const u16*)d_in[4]; p.b1 = (const u16*)d_in[5]; p.w2 = (const u16*)d_in[6]; p.b2 = (const u16*)d_in[7];
  p.c = (const u16*)d_in[8]; p.wmod = (const u16*)d_in[9]; p.out = (u16*)d_out; p.ws = (char*)d_ws;
  fused_block<<<256, 512, 0, stream>>>(p);
}
```

```cpp
#include <hip/hip_runtime.h>
#include <hip/hip_bf16.h>
#include <stdint.h>

typedef unsigned short u16;
typedef short v4s __attribute__((ext_vector_type(4)));
typedef short v8s __attribute__((ext_vector_type(8)));
typedef float f2 __attribute__((ext_vector_type(2)));
typedef float f4 __attribute__((ext_vector_type(4)));
typedef float f16v __attribute__((ext_vector_type(16)));
typedef unsigned v2u __attribute__((ext_vector_type(2)));
typedef unsigned v4u __attribute__((ext_vector_type(4)));
typedef __bf16 b2 __attribute__((ext_vector_type(2)));
#define LAS __attribute__((address_space(3)))
#define GAS __attribute__((address_space(1)))
#define DI __device__ __forceinline__
typedef __amdgpu_buffer_rsrc_t srd_t;

constexpr int SEQ = 2048, DM = 512, FF = 1792;
constexpr unsigned WS_EPOCH = 0;
constexpr unsigned WS_F0 = 4096;
constexpr unsigned WS_FP = WS_F0 + 8192;
constexpr unsigned WS_FM = WS_FP + 4 * 16 * 128;
constexpr unsigned WS_FQ = WS_FM + 2048;
constexpr unsigned WS_FW1 = WS_FQ + 2048;
constexpr unsigned WS_FWO = WS_FW1 + 2048;
constexpr unsigned WS_FW2 = WS_FWO + 1024;
static_assert(WS_FW2 + 4096 <= (64 << 10), "flags");
constexpr unsigned WS_WV = 64 << 10;
constexpr unsigned WS_MOD = 128 << 10;
constexpr unsigned WS_ST1 = 192 << 10;
constexpr unsigned WS_ST2A = 512 << 10;
constexpr unsigned WS_ST2B = 1024 << 10;
constexpr unsigned WS_HF = 2u << 20;
constexpr unsigned WS_KV = 4u << 20;
constexpr unsigned WS_OF = 8u << 20;
constexpr unsigned WS_X1F = 10u << 20;
constexpr unsigned WS_H1F = 12u << 20;
constexpr unsigned WS_TQ = 19u << 20;
constexpr unsigned WS_TW1 = WS_TQ + (1536u << 10);
constexpr unsigned WS_TWO = WS_TW1 + (1792u << 10);
constexpr unsigned WS_TW2 = WS_TWO + (512u << 10);
constexpr unsigned WS_STAMP = 25u << 20;
constexpr unsigned WS_BYTES = 27u << 20;
static_assert(WS_TW2 + (1792u << 10) <= WS_STAMP, "ws");
constexpr unsigned L_CA = 98304;
constexpr unsigned L_XS = 131072;
constexpr unsigned L_S1 = L_XS + 8192;
constexpr unsigned L_SO = L_S1 + 7168;
constexpr unsigned L_SC = L_SO + 12288;
constexpr unsigned L_CAP = L_SC + 2048;
constexpr unsigned L_CNT = L_CAP + 512;
static_assert(L_CNT + 16 <= 163840, "lds");
constexpr unsigned L_SQ = 0;
constexpr unsigned L_M2 = 8192;
constexpr unsigned L_WQKV = 0;
constexpr unsigned L_PARTU = 98304;
constexpr unsigned L_PARTV = 32768;
constexpr unsigned L_AK = 98304;
constexpr unsigned KPL = 6144;
constexpr unsigned L_AV = L_AK + 4 * KPL;
constexpr unsigned VPL = 6208, VHD = 12416;
constexpr unsigned L_PSA = L_AK + KPL, L_PSB = L_AK + 3 * KPL, L_PCB = L_AV + VPL, L_PGA = L_AV + VHD + VPL, L_PGB = L_PGA + 1024, L_B2C = L_PGA + 2048;
constexpr unsigned L_PSA2 = L_AK + 2 * KPL, L_PSB2 = L_AV, L_PCB2 = L_AV + VHD;
constexpr unsigned L_FIN = L_AK;
constexpr unsigned L_ONE = L_AV + 2 * VHD;
constexpr unsigned L_AQ = L_ONE + 6400;
constexpr unsigned L_PERS = L_AQ + 8192;
static_assert(L_PERS + 1024 <= 163840, "lds");
static_assert((L_AV % 256) == 0 && (L_ONE % 256) == 0, "bank plan");
constexpr unsigned L_WO = 0;
constexpr unsigned L_W1 = 65536;
constexpr unsigned L_W1S = 0;
constexpr unsigned L_XCH = 16384;
constexpr unsigned L_XF = 16384 + 12288;
constexpr unsigned L_W2X = 98304;

struct Params { const u16* x; const u16* wqkv; const u16* wo; const u16* w1; const u16* b1; const u16* w2; const u16* b2; const u16* c; const u16* wmod; u16* out; char* ws; };

DI float bflo(unsigned v) { return __uint_as_float(v << 16); }
DI float bfhi(unsigned v) { return __uint_as_float(v & 0xffff0000u); }
DI unsigned pk(float a, float b) { f2 v = {a, b}; b2 r = __builtin_convertvector(v, b2); return __builtin_bit_cast(unsigned, r); }
DI float rbf(float a) { return bflo(pk(a, 0.f)); }
DI v8s cat(v4s a, v4s b) { return __builtin_shufflevector(a, b, 0, 1, 2, 3, 4, 5, 6, 7); }
DI v4s trd(unsigned a) { return __builtin_amdgcn_ds_read_tr16_b64_v4i16((LAS v4s*)(uintptr_t)a); }
DI v8s asv8(v4u v) { return __builtin_bit_cast(v8s, v); }
DI v4u asv4u(v8s v) { return __builtin_bit_cast(v4u, v); }
DI v8s lds16(unsigned a) { return *(const LAS v8s*)(uintptr_t)a; }
DI f4 ldsf4(unsigned a) { return *(const LAS f4*)(uintptr_t)a; }
DI v2u lds8(unsigned a) { return *(const LAS v2u*)(uintptr_t)a; }
#define MFMA16(a, b, c) __builtin_amdgcn_mfma_f32_16x16x32_bf16((a), (b), (c), 0, 0, 0)
#define MFMA32(a, b, c) __builtin_amdgcn_mfma_f32_32x32x16_bf16((a), (b), (c), 0, 0, 0)
#define BAR() asm volatile("s_waitcnt lgkmcnt(0)\n\ts_barrier" ::: "memory")
#define VM0() asm volatile("s_waitcnt vmcnt(0)" ::: "memory")
#define LG0() asm volatile("s_waitcnt lgkmcnt(0)" ::: "memory")
#define CFENCE() asm volatile("" ::: "memory")
#define SB() __builtin_amdgcn_sched_barrier(0)
DI unsigned opq(unsigned v) { asm volatile("" : "+v"(v)); return v; }
DI void glds(srd_t r, unsigned voff, unsigned soff, unsigned ldsaddr) {
  asm volatile("s_mov_b32 m0, %0\n\ts_nop 0\n\tbuffer_load_dwordx4 %1, %2, %3 offen lds" ::"s"(ldsaddr), "v"(voff), "s"(r), "s"(soff) : "memory");
}
DI void glds1(srd_t r, unsigned voff, unsigned soff, unsigned ldsaddr) {
  asm volatile("s_mov_b32 m0, %0\n\ts_nop 0\n\tbuffer_load_dwordx4 %1, %2, %3 offen sc1 lds" ::"s"(ldsaddr), "v"(voff), "s"(r), "s"(soff) : "memory");
}
DI void glds4(srd_t r, unsigned voff, unsigned soff, unsigned ldsaddr) {
  asm volatile("s_mov_b32 m0, %0\n\ts_nop 0\n\tbuffer_load_dwordx4 %1, %2, %3 offen lds\n\tbuffer_load_dwordx4 %1, %2, %3 offen offset:1024 lds\n\t"
               "buffer_load_dwordx4 %1, %2, %3 offen offset:2048 lds\n\tbuffer_load_dwordx4 %1, %2, %3 offen offset:3072 lds" ::"s"(ldsaddr), "v"(voff), "s"(r), "s"(soff) : "memory");
}
DI void glds4x1(srd_t r, unsigned voff, unsigned soff, unsigned ldsaddr) {
  asm volatile("s_mov_b32 m0, %0\n\ts_nop 0\n\tbuffer_load_dwordx4 %1, %2, %3 offen sc1 lds\n\tbuffer_load_dwordx4 %1, %2, %3 offen offset:1024 sc1 lds\n\t"
               "buffer_load_dwordx4 %1, %2, %3 offen offset:2048 sc1 lds\n\tbuffer_load_dwordx4 %1, %2, %3 offen offset:3072 sc1 lds" ::"s"(ldsaddr), "v"(voff), "s"(r), "s"(soff) : "memory");
}
DI void glds2x1(srd_t r, unsigned voff, unsigned soff, unsigned ldsaddr) {
  asm volatile("s_mov_b32 m0, %0\n\ts_nop 0\n\tbuffer_load_dwordx4 %1, %2, %3 offen sc1 lds\n\tbuffer_load_dwordx4 %1, %2, %3 offen offset:1024 sc1 lds" ::"s"(ldsaddr), "v"(voff), "s"(r), "s"(soff) : "memory");
}
DI v4u ld16(srd_t r, unsigned voff, unsigned soff) { return __builtin_amdgcn_raw_buffer_load_b128(r, voff, soff, 16); }
DI v2u ld8(srd_t r, unsigned voff, unsigned soff) { return __builtin_amdgcn_raw_buffer_load_b64(r, voff, soff, 16); }
DI v4u ldp16(srd_t r, unsigned voff, unsigned soff) { return __builtin_amdgcn_raw_buffer_load_b128(r, voff, soff, 0); }
DI void stp16(srd_t r, v4u v, unsigned off) { __builtin_amdgcn_raw_buffer_store_b128(v, r, off, 0, 0); }
DI void st16(srd_t r, v4u v, unsigned off) { __builtin_amdgcn_raw_buffer_store_b128(v, r, off, 0, 16); }
DI void st8(srd_t r, v2u v, unsigned off) { __builtin_amdgcn_raw_buffer_store_b64(v, r, off, 0, 16); }
DI float sum16(float v) { auto r = __builtin_amdgcn_permlane16_swap(__float_as_uint(v), __float_as_uint(v), false, false); return __uint_as_float(r[0]) + __uint_as_float(r[1]); }
DI float sum32(float v) { auto r = __builtin_amdgcn_permlane32_swap(__float_as_uint(v), __float_as_uint(v), false, false); return __uint_as_float(r[0]) + __uint_as_float(r[1]); }
DI float max32(float v) { auto r = __builtin_amdgcn_permlane32_swap(__float_as_uint(v), __float_as_uint(v), false, false); return fmaxf(__uint_as_float(r[0]), __uint_as_float(r[1])); }
DI float sumg(float v) { return sum32(sum16(v)); }
#define RLX_AGENT __ATOMIC_RELAXED, __HIP_MEMORY_SCOPE_AGENT
typedef unsigned long long u64;
DI void arrive(char* ws, unsigned off, u64 tag) { (void)__hip_atomic_exchange((GAS u64*)(ws + off), tag, RLX_AGENT); }
DI void wait_words(char* ws, unsigned off, int n, int lane, u64 tag) {
  GAS u64* p = (GAS u64*)(ws + off) + (lane < n ? lane : 0);
  for (unsigned spins = 0; spins < (1u << 17); ++spins)
    if (__all(__hip_atomic_load(p, RLX_AGENT) == tag)) break;
  CFENCE();
}
template <int C> DI float dpp(float v) { return __uint_as_float(__builtin_amdgcn_update_dpp(0u, __float_as_uint(v), C, 0xf, 0xf, false)); }
DI float rowsum16(float v) { v += dpp<0x128>(v); v += dpp<0x124>(v); v += dpp<0x122>(v); v += dpp<0x121>(v); return v; }
#define PIN(x) asm volatile("" : "+v"(x))
#define STAMP(k)
#define STAMP_FLUSH(o)
#define CSTAMP(k)
DI float silu(float v) { return v * __builtin_amdgcn_rcpf(1.f + __builtin_amdgcn_exp2f(-1.4426950408889634f * v)); }

DI v8s trfragA(unsigned img, int half, int lane) {
  const int g = lane >> 4, q = (lane & 15) >> 2, pp = lane & 3;
  const unsigned a = img + (4 * g + q) * 64 + ((half ^ (g & 1)) * 32) + pp * 8;
  return cat(trd(a), trd(a + 1024));
}

template <bool FULL>
DI void attention(int t, int hd, int kt0, int lane, f16v mU, f16v mL, f16v& O, srd_t rWS, unsigned wo_src, unsigned w1_src, int w, const u16* xres, v2u& xr0, v2u& xr1, unsigned& stampv) {
  const int qr = lane & 31, h = lane >> 5, blk = (lane >> 4) & 1, q = (lane & 15) >> 2, pp = lane & 3;
  const v8s Qf = lds16(L_AQ + (hd * 2 + h) * 2048 + (32 * t + qr) * 16);
  const unsigned kb = opq(L_AK + (hd * 2 + h) * KPL + (32 * t + qr) * 16);
  const unsigned vb = opq((blk == 0 ? L_AV + hd * VHD + (pp >> 1) * VPL + (pp & 1) * 8 + (4 * h + q) * 16 : L_ONE + ((hd * 128 + (h ? 0 : 128)) & 255)) + t * 512);
  const f16v zero = {};
#define RIDE(n) do { if ((n) < 2) glds4x1(rWS, lane * 16, wo_src + (w * 2 + (n)) * 4096, L_WO + (w * 2 + (n)) * 4096); \
                     else glds4x1(rWS, lane * 16, w1_src + w * 4096, L_W1 + w * 4096); } while (0)
  if (FULL) {
    v8s Kf[9];
#pragma unroll
    for (int kt = 0; kt < 9; ++kt) Kf[kt] = lds16(kb + kt * 512);
    v8s V0 = cat(trd(vb), trd(vb + 128)), V1 = cat(trd(vb + 256), trd(vb + 384));
    f16v S[2];
    float m0 = -3e38f, m1 = -3e38f;
    S[0] = MFMA32(Kf[0], Qf, mL);
    SB();
#pragma unroll
    for (int kt = 0; kt < 9; ++kt) {
      if (kt < 8) S[(kt + 1) & 1] = MFMA32(Kf[kt + 1], Qf, kt + 1 == 8 ? mU : zero);
      SB();
#pragma unroll
      for (int r = 0; r < 16; r += 4) { m0 = fmaxf(fmaxf(m0, S[kt & 1][r]), S[kt & 1][r + 1]); m1 = fmaxf(fmaxf(m1, S[kt & 1][r + 2]), S[kt & 1][r + 3]); }
      SB();
    }
    const float m = max32(fmaxf(m0, m1));
    STAMP(23);
    f16v nm;
#pragma unroll
    for (int r = 0; r < 16; ++r) { nm[r] = -m; mU[r] -= m; mL[r] -= m; }
    O = zero;
    S[0] = MFMA32(Kf[0], Qf, mL);
    SB();
#pragma unroll
    for (int kt = 0; kt < 9; ++kt) {
      if (kt < 8) S[(kt + 1) & 1] = MFMA32(Kf[kt + 1], Qf, kt + 1 == 8 ? mU : nm);
      if (kt == 0) RIDE(0);
      if (kt == 3) RIDE(1);
      if (kt == 6) RIDE(2);
      SB();
      v4u P0, P1;
#pragma unroll
      for (int r = 0; r < 4; ++r) P0[r] = pk(__builtin_amdgcn_exp2f(S[kt & 1][2 * r]), __builtin_amdgcn_exp2f(S[kt & 1][2 * r + 1]));
      O = MFMA32(V0, asv8(P0), O);
      if (kt < 8) V0 = cat(trd(vb + (kt + 1) * 512), trd(vb + (kt + 1) * 512 + 128));
      if (kt == 2) xr0 = *(const v2u*)xres;
      if (kt == 5) xr1 = *(const v2u*)(xres + 16);
      SB();
#pragma unroll
      for (int r = 0; r < 4; ++r) P1[r] = pk(__builtin_amdgcn_exp2f(S[kt & 1][8 + 2 * r]), __builtin_amdgcn_exp2f(S[kt & 1][8 + 2 * r + 1]));
      O = MFMA32(V1, asv8(P1), O);
      if (kt < 8) V1 = cat(trd(vb + (kt + 1) * 512 + 256), trd(vb + (kt + 1) * 512 + 384));
      SB();
    }
  } else {
#pragma unroll
    for (int n = 0; n < 3; ++n) RIDE(n);
    xr0 = *(const v2u*)xres; xr1 = *(const v2u*)(xres + 16);
    float m = -3e38f;
#pragma unroll
    for (int kt = 0; kt < 9; ++kt) {
      if (kt >= kt0) {
        const f16v S = MFMA32(lds16(kb + kt * 512), Qf, kt == 8 ? mU : zero);
#pragma unroll
        for (int r = 0; r < 16; r += 2) m = fmaxf(fmaxf(m, S[r]), S[r + 1]);
      }
    }
    m = max32(m);
    f16v nm;
#pragma unroll
    for (int r = 0; r < 16; ++r) { nm[r] = -m; mU[r] -= m; }
    O = zero;
#pragma unroll
    for (int kt = 0; kt < 9; ++kt) {
      if (kt >= kt0) {
        const f16v S = MFMA32(lds16(kb + kt * 512), Qf, kt == 8 ? mU : nm);
        v4u P0, P1;
#pragma unroll
        for (int r = 0; r < 4; ++r) {
          P0[r] = pk(__builtin_amdgcn_exp2f(S[2 * r]), __builtin_amdgcn_exp2f(S[2 * r + 1]));
          P1[r] = pk(__builtin_amdgcn_exp2f(S[8 + 2 * r]), __builtin_amdgcn_exp2f(S[8 + 2 * r + 1]));
        }
        const v8s V0 = cat(trd(vb + kt * 512), trd(vb + kt * 512 + 128));
        const v8s V1 = cat(trd(vb + kt * 512 + 256), trd(vb + kt * 512 + 384));
        O = MFMA32(V0, asv8(P0), O);
        O = MFMA32(V1, asv8(P1), O);
      }
    }
  }
#undef RIDE
}

__global__ void __launch_bounds__(512, 2) fused_block(Params p) {
  __shared__ __attribute__((aligned(16))) char smem[163840];
  LAS char* sm = (LAS char*)smem;
  const int tid = threadIdx.x, lane = tid & 63;
  const int w = __builtin_amdgcn_readfirstlane(tid >> 6);
  const int b = blockIdx.x, ci = ((b & 7) >> 1) * 4 + ((b >> 3) & 3), cj = (b & 1) * 8 + (b >> 5);
  const int tok = lane & 15, g = lane >> 4, q4 = (lane & 15) >> 2, pp = lane & 3;
  const int hf = cj & 1, ct = cj >> 1, par = cj & 1;
  const int wq = w < 4 ? w : w - 1;
  unsigned stampv = 0;
  const srd_t rWS = __builtin_amdgcn_make_buffer_rsrc(p.ws, 0, WS_BYTES, 0x00020000);
  const int rA = lane >> 2, pA = lane & 3;
  const unsigned colA = (((pA >> 1) ^ ((rA >> 2) & 1)) * 32) + (pA & 1) * 16;
  const int rB = lane >> 3, pB = lane & 7;
  const unsigned colB = (((pB >> 1) ^ ((rB >> 1) & 3)) * 32) + (pB & 1) * 16;
  STAMP(0);
  CSTAMP(40);

  const bool m2 = b < 160;
  const int msl = b % 40, mkq = b / 40;
  const srd_t rWmod = __builtin_amdgcn_make_buffer_rsrc((void*)p.wmod, 0, DM * 3072 * 2, 0x00020000);
  if (w < 4) {
    __builtin_amdgcn_s_setprio(2);
    const srd_t rX = __builtin_amdgcn_make_buffer_rsrc((void*)p.x, 0, SEQ * DM * 2, 0x00020000);
#pragma unroll
    for (int i0 = 0; i0 < 8; ++i0) { const int ii = (i0 + (ci & 3) * 2 + (cj & 1)) & 7; glds(rWmod, rA * 6144 + colA, (w * 8 + ii) * 16 * 6144 + (512 + 32 * cj) * 2, L_CA + (w * 8 + ii) * 1024); }
#pragma unroll
    for (int u = 0; u < 2; ++u) glds(rX, rA * 1024 + pA * 16, (128 * ci + 16 * (2 * w + u)) * 1024 + 64 * cj, L_XS + (2 * w + u) * 1024);
  }
  if (tid == 256) { *(LAS unsigned*)(uintptr_t)L_CNT = 0u; *(LAS unsigned*)(uintptr_t)(L_CNT + 4) = m2 ? 0u : 1u; *(LAS unsigned*)(uintptr_t)(L_CNT + 8) = 0u; }
  BAR();
#define READ_TAG() do { const v2u ep_ = ld8(rWS, WS_EPOCH, 0); TAG = (((u64)__builtin_amdgcn_readfirstlane(ep_[1]) << 32) | __builtin_amdgcn_readfirstlane(ep_[0])) + 1; } while (0)
  v8s rt[12];
  f16v mU, mL;
  u64 TAG;
  if (w < 4) {
    const v2u ep = ld8(rWS, WS_EPOCH, 0);
    const v4u cr = *(const v4u*)(p.c + 128 * w + 8 * (lane & 15));
    const unsigned sc = L_SC + w * 256;
    if (lane < 16) *(LAS v4u*)(uintptr_t)(sc + lane * 16) = v4u{pk(silu(bflo(cr[0])), silu(bfhi(cr[0]))), pk(silu(bflo(cr[1])), silu(bfhi(cr[1]))), pk(silu(bflo(cr[2])), silu(bfhi(cr[2]))), pk(silu(bflo(cr[3])), silu(bfhi(cr[3])))};
    LG0();
    v8s sf[4];
#pragma unroll
    for (int s = 0; s < 4; ++s) { const v2u lo = lds8(sc + (32 * s + 4 * g) * 2), hi = lds8(sc + (32 * s + 16 + 4 * g) * 2); sf[s] = asv8(v4u{lo[0], lo[1], hi[0], hi[1]}); }
    TAG = (((u64)__builtin_amdgcn_readfirstlane(ep[1]) << 32) | __builtin_amdgcn_readfirstlane(ep[0])) + 1;
    VM0();
    STAMP(1);
    v2u xa[2][2];
#pragma unroll
    for (int u = 0; u < 2; ++u) { const unsigned a = L_XS + (2 * w + u) * 1024 + tok * 64 + g * 8; xa[u][0] = lds8(a); xa[u][1] = lds8(a + 32); }
    f4 a0 = {0, 0, 0, 0}, a1 = {0, 0, 0, 0};
    {
      v8s W[4][2];
#pragma unroll
      for (int s = 0; s < 4; ++s) { W[s][0] = trfragA(L_CA + (4 * w + s) * 2048, 0, lane); W[s][1] = trfragA(L_CA + (4 * w + s) * 2048, 1, lane); }
#pragma unroll
      for (int s = 0; s < 4; ++s) { a0 = MFMA16(W[s][0], sf[s], a0); a1 = MFMA16(W[s][1], sf[s], a1); }
    }
    if (tok == 0) { *(LAS f4*)(uintptr_t)(L_CAP + (w * 32 + 4 * g) * 4) = a0; *(LAS f4*)(uintptr_t)(L_CAP + (w * 32 + 16 + 4 * g) * 4) = a1; }
    LG0();
    if (lane == 0) __hip_atomic_fetch_add((LAS unsigned*)(uintptr_t)L_CNT, 1u, __ATOMIC_RELAXED, __HIP_MEMORY_SCOPE_WORKGROUP);
    while (*(volatile LAS unsigned*)(uintptr_t)L_CNT < 4u) {}
    CFENCE();
    STAMP(19);
    a0 = ldsf4(L_CAP + 4 * g * 4); a1 = ldsf4(L_CAP + (16 + 4 * g) * 4);
#pragma unroll
    for (int e = 1; e < 4; ++e) { a0 += ldsf4(L_CAP + (e * 32 + 4 * g) * 4); a1 += ldsf4(L_CAP + (e * 32 + 16 + 4 * g) * 4); }
    float wv[8];
#pragma unroll
    for (int r = 0; r < 4; ++r) { wv[r] = rbf(1.f + rbf(a0[r])); wv[4 + r] = rbf(1.f + rbf(a1[r])); }
#pragma unroll
    for (int u = 0; u < 2; ++u) {
      const float x0 = bflo(xa[u][0][0]), x1 = bfhi(xa[u][0][0]), x2 = bflo(xa[u][0][1]), x3 = bfhi(xa[u][0][1]);
      const float x4 = bflo(xa[u][1][0]), x5 = bfhi(xa[u][1][0]), x6 = bflo(xa[u][1][1]), x7 = bfhi(xa[u][1][1]);
      const int T = 2 * w + u;
      st16(rWS, v4u{pk(x0 * wv[0], x1 * wv[1]), pk(x2 * wv[2], x3 * wv[3]), pk(x4 * wv[4], x5 * wv[5]), pk(x6 * wv[6], x7 * wv[7])},
           WS_HF + ((ci * 8 + T) * 16 + cj) * 1024 + lane * 16);
      if (u == 0 && w == 0 && tok == 0) {
        st8(rWS, v2u{pk(wv[0], wv[1]), pk(wv[2], wv[3])}, WS_WV + ci * 1024 + cj * 64 + g * 8);
        st8(rWS, v2u{pk(wv[4], wv[5]), pk(wv[6], wv[7])}, WS_WV + ci * 1024 + cj * 64 + 32 + g * 8);
      }
      const float s1 = sumg(((x0 + x1) + (x2 + x3)) + ((x4 + x5) + (x6 + x7)));
      const float s2 = sumg(((x0 * x0 + x1 * x1) + (x2 * x2 + x3 * x3)) + ((x4 * x4 + x5 * x5) + (x6 * x6 + x7 * x7)));
      if (g == 0) st8(rWS, v2u{__float_as_uint(s1), __float_as_uint(s2)}, WS_ST1 + ((ci * 16 + cj) * 128 + 16 * T + tok) * 8);
    }
    STAMP(21);
    VM0();
    if (lane == 0) { arrive(p.ws, WS_F0 + (ci * 64 + cj * 4 + w) * 8, TAG); __hip_atomic_fetch_add((LAS unsigned*)(uintptr_t)(L_CNT + 8), 1u, __ATOMIC_RELAXED, __HIP_MEMORY_SCOPE_WORKGROUP); }
    __builtin_amdgcn_s_setprio(0);
  } else if (w == 4) {
    if (m2) {
      const int mvec = msl >> 3, mcol = (mvec == 0 ? 0 : 512 + 512 * mvec) + (msl & 7) * 64;
      __builtin_amdgcn_s_sleep(8);
#pragma unroll
      for (int ii = 0; ii < 16; ++ii) glds(rWmod, rB * 6144 + colB, (128 * mkq + 8 * ii) * 6144 + mcol * 2, L_M2 + ii * 1024);
      const v4u cr = *(const v4u*)(p.c + 128 * mkq + 8 * (lane & 15));
      const unsigned sc = L_SC + 1024;
      if (lane < 16) *(LAS v4u*)(uintptr_t)(sc + lane * 16) = v4u{pk(silu(bflo(cr[0])), silu(bfhi(cr[0]))), pk(silu(bflo(cr[1])), silu(bfhi(cr[1]))), pk(silu(bflo(cr[2])), silu(bfhi(cr[2]))), pk(silu(bflo(cr[3])), silu(bfhi(cr[3])))};
      LG0();
      v8s sf[4];
#pragma unroll
      for (int s = 0; s < 4; ++s) { const v2u lo = lds8(sc + (32 * s + 4 * g) * 2), hi = lds8(sc + (32 * s + 16 + 4 * g) * 2); sf[s] = asv8(v4u{lo[0], lo[1], hi[0], hi[1]}); }
      VM0();
      f4 a[4];
#pragma unroll
      for (int nt = 0; nt < 4; ++nt) a[nt] = f4{0, 0, 0, 0};
      const int row = 4 * g + q4;
#pragma unroll
      for (int s = 0; s < 4; ++s) {
        v8s W[4];
#pragma unroll
        for (int nt = 0; nt < 4; ++nt) { const unsigned ad = L_M2 + s * 4096 + row * 128 + ((nt ^ ((row >> 1) & 3)) * 32) + pp * 8; W[nt] = cat(trd(ad), trd(ad + 2048)); }
#pragma unroll
        for (int nt = 0; nt < 4; ++nt) a[nt] = MFMA16(W[nt], sf[s], a[nt]);
      }
      if (tok == 0) {
#pragma unroll
        for (int nt = 0; nt < 4; ++nt)
          st16(rWS, v4u{__float_as_uint(a[nt][0]), __float_as_uint(a[nt][1]), __float_as_uint(a[nt][2]), __float_as_uint(a[nt][3])},
               WS_MOD + mkq * 10240 + mvec * 2048 + ((msl & 7) * 2 + (nt >> 1)) * 128 + g * 32 + (nt & 1) * 16);
      }
      LG0();
      if (lane == 0) *(volatile LAS unsigned*)(uintptr_t)(L_CNT + 4) = 1u;
      READ_TAG();
      VM0();
      if (lane == 0) arrive(p.ws, WS_FM + b * 8, TAG);
    } else READ_TAG();
    STAMP(1);
  } else {
    if (w == 5) {
      const srd_t r = __builtin_amdgcn_make_buffer_rsrc((void*)p.wqkv, 0, DM * 1536 * 2, 0x00020000);
#pragma unroll
      for (int e = 0; e < 6; ++e) glds(r, rA * 3072 + colA, (32 * ci + 16 * (e & 1)) * 3072 + ((e >> 1) * 512 + 32 * cj) * 2, L_SQ + e * 1024);
      READ_TAG();
      VM0();
#pragma unroll
      for (int nt = 0; nt < 6; ++nt) st16(rWS, asv4u(trfragA(L_SQ + (nt >> 1) * 2048, nt & 1, lane)), WS_TQ + ((cj * 6 + nt) * 16 + ci) * 1024 + lane * 16);
      VM0();
      if (lane == 0) arrive(p.ws, WS_FQ + (cj * 16 + ci) * 8, TAG);
    } else {
      READ_TAG();
      __builtin_amdgcn_s_sleep(16);
      if (w == 6) {
        const srd_t r1 = __builtin_amdgcn_make_buffer_rsrc((void*)p.w1, 0, DM * FF * 2, 0x00020000);
#pragma unroll
        for (int e = 0; e < 6; ++e) glds(r1, rA * (FF * 2) + colA, (32 * ci + 16 * (e & 1)) * (FF * 2) + (112 * cj + 16 * par + (e >> 1) * 32) * 2, L_S1 + e * 1024);
        glds(r1, (lane >> 1) * (FF * 2) + (lane & 1) * 16, 32 * ci * (FF * 2) + (112 * cj + (par ? 0 : 96)) * 2, L_S1 + 6144);
        VM0();
#pragma unroll
        for (int nt = 0; nt < 6; ++nt) rt[nt] = trfragA(L_S1 + (nt >> 1) * 2048, nt & 1, lane);
        { const unsigned a = L_S1 + 6144 + (4 * g + q4) * 32 + pp * 8; rt[6] = cat(trd(a), trd(a + 512)); }
      } else {
        const srd_t ro = __builtin_amdgcn_make_buffer_rsrc((void*)p.wo, 0, DM * DM * 2, 0x00020000);
#pragma unroll
        for (int e = 0; e < 4; ++e) glds(ro, rB * 1024 + colB, (32 * ((b & 127) >> 3) + 8 * e) * 1024 + (b & 7) * 128, L_SO + e * 1024);
        const srd_t r2 = __builtin_amdgcn_make_buffer_rsrc((void*)p.w2, 0, FF * DM * 2, 0x00020000);
        const int b2 = b < 224 ? b : 0;
#pragma unroll
        for (int e = 0; e < 8; ++e) { const int id = 2 * b2 + (e >> 2); glds(r2, rB * 1024 + colB, (32 * (id >> 3) + 8 * (e & 3)) * 1024 + (id & 7) * 128, L_SO + 4096 + e * 1024); }
        VM0();
        {
          const int row = 16 * (g >> 1) + 4 * (g & 1) + q4;
#pragma unroll
          for (int nt = 0; nt < 4; ++nt) { const unsigned a = L_SO + row * 128 + ((nt ^ ((row >> 1) & 3)) * 32) + pp * 8; rt[8 + nt] = cat(trd(a), trd(a + 1024)); }
        }
        const int row = 4 * g + q4;
#pragma unroll
        for (int e = 0; e < 8; ++e) { const int nt = e & 3; const unsigned a = L_SO + 4096 + (e >> 2) * 4096 + row * 128 + ((nt ^ ((row >> 1) & 3)) * 32) + pp * 8; rt[e] = cat(trd(a), trd(a + 2048)); }
      }
    }
    STAMP(1);
    if (w >= 6) {
      while (*(volatile LAS unsigned*)(uintptr_t)(L_CNT + 8) < 4u) __builtin_amdgcn_s_sleep(1);
      CFENCE();
      if (w == 6) {
#pragma unroll
        for (int e = 0; e < 7; ++e) st16(rWS, asv4u(rt[e]), WS_TW1 + ((cj * 7 + e) * 16 + ci) * 1024 + lane * 16);
      } else {
        if (b < 224) {
#pragma unroll
          for (int e = 0; e < 8; ++e) { const int id = 2 * b + (e >> 2); st16(rWS, asv4u(rt[e]), WS_TW2 + (((id & 7) * 56 + (id >> 3)) * 4 + (e & 3)) * 1024 + lane * 16); }
        }
        if (b < 128) {
#pragma unroll
          for (int e = 0; e < 4; ++e) st16(rWS, asv4u(rt[8 + e]), WS_TWO + (((b & 7) * 16 + (b >> 3)) * 4 + e) * 1024 + lane * 16);
        }
      }
    }
    wait_words(p.ws, WS_FQ + cj * 128, 16, lane, TAG);
    while (*(volatile LAS unsigned*)(uintptr_t)(L_CNT + 4) == 0u) __builtin_amdgcn_s_sleep(1);
    CFENCE();
    STAMP(19);
#pragma unroll
    for (int e = 0; e < 8; ++e) glds4x1(rWS, lane * 16, WS_TQ + cj * 98304 + ((w - 5) * 8 + ((e + 2 * (ci & 3)) & 7)) * 4096, L_WQKV + ((w - 5) * 8 + ((e + 2 * (ci & 3)) & 7)) * 4096);
  }
  {
    const int qr = lane & 31, h = lane >> 5;
#pragma unroll
    for (int r = 0; r < 16; ++r) { const int kr = (r & 3) + 8 * (r >> 2) + 4 * h; mU[r] = kr > qr ? -1e30f : 0.f; mL[r] = kr > qr ? 0.f : -1e30f; }
    asm volatile("" : "+v"(mU), "+v"(mL));
  }
  if (w == 0) wait_words(p.ws, WS_F0 + ci * 512, 64, lane, TAG);
  STAMP(2);
  if (w >= 5) VM0();
  BAR();
  STAMP(3);
  if (lane == 0) {
    if (w == 6) arrive(p.ws, WS_FW1 + (cj * 16 + ci) * 8, TAG);
    if (w == 7 && b < 128) arrive(p.ws, WS_FWO + ((b & 7) * 16 + (b >> 3)) * 8, TAG);
    if (w == 7 && b < 224) { arrive(p.ws, WS_FW2 + (((2 * b) & 7) * 64 + ((2 * b) >> 3)) * 8, TAG); arrive(p.ws, WS_FW2 + (((2 * b + 1) & 7) * 64 + ((2 * b + 1) >> 3)) * 8, TAG); }
  }

  f4 accQ[2], accKV[4];
  float rstd, nmu;
  v4u gaR, cbR, sbR, gbR;
  v2u b2R[2];
  v4u mc0, md0;
  v4u mx[2];
  const int tt = w & 3, kh = w >> 2;
  const int ocol = 64 * ct + 32 * kh + 4 * g;
  {
    const unsigned bq = opq(L_WQKV + lane * 16), bv = opq(L_WQKV + 65536 + lane * 16);
#define QF(nt, s) lds16(((nt) < 4 ? bq : bv) + ((nt) & 3) * 16384 + (s) * 1024)
    v8s act[16];
    const unsigned hsrc = WS_HF + (ci * 8 + w) * 16 * 1024;
    v2u wl[2], wh[2];
#pragma unroll
    for (int u = 0; u < 2; ++u) { const unsigned o = (32 * (2 * w + u) + 4 * g) * 2; wl[u] = ld8(rWS, o, WS_WV + ci * 1024); wh[u] = ld8(rWS, o + 32, WS_WV + ci * 1024); }
#pragma unroll
    for (int s = 0; s < 4; ++s) act[s] = asv8(ld16(rWS, lane * 16, hsrc + s * 1024));
    {
      f4 au[6];
      v8s W[2][6];
      const unsigned c0 = opq(bq + w * 2048), d0 = opq(bv + w * 2048);
#pragma unroll
      for (int u = 0; u < 2; ++u)
#pragma unroll
        for (int nt = 0; nt < 6; ++nt) W[u][nt] = lds16((nt < 4 ? c0 : d0) + (nt & 3) * 16384 + u * 1024);
#pragma unroll
      for (int nt = 0; nt < 6; ++nt) au[nt] = f4{0, 0, 0, 0};
#pragma unroll
      for (int u = 0; u < 2; ++u) {
        const v8s a = asv8(v4u{wl[u][0], wl[u][1], wh[u][0], wh[u][1]});
#pragma unroll
        for (int nt = 0; nt < 6; ++nt) au[nt] = MFMA16(W[u][nt], a, au[nt]);
      }
      if (tok == 0) {
#pragma unroll
        for (int nt = 0; nt < 6; ++nt) *(LAS f4*)(sm + L_PARTU + (w * 96 + nt * 16 + 4 * g) * 4) = au[nt];
      }
    }
    BAR();
    if (tid >= 384 && tid < 480) {
      const int e0 = tid - 384;
      float s = 0;
#pragma unroll
      for (int e = 0; e < 8; ++e) s += *(const LAS float*)(sm + L_PARTU + (e * 96 + e0) * 4);
      *(LAS float*)(sm + L_PERS + e0 * 4) = s;
    }
    STAMP(16);
    v2u st[4];
    u64 fm0 = 0, fm1 = 0, fm2 = 0;
#pragma unroll
    for (int nt = 0; nt < 4; ++nt) accKV[nt] = f4{0, 0, 0, 0};
#pragma unroll
    for (int nt = 0; nt < 2; ++nt) accQ[nt] = f4{0, 0, 0, 0};
    {
      v8s W[6];
#pragma unroll
      for (int nt = 0; nt < 6; ++nt) W[nt] = QF(nt, 0);
      SB();
#pragma unroll
      for (int s = 0; s < 16; ++s) {
        if (s == 0) act[4] = asv8(ld16(rWS, lane * 16, hsrc + 4 * 1024));
        if (s + 5 < 16) act[s + 5] = asv8(ld16(rWS, lane * 16, hsrc + (s + 5) * 1024));
        else if (s < 15) st[s - 11] = ld8(rWS, ((4 * g + s - 11) * 128 + 16 * w + tok) * 8, WS_ST1 + ci * 16 * 128 * 8);
        if (s == 12) { GAS u64* f = (GAS u64*)(p.ws + WS_FM); fm0 = __hip_atomic_load(f + lane, RLX_AGENT); fm1 = __hip_atomic_load(f + 64 + lane, RLX_AGENT); fm2 = __hip_atomic_load(f + 128 + (lane & 31), RLX_AGENT); }
#pragma unroll
        for (int nt = 0; nt < 6; ++nt) {
          if (nt < 2) accQ[nt] = MFMA16(W[nt], act[s], accQ[nt]); else accKV[nt - 2] = MFMA16(W[nt], act[s], accKV[nt - 2]);
          if (s + 1 < 16) W[nt] = QF(nt, s + 1);
          SB();
        }
      }
    }
    float s1 = 0, s2 = 0;
#pragma unroll
    for (int e = 0; e < 4; ++e) { s1 += __uint_as_float(st[e][0]); s2 += __uint_as_float(st[e][1]); }
    s1 = sumg(s1); s2 = sumg(s2);
    const float mu = s1 * (1.f / 512), var = fmaxf(s2 * (1.f / 512) - mu * mu, 0.f);
    rstd = __builtin_amdgcn_rsqf(var + 1e-5f); nmu = -rstd * mu;
    if (w == 2 && lane < 8) *(LAS v4u*)(sm + L_B2C + lane * 16) = *(const v4u*)(p.b2 + 64 * ct + lane * 8);
    PIN(rstd); PIN(nmu);
    {
          GAS u64* f = (GAS u64*)(p.ws + WS_FM);
          for (unsigned spins = 0; spins < (1u << 17) && !__all(fm0 == TAG && fm1 == TAG && fm2 == TAG); ++spins) {
            fm0 = __hip_atomic_load(f + lane, RLX_AGENT); fm1 = __hip_atomic_load(f + 64 + lane, RLX_AGENT); fm2 = __hip_atomic_load(f + 128 + (lane & 31), RLX_AGENT); }
          CFENCE();
          if (w < 6) {
            const int vec = w < 2 ? 0 : w < 4 ? 2 : 3, hq = w & 1;
            const unsigned dst = w == 0 ? L_PSA : w == 1 ? L_PSA2 : w == 2 ? L_PSB : w == 3 ? L_PSB2 : w == 4 ? L_PCB : L_PCB2;
            glds2x1(rWS, lane * 16, WS_MOD + (2 * hq) * 10240 + vec * 2048, dst);
            glds2x1(rWS, lane * 16, WS_MOD + (2 * hq + 1) * 10240 + vec * 2048, dst + 2048);
          } else glds1(rWS, (lane >> 4) * 10240 + (lane & 15) * 16, WS_MOD + (w == 6 ? 1 : 4) * 2048 + ct * 256, w == 6 ? L_PGA : L_PGB);
    }
    STAMP(4);
    BAR();
    {
      v4u kk, vv;
      unsigned pkd[4][2];
#pragma unroll
      for (int nt = 0; nt < 4; ++nt) {
        const f4 u = ldsf4(L_PERS + (32 + nt * 16 + 4 * g) * 4);
        pkd[nt][0] = pk(rstd * accKV[nt][0] + nmu * u[0], rstd * accKV[nt][1] + nmu * u[1]);
        pkd[nt][1] = pk(rstd * accKV[nt][2] + nmu * u[2], rstd * accKV[nt][3] + nmu * u[3]);
      }
      { auto r0 = __builtin_amdgcn_permlane16_swap(pkd[0][0], pkd[1][0], false, false); auto r1 = __builtin_amdgcn_permlane16_swap(pkd[0][1], pkd[1][1], false, false);
        kk = v4u{r0[0], r1[0], r0[1], r1[1]}; }
      { auto r0 = __builtin_amdgcn_permlane16_swap(pkd[2][0], pkd[3][0], false, false); auto r1 = __builtin_amdgcn_permlane16_swap(pkd[2][1], pkd[3][1], false, false);
        vv = v4u{r0[0], r1[0], r0[1], r1[1]}; }
      const int hd = g & 1, pl = g >> 1;
      st16(rWS, kk, WS_KV + (ci * 16 + cj) * 16384 + ((0 * 2 + hd) * 2 + pl) * 2048 + (16 * w + tok) * 16);
      st16(rWS, vv, WS_KV + (ci * 16 + cj) * 16384 + ((1 * 2 + hd) * 2 + pl) * 2048 + (16 * w + tok) * 16);
      *(LAS v4u*)(sm + L_AK + (hd * 2 + pl) * KPL + (256 + 16 * w + tok) * 16) = kk;
      *(LAS v4u*)(sm + L_AV + hd * VHD + pl * VPL + (256 + 16 * w + tok) * 16) = vv;
    }
    VM0();
    BAR();
    if (tid == 256) arrive(p.ws, WS_FP + (0 * 16 + ci) * 128 + cj * 8, TAG);
    STAMP(5);
    if (tid < 400) *(LAS v4u*)(sm + L_ONE + tid * 16) = v4u{0x3f803f80u, 0x3f803f80u, 0x3f803f80u, 0x3f803f80u};
    if (tid < 416) {
      const int seg = tid < 384 ? tid >> 7 : 3 + ((tid - 384) >> 4), e = tid < 384 ? tid & 127 : (tid - 384) & 15;
      f4 v;
      if (seg < 3) {
        const unsigned a0 = (seg == 0 ? L_PSA : seg == 1 ? L_PSB : L_PCB) + e * 16, a1 = (seg == 0 ? L_PSA2 : seg == 1 ? L_PSB2 : L_PCB2) + e * 16;
        v = (ldsf4(a0) + ldsf4(a0 + 2048)) + (ldsf4(a1) + ldsf4(a1 + 2048));
      } else {
        const unsigned a0 = (seg == 3 ? L_PGA : L_PGB) + e * 16;
        v = (ldsf4(a0) + ldsf4(a0 + 256)) + (ldsf4(a0 + 512) + ldsf4(a0 + 768));
      }
      *(LAS v2u*)(sm + L_FIN + (seg < 3 ? seg * 1024 : 3072 + (seg - 3) * 128) + e * 8) = v2u{pk(v[0], v[1]), pk(v[2], v[3])};
    }
    BAR();
    {
      f4 av[4];
      v8s W[2][4], a[2];
      const unsigned c0 = opq(bq + w * 2048), d0 = opq(bv + w * 2048);
#pragma unroll
      for (int u = 0; u < 2; ++u) {
        a[u] = lds16(L_FIN + (2 * w + u) * 64 + g * 16);
#pragma unroll
        for (int nt = 0; nt < 4; ++nt) W[u][nt] = lds16((nt < 2 ? c0 : d0) + (nt & 1) * 16384 + u * 1024);
      }
#pragma unroll
      for (int nt = 0; nt < 4; ++nt) av[nt] = f4{0, 0, 0, 0};
#pragma unroll
      for (int u = 0; u < 2; ++u)
#pragma unroll
        for (int nt = 0; nt < 4; ++nt) av[nt] = MFMA16(W[u][nt], a[u], av[nt]);
      if (tok == 0) {
#pragma unroll
        for (int nt = 0; nt < 4; ++nt) *(LAS f4*)(sm + L_PARTV + (w * 64 + nt * 16 + 4 * g) * 4) = av[nt];
      }
    }
    {
      gaR = asv4u(lds16(L_FIN + 3072 + kh * 64 + g * 16)); gbR = asv4u(lds16(L_FIN + 3200 + kh * 64 + g * 16));
      sbR = asv4u(lds16(L_FIN + 1024 + (2 * ct + kh) * 64 + g * 16)); cbR = asv4u(lds16(L_FIN + 2048 + (2 * ct + kh) * 64 + g * 16));
#pragma unroll
      for (int e = 0; e < 2; ++e) b2R[e] = lds8(L_B2C + (32 * kh + 4 * g + 16 * e) * 2);
      mc0 = asv4u(lds16(L_FIN + 2048 + 16 * lane)); md0 = asv4u(lds16(L_FIN + 1024 + 16 * lane));
#pragma unroll
      for (int u = 0; u < 2; ++u) mx[u] = asv4u(lds16(L_FIN + (tok == 1 ? 1024 : 2048) + (2 * w + u) * 64 + g * 16));
      BAR();
      if (tid < 64) {
        float s = 0;
#pragma unroll
        for (int e = 0; e < 8; ++e) s += *(const LAS float*)(sm + L_PARTV + (e * 64 + tid) * 4);
        *(LAS float*)(sm + L_PERS + (96 + tid) * 4) = s;
      }
    }
    if (ci < 2) {
      for (int e = tid; e < 8 * 256; e += 512) {
        const int pln = e >> 8, key = e & 255;
        if (ci == 0 || key < 128) {
          const unsigned a = pln < 4 ? L_AK + pln * KPL : L_AV + ((pln - 4) >> 1) * VHD + (pln & 1) * VPL;
          *(LAS v4u*)(sm + a + key * 16) = v4u{0, 0, 0, 0};
        }
      }
    }
    if (w == 4) {
      GAS u64* f0 = (GAS u64*)(p.ws + (lane == 0 && ci >= 1 ? WS_FP + (ci - 1) * 128 + cj * 8 : lane == 1 && ci >= 2 ? WS_FP + (ci - 2) * 128 + cj * 8
                                       : lane >= 16 && lane < 32 ? WS_FW1 + (cj * 16 + lane - 16) * 8 : WS_FWO + (ct * 16 + (lane & 15)) * 8));
      GAS u64* f1 = (GAS u64*)(p.ws + WS_FW2 + ct * 512) + (lane < 56 ? lane : 0);
      for (unsigned spins = 0; spins < (1u << 17); ++spins) {
        const u64 a = __hip_atomic_load(f0, RLX_AGENT), c = __hip_atomic_load(f1, RLX_AGENT);
        if (__all(a == TAG && c == TAG)) break;
      }
      CFENCE();
    }
    BAR();
    STAMP(6);
    {
      const unsigned dst = w < 4 ? L_AK + w * KPL : L_AV + ((w - 4) >> 1) * VHD + (w & 1) * VPL;
#pragma unroll
      for (int src = 0; src < 2; ++src)
        if (ci - 2 + src >= 0) glds2x1(rWS, lane * 16, WS_KV + ((ci - 2 + src) * 16 + cj) * 16384 + w * 2048, dst + src * 2048);
    }
    {
      const float qs = 0.25f * 1.4426950408889634f;
      unsigned pq[2][2];
#pragma unroll
      for (int nt = 0; nt < 2; ++nt) {
        const f4 u = ldsf4(L_PERS + (nt * 16 + 4 * g) * 4), v = ldsf4(L_PERS + (96 + nt * 16 + 4 * g) * 4);
        pq[nt][0] = pk(qs * (rstd * accQ[nt][0] + nmu * u[0] + v[0]), qs * (rstd * accQ[nt][1] + nmu * u[1] + v[1]));
        pq[nt][1] = pk(qs * (rstd * accQ[nt][2] + nmu * u[2] + v[2]), qs * (rstd * accQ[nt][3] + nmu * u[3] + v[3]));
      }
      auto r0 = __builtin_amdgcn_permlane16_swap(pq[0][0], pq[1][0], false, false); auto r1 = __builtin_amdgcn_permlane16_swap(pq[0][1], pq[1][1], false, false);
      *(LAS v4u*)(sm + L_AQ + ((g & 1) * 2 + (g >> 1)) * 2048 + (16 * w + tok) * 16) = v4u{r0[0], r1[0], r0[1], r1[1]};
    }
    VM0();
    BAR();
    STAMP(7);
  }

  const int orow = 128 * ci + 64 * hf + 16 * tt + tok;
  v2u xr0, xr1;
  {
    const int t = w & 3, hd = w >> 2;
    const int kt0 = ci >= 2 ? 0 : 8 - 4 * ci - t;
    f16v O;
    const u16* xres = p.x + (size_t)orow * DM + ocol;
    if (ci >= 2) attention<true>(t, hd, 0, lane, mU, mL, O, rWS, WS_TWO + ct * 65536, WS_TW1 + cj * 7 * 16384, w, xres, xr0, xr1, stampv);
    else attention<false>(t, hd, kt0, lane, mU, mL, O, rWS, WS_TWO + ct * 65536, WS_TW1 + cj * 7 * 16384, w, xres, xr0, xr1, stampv);
    const int h = lane >> 5;
    const float inv = __builtin_amdgcn_rcpf(O[8]);
    const f4 b0 = ldsf4(L_PERS + (96 + 32 + hd * 16 + 4 * h) * 4), b1 = ldsf4(L_PERS + (96 + 32 + hd * 16 + 8 + 4 * h) * 4);
    const v4u o = {pk(O[0] * inv + b0[0], O[1] * inv + b0[1]), pk(O[2] * inv + b0[2], O[3] * inv + b0[3]),
                   pk(O[4] * inv + b1[0], O[5] * inv + b1[1]), pk(O[6] * inv + b1[2], O[7] * inv + b1[3])};
    const int qr = lane & 31;
    st16(rWS, o, WS_OF + (ci * 8 * 16 + cj) * 1024 + ((2 * t + (qr >> 4)) * 16) * 1024 + ((2 * hd + h) * 16 + (qr & 15)) * 16);
  }
  STAMP(24);
  VM0();
  BAR();
  if (tid == 256) arrive(p.ws, WS_FP + (1 * 16 + ci) * 128 + cj * 8, TAG);
  STAMP(8);
  float b1v = 0.f;
  v8s aux2[2];
  float Cw = 0, Cw2 = 0, Cws = 0, Cs = 0, Cs2 = 0;
#define MODSUMS() do { \
      _Pragma("unroll") for (int r = 0; r < 4; ++r) { \
        const float w0 = rbf(1.f + bflo(mc0[r])), w1 = rbf(1.f + bfhi(mc0[r])), s0 = bflo(md0[r]), s1_ = bfhi(md0[r]); \
        Cw += w0 + w1; Cw2 += w0 * w0 + w1 * w1; Cws += w0 * s0 + w1 * s1_; Cs += s0 + s1_; Cs2 += s0 * s0 + s1_ * s1_; } \
      Cw = sumg(rowsum16(Cw)); Cw2 = sumg(rowsum16(Cw2)); Cws = sumg(rowsum16(Cws)); Cs = sumg(rowsum16(Cs)); Cs2 = sumg(rowsum16(Cs2)); \
      _Pragma("unroll") for (int u = 0; u < 2; ++u) { v4u t; \
        _Pragma("unroll") for (int d = 0; d < 4; ++d) t[d] = tok == 0 ? pk(1.f + bflo(mx[u][d]), 1.f + bfhi(mx[u][d])) : (tok == 1 ? mx[u][d] : 0x3f803f80u); \
        aux2[u] = asv8(t); } \
      asm volatile("" : "+v"(Cw), "+v"(Cw2), "+v"(Cws), "+v"(Cs), "+v"(Cs2), "+v"(aux2[0]), "+v"(aux2[1])); } while (0)
  if (w != 4) {
#pragma unroll
    for (int e = 0; e < 3; ++e) { const int gi = wq + 7 * e; if (gi < 16) glds4x1(rWS, lane * 16, WS_TW1 + (cj * 7 + 2) * 16384 + gi * 4096, L_W1 + 32768 + gi * 4096); }
  }
  MODSUMS();
  if (w == 4) wait_words(p.ws, WS_FP + (1 * 16 + ci) * 128, 16, lane, TAG);
  BAR();
  STAMP(9);

  float x1[8];
  {
    v8s oa[8];
    const unsigned osrc = WS_OF + ((ci * 8 + 4 * hf + tt) * 16 + 8 * kh) * 1024;
#pragma unroll
    for (int s = 0; s < 3; ++s) oa[s] = asv8(ld16(rWS, lane * 16, osrc + s * 1024));
    const unsigned bo = opq(L_WO + kh * 32768 + lane * 16);
    f4 acc[4];
#pragma unroll
    for (int nt = 0; nt < 4; ++nt) acc[nt] = f4{0, 0, 0, 0};
    {
      v8s W[2][4];
#pragma unroll
      for (int s = 0; s < 2; ++s)
#pragma unroll
        for (int nt = 0; nt < 4; ++nt) W[s][nt] = lds16(bo + (s * 4 + nt) * 1024);
      SB();
#pragma unroll
      for (int s = 3; s < 8; ++s) oa[s] = asv8(ld16(rWS, lane * 16, osrc + s * 1024));
      if (tid < 112) { const int nt = tid >> 4; b1v = bflo(p.b1[(nt == 6 ? 112 * cj + (par ? 0 : 96) : 112 * cj + 16 * par + nt * 16) + (tid & 15)]); }
      SB();
#pragma unroll
      for (int s = 0; s < 8; ++s)
#pragma unroll
        for (int nt = 0; nt < 4; ++nt) {
          acc[nt] = MFMA16(W[s & 1][nt], oa[s], acc[nt]);
          if (s + 2 < 8) W[s & 1][nt] = lds16(bo + ((s + 2) * 4 + nt) * 1024);
          SB();
        }
    }
    STAMP(26);
    VM0();
    BAR();
    *(LAS f4*)(sm + L_XCH + w * 2048 + lane * 16) = kh ? acc[0] : acc[2];
    *(LAS f4*)(sm + L_XCH + w * 2048 + 1024 + lane * 16) = kh ? acc[1] : acc[3];
    BAR();
    const f4 o0 = ldsf4(L_XCH + (w ^ 4) * 2048 + lane * 16), o1 = ldsf4(L_XCH + (w ^ 4) * 2048 + 1024 + lane * 16);
    const f4 t0 = (kh ? acc[2] : acc[0]) + o0, t1 = (kh ? acc[3] : acc[1]) + o1;
    const float xin[8] = {bflo(xr0[0]), bfhi(xr0[0]), bflo(xr0[1]), bfhi(xr0[1]), bflo(xr1[0]), bfhi(xr1[0]), bflo(xr1[1]), bfhi(xr1[1])};
    const float ga[8] = {bflo(gaR[0]), bfhi(gaR[0]), bflo(gaR[1]), bfhi(gaR[1]), bflo(gaR[2]), bfhi(gaR[2]), bflo(gaR[3]), bfhi(gaR[3])};
    const float cb[8] = {bflo(cbR[0]), bfhi(cbR[0]), bflo(cbR[1]), bfhi(cbR[1]), bflo(cbR[2]), bfhi(cbR[2]), bflo(cbR[3]), bfhi(cbR[3])};
    const float sv[8] = {bflo(sbR[0]), bfhi(sbR[0]), bflo(sbR[1]), bfhi(sbR[1]), bflo(sbR[2]), bfhi(sbR[2]), bflo(sbR[3]), bfhi(sbR[3])};
    float wv[8], xw[8];
#pragma unroll
    for (int r = 0; r < 8; ++r) { x1[r] = rbf(xin[r] + ga[r] * (r < 4 ? t0[r & 3] : t1[r & 3])); wv[r] = rbf(1.f + cb[r]); xw[r] = x1[r] * wv[r]; }
    st16(rWS, v4u{pk(xw[0], xw[1]), pk(xw[2], xw[3]), pk(xw[4], xw[5]), pk(xw[6], xw[7])}, WS_X1F + ((ci * 8 + 4 * hf + tt) * 16 + 2 * ct + kh) * 1024 + lane * 16);
    SB();
    float s1 = 0, s2 = 0, s3 = 0, s4 = 0, s5 = 0, s6 = 0;
#pragma unroll
    for (int r = 0; r < 8; ++r) { s1 += x1[r]; s2 += x1[r] * x1[r]; s3 += xw[r]; s4 += xw[r] * wv[r]; s5 += xw[r] * xw[r]; s6 += xw[r] * sv[r]; }
    s1 = sumg(s1); s2 = sumg(s2); s3 = sumg(s3); s4 = sumg(s4); s5 = sumg(s5); s6 = sumg(s6);
    const unsigned rec = (ci * 16 + 2 * ct + kh) * 128 + 64 * hf + 16 * tt + tok;
    if (g == 0) st16(rWS, v4u{__float_as_uint(s1), __float_as_uint(s2), __float_as_uint(s3), __float_as_uint(s4)}, WS_ST2A + rec * 16);
    if (g == 1) st8(rWS, v2u{__float_as_uint(s5), __float_as_uint(s6)}, WS_ST2B + rec * 8);
  }
  if (w < 4) {
    glds4x1(rWS, lane * 16, WS_TW1 + (cj * 7 + 6) * 16384 + w * 4096, L_W1S + w * 4096);
    glds4x1(rWS, lane * 16, WS_TW2 + ct * 229376 + 7 * 16384 + w * 4096, 49152 + w * 4096);
    asm volatile("s_waitcnt vmcnt(8)" ::: "memory");
  } else {
    glds4x1(rWS, lane * 16, WS_TW2 + ct * 229376 + (w - 4) * 4096, 32768 + (w - 4) * 4096);
    asm volatile("s_waitcnt vmcnt(4)" ::: "memory");
  }
  BAR();
  if (tid == 256) arrive(p.ws, WS_FP + (2 * 16 + ci) * 128 + cj * 8, TAG);
  STAMP(10);
  if (w == 4) asm volatile("s_waitcnt vmcnt(1)" ::: "memory"); else VM0();
  BAR();

  v4u stg[8];
  {
    const unsigned bP = opq(L_W1 + lane * 16), bR = opq(L_W1 + 65536 + lane * 16), bS = opq(L_W1S + lane * 16);
#define W1F(nt, s) lds16((nt) == 6 ? bS + (s) * 1024 : (nt) < 4 ? bP + (nt) * 16384 + (s) * 1024 : bR + ((nt) - 4) * 16384 + (s) * 1024)
    {
      f4 au[7];
#pragma unroll
      for (int nt = 0; nt < 7; ++nt) au[nt] = f4{0, 0, 0, 0};
      const unsigned c0 = opq(bP + w * 2048), d0 = opq(bR + w * 2048), e0 = opq(bS + w * 2048);
#pragma unroll
      for (int u = 0; u < 2; ++u) {
        v8s W[7];
#pragma unroll
        for (int nt = 0; nt < 7; ++nt) W[nt] = lds16(nt == 6 ? e0 + u * 1024 : nt < 4 ? c0 + nt * 16384 + u * 1024 : d0 + (nt - 4) * 16384 + u * 1024);
#pragma unroll
        for (int nt = 0; nt < 7; ++nt) au[nt] = MFMA16(W[nt], aux2[u], au[nt]);
      }
      if (tok < 3) {
#pragma unroll
        for (int nt = 0; nt < 7; ++nt) *(LAS f4*)(sm + L_XCH + ((w * 3 + tok) * 112 + nt * 16 + 4 * g) * 4) = au[nt];
      }
    }
    BAR();
    if (tid < 112) *(LAS float*)(sm + L_XF + (336 + tid) * 4) = b1v;
    {
      const int e0 = w < 4 ? tid : tid - 64;
      if (w != 4 && e0 < 336) {
        float s = 0;
#pragma unroll
        for (int e = 0; e < 8; ++e) s += *(const LAS float*)(sm + L_XCH + (e * 336 + e0) * 4);
        *(LAS float*)(sm + L_XF + e0 * 4) = s;
      }
    }
    if (w == 4) wait_words(p.ws, WS_FP + (2 * 16 + ci) * 128, 16, lane, TAG);
    BAR();
    STAMP(11);
    v8s act2[16];
    const unsigned asrc = WS_X1F + (ci * 8 + w) * 16 * 1024;
#pragma unroll
    for (int s = 0; s < 4; ++s) act2[s] = asv8(ld16(rWS, lane * 16, asrc + s * 1024));
    v4u sa[4]; v2u sb[4];
    f4 acc[7];
#pragma unroll
    for (int nt = 0; nt < 7; ++nt) acc[nt] = f4{0, 0, 0, 0};
    {
      v8s W[6];
#pragma unroll
      for (int nt = 0; nt < 6; ++nt) W[nt] = W1F(nt, 0);
      SB();
#pragma unroll
      for (int s = 0; s < 16; ++s) {
        if (s == 0) act2[4] = asv8(ld16(rWS, lane * 16, asrc + 4 * 1024));
        if (s + 5 < 16) act2[s + 5] = asv8(ld16(rWS, lane * 16, asrc + (s + 5) * 1024));
        else if (s < 15) { sa[s - 11] = ld16(rWS, ((4 * g + s - 11) * 128 + 16 * w + tok) * 16, WS_ST2A + ci * 16 * 128 * 16);
                           sb[s - 11] = ld8(rWS, ((4 * g + s - 11) * 128 + 16 * w + tok) * 8, WS_ST2B + ci * 16 * 128 * 8); }
#pragma unroll
        for (int nt = 0; nt < 6; ++nt) {
          acc[nt] = MFMA16(W[nt], act2[s], acc[nt]);
          if (s + 1 < 16) W[nt] = W1F(nt, s + 1);
          SB();
        }
      }
    }
    STAMP(18);
    CSTAMP(42);
    BAR();
    v8s WB[4];
#pragma unroll
    for (int s = 0; s < 4; ++s) WB[s] = W1F(6, s);
    float s1 = 0, s2 = 0, s3 = 0, s4 = 0, s5 = 0, s6 = 0;
#pragma unroll
    for (int e = 0; e < 4; ++e) { s1 += __uint_as_float(sa[e][0]); s2 += __uint_as_float(sa[e][1]); s3 += __uint_as_float(sa[e][2]); s4 += __uint_as_float(sa[e][3]); s5 += __uint_as_float(sb[e][0]); s6 += __uint_as_float(sb[e][1]); }
    s1 = sumg(s1); s2 = sumg(s2); s3 = sumg(s3); s4 = sumg(s4); s5 = sumg(s5); s6 = sumg(s6);
    const float mu1 = s1 * (1.f / 512), var1 = fmaxf(s2 * (1.f / 512) - mu1 * mu1, 0.f), r1 = __builtin_amdgcn_rsqf(var1 + 1e-5f);
    const float mu2 = (r1 * (s3 - mu1 * Cw) + Cs) * (1.f / 512);
    const float hh = (r1 * r1 * (s5 - 2.f * mu1 * s4 + mu1 * mu1 * Cw2) + 2.f * r1 * (s6 - mu1 * Cws) + Cs2) * (1.f / 512);
    const float r2 = __builtin_amdgcn_rsqf(fmaxf(hh - mu2 * mu2, 0.f) + 1e-5f);
    const float cA = r2 * r1, cB = -cA * mu1, cC = r2, cD = -r2 * mu2;
    const unsigned S0 = (7 * cj + par) >> 1;
    const unsigned hb = WS_H1F + (ci * 8 + w) * 56 * 1024;
    const unsigned xf = opq(L_XF + 16 * g);
#define BIAS4(nt) __builtin_elementwise_fma(f4{cB, cB, cB, cB}, ldsf4(xf + (nt) * 64), __builtin_elementwise_fma(f4{cC, cC, cC, cC}, ldsf4(xf + 448 + (nt) * 64), \
                    __builtin_elementwise_fma(f4{cD, cD, cD, cD}, ldsf4(xf + 896 + (nt) * 64), ldsf4(xf + 1344 + (nt) * 64))))
#define GELU4(o, a, bias) do { const f4 z_ = __builtin_elementwise_fma(f4{cA, cA, cA, cA}, a, bias); const f4 q_ = __builtin_elementwise_fma(z_ * z_, f4{-0.1029432397f, -0.1029432397f, -0.1029432397f, -0.1029432397f}, \
        f4{-2.3022081985f, -2.3022081985f, -2.3022081985f, -2.3022081985f}) * z_; f4 e_; \
        _Pragma("unroll") for (int r = 0; r < 4; ++r) e_[r] = __builtin_amdgcn_exp2f(q_[r]); \
        e_ += f4{1.f, 1.f, 1.f, 1.f}; \
        _Pragma("unroll") for (int r = 0; r < 4; ++r) e_[r] = __builtin_amdgcn_rcpf(e_[r]); \
        o = z_ * e_; } while (0)
    {
      unsigned hp[2][2];
      f4 bias = BIAS4(0);
      PIN(bias);
      SB();
#pragma unroll
      for (int nt = 0; nt < 6; ++nt) {
#pragma unroll
        for (int k = (nt * 16) / 6; k < ((nt + 1) * 16) / 6; ++k) { acc[6] = MFMA16(WB[k & 3], act2[k], acc[6]); if (k + 4 < 16) WB[k & 3] = W1F(6, k + 4); }
        if ((nt & 1) == 0) { const int gi = w * 3 + (nt >> 1), it = 1 + (gi >> 3), khh = (gi >> 2) & 1, pc4 = gi & 3;
          glds4x1(rWS, lane * 16, WS_TW2 + ct * 229376 + (khh * 7 + it) * 16384 + pc4 * 4096, 65536u + (it - 1) * 32768u + khh * 16384u + pc4 * 4096u); }
        if (nt >= 1 && nt < 5) {
#pragma unroll
          for (int e = 2 * (nt - 1); e < 2 * nt; ++e) { const int pc = w * 8 + e; const int chunk = (pc >> 4) == 0 ? 5 : ((pc >> 4) == 1 ? 12 : ((pc >> 4) == 2 ? 6 : 13));
            stg[e] = ld16(rWS, lane * 16, WS_TW2 + ct * 229376 + chunk * 16384 + (pc & 15) * 1024); }
        }
        f4 nb = bias;
        if (nt < 5) nb = BIAS4(nt + 1); else nb = BIAS4(6);
        f4 o;
        GELU4(o, acc[nt], bias);
        hp[nt & 1][0] = pk(o[0], o[1]); hp[nt & 1][1] = pk(o[2], o[3]);
        PIN(hp[nt & 1][0]); PIN(hp[nt & 1][1]); PIN(nb);
        bias = nb;
        if (nt & 1) st16(rWS, v4u{hp[0][0], hp[0][1], hp[1][0], hp[1][1]}, hb + (S0 + (nt >> 1)) * 1024 + lane * 16);
        SB();
      }
      STAMP(20);
      CSTAMP(43);
      f4 o;
      GELU4(o, acc[6], bias);
      const unsigned NT = 7 * cj + (par ? 0 : 6);
      st8(rWS, v2u{pk(o[0], o[1]), pk(o[2], o[3])}, hb + (NT >> 1) * 1024 + (NT & 1) * 512 + lane * 8);
    }
  }
  STAMP(25);
  VM0();
  BAR();
  if (tid == 256) arrive(p.ws, WS_FP + (3 * 16 + ci) * 128 + cj * 8, TAG);
  STAMP(13);
  if (w != 4) {
#pragma unroll
    for (int e = 0; e < 2; ++e) {
      const int gi = wq + 7 * e;
      if (gi < 8) glds4x1(rWS, lane * 16, WS_TW2 + ct * 229376 + ((gi >> 2) * 7 + 4) * 16384 + (gi & 3) * 4096, (gi >> 2) * 16384u + (gi & 3) * 4096u);
    }
  } else {
    if (b == 0) {
      GAS u64* f = (GAS u64*)(p.ws + WS_F0) + lane;
      for (unsigned spins = 0; spins < (1u << 17); ++spins) {
        bool ok = true;
#pragma unroll
        for (int i = 0; i < 16; ++i) ok &= __hip_atomic_load(f + i * 64, RLX_AGENT) == TAG;
        if (__all(ok)) break;
      }
      if (lane == 0) __hip_atomic_store((GAS u64*)(p.ws + WS_EPOCH), TAG, RLX_AGENT);
    }
    wait_words(p.ws, WS_FP + (3 * 16 + ci) * 128, 16, lane, TAG);
  }
  BAR();
  STAMP(14);

  {
    v8s ha[28];
    const unsigned hb = WS_H1F + ((ci * 8 + 4 * hf + tt) * 56 + 28 * kh) * 1024;
#define HLOAD(s) do { if ((s) % 7 == 3) { const v2u lo_ = ld8(rWS, lane * 8, hb + (s) * 1024), hi_ = ld8(rWS, lane * 8 + 512, hb + (s) * 1024); ha[s] = asv8(v4u{lo_[0], lo_[1], hi_[0], hi_[1]}); } \
                      else ha[s] = asv8(ld16(rWS, lane * 16, hb + (s) * 1024)); } while (0)
#pragma unroll
    for (int s = 0; s < 3; ++s) HLOAD(s);
    const unsigned bw = opq(kh * 16384 + lane * 16), bw2 = opq(bw + 65536), bw3 = opq(bw + 131072);
    f4 acc[4];
#pragma unroll
    for (int nt = 0; nt < 4; ++nt) acc[nt] = f4{0, 0, 0, 0};
#define W2F(nt, S) lds16((((S) >> 2) == 0 || ((S) >> 2) == 5 ? bw + 32768 : ((S) >> 2) == 1 || ((S) >> 2) == 6 ? bw2 : ((S) >> 2) == 2 ? bw2 + 32768 : ((S) >> 2) == 3 ? bw3 : bw) + (((S) & 3) * 4 + (nt)) * 1024)
    {
      v8s W[2][4];
#pragma unroll
      for (int S = 0; S < 2; ++S)
#pragma unroll
        for (int nt = 0; nt < 4; ++nt) W[S][nt] = W2F(nt, S);
      SB();
#pragma unroll
      for (int s = 3; s < 8; ++s) HLOAD(s);
      SB();
#pragma unroll
      for (int S = 0; S < 28; ++S) {
        if (S + 8 < 28) HLOAD(S + 8);
        if (S == 8) BAR();
        if (S >= 8 && S < 16) { const int e = S - 8, pc = w * 8 + e; *(LAS v4u*)(sm + 32768 + (pc >> 5) * 32768 + ((pc >> 4) & 1) * 16384 + (pc & 15) * 1024 + lane * 16) = stg[e]; }
        if (S == 18) BAR();
#pragma unroll
        for (int nt = 0; nt < 4; ++nt) {
          acc[nt] = MFMA16(W[S & 1][nt], ha[S], acc[nt]);
          if (S + 2 < 28) W[S & 1][nt] = W2F(nt, S + 2);
          SB();
        }
      }
    }
    STAMP(22);
    BAR();
    *(LAS f4*)(sm + L_W2X + w * 2048 + lane * 16) = kh ? acc[0] : acc[2];
    *(LAS f4*)(sm + L_W2X + w * 2048 + 1024 + lane * 16) = kh ? acc[1] : acc[3];
    BAR();
    const f4 o0 = ldsf4(L_W2X + (w ^ 4) * 2048 + lane * 16), o1 = ldsf4(L_W2X + (w ^ 4) * 2048 + 1024 + lane * 16);
    const f4 t0 = (kh ? acc[2] : acc[0]) + o0, t1 = (kh ? acc[3] : acc[1]) + o1;
    const float gb[8] = {bflo(gbR[0]), bfhi(gbR[0]), bflo(gbR[1]), bfhi(gbR[1]), bflo(gbR[2]), bfhi(gbR[2]), bflo(gbR[3]), bfhi(gbR[3])};
    const float bb[8] = {bflo(b2R[0][0]), bfhi(b2R[0][0]), bflo(b2R[0][1]), bfhi(b2R[0][1]), bflo(b2R[1][0]), bfhi(b2R[1][0]), bflo(b2R[1][1]), bfhi(b2R[1][1])};
    float o[8];
#pragma unroll
    for (int r = 0; r < 8; ++r) o[r] = x1[r] + gb[r] * ((r < 4 ? t0[r & 3] : t1[r & 3]) + bb[r]);
    auto r0 = __builtin_amdgcn_permlane16_swap(pk(o[0], o[1]), pk(o[4], o[5]), false, false);
    auto r1 = __builtin_amdgcn_permlane16_swap(pk(o[2], o[3]), pk(o[6], o[7]), false, false);
    *(v4u*)(p.out + (size_t)orow * DM + 64 * ct + 32 * kh + (g & 1) * 16 + (g >> 1) * 8) = v4u{r0[0], r1[0], r0[1], r1[1]};
  }
  STAMP(15);
  CSTAMP(41);
  STAMP_FLUSH(0);
}

extern "C" void kernel_launch(void* const* d_in, const int* in_sizes, int n_in, void* d_out, int out_size, void* d_ws, size_t ws_size, hipStream_t stream) {
  Params p;
  p.x = (const u16*)d_in[0]; p.wqkv = (const u16*)d_in[1]; p.wo = (const u16*)d_in[2];
  p.w1 = (const u16*)d_in[4]; p.b1 = (const u16*)d_in[5]; p.w2 = (const u16*)d_in[6]; p.b2 = (const u16*)d_in[7];
  p.c = (const u16*)d_in[8]; p.wmod = (const u16*)d_in[9]; p.out = (u16*)d_out; p.ws = (char*)d_ws;
  fused_block<<<256, 512, 0, stream>>>(p);
}
```

```cpp
#include <hip/hip_runtime.h>
#include <hip/hip_bf16.h>
#include <stdint.h>
#include <stdio.h>
#define STAMPS 1

typedef unsigned short u16;
typedef short v4s __attribute__((ext_vector_type(4)));
typedef short v8s __attribute__((ext_vector_type(8)));
typedef float f2 __attribute__((ext_vector_type(2)));
typedef float f4 __attribute__((ext_vector_type(4)));
typedef float f16v __attribute__((ext_vector_type(16)));
typedef unsigned v2u __attribute__((ext_vector_type(2)));
typedef unsigned v4u __attribute__((ext_vector_type(4)));
typedef __bf16 b2 __attribute__((ext_vector_type(2)));
#define LAS __attribute__((address_space(3)))
#define GAS __attribute__((address_space(1)))
#define DI __device__ __forceinline__
typedef __amdgpu_buffer_rsrc_t srd_t;

constexpr int SEQ = 2048, DM = 512, FF = 1792;
constexpr unsigned WS_EPOCH = 0;
constexpr unsigned WS_F0 = 4096;
constexpr unsigned WS_FP = WS_F0 + 8192;
constexpr unsigned WS_FM = WS_FP + 4 * 16 * 128;
constexpr unsigned WS_FQ = WS_FM + 2048;
constexpr unsigned WS_FW1 = WS_FQ + 2048;
constexpr unsigned WS_FWO = WS_FW1 + 2048;
constexpr unsigned WS_FW2 = WS_FWO + 1024;
static_assert(WS_FW2 + 4096 <= (64 << 10), "flags");
constexpr unsigned WS_WV = 64 << 10;
constexpr unsigned WS_MOD = 128 << 10;
constexpr unsigned WS_ST1 = 192 << 10;
constexpr unsigned WS_ST2A = 512 << 10;
constexpr unsigned WS_ST2B = 1024 << 10;
constexpr unsigned WS_HF = 2u << 20;
constexpr unsigned WS_KV = 4u << 20;
constexpr unsigned WS_OF = 8u << 20;
constexpr unsigned WS_X1F = 10u << 20;
constexpr unsigned WS_H1F = 12u << 20;
constexpr unsigned WS_TQ = 19u << 20;
constexpr unsigned WS_TW1 = WS_TQ + (1536u << 10);
constexpr unsigned WS_TWO = WS_TW1 + (1792u << 10);
constexpr unsigned WS_TW2 = WS_TWO + (512u << 10);
constexpr unsigned WS_STAMP = 25u << 20;
constexpr unsigned WS_BYTES = 27u << 20;
static_assert(WS_TW2 + (1792u << 10) <= WS_STAMP, "ws");
constexpr unsigned L_CA = 98304;
constexpr unsigned L_XS = 131072;
constexpr unsigned L_S1 = L_XS + 8192;
constexpr unsigned L_SO = L_S1 + 7168;
constexpr unsigned L_SC = L_SO + 12288;
constexpr unsigned L_CAP = L_SC + 2048;
constexpr unsigned L_CNT = L_CAP + 512;
static_assert(L_CNT + 16 <= 163840, "lds");
constexpr unsigned L_SQ = 0;
constexpr unsigned L_M2 = 8192;
constexpr unsigned L_WQKV = 0;
constexpr unsigned L_PARTU = 98304;
constexpr unsigned L_PARTV = 32768;
constexpr unsigned L_AK = 98304;
constexpr unsigned KPL = 6144;
constexpr unsigned L_AV = L_AK + 4 * KPL;
constexpr unsigned VPL = 6208, VHD = 12416;
constexpr unsigned L_PSA = L_AK + KPL, L_PSB = L_AK + 3 * KPL, L_PCB = L_AV + VPL, L_PGA = L_AV + VHD + VPL, L_PGB = L_PGA + 1024, L_B2C = L_PGA + 2048;
constexpr unsigned L_PSA2 = L_AK + 2 * KPL, L_PSB2 = L_AV, L_PCB2 = L_AV + VHD;
constexpr unsigned L_FIN = L_AK;
constexpr unsigned L_ONE = L_AV + 2 * VHD;
constexpr unsigned L_AQ = L_ONE + 6400;
constexpr unsigned L_PERS = L_AQ + 8192;
static_assert(L_PERS + 1024 <= 163840, "lds");
static_assert((L_AV % 256) == 0 && (L_ONE % 256) == 0, "bank plan");
constexpr unsigned L_WO = 0;
constexpr unsigned L_W1 = 65536;
constexpr unsigned L_W1S = 0;
constexpr unsigned L_XCH = 16384;
constexpr unsigned L_XF = 16384 + 12288;
constexpr unsigned L_W2X = 98304;

struct Params { const u16* x; const u16* wqkv; const u16* wo; const u16* w1; const u16* b1; const u16* w2; const u16* b2; const u16* c; const u16* wmod; u16* out; char* ws; };

DI float bflo(unsigned v) { return __uint_as_float(v << 16); }
DI float bfhi(unsigned v) { return __uint_as_float(v & 0xffff0000u); }
DI unsigned pk(float a, float b) { f2 v = {a, b}; b2 r = __builtin_convertvector(v, b2); return __builtin_bit_cast(unsigned, r); }
DI float rbf(float a) { return bflo(pk(a, 0.f)); }
DI v8s cat(v4s a, v4s b) { return __builtin_shufflevector(a, b, 0, 1, 2, 3, 4, 5, 6, 7); }
DI v4s trd(unsigned a) { return __builtin_amdgcn_ds_read_tr16_b64_v4i16((LAS v4s*)(uintptr_t)a); }
DI v8s asv8(v4u v) { return __builtin_bit_cast(v8s, v); }
DI v4u asv4u(v8s v) { return __builtin_bit_cast(v4u, v); }
DI v8s lds16(unsigned a) { return *(const LAS v8s*)(uintptr_t)a; }
DI f4 ldsf4(unsigned a) { return *(const LAS f4*)(uintptr_t)a; }
DI v2u lds8(unsigned a) { return *(const LAS v2u*)(uintptr_t)a; }
#define MFMA16(a, b, c) __builtin_amdgcn_mfma_f32_16x16x32_bf16((a), (b), (c), 0, 0, 0)
#define MFMA32(a, b, c) __builtin_amdgcn_mfma_f32_32x32x16_bf16((a), (b), (c), 0, 0, 0)
#define BAR() asm volatile("s_waitcnt lgkmcnt(0)\n\ts_barrier" ::: "memory")
#define VM0() asm volatile("s_waitcnt vmcnt(0)" ::: "memory")
#define LG0() asm volatile("s_waitcnt lgkmcnt(0)" ::: "memory")
#define CFENCE() asm volatile("" ::: "memory")
#define SB() __builtin_amdgcn_sched_barrier(0)
DI unsigned opq(unsigned v) { asm volatile("" : "+v"(v)); return v; }
DI void glds(srd_t r, unsigned voff, unsigned soff, unsigned ldsaddr) {
  asm volatile("s_mov_b32 m0, %0\n\ts_nop 0\n\tbuffer_load_dwordx4 %1, %2, %3 offen lds" ::"s"(ldsaddr), "v"(voff), "s"(r), "s"(soff) : "memory");
}
DI void glds1(srd_t r, unsigned voff, unsigned soff, unsigned ldsaddr) {
  asm volatile("s_mov_b32 m0, %0\n\ts_nop 0\n\tbuffer_load_dwordx4 %1, %2, %3 offen sc1 lds" ::"s"(ldsaddr), "v"(voff), "s"(r), "s"(soff) : "memory");
}
DI void glds4(srd_t r, unsigned voff, unsigned soff, unsigned ldsaddr) {
  asm volatile("s_mov_b32 m0, %0\n\ts_nop 0\n\tbuffer_load_dwordx4 %1, %2, %3 offen lds\n\tbuffer_load_dwordx4 %1, %2, %3 offen offset:1024 lds\n\t"
               "buffer_load_dwordx4 %1, %2, %3 offen offset:2048 lds\n\tbuffer_load_dwordx4 %1, %2, %3 offen offset:3072 lds" ::"s"(ldsaddr), "v"(voff), "s"(r), "s"(soff) : "memory");
}
DI void glds4x1(srd_t r, unsigned voff, unsigned soff, unsigned ldsaddr) {
  asm volatile("s_mov_b32 m0, %0\n\ts_nop 0\n\tbuffer_load_dwordx4 %1, %2, %3 offen sc1 lds\n\tbuffer_load_dwordx4 %1, %2, %3 offen offset:1024 sc1 lds\n\t"
               "buffer_load_dwordx4 %1, %2, %3 offen offset:2048 sc1 lds\n\tbuffer_load_dwordx4 %1, %2, %3 offen offset:3072 sc1 lds" ::"s"(ldsaddr), "v"(voff), "s"(r), "s"(soff) : "memory");
}
DI void glds2x1(srd_t r, unsigned voff, unsigned soff, unsigned ldsaddr) {
  asm volatile("s_mov_b32 m0, %0\n\ts_nop 0\n\tbuffer_load_dwordx4 %1, %2, %3 offen sc1 lds\n\tbuffer_load_dwordx4 %1, %2, %3 offen offset:1024 sc1 lds" ::"s"(ldsaddr), "v"(voff), "s"(r), "s"(soff) : "memory");
}
DI v4u ld16(srd_t r, unsigned voff, unsigned soff) { return __builtin_amdgcn_raw_buffer_load_b128(r, voff, soff, 16); }
DI v2u ld8(srd_t r, unsigned voff, unsigned soff) { return __builtin_amdgcn_raw_buffer_load_b64(r, voff, soff, 16); }
DI v4u ldp16(srd_t r, unsigned voff, unsigned soff) { return __builtin_amdgcn_raw_buffer_load_b128(r, voff, soff, 0); }
DI void stp16(srd_t r, v4u v, unsigned off) { __builtin_amdgcn_raw_buffer_store_b128(v, r, off, 0, 0); }
DI void st16(srd_t r, v4u v, unsigned off) { __builtin_amdgcn_raw_buffer_store_b128(v, r, off, 0, 16); }
DI void st8(srd_t r, v2u v, unsigned off) { __builtin_amdgcn_raw_buffer_store_b64(v, r, off, 0, 16); }
DI float sum16(float v) { auto r = __builtin_amdgcn_permlane16_swap(__float_as_uint(v), __float_as_uint(v), false, false); return __uint_as_float(r[0]) + __uint_as_float(r[1]); }
DI float sum32(float v) { auto r = __builtin_amdgcn_permlane32_swap(__float_as_uint(v), __float_as_uint(v), false, false); return __uint_as_float(r[0]) + __uint_as_float(r[1]); }
DI float max32(float v) { auto r = __builtin_amdgcn_permlane32_swap(__float_as_uint(v), __float_as_uint(v), false, false); return fmaxf(__uint_as_float(r[0]), __uint_as_float(r[1])); }
DI float sumg(float v) { return sum32(sum16(v)); }
#define RLX_AGENT __ATOMIC_RELAXED, __HIP_MEMORY_SCOPE_AGENT
typedef unsigned long long u64;
DI void arrive(char* ws, unsigned off, u64 tag) { (void)__hip_atomic_exchange((GAS u64*)(ws + off), tag, RLX_AGENT); }
DI void wait_words(char* ws, unsigned off, int n, int lane, u64 tag) {
  GAS u64* p = (GAS u64*)(ws + off) + (lane < n ? lane : 0);
  for (unsigned spins = 0; spins < (1u << 17); ++spins)
    if (__all(__hip_atomic_load(p, RLX_AGENT) == tag)) break;
  CFENCE();
}
template <int C> DI float dpp(float v) { return __uint_as_float(__builtin_amdgcn_update_dpp(0u, __float_as_uint(v), C, 0xf, 0xf, false)); }
DI float rowsum16(float v) { v += dpp<0x128>(v); v += dpp<0x124>(v); v += dpp<0x122>(v); v += dpp<0x121>(v); return v; }
#define PIN(x) asm volatile("" : "+v"(x))
#define STAMP(k) do { unsigned long long t_; asm volatile("s_memrealtime %0\n\ts_waitcnt lgkmcnt(0)" : "=s"(t_) :: "memory"); asm volatile("v_writelane_b32 %0, %1, %2" : "+v"(stampv) : "s"((unsigned)t_), "i"(k)); } while (0)
#define CSTAMP(k) do { unsigned long long t_; asm volatile("s_memtime %0\n\ts_waitcnt lgkmcnt(0)" : "=s"(t_) :: "memory"); asm volatile("v_writelane_b32 %0, %1, %2" : "+v"(stampv) : "s"((unsigned)t_), "i"(k)); } while (0)
#define STAMP_FLUSH(o) do { ((GAS unsigned*)(p.ws + WS_STAMP + (o)))[(blockIdx.x * 8 + w) * 64 + lane] = stampv; } while (0)
DI float silu(float v) { return v * __builtin_amdgcn_rcpf(1.f + __builtin_amdgcn_exp2f(-1.4426950408889634f * v)); }

DI v8s trfragA(unsigned img, int half, int lane) {
  const int g = lane >> 4, q = (lane & 15) >> 2, pp = lane & 3;
  const unsigned a = img + (4 * g + q) * 64 + ((half ^ (g & 1)) * 32) + pp * 8;
  return cat(trd(a), trd(a + 1024));
}

template <bool FULL>
DI void attention(int t, int hd, int kt0, int lane, f16v mU, f16v mL, f16v& O, srd_t rWS, unsigned wo_src, unsigned w1_src, int w, const u16* xres, v2u& xr0, v2u& xr1, unsigned& stampv) {
  const int qr = lane & 31, h = lane >> 5, blk = (lane >> 4) & 1, q = (lane & 15) >> 2, pp = lane & 3;
  const v8s Qf = lds16(L_AQ + (hd * 2 + h) * 2048 + (32 * t + qr) * 16);
  const unsigned kb = opq(L_AK + (hd * 2 + h) * KPL + (32 * t + qr) * 16);
  const unsigned vb = opq((blk == 0 ? L_AV + hd * VHD + (pp >> 1) * VPL + (pp & 1) * 8 + (4 * h + q) * 16 : L_ONE + ((hd * 128 + (h ? 0 : 128)) & 255)) + t * 512);
  const f16v zero = {};
#define RIDE(n) do { if ((n) < 2) glds4x1(rWS, lane * 16, wo_src + (w * 2 + (n)) * 4096, L_WO + (w * 2 + (n)) * 4096); \
                     else glds4x1(rWS, lane * 16, w1_src + w * 4096, L_W1 + w * 4096); } while (0)
  if (FULL) {
    v8s Kf[9];
#pragma unroll
    for (int kt = 0; kt < 9; ++kt) Kf[kt] = lds16(kb + kt * 512);
    v8s V0 = cat(trd(vb), trd(vb + 128)), V1 = cat(trd(vb + 256), trd(vb + 384));
    f16v S[2];
    float m0 = -3e38f, m1 = -3e38f;
    S[0] = MFMA32(Kf[0], Qf, mL);
    SB();
#pragma unroll
    for (int kt = 0; kt < 9; ++kt) {
      if (kt < 8) S[(kt + 1) & 1] = MFMA32(Kf[kt + 1], Qf, kt + 1 == 8 ? mU : zero);
      SB();
#pragma unroll
      for (int r = 0; r < 16; r += 4) { m0 = fmaxf(fmaxf(m0, S[kt & 1][r]), S[kt & 1][r + 1]); m1 = fmaxf(fmaxf(m1, S[kt & 1][r + 2]), S[kt & 1][r + 3]); }
      SB();
    }
    const float m = max32(fmaxf(m0, m1));
    STAMP(23);
    f16v nm;
#pragma unroll
    for (int r = 0; r < 16; ++r) { nm[r] = -m; mU[r] -= m; mL[r] -= m; }
    O = zero;
    S[0] = MFMA32(Kf[0], Qf, mL);
    SB();
#pragma unroll
    for (int kt = 0; kt < 9; ++kt) {
      if (kt < 8) S[(kt + 1) & 1] = MFMA32(Kf[kt + 1], Qf, kt + 1 == 8 ? mU : nm);
      if (kt == 0) RIDE(0);
      if (kt == 3) RIDE(1);
      if (kt == 6) RIDE(2);
      SB();
      v4u P0, P1;
#pragma unroll
      for (int r = 0; r < 4; ++r) P0[r] = pk(__builtin_amdgcn_exp2f(S[kt & 1][2 * r]), __builtin_amdgcn_exp2f(S[kt & 1][2 * r + 1]));
      O = MFMA32(V0, asv8(P0), O);
      if (kt < 8) V0 = cat(trd(vb + (kt + 1) * 512), trd(vb + (kt + 1) * 512 + 128));
      if (kt == 2) xr0 = *(const v2u*)xres;
      if (kt == 5) xr1 = *(const v2u*)(xres + 16);
      SB();
#pragma unroll
      for (int r = 0; r < 4; ++r) P1[r] = pk(__builtin_amdgcn_exp2f(S[kt & 1][8 + 2 * r]), __builtin_amdgcn_exp2f(S[kt & 1][8 + 2 * r + 1]));
      O = MFMA32(V1, asv8(P1), O);
      if (kt < 8) V1 = cat(trd(vb + (kt + 1) * 512 + 256), trd(vb + (kt + 1) * 512 + 384));
      SB();
    }
  } else {
#pragma unroll
    for (int n = 0; n < 3; ++n) RIDE(n);
    xr0 = *(const v2u*)xres; xr1 = *(const v2u*)(xres + 16);
    float m = -3e38f;
#pragma unroll
    for (int kt = 0; kt < 9; ++kt) {
      if (kt >= kt0) {
        const f16v S = MFMA32(lds16(kb + kt * 512), Qf, kt == 8 ? mU : zero);
#pragma unroll
        for (int r = 0; r < 16; r += 2) m = fmaxf(fmaxf(m, S[r]), S[r + 1]);
      }
    }
    m = max32(m);
    f16v nm;
#pragma unroll
    for (int r = 0; r < 16; ++r) { nm[r] = -m; mU[r] -= m; }
    O = zero;
#pragma unroll
    for (int kt = 0; kt < 9; ++kt) {
      if (kt >= kt0) {
        const f16v S = MFMA32(lds16(kb + kt * 512), Qf, kt == 8 ? mU : nm);
        v4u P0, P1;
#pragma unroll
        for (int r = 0; r < 4; ++r) {
          P0[r] = pk(__builtin_amdgcn_exp2f(S[2 * r]), __builtin_amdgcn_exp2f(S[2 * r + 1]));
          P1[r] = pk(__builtin_amdgcn_exp2f(S[8 + 2 * r]), __builtin_amdgcn_exp2f(S[8 + 2 * r + 1]));
        }
        const v8s V0 = cat(trd(vb + kt * 512), trd(vb + kt * 512 + 128));
        const v8s V1 = cat(trd(vb + kt * 512 + 256), trd(vb + kt * 512 + 384));
        O = MFMA32(V0, asv8(P0), O);
        O = MFMA32(V1, asv8(P1), O);
      }
    }
  }
#undef RIDE
}

__global__ void __launch_bounds__(512, 2) fused_block(Params p) {
  __shared__ __attribute__((aligned(16))) char smem[163840];
  LAS char* sm = (LAS char*)smem;
  const int tid = threadIdx.x, lane = tid & 63;
  const int w = __builtin_amdgcn_readfirstlane(tid >> 6);
  const int b = blockIdx.x, ci = ((b & 7) >> 1) * 4 + ((b >> 3) & 3), cj = (b & 1) * 8 + (b >> 5);
  const int tok = lane & 15, g = lane >> 4, q4 = (lane & 15) >> 2, pp = lane & 3;
  const int hf = cj & 1, ct = cj >> 1, par = cj & 1;
  const int wq = w < 4 ? w : w - 1;
  unsigned stampv = 0;
  const srd_t rWS = __builtin_amdgcn_make_buffer_rsrc(p.ws, 0, WS_BYTES, 0x00020000);
  const int rA = lane >> 2, pA = lane & 3;
  const unsigned colA = (((pA >> 1) ^ ((rA >> 2) & 1)) * 32) + (pA & 1) * 16;
  const int rB = lane >> 3, pB = lane & 7;
  const unsigned colB = (((pB >> 1) ^ ((rB >> 1) & 3)) * 32) + (pB & 1) * 16;
  STAMP(0);
  CSTAMP(40);

  const bool m2 = b < 160;
  const int msl = b % 40, mkq = b / 40;
  const srd_t rWmod = __builtin_amdgcn_make_buffer_rsrc((void*)p.wmod, 0, DM * 3072 * 2, 0x00020000);
  if (w < 4) {
    __builtin_amdgcn_s_setprio(2);
    const srd_t rX = __builtin_amdgcn_make_buffer_rsrc((void*)p.x, 0, SEQ * DM * 2, 0x00020000);
#pragma unroll
    for (int i0 = 0; i0 < 8; ++i0) { const int ii = (i0 + (ci & 3) * 2 + (cj & 1)) & 7; glds(rWmod, rA * 6144 + colA, (w * 8 + ii) * 16 * 6144 + (512 + 32 * cj) * 2, L_CA + (w * 8 + ii) * 1024); }
#pragma unroll
    for (int u = 0; u < 2; ++u) glds(rX, rA * 1024 + pA * 16, (128 * ci + 16 * (2 * w + u)) * 1024 + 64 * cj, L_XS + (2 * w + u) * 1024);
  }
  if (tid == 256) { *(LAS unsigned*)(uintptr_t)L_CNT = 0u; *(LAS unsigned*)(uintptr_t)(L_CNT + 4) = m2 ? 0u : 1u; *(LAS unsigned*)(uintptr_t)(L_CNT + 8) = 0u; }
  BAR();
#define READ_TAG() do { const v2u ep_ = ld8(rWS, WS_EPOCH, 0); TAG = (((u64)__builtin_amdgcn_readfirstlane(ep_[1]) << 32) | __builtin_amdgcn_readfirstlane(ep_[0])) + 1; } while (0)
  v8s rt[12];
  f16v mU, mL;
  u64 TAG;
  if (w < 4) {
    const v2u ep = ld8(rWS, WS_EPOCH, 0);
    const v4u cr = *(const v4u*)(p.c + 128 * w + 8 * (lane & 15));
    const unsigned sc = L_SC + w * 256;
    if (lane < 16) *(LAS v4u*)(uintptr_t)(sc + lane * 16) = v4u{pk(silu(bflo(cr[0])), silu(bfhi(cr[0]))), pk(silu(bflo(cr[1])), silu(bfhi(cr[1]))), pk(silu(bflo(cr[2])), silu(bfhi(cr[2]))), pk(silu(bflo(cr[3])), silu(bfhi(cr[3])))};
    LG0();
    v8s sf[4];
#pragma unroll
    for (int s = 0; s < 4; ++s) { const v2u lo = lds8(sc + (32 * s + 4 * g) * 2), hi = lds8(sc + (32 * s + 16 + 4 * g) * 2); sf[s] = asv8(v4u{lo[0], lo[1], hi[0], hi[1]}); }
    TAG = (((u64)__builtin_amdgcn_readfirstlane(ep[1]) << 32) | __builtin_amdgcn_readfirstlane(ep[0])) + 1;
    VM0();
    STAMP(1);
    v2u xa[2][2];
#pragma unroll
    for (int u = 0; u < 2; ++u) { const unsigned a = L_XS + (2 * w + u) * 1024 + tok * 64 + g * 8; xa[u][0] = lds8(a); xa[u][1] = lds8(a + 32); }
    f4 a0 = {0, 0, 0, 0}, a1 = {0, 0, 0, 0};
    {
      v8s W[4][2];
#pragma unroll
      for (int s = 0; s < 4; ++s) { W[s][0] = trfragA(L_CA + (4 * w + s) * 2048, 0, lane); W[s][1] = trfragA(L_CA + (4 * w + s) * 2048, 1, lane); }
#pragma unroll
      for (int s = 0; s < 4; ++s) { a0 = MFMA16(W[s][0], sf[s], a0); a1 = MFMA16(W[s][1], sf[s], a1); }
    }
    if (tok == 0) { *(LAS f4*)(uintptr_t)(L_CAP + (w * 32 + 4 * g) * 4) = a0; *(LAS f4*)(uintptr_t)(L_CAP + (w * 32 + 16 + 4 * g) * 4) = a1; }
    LG0();
    if (lane == 0) __hip_atomic_fetch_add((LAS unsigned*)(uintptr_t)L_CNT, 1u, __ATOMIC_RELAXED, __HIP_MEMORY_SCOPE_WORKGROUP);
    while (*(volatile LAS unsigned*)(uintptr_t)L_CNT < 4u) {}
    CFENCE();
    STAMP(19);
    a0 = ldsf4(L_CAP + 4 * g * 4); a1 = ldsf4(L_CAP + (16 + 4 * g) * 4);
#pragma unroll
    for (int e = 1; e < 4; ++e) { a0 += ldsf4(L_CAP + (e * 32 + 4 * g) * 4); a1 += ldsf4(L_CAP + (e * 32 + 16 + 4 * g) * 4); }
    float wv[8];
#pragma unroll
    for (int r = 0; r < 4; ++r) { wv[r] = rbf(1.f + rbf(a0[r])); wv[4 + r] = rbf(1.f + rbf(a1[r])); }
#pragma unroll
    for (int u = 0; u < 2; ++u) {
      const float x0 = bflo(xa[u][0][0]), x1 = bfhi(xa[u][0][0]), x2 = bflo(xa[u][0][1]), x3 = bfhi(xa[u][0][1]);
      const float x4 = bflo(xa[u][1][0]), x5 = bfhi(xa[u][1][0]), x6 = bflo(xa[u][1][1]), x7 = bfhi(xa[u][1][1]);
      const int T = 2 * w + u;
      st16(rWS, v4u{pk(x0 * wv[0], x1 * wv[1]), pk(x2 * wv[2], x3 * wv[3]), pk(x4 * wv[4], x5 * wv[5]), pk(x6 * wv[6], x7 * wv[7])},
           WS_HF + ((ci * 8 + T) * 16 + cj) * 1024 + lane * 16);
      if (u == 0 && w == 0 && tok == 0) {
        st8(rWS, v2u{pk(wv[0], wv[1]), pk(wv[2], wv[3])}, WS_WV + ci * 1024 + cj * 64 + g * 8);
        st8(rWS, v2u{pk(wv[4], wv[5]), pk(wv[6], wv[7])}, WS_WV + ci * 1024 + cj * 64 + 32 + g * 8);
      }
      const float s1 = sumg(((x0 + x1) + (x2 + x3)) + ((x4 + x5) + (x6 + x7)));
      const float s2 = sumg(((x0 * x0 + x1 * x1) + (x2 * x2 + x3 * x3)) + ((x4 * x4 + x5 * x5) + (x6 * x6 + x7 * x7)));
      if (g == 0) st8(rWS, v2u{__float_as_uint(s1), __float_as_uint(s2)}, WS_ST1 + ((ci * 16 + cj) * 128 + 16 * T + tok) * 8);
    }
    STAMP(21);
    VM0();
    if (lane == 0) { arrive(p.ws, WS_F0 + (ci * 64 + cj * 4 + w) * 8, TAG); __hip_atomic_fetch_add((LAS unsigned*)(uintptr_t)(L_CNT + 8), 1u, __ATOMIC_RELAXED, __HIP_MEMORY_SCOPE_WORKGROUP); }
    __builtin_amdgcn_s_setprio(0);
  } else if (w == 4) {
    if (m2) {
      const int mvec = msl >> 3, mcol = (mvec == 0 ? 0 : 512 + 512 * mvec) + (msl & 7) * 64;
      __builtin_amdgcn_s_sleep(8);
#pragma unroll
      for (int ii = 0; ii < 16; ++ii) glds(rWmod, rB * 6144 + colB, (128 * mkq + 8 * ii) * 6144 + mcol * 2, L_M2 + ii * 1024);
      const v4u cr = *(const v4u*)(p.c + 128 * mkq + 8 * (lane & 15));
      const unsigned sc = L_SC + 1024;
      if (lane < 16) *(LAS v4u*)(uintptr_t)(sc + lane * 16) = v4u{pk(silu(bflo(cr[0])), silu(bfhi(cr[0]))), pk(silu(bflo(cr[1])), silu(bfhi(cr[1]))), pk(silu(bflo(cr[2])), silu(bfhi(cr[2]))), pk(silu(bflo(cr[3])), silu(bfhi(cr[3])))};
      LG0();
      v8s sf[4];
#pragma unroll
      for (int s = 0; s < 4; ++s) { const v2u lo = lds8(sc + (32 * s + 4 * g) * 2), hi = lds8(sc + (32 * s + 16 + 4 * g) * 2); sf[s] = asv8(v4u{lo[0], lo[1], hi[0], hi[1]}); }
      VM0();
      f4 a[4];
#pragma unroll
      for (int nt = 0; nt < 4; ++nt) a[nt] = f4{0, 0, 0, 0};
      const int row = 4 * g + q4;
#pragma unroll
      for (int s = 0; s < 4; ++s) {
        v8s W[4];
#pragma unroll
        for (int nt = 0; nt < 4; ++nt) { const unsigned ad = L_M2 + s * 4096 + row * 128 + ((nt ^ ((row >> 1) & 3)) * 32) + pp * 8; W[nt] = cat(trd(ad), trd(ad + 2048)); }
#pragma unroll
        for (int nt = 0; nt < 4; ++nt) a[nt] = MFMA16(W[nt], sf[s], a[nt]);
      }
      if (tok == 0) {
#pragma unroll
        for (int nt = 0; nt < 4; ++nt)
          st16(rWS, v4u{__float_as_uint(a[nt][0]), __float_as_uint(a[nt][1]), __float_as_uint(a[nt][2]), __float_as_uint(a[nt][3])},
               WS_MOD + mkq * 10240 + mvec * 2048 + ((msl & 7) * 2 + (nt >> 1)) * 128 + g * 32 + (nt & 1) * 16);
      }
      LG0();
      if (lane == 0) *(volatile LAS unsigned*)(uintptr_t)(L_CNT + 4) = 1u;
      READ_TAG();
      VM0();
      if (lane == 0) arrive(p.ws, WS_FM + b * 8, TAG);
    } else READ_TAG();
    STAMP(1);
  } else {
    if (w == 5) {
      const srd_t r = __builtin_amdgcn_make_buffer_rsrc((void*)p.wqkv, 0, DM * 1536 * 2, 0x00020000);
#pragma unroll
      for (int e = 0; e < 6; ++e) glds(r, rA * 3072 + colA, (32 * ci + 16 * (e & 1)) * 3072 + ((e >> 1) * 512 + 32 * cj) * 2, L_SQ + e * 1024);
      READ_TAG();
      VM0();
#pragma unroll
      for (int nt = 0; nt < 6; ++nt) st16(rWS, asv4u(trfragA(L_SQ + (nt >> 1) * 2048, nt & 1, lane)), WS_TQ + ((cj * 6 + nt) * 16 + ci) * 1024 + lane * 16);
      VM0();
      if (lane == 0) arrive(p.ws, WS_FQ + (cj * 16 + ci) * 8, TAG);
    } else {
      READ_TAG();
      __builtin_amdgcn_s_sleep(16);
      if (w == 6) {
        const srd_t r1 = __builtin_amdgcn_make_buffer_rsrc((void*)p.w1, 0, DM * FF * 2, 0x00020000);
#pragma unroll
        for (int e = 0; e < 6; ++e) glds(r1, rA * (FF * 2) + colA, (32 * ci + 16 * (e & 1)) * (FF * 2) + (112 * cj + 16 * par + (e >> 1) * 32) * 2, L_S1 + e * 1024);
        glds(r1, (lane >> 1) * (FF * 2) + (lane & 1) * 16, 32 * ci * (FF * 2) + (112 * cj + (par ? 0 : 96)) * 2, L_S1 + 6144);
        VM0();
#pragma unroll
        for (int nt = 0; nt < 6; ++nt) rt[nt] = trfragA(L_S1 + (nt >> 1) * 2048, nt & 1, lane);
        { const unsigned a = L_S1 + 6144 + (4 * g + q4) * 32 + pp * 8; rt[6] = cat(trd(a), trd(a + 512)); }
      } else {
        const srd_t ro = __builtin_amdgcn_make_buffer_rsrc((void*)p.wo, 0, DM * DM * 2, 0x00020000);
#pragma unroll
        for (int e = 0; e < 4; ++e) glds(ro, rB * 1024 + colB, (32 * ((b & 127) >> 3) + 8 * e) * 1024 + (b & 7) * 128, L_SO + e * 1024);
        const srd_t r2 = __builtin_amdgcn_make_buffer_rsrc((void*)p.w2, 0, FF * DM * 2, 0x00020000);
        const int b2 = b < 224 ? b : 0;
#pragma unroll
        for (int e = 0; e < 8; ++e) { const int id = 2 * b2 + (e >> 2); glds(r2, rB * 1024 + colB, (32 * (id >> 3) + 8 * (e & 3)) * 1024 + (id & 7) * 128, L_SO + 4096 + e * 1024); }
        VM0();
        {
          const int row = 16 * (g >> 1) + 4 * (g & 1) + q4;
#pragma unroll
          for (int nt = 0; nt < 4; ++nt) { const unsigned a = L_SO + row * 128 + ((nt ^ ((row >> 1) & 3)) * 32) + pp * 8; rt[8 + nt] = cat(trd(a), trd(a + 1024)); }
        }
        const int row = 4 * g + q4;
#pragma unroll
        for (int e = 0; e < 8; ++e) { const int nt = e & 3; const unsigned a = L_SO + 4096 + (e >> 2) * 4096 + row * 128 + ((nt ^ ((row >> 1) & 3)) * 32) + pp * 8; rt[e] = cat(trd(a), trd(a + 2048)); }
      }
    }
    STAMP(1);
    if (w >= 6) {
      while (*(volatile LAS unsigned*)(uintptr_t)(L_CNT + 8) < 4u) __builtin_amdgcn_s_sleep(1);
      CFENCE();
      if (w == 6) {
#pragma unroll
        for (int e = 0; e < 7; ++e) st16(rWS, asv4u(rt[e]), WS_TW1 + ((cj * 7 + e) * 16 + ci) * 1024 + lane * 16);
      } else {
        if (b < 224) {
#pragma unroll
          for (int e = 0; e < 8; ++e) { const int id = 2 * b + (e >> 2); st16(rWS, asv4u(rt[e]), WS_TW2 + (((id & 7) * 56 + (id >> 3)) * 4 + (e & 3)) * 1024 + lane * 16); }
        }
        if (b < 128) {
#pragma unroll
          for (int e = 0; e < 4; ++e) st16(rWS, asv4u(rt[8 + e]), WS_TWO + (((b & 7) * 16 + (b >> 3)) * 4 + e) * 1024 + lane * 16);
        }
      }
    }
    wait_words(p.ws, WS_FQ + cj * 128, 16, lane, TAG);
    while (*(volatile LAS unsigned*)(uintptr_t)(L_CNT + 4) == 0u) __builtin_amdgcn_s_sleep(1);
    CFENCE();
    STAMP(19);
#pragma unroll
    for (int e = 0; e < 8; ++e) glds4x1(rWS, lane * 16, WS_TQ + cj * 98304 + ((w - 5) * 8 + ((e + 2 * (ci & 3)) & 7)) * 4096, L_WQKV + ((w - 5) * 8 + ((e + 2 * (ci & 3)) & 7)) * 4096);
  }
  {
    const int qr = lane & 31, h = lane >> 5;
#pragma unroll
    for (int r = 0; r < 16; ++r) { const int kr = (r & 3) + 8 * (r >> 2) + 4 * h; mU[r] = kr > qr ? -1e30f : 0.f; mL[r] = kr > qr ? 0.f : -1e30f; }
    asm volatile("" : "+v"(mU), "+v"(mL));
  }
  if (w == 0) wait_words(p.ws, WS_F0 + ci * 512, 64, lane, TAG);
  STAMP(2);
  if (w >= 5) VM0();
  BAR();
  STAMP(3);
  if (lane == 0) {
    if (w == 6) arrive(p.ws, WS_FW1 + (cj * 16 + ci) * 8, TAG);
    if (w == 7 && b < 128) arrive(p.ws, WS_FWO + ((b & 7) * 16 + (b >> 3)) * 8, TAG);
    if (w == 7 && b < 224) { arrive(p.ws, WS_FW2 + (((2 * b) & 7) * 64 + ((2 * b) >> 3)) * 8, TAG); arrive(p.ws, WS_FW2 + (((2 * b + 1) & 7) * 64 + ((2 * b + 1) >> 3)) * 8, TAG); }
  }

  f4 accQ[2], accKV[4];
  float rstd, nmu;
  v4u gaR, cbR, sbR, gbR;
  v2u b2R[2];
  v4u mc0, md0;
  v4u mx[2];
  const int tt = w & 3, kh = w >> 2;
  const int ocol = 64 * ct + 32 * kh + 4 * g;
  {
    const unsigned bq = opq(L_WQKV + lane * 16), bv = opq(L_WQKV + 65536 + lane * 16);
#define QF(nt, s) lds16(((nt) < 4 ? bq : bv) + ((nt) & 3) * 16384 + (s) * 1024)
    v8s act[16];
    const unsigned hsrc = WS_HF + (ci * 8 + w) * 16 * 1024;
    v2u wl[2], wh[2];
#pragma unroll
    for (int u = 0; u < 2; ++u) { const unsigned o = (32 * (2 * w + u) + 4 * g) * 2; wl[u] = ld8(rWS, o, WS_WV + ci * 1024); wh[u] = ld8(rWS, o + 32, WS_WV + ci * 1024); }
#pragma unroll
    for (int s = 0; s < 4; ++s) act[s] = asv8(ld16(rWS, lane * 16, hsrc + s * 1024));
    {
      f4 au[6];
      v8s W[2][6];
      const unsigned c0 = opq(bq + w * 2048), d0 = opq(bv + w * 2048);
#pragma unroll
      for (int u = 0; u < 2; ++u)
#pragma unroll
        for (int nt = 0; nt < 6; ++nt) W[u][nt] = lds16((nt < 4 ? c0 : d0) + (nt & 3) * 16384 + u * 1024);
#pragma unroll
      for (int nt = 0; nt < 6; ++nt) au[nt] = f4{0, 0, 0, 0};
#pragma unroll
      for (int u = 0; u < 2; ++u) {
        const v8s a = asv8(v4u{wl[u][0], wl[u][1], wh[u][0], wh[u][1]});
#pragma unroll
        for (int nt = 0; nt < 6; ++nt) au[nt] = MFMA16(W[u][nt], a, au[nt]);
      }
      if (tok == 0) {
#pragma unroll
        for (int nt = 0; nt < 6; ++nt) *(LAS f4*)(sm + L_PARTU + (w * 96 + nt * 16 + 4 * g) * 4) = au[nt];
      }
    }
    BAR();
    if (tid >= 384 && tid < 480) {
      const int e0 = tid - 384;
      float s = 0;
#pragma unroll
      for (int e = 0; e < 8; ++e) s += *(const LAS float*)(sm + L_PARTU + (e * 96 + e0) * 4);
      *(LAS float*)(sm + L_PERS + e0 * 4) = s;
    }
    STAMP(16);
    v2u st[4];
    u64 fm0 = 0, fm1 = 0, fm2 = 0;
#pragma unroll
    for (int nt = 0; nt < 4; ++nt) accKV[nt] = f4{0, 0, 0, 0};
#pragma unroll
    for (int nt = 0; nt < 2; ++nt) accQ[nt] = f4{0, 0, 0, 0};
    {
      v8s W[6];
#pragma unroll
      for (int nt = 0; nt < 6; ++nt) W[nt] = QF(nt, 0);
      SB();
#pragma unroll
      for (int s = 0; s < 16; ++s) {
        if (s == 0) act[4] = asv8(ld16(rWS, lane * 16, hsrc + 4 * 1024));
        if (s + 5 < 16) act[s + 5] = asv8(ld16(rWS, lane * 16, hsrc + (s + 5) * 1024));
        else if (s < 15) st[s - 11] = ld8(rWS, ((4 * g + s - 11) * 128 + 16 * w + tok) * 8, WS_ST1 + ci * 16 * 128 * 8);
        if (s == 12) { GAS u64* f = (GAS u64*)(p.ws + WS_FM); fm0 = __hip_atomic_load(f + lane, RLX_AGENT); fm1 = __hip_atomic_load(f + 64 + lane, RLX_AGENT); fm2 = __hip_atomic_load(f + 128 + (lane & 31), RLX_AGENT); }
#pragma unroll
        for (int nt = 0; nt < 6; ++nt) {
          if (nt < 2) accQ[nt] = MFMA16(W[nt], act[s], accQ[nt]); else accKV[nt - 2] = MFMA16(W[nt], act[s], accKV[nt - 2]);
          if (s + 1 < 16) W[nt] = QF(nt, s + 1);
          SB();
        }
      }
    }
    float s1 = 0, s2 = 0;
#pragma unroll
    for (int e = 0; e < 4; ++e) { s1 += __uint_as_float(st[e][0]); s2 += __uint_as_float(st[e][1]); }
    s1 = sumg(s1); s2 = sumg(s2);
    const float mu = s1 * (1.f / 512), var = fmaxf(s2 * (1.f / 512) - mu * mu, 0.f);
    rstd = __builtin_amdgcn_rsqf(var + 1e-5f); nmu = -rstd * mu;
    if (w == 2 && lane < 8) *(LAS v4u*)(sm + L_B2C + lane * 16) = *(const v4u*)(p.b2 + 64 * ct + lane * 8);
    PIN(rstd); PIN(nmu);
    {
          GAS u64* f = (GAS u64*)(p.ws + WS_FM);
          for (unsigned spins = 0; spins < (1u << 17) && !__all(fm0 == TAG && fm1 == TAG && fm2 == TAG); ++spins) {
            fm0 = __hip_atomic_load(f + lane, RLX_AGENT); fm1 = __hip_atomic_load(f + 64 + lane, RLX_AGENT); fm2 = __hip_atomic_load(f + 128 + (lane & 31), RLX_AGENT); }
          CFENCE();
          if (w < 6) {
            const int vec = w < 2 ? 0 : w < 4 ? 2 : 3, hq = w & 1;
            const unsigned dst = w == 0 ? L_PSA : w == 1 ? L_PSA2 : w == 2 ? L_PSB : w == 3 ? L_PSB2 : w == 4 ? L_PCB : L_PCB2;
            glds2x1(rWS, lane * 16, WS_MOD + (2 * hq) * 10240 + vec * 2048, dst);
            glds2x1(rWS, lane * 16, WS_MOD + (2 * hq + 1) * 10240 + vec * 2048, dst + 2048);
          } else glds1(rWS, (lane >> 4) * 10240 + (lane & 15) * 16, WS_MOD + (w == 6 ? 1 : 4) * 2048 + ct * 256, w == 6 ? L_PGA : L_PGB);
    }
    STAMP(4);
    BAR();
    {
      v4u kk, vv;
      unsigned pkd[4][2];
#pragma unroll
      for (int nt = 0; nt < 4; ++nt) {
        const f4 u = ldsf4(L_PERS + (32 + nt * 16 + 4 * g) * 4);
        pkd[nt][0] = pk(rstd * accKV[nt][0] + nmu * u[0], rstd * accKV[nt][1] + nmu * u[1]);
        pkd[nt][1] = pk(rstd * accKV[nt][2] + nmu * u[2], rstd * accKV[nt][3] + nmu * u[3]);
      }
      { auto r0 = __builtin_amdgcn_permlane16_swap(pkd[0][0], pkd[1][0], false, false); auto r1 = __builtin_amdgcn_permlane16_swap(pkd[0][1], pkd[1][1], false, false);
        kk = v4u{r0[0], r1[0], r0[1], r1[1]}; }
      { auto r0 = __builtin_amdgcn_permlane16_swap(pkd[2][0], pkd[3][0], false, false); auto r1 = __builtin_amdgcn_permlane16_swap(pkd[2][1], pkd[3][1], false, false);
        vv = v4u{r0[0], r1[0], r0[1], r1[1]}; }
      const int hd = g & 1, pl = g >> 1;
      st16(rWS, kk, WS_KV + (ci * 16 + cj) * 16384 + ((0 * 2 + hd) * 2 + pl) * 2048 + (16 * w + tok) * 16);
      st16(rWS, vv, WS_KV + (ci * 16 + cj) * 16384 + ((1 * 2 + hd) * 2 + pl) * 2048 + (16 * w + tok) * 16);
      *(LAS v4u*)(sm + L_AK + (hd * 2 + pl) * KPL + (256 + 16 * w + tok) * 16) = kk;
      *(LAS v4u*)(sm + L_AV + hd * VHD + pl * VPL + (256 + 16 * w + tok) * 16) = vv;
    }
    VM0();
    BAR();
    if (tid == 256) arrive(p.ws, WS_FP + (0 * 16 + ci) * 128 + cj * 8, TAG);
    STAMP(5);
    if (tid < 400) *(LAS v4u*)(sm + L_ONE + tid * 16) = v4u{0x3f803f80u, 0x3f803f80u, 0x3f803f80u, 0x3f803f80u};
    if (tid < 416) {
      const int seg = tid < 384 ? tid >> 7 : 3 + ((tid - 384) >> 4), e = tid < 384 ? tid & 127 : (tid - 384) & 15;
      f4 v;
      if (seg < 3) {
        const unsigned a0 = (seg == 0 ? L_PSA : seg == 1 ? L_PSB : L_PCB) + e * 16, a1 = (seg == 0 ? L_PSA2 : seg == 1 ? L_PSB2 : L_PCB2) + e * 16;
        v = (ldsf4(a0) + ldsf4(a0 + 2048)) + (ldsf4(a1) + ldsf4(a1 + 2048));
      } else {
        const unsigned a0 = (seg == 3 ? L_PGA : L_PGB) + e * 16;
        v = (ldsf4(a0) + ldsf4(a0 + 256)) + (ldsf4(a0 + 512) + ldsf4(a0 + 768));
      }
      *(LAS v2u*)(sm + L_FIN + (seg < 3 ? seg * 1024 : 3072 + (seg - 3) * 128) + e * 8) = v2u{pk(v[0], v[1]), pk(v[2], v[3])};
    }
    BAR();
    {
      f4 av[4];
      v8s W[2][4], a[2];
      const unsigned c0 = opq(bq + w * 2048), d0 = opq(bv + w * 2048);
#pragma unroll
      for (int u = 0; u < 2; ++u) {
        a[u] = lds16(L_FIN + (2 * w + u) * 64 + g * 16);
#pragma unroll
        for (int nt = 0; nt < 4; ++nt) W[u][nt] = lds16((nt < 2 ? c0 : d0) + (nt & 1) * 16384 + u * 1024);
      }
#pragma unroll
      for (int nt = 0; nt < 4; ++nt) av[nt] = f4{0, 0, 0, 0};
#pragma unroll
      for (int u = 0; u < 2; ++u)
#pragma unroll
        for (int nt = 0; nt < 4; ++nt) av[nt] = MFMA16(W[u][nt], a[u], av[nt]);
      if (tok == 0) {
#pragma unroll
        for (int nt = 0; nt < 4; ++nt) *(LAS f4*)(sm + L_PARTV + (w * 64 + nt * 16 + 4 * g) * 4) = av[nt];
      }
    }
    {
      gaR = asv4u(lds16(L_FIN + 3072 + kh * 64 + g * 16)); gbR = asv4u(lds16(L_FIN + 3200 + kh * 64 + g * 16));
      sbR = asv4u(lds16(L_FIN + 1024 + (2 * ct + kh) * 64 + g * 16)); cbR = asv4u(lds16(L_FIN + 2048 + (2 * ct + kh) * 64 + g * 16));
#pragma unroll
      for (int e = 0; e < 2; ++e) b2R[e] = lds8(L_B2C + (32 * kh + 4 * g + 16 * e) * 2);
      mc0 = asv4u(lds16(L_FIN + 2048 + 16 * lane)); md0 = asv4u(lds16(L_FIN + 1024 + 16 * lane));
#pragma unroll
      for (int u = 0; u < 2; ++u) mx[u] = asv4u(lds16(L_FIN + (tok == 1 ? 1024 : 2048) + (2 * w + u) * 64 + g * 16));
      BAR();
      if (tid < 64) {
        float s = 0;
#pragma unroll
        for (int e = 0; e < 8; ++e) s += *(const LAS float*)(sm + L_PARTV + (e * 64 + tid) * 4);
        *(LAS float*)(sm + L_PERS + (96 + tid) * 4) = s;
      }
    }
    if (ci < 2) {
      for (int e = tid; e < 8 * 256; e += 512) {
        const int pln = e >> 8, key = e & 255;
        if (ci == 0 || key < 128) {
          const unsigned a = pln < 4 ? L_AK + pln * KPL : L_AV + ((pln - 4) >> 1) * VHD + (pln & 1) * VPL;
          *(LAS v4u*)(sm + a + key * 16) = v4u{0, 0, 0, 0};
        }
      }
    }
    if (w == 4) {
      GAS u64* f0 = (GAS u64*)(p.ws + (lane == 0 && ci >= 1 ? WS_FP + (ci - 1) * 128 + cj * 8 : lane == 1 && ci >= 2 ? WS_FP + (ci - 2) * 128 + cj * 8
                                       : lane >= 16 && lane < 32 ? WS_FW1 + (cj * 16 + lane - 16) * 8 : WS_FWO + (ct * 16 + (lane & 15)) * 8));
      GAS u64* f1 = (GAS u64*)(p.ws + WS_FW2 + ct * 512) + (lane < 56 ? lane : 0);
      for (unsigned spins = 0; spins < (1u << 17); ++spins) {
        const u64 a = __hip_atomic_load(f0, RLX_AGENT), c = __hip_atomic_load(f1, RLX_AGENT);
        if (__all(a == TAG && c == TAG)) break;
      }
      CFENCE();
    }
    BAR();
    STAMP(6);
    {
      const unsigned dst = w < 4 ? L_AK + w * KPL : L_AV + ((w - 4) >> 1) * VHD + (w & 1) * VPL;
#pragma unroll
      for (int src = 0; src < 2; ++src)
        if (ci - 2 + src >= 0) glds2x1(rWS, lane * 16, WS_KV + ((ci - 2 + src) * 16 + cj) * 16384 + w * 2048, dst + src * 2048);
    }
    {
      const float qs = 0.25f * 1.4426950408889634f;
      unsigned pq[2][2];
#pragma unroll
      for (int nt = 0; nt < 2; ++nt) {
        const f4 u = ldsf4(L_PERS + (nt * 16 + 4 * g) * 4), v = ldsf4(L_PERS + (96 + nt * 16 + 4 * g) * 4);
        pq[nt][0] = pk(qs * (rstd * accQ[nt][0] + nmu * u[0] + v[0]), qs * (rstd * accQ[nt][1] + nmu * u[1] + v[1]));
        pq[nt][1] = pk(qs * (rstd * accQ[nt][2] + nmu * u[2] + v[2]), qs * (rstd * accQ[nt][3] + nmu * u[3] + v[3]));
      }
      auto r0 = __builtin_amdgcn_permlane16_swap(pq[0][0], pq[1][0], false, false); auto r1 = __builtin_amdgcn_permlane16_swap(pq[0][1], pq[1][1], false, false);
      *(LAS v4u*)(sm + L_AQ + ((g & 1) * 2 + (g >> 1)) * 2048 + (16 * w + tok) * 16) = v4u{r0[0], r1[0], r0[1], r1[1]};
    }
    VM0();
    BAR();
    STAMP(7);
  }

  const int orow = 128 * ci + 64 * hf + 16 * tt + tok;
  v2u xr0, xr1;
  {
    const int t = w & 3, hd = w >> 2;
    const int kt0 = ci >= 2 ? 0 : 8 - 4 * ci - t;
    f16v O;
    const u16* xres = p.x + (size_t)orow * DM + ocol;
    if (ci >= 2) attention<true>(t, hd, 0, lane, mU, mL, O, rWS, WS_TWO + ct * 65536, WS_TW1 + cj * 7 * 16384, w, xres, xr0, xr1, stampv);
    else attention<false>(t, hd, kt0, lane, mU, mL, O, rWS, WS_TWO + ct * 65536, WS_TW1 + cj * 7 * 16384, w, xres, xr0, xr1, stampv);
    const int h = lane >> 5;
    const float inv = __builtin_amdgcn_rcpf(O[8]);
    const f4 b0 = ldsf4(L_PERS + (96 + 32 + hd * 16 + 4 * h) * 4), b1 = ldsf4(L_PERS + (96 + 32 + hd * 16 + 8 + 4 * h) * 4);
    const v4u o = {pk(O[0] * inv + b0[0], O[1] * inv + b0[1]), pk(O[2] * inv + b0[2], O[3] * inv + b0[3]),
                   pk(O[4] * inv + b1[0], O[5] * inv + b1[1]), pk(O[6] * inv + b1[2], O[7] * inv + b1[3])};
    const int qr = lane & 31;
    st16(rWS, o, WS_OF + (ci * 8 * 16 + cj) * 1024 + ((2 * t + (qr >> 4)) * 16) * 1024 + ((2 * hd + h) * 16 + (qr & 15)) * 16);
  }
  STAMP(24);
  VM0();
  BAR();
  if (tid == 256) arrive(p.ws, WS_FP + (1 * 16 + ci) * 128 + cj * 8, TAG);
  STAMP(8);
  float b1v = 0.f;
  v8s aux2[2];
  float Cw = 0, Cw2 = 0, Cws = 0, Cs = 0, Cs2 = 0;
#define MODSUMS() do { \
      _Pragma("unroll") for (int r = 0; r < 4; ++r) { \
        const float w0 = rbf(1.f + bflo(mc0[r])), w1 = rbf(1.f + bfhi(mc0[r])), s0 = bflo(md0[r]), s1_ = bfhi(md0[r]); \
        Cw += w0 + w1; Cw2 += w0 * w0 + w1 * w1; Cws += w0 * s0 + w1 * s1_; Cs += s0 + s1_; Cs2 += s0 * s0 + s1_ * s1_; } \
      Cw = sumg(rowsum16(Cw)); Cw2 = sumg(rowsum16(Cw2)); Cws = sumg(rowsum16(Cws)); Cs = sumg(rowsum16(Cs)); Cs2 = sumg(rowsum16(Cs2)); \
      _Pragma("unroll") for (int u = 0; u < 2; ++u) { v4u t; \
        _Pragma("unroll") for (int d = 0; d < 4; ++d) t[d] = tok == 0 ? pk(1.f + bflo(mx[u][d]), 1.f + bfhi(mx[u][d])) : (tok == 1 ? mx[u][d] : 0x3f803f80u); \
        aux2[u] = asv8(t); } \
      asm volatile("" : "+v"(Cw), "+v"(Cw2), "+v"(Cws), "+v"(Cs), "+v"(Cs2), "+v"(aux2[0]), "+v"(aux2[1])); } while (0)
  if (w != 4) {
#pragma unroll
    for (int e = 0; e < 3; ++e) { const int gi = wq + 7 * e; if (gi < 16) glds4x1(rWS, lane * 16, WS_TW1 + (cj * 7 + 2) * 16384 + gi * 4096, L_W1 + 32768 + gi * 4096); }
  }
  MODSUMS();
  if (w == 4) wait_words(p.ws, WS_FP + (1 * 16 + ci) * 128, 16, lane, TAG);
  BAR();
  STAMP(9);

  float x1[8];
  {
    v8s oa[8];
    const unsigned osrc = WS_OF + ((ci * 8 + 4 * hf + tt) * 16 + 8 * kh) * 1024;
#pragma unroll
    for (int s = 0; s < 3; ++s) oa[s] = asv8(ld16(rWS, lane * 16, osrc + s * 1024));
    const unsigned bo = opq(L_WO + kh * 32768 + lane * 16);
    f4 acc[4];
#pragma unroll
    for (int nt = 0; nt < 4; ++nt) acc[nt] = f4{0, 0, 0, 0};
    {
      v8s W[2][4];
#pragma unroll
      for (int s = 0; s < 2; ++s)
#pragma unroll
        for (int nt = 0; nt < 4; ++nt) W[s][nt] = lds16(bo + (s * 4 + nt) * 1024);
      SB();
#pragma unroll
      for (int s = 3; s < 8; ++s) oa[s] = asv8(ld16(rWS, lane * 16, osrc + s * 1024));
      if (tid < 112) { const int nt = tid >> 4; b1v = bflo(p.b1[(nt == 6 ? 112 * cj + (par ? 0 : 96) : 112 * cj + 16 * par + nt * 16) + (tid & 15)]); }
      SB();
#pragma unroll
      for (int s = 0; s < 8; ++s)
#pragma unroll
        for (int nt = 0; nt < 4; ++nt) {
          acc[nt] = MFMA16(W[s & 1][nt], oa[s], acc[nt]);
          if (s + 2 < 8) W[s & 1][nt] = lds16(bo + ((s + 2) * 4 + nt) * 1024);
          SB();
        }
    }
    STAMP(26);
    VM0();
    BAR();
    *(LAS f4*)(sm + L_XCH + w * 2048 + lane * 16) = kh ? acc[0] : acc[2];
    *(LAS f4*)(sm + L_XCH + w * 2048 + 1024 + lane * 16) = kh ? acc[1] : acc[3];
    BAR();
    const f4 o0 = ldsf4(L_XCH + (w ^ 4) * 2048 + lane * 16), o1 = ldsf4(L_XCH + (w ^ 4) * 2048 + 1024 + lane * 16);
    const f4 t0 = (kh ? acc[2] : acc[0]) + o0, t1 = (kh ? acc[3] : acc[1]) + o1;
    const float xin[8] = {bflo(xr0[0]), bfhi(xr0[0]), bflo(xr0[1]), bfhi(xr0[1]), bflo(xr1[0]), bfhi(xr1[0]), bflo(xr1[1]), bfhi(xr1[1])};
    const float ga[8] = {bflo(gaR[0]), bfhi(gaR[0]), bflo(gaR[1]), bfhi(gaR[1]), bflo(gaR[2]), bfhi(gaR[2]), bflo(gaR[3]), bfhi(gaR[3])};
    const float cb[8] = {bflo(cbR[0]), bfhi(cbR[0]), bflo(cbR[1]), bfhi(cbR[1]), bflo(cbR[2]), bfhi(cbR[2]), bflo(cbR[3]), bfhi(cbR[3])};
    const float sv[8] = {bflo(sbR[0]), bfhi(sbR[0]), bflo(sbR[1]), bfhi(sbR[1]), bflo(sbR[2]), bfhi(sbR[2]), bflo(sbR[3]), bfhi(sbR[3])};
    float wv[8], xw[8];
#pragma unroll
    for (int r = 0; r < 8; ++r) { x1[r] = rbf(xin[r] + ga[r] * (r < 4 ? t0[r & 3] : t1[r & 3])); wv[r] = rbf(1.f + cb[r]); xw[r] = x1[r] * wv[r]; }
    st16(rWS, v4u{pk(xw[0], xw[1]), pk(xw[2], xw[3]), pk(xw[4], xw[5]), pk(xw[6], xw[7])}, WS_X1F + ((ci * 8 + 4 * hf + tt) * 16 + 2 * ct + kh) * 1024 + lane * 16);
    SB();
    float s1 = 0, s2 = 0, s3 = 0, s4 = 0, s5 = 0, s6 = 0;
#pragma unroll
    for (int r = 0; r < 8; ++r) { s1 += x1[r]; s2 += x1[r] * x1[r]; s3 += xw[r]; s4 += xw[r] * wv[r]; s5 += xw[r] * xw[r]; s6 += xw[r] * sv[r]; }
    s1 = sumg(s1); s2 = sumg(s2); s3 = sumg(s3); s4 = sumg(s4); s5 = sumg(s5); s6 = sumg(s6);
    const unsigned rec = (ci * 16 + 2 * ct + kh) * 128 + 64 * hf + 16 * tt + tok;
    if (g == 0) st16(rWS, v4u{__float_as_uint(s1), __float_as_uint(s2), __float_as_uint(s3), __float_as_uint(s4)}, WS_ST2A + rec * 16);
    if (g == 1) st8(rWS, v2u{__float_as_uint(s5), __float_as_uint(s6)}, WS_ST2B + rec * 8);
  }
  if (w < 4) {
    glds4x1(rWS, lane * 16, WS_TW1 + (cj * 7 + 6) * 16384 + w * 4096, L_W1S + w * 4096);
    glds4x1(rWS, lane * 16, WS_TW2 + ct * 229376 + 7 * 16384 + w * 4096, 49152 + w * 4096);
    asm volatile("s_waitcnt vmcnt(8)" ::: "memory");
  } else {
    glds4x1(rWS, lane * 16, WS_TW2 + ct * 229376 + (w - 4) * 4096, 32768 + (w - 4) * 4096);
    asm volatile("s_waitcnt vmcnt(4)" ::: "memory");
  }
  BAR();
  if (tid == 256) arrive(p.ws, WS_FP + (2 * 16 + ci) * 128 + cj * 8, TAG);
  STAMP(10);
  if (w == 4) asm volatile("s_waitcnt vmcnt(1)" ::: "memory"); else VM0();
  BAR();

  v4u stg[8];
  {
    const unsigned bP = opq(L_W1 + lane * 16), bR = opq(L_W1 + 65536 + lane * 16), bS = opq(L_W1S + lane * 16);
#define W1F(nt, s) lds16((nt) == 6 ? bS + (s) * 1024 : (nt) < 4 ? bP + (nt) * 16384 + (s) * 1024 : bR + ((nt) - 4) * 16384 + (s) * 1024)
    {
      f4 au[7];
#pragma unroll
      for (int nt = 0; nt < 7; ++nt) au[nt] = f4{0, 0, 0, 0};
      const unsigned c0 = opq(bP + w * 2048), d0 = opq(bR + w * 2048), e0 = opq(bS + w * 2048);
#pragma unroll
      for (int u = 0; u < 2; ++u) {
        v8s W[7];
#pragma unroll
        for (int nt = 0; nt < 7; ++nt) W[nt] = lds16(nt == 6 ? e0 + u * 1024 : nt < 4 ? c0 + nt * 16384 + u * 1024 : d0 + (nt - 4) * 16384 + u * 1024);
#pragma unroll
        for (int nt = 0; nt < 7; ++nt) au[nt] = MFMA16(W[nt], aux2[u], au[nt]);
      }
      if (tok < 3) {
#pragma unroll
        for (int nt = 0; nt < 7; ++nt) *(LAS f4*)(sm + L_XCH + ((w * 3 + tok) * 112 + nt * 16 + 4 * g) * 4) = au[nt];
      }
    }
    BAR();
    if (tid < 112) *(LAS float*)(sm + L_XF + (336 + tid) * 4) = b1v;
    {
      const int e0 = w < 4 ? tid : tid - 64;
      if (w != 4 && e0 < 336) {
        float s = 0;
#pragma unroll
        for (int e = 0; e < 8; ++e) s += *(const LAS float*)(sm + L_XCH + (e * 336 + e0) * 4);
        *(LAS float*)(sm + L_XF + e0 * 4) = s;
      }
    }
    if (w == 4) wait_words(p.ws, WS_FP + (2 * 16 + ci) * 128, 16, lane, TAG);
    BAR();
    STAMP(11);
    v8s act2[16];
    const unsigned asrc = WS_X1F + (ci * 8 + w) * 16 * 1024;
#pragma unroll
    for (int s = 0; s < 4; ++s) act2[s] = asv8(ld16(rWS, lane * 16, asrc + s * 1024));
    v4u sa[4]; v2u sb[4];
    f4 acc[7];
#pragma unroll
    for (int nt = 0; nt < 7; ++nt) acc[nt] = f4{0, 0, 0, 0};
    {
      v8s W[6];
#pragma unroll
      for (int nt = 0; nt < 6; ++nt) W[nt] = W1F(nt, 0);
      SB();
#pragma unroll
      for (int s = 0; s < 16; ++s) {
        if (s == 0) act2[4] = asv8(ld16(rWS, lane * 16, asrc + 4 * 1024));
        if (s + 5 < 16) act2[s + 5] = asv8(ld16(rWS, lane * 16, asrc + (s + 5) * 1024));
        else if (s < 15) { sa[s - 11] = ld16(rWS, ((4 * g + s - 11) * 128 + 16 * w + tok) * 16, WS_ST2A + ci * 16 * 128 * 16);
                           sb[s - 11] = ld8(rWS, ((4 * g + s - 11) * 128 + 16 * w + tok) * 8, WS_ST2B + ci * 16 * 128 * 8); }
#pragma unroll
        for (int nt = 0; nt < 6; ++nt) {
          acc[nt] = MFMA16(W[nt], act2[s], acc[nt]);
          if (s + 1 < 16) W[nt] = W1F(nt, s + 1);
          SB();
        }
      }
    }
    STAMP(18);
    CSTAMP(42);
    BAR();
    v8s WB[4];
#pragma unroll
    for (int s = 0; s < 4; ++s) WB[s] = W1F(6, s);
    float s1 = 0, s2 = 0, s3 = 0, s4 = 0, s5 = 0, s6 = 0;
#pragma unroll
    for (int e = 0; e < 4; ++e) { s1 += __uint_as_float(sa[e][0]); s2 += __uint_as_float(sa[e][1]); s3 += __uint_as_float(sa[e][2]); s4 += __uint_as_float(sa[e][3]); s5 += __uint_as_float(sb[e][0]); s6 += __uint_as_float(sb[e][1]); }
    s1 = sumg(s1); s2 = sumg(s2); s3 = sumg(s3); s4 = sumg(s4); s5 = sumg(s5); s6 = sumg(s6);
    const float mu1 = s1 * (1.f / 512), var1 = fmaxf(s2 * (1.f / 512) - mu1 * mu1, 0.f), r1 = __builtin_amdgcn_rsqf(var1 + 1e-5f);
    const float mu2 = (r1 * (s3 - mu1 * Cw) + Cs) * (1.f / 512);
    const float hh = (r1 * r1 * (s5 - 2.f * mu1 * s4 + mu1 * mu1 * Cw2) + 2.f * r1 * (s6 - mu1 * Cws) + Cs2) * (1.f / 512);
    const float r2 = __builtin_amdgcn_rsqf(fmaxf(hh - mu2 * mu2, 0.f) + 1e-5f);
    const float cA = r2 * r1, cB = -cA * mu1, cC = r2, cD = -r2 * mu2;
    const unsigned S0 = (7 * cj + par) >> 1;
    const unsigned hb = WS_H1F + (ci * 8 + w) * 56 * 1024;
    const unsigned xf = opq(L_XF + 16 * g);
#define BIAS4(nt) __builtin_elementwise_fma(f4{cB, cB, cB, cB}, ldsf4(xf + (nt) * 64), __builtin_elementwise_fma(f4{cC, cC, cC, cC}, ldsf4(xf + 448 + (nt) * 64), \
                    __builtin_elementwise_fma(f4{cD, cD, cD, cD}, ldsf4(xf + 896 + (nt) * 64), ldsf4(xf + 1344 + (nt) * 64))))
#define GELU4(o, a, bias) do { const f4 z_ = __builtin_elementwise_fma(f4{cA, cA, cA, cA}, a, bias); const f4 q_ = __builtin_elementwise_fma(z_ * z_, f4{-0.1029432397f, -0.1029432397f, -0.1029432397f, -0.1029432397f}, \
        f4{-2.3022081985f, -2.3022081985f, -2.3022081985f, -2.3022081985f}) * z_; f4 e_; \
        _Pragma("unroll") for (int r = 0; r < 4; ++r) e_[r] = __builtin_amdgcn_exp2f(q_[r]); \
        e_ += f4{1.f, 1.f, 1.f, 1.f}; \
        _Pragma("unroll") for (int r = 0; r < 4; ++r) e_[r] = __builtin_amdgcn_rcpf(e_[r]); \
        o = z_ * e_; } while (0)
    {
      unsigned hp[2][2];
      f4 bias = BIAS4(0);
      PIN(bias);
      SB();
#pragma unroll
      for (int nt = 0; nt < 6; ++nt) {
#pragma unroll
        for (int k = (nt * 16) / 6; k < ((nt + 1) * 16) / 6; ++k) { acc[6] = MFMA16(WB[k & 3], act2[k], acc[6]); if (k + 4 < 16) WB[k & 3] = W1F(6, k + 4); }
        if ((nt & 1) == 0) { const int gi = w * 3 + (nt >> 1), it = 1 + (gi >> 3), khh = (gi >> 2) & 1, pc4 = gi & 3;
          glds4x1(rWS, lane * 16, WS_TW2 + ct * 229376 + (khh * 7 + it) * 16384 + pc4 * 4096, 65536u + (it - 1) * 32768u + khh * 16384u + pc4 * 4096u); }
        if (nt >= 1 && nt < 5) {
#pragma unroll
          for (int e = 2 * (nt - 1); e < 2 * nt; ++e) { const int pc = w * 8 + e; const int chunk = (pc >> 4) == 0 ? 5 : ((pc >> 4) == 1 ? 12 : ((pc >> 4) == 2 ? 6 : 13));
            stg[e] = ld16(rWS, lane * 16, WS_TW2 + ct * 229376 + chunk * 16384 + (pc & 15) * 1024); }
        }
        f4 nb = bias;
        if (nt < 5) nb = BIAS4(nt + 1); else nb = BIAS4(6);
        f4 o;
        GELU4(o, acc[nt], bias);
        hp[nt & 1][0] = pk(o[0], o[1]); hp[nt & 1][1] = pk(o[2], o[3]);
        PIN(hp[nt & 1][0]); PIN(hp[nt & 1][1]); PIN(nb);
        bias = nb;
        if (nt & 1) st16(rWS, v4u{hp[0][0], hp[0][1], hp[1][0], hp[1][1]}, hb + (S0 + (nt >> 1)) * 1024 + lane * 16);
        SB();
      }
      STAMP(20);
      CSTAMP(43);
      f4 o;
      GELU4(o, acc[6], bias);
      const unsigned NT = 7 * cj + (par ? 0 : 6);
      st8(rWS, v2u{pk(o[0], o[1]), pk(o[2], o[3])}, hb + (NT >> 1) * 1024 + (NT & 1) * 512 + lane * 8);
    }
  }
  STAMP(25);
  VM0();
  BAR();
  if (tid == 256) arrive(p.ws, WS_FP + (3 * 16 + ci) * 128 + cj * 8, TAG);
  STAMP(13);
  if (w != 4) {
#pragma unroll
    for (int e = 0; e < 2; ++e) {
      const int gi = wq + 7 * e;
      if (gi < 8) glds4x1(rWS, lane * 16, WS_TW2 + ct * 229376 + ((gi >> 2) * 7 + 4) * 16384 + (gi & 3) * 4096, (gi >> 2) * 16384u + (gi & 3) * 4096u);
    }
  } else {
    if (b == 0) {
      GAS u64* f = (GAS u64*)(p.ws + WS_F0) + lane;
      for (unsigned spins = 0; spins < (1u << 17); ++spins) {
        bool ok = true;
#pragma unroll
        for (int i = 0; i < 16; ++i) ok &= __hip_atomic_load(f + i * 64, RLX_AGENT) == TAG;
        if (__all(ok)) break;
      }
      if (lane == 0) __hip_atomic_store((GAS u64*)(p.ws + WS_EPOCH), TAG, RLX_AGENT);
    }
    wait_words(p.ws, WS_FP + (3 * 16 + ci) * 128, 16, lane, TAG);
  }
  BAR();
  STAMP(14);

  {
    v8s ha[28];
    const unsigned hb = WS_H1F + ((ci * 8 + 4 * hf + tt) * 56 + 28 * kh) * 1024;
#define HLOAD(s) do { if ((s) % 7 == 3) { const v2u lo_ = ld8(rWS, lane * 8, hb + (s) * 1024), hi_ = ld8(rWS, lane * 8 + 512, hb + (s) * 1024); ha[s] = asv8(v4u{lo_[0], lo_[1], hi_[0], hi_[1]}); } \
                      else ha[s] = asv8(ld16(rWS, lane * 16, hb + (s) * 1024)); } while (0)
#pragma unroll
    for (int s = 0; s < 3; ++s) HLOAD(s);
    const unsigned bw = opq(kh * 16384 + lane * 16), bw2 = opq(bw + 65536), bw3 = opq(bw + 131072);
    f4 acc[4];
#pragma unroll
    for (int nt = 0; nt < 4; ++nt) acc[nt] = f4{0, 0, 0, 0};
#define W2F(nt, S) lds16((((S) >> 2) == 0 || ((S) >> 2) == 5 ? bw + 32768 : ((S) >> 2) == 1 || ((S) >> 2) == 6 ? bw2 : ((S) >> 2) == 2 ? bw2 + 32768 : ((S) >> 2) == 3 ? bw3 : bw) + (((S) & 3) * 4 + (nt)) * 1024)
    {
      v8s W[2][4];
#pragma unroll
      for (int S = 0; S < 2; ++S)
#pragma unroll
        for (int nt = 0; nt < 4; ++nt) W[S][nt] = W2F(nt, S);
      SB();
#pragma unroll
      for (int s = 3; s < 8; ++s) HLOAD(s);
      SB();
#pragma unroll
      for (int S = 0; S < 28; ++S) {
        if (S + 8 < 28) HLOAD(S + 8);
        if (S == 8) BAR();
        if (S >= 8 && S < 16) { const int e = S - 8, pc = w * 8 + e; *(LAS v4u*)(sm + 32768 + (pc >> 5) * 32768 + ((pc >> 4) & 1) * 16384 + (pc & 15) * 1024 + lane * 16) = stg[e]; }
        if (S == 18) BAR();
#pragma unroll
        for (int nt = 0; nt < 4; ++nt) {
          acc[nt] = MFMA16(W[S & 1][nt], ha[S], acc[nt]);
          if (S + 2 < 28) W[S & 1][nt] = W2F(nt, S + 2);
          SB();
        }
      }
    }
    STAMP(22);
    BAR();
    *(LAS f4*)(sm + L_W2X + w * 2048 + lane * 16) = kh ? acc[0] : acc[2];
    *(LAS f4*)(sm + L_W2X + w * 2048 + 1024 + lane * 16) = kh ? acc[1] : acc[3];
    BAR();
    const f4 o0 = ldsf4(L_W2X + (w ^ 4) * 2048 + lane * 16), o1 = ldsf4(L_W2X + (w ^ 4) * 2048 + 1024 + lane * 16);
    const f4 t0 = (kh ? acc[2] : acc[0]) + o0, t1 = (kh ? acc[3] : acc[1]) + o1;
    const float gb[8] = {bflo(gbR[0]), bfhi(gbR[0]), bflo(gbR[1]), bfhi(gbR[1]), bflo(gbR[2]), bfhi(gbR[2]), bflo(gbR[3]), bfhi(gbR[3])};
    const float bb[8] = {bflo(b2R[0][0]), bfhi(b2R[0][0]), bflo(b2R[0][1]), bfhi(b2R[0][1]), bflo(b2R[1][0]), bfhi(b2R[1][0]), bflo(b2R[1][1]), bfhi(b2R[1][1])};
    float o[8];
#pragma unroll
    for (int r = 0; r < 8; ++r) o[r] = x1[r] + gb[r] * ((r < 4 ? t0[r & 3] : t1[r & 3]) + bb[r]);
    auto r0 = __builtin_amdgcn_permlane16_swap(pk(o[0], o[1]), pk(o[4], o[5]), false, false);
    auto r1 = __builtin_amdgcn_permlane16_swap(pk(o[2], o[3]), pk(o[6], o[7]), false, false);
    *(v4u*)(p.out + (size_t)orow * DM + 64 * ct + 32 * kh + (g & 1) * 16 + (g >> 1) * 8) = v4u{r0[0], r1[0], r0[1], r1[1]};
  }
  STAMP(15);
  CSTAMP(41);
  if (w == 0 && (blockIdx.x == 0 || blockIdx.x == 77 || blockIdx.x == 130 || blockIdx.x == 255)) {
    unsigned v[27];
#pragma unroll
    for (int k = 0; k < 27; ++k) v[k] = __builtin_amdgcn_readlane(stampv, k);
    if (lane == 0) printf("WG %d: [3] %.2f [4] %.2f [5] %.2f [7] %.2f [8] %.2f [9] %.2f [10] %.2f [11] %.2f [18] %.2f [13] %.2f [14] %.2f [22] %.2f [15] %.2f clk %.3f\n", blockIdx.x,
      (v[3]-v[0])*0.01f,(v[4]-v[0])*0.01f,(v[5]-v[0])*0.01f,(v[7]-v[0])*0.01f,(v[8]-v[0])*0.01f,(v[9]-v[0])*0.01f,(v[10]-v[0])*0.01f,(v[11]-v[0])*0.01f,(v[18]-v[0])*0.01f,(v[13]-v[0])*0.01f,(v[14]-v[0])*0.01f,(v[22]-v[0])*0.01f,(v[15]-v[0])*0.01f,
      (float)(__builtin_amdgcn_readlane(stampv, 41) - __builtin_amdgcn_readlane(stampv, 40)) / ((v[15]-v[0]) * 10.f));
  }
}

extern "C" void kernel_launch(void* const* d_in, const int* in_sizes, int n_in, void* d_out, int out_size, void* d_ws, size_t ws_size, hipStream_t stream) {
  Params p;
  p.x = (const u16*)d_in[0]; p.wqkv = (const u16*)d_in[1]; p.wo = (const u16*)d_in[2];
  p.w1 = (const u16*)d_in[4]; p.b1 = (const u16*)d_in[5]; p.w2 = (const u16*)d_in[6]; p.b2 = (const u16*)d_in[7];
  p.c = (const u16*)d_in[8]; p.wmod = (const u16*)d_in[9]; p.out = (u16*)d_out; p.ws = (char*)d_ws;
  fused_block<<<256, 512, 0, stream>>>(p);
}
```

```cpp
#include <hip/hip_runtime.h>
#include <hip/hip_bf16.h>
#include <stdint.h>

typedef unsigned short u16;
typedef short v4s __attribute__((ext_vector_type(4)));
typedef short v8s __attribute__((ext_vector_type(8)));
typedef float f2 __attribute__((ext_vector_type(2)));
typedef float f4 __attribute__((ext_vector_type(4)));
typedef float f16v __attribute__((ext_vector_type(16)));
typedef unsigned v2u __attribute__((ext_vector_type(2)));
typedef unsigned v4u __attribute__((ext_vector_type(4)));
typedef __bf16 b2 __attribute__((ext_vector_type(2)));
#define LAS __attribute__((address_space(3)))
#define GAS __attribute__((address_space(1)))
#define DI __device__ __forceinline__
typedef __amdgpu_buffer_rsrc_t srd_t;

constexpr int SEQ = 2048, DM = 512, FF = 1792;
constexpr unsigned WS_EPOCH = 0;
constexpr unsigned WS_F0 = 4096;
constexpr unsigned WS_FP = WS_F0 + 8192;
constexpr unsigned WS_FM = WS_FP + 4 * 16 * 128;
constexpr unsigned WS_FQ = WS_FM + 2048;
constexpr unsigned WS_FW1 = WS_FQ + 2048;
constexpr unsigned WS_FWO = WS_FW1 + 2048;
constexpr unsigned WS_FW2 = WS_FWO + 1024;
static_assert(WS_FW2 + 4096 <= (64 << 10), "flags");
constexpr unsigned WS_WV = 64 << 10;
constexpr unsigned WS_MOD = 128 << 10;
constexpr unsigned WS_ST1 = 192 << 10;
constexpr unsigned WS_ST2A = 512 << 10;
constexpr unsigned WS_ST2B = 1024 << 10;
constexpr unsigned WS_HF = 2u << 20;
constexpr unsigned WS_KV = 4u << 20;
constexpr unsigned WS_OF = 8u << 20;
constexpr unsigned WS_X1F = 10u << 20;
constexpr unsigned WS_H1F = 12u << 20;
constexpr unsigned WS_TQ = 19u << 20;
constexpr unsigned WS_TW1 = WS_TQ + (1536u << 10);
constexpr unsigned WS_TWO = WS_TW1 + (1792u << 10);
constexpr unsigned WS_TW2 = WS_TWO + (512u << 10);
constexpr unsigned WS_STAMP = 25u << 20;
constexpr unsigned WS_BYTES = 27u << 20;
static_assert(WS_TW2 + (1792u << 10) <= WS_STAMP, "ws");
constexpr unsigned L_CA = 98304;
constexpr unsigned L_XS = 131072;
constexpr unsigned L_S1 = L_XS + 8192;
constexpr unsigned L_SO = L_S1 + 7168;
constexpr unsigned L_SC = L_SO + 12288;
constexpr unsigned L_CAP = L_SC + 2048;
constexpr unsigned L_CNT = L_CAP + 512;
static_assert(L_CNT + 16 <= 163840, "lds");
constexpr unsigned L_SQ = 0;
constexpr unsigned L_M2 = 8192;
constexpr unsigned L_WQKV = 0;
constexpr unsigned L_PARTU = 98304;
constexpr unsigned L_PARTV = 32768;
constexpr unsigned L_AK = 98304;
constexpr unsigned KPL = 6144;
constexpr unsigned L_AV = L_AK + 4 * KPL;
constexpr unsigned VPL = 6208, VHD = 12416;
constexpr unsigned L_PSA = L_AK + KPL, L_PSB = L_AK + 3 * KPL, L_PCB = L_AV + VPL, L_PGA = L_AV + VHD + VPL, L_PGB = L_PGA + 1024, L_B2C = L_PGA + 2048;
constexpr unsigned L_PSA2 = L_AK + 2 * KPL, L_PSB2 = L_AV, L_PCB2 = L_AV + VHD;
constexpr unsigned L_FIN = L_AK;
constexpr unsigned L_ONE = L_AV + 2 * VHD;
constexpr unsigned L_AQ = L_ONE + 6400;
constexpr unsigned L_PERS = L_AQ + 8192;
static_assert(L_PERS + 1024 <= 163840, "lds");
static_assert((L_AV % 256) == 0 && (L_ONE % 256) == 0, "bank plan");
constexpr unsigned L_WO = 0;
constexpr unsigned L_W1 = 65536;
constexpr unsigned L_W1S = 0;
constexpr unsigned L_XCH = 16384;
constexpr unsigned L_XF = 16384 + 12288;
constexpr unsigned L_W2X = 98304;

struct Params { const u16* x; const u16* wqkv; const u16* wo; const u16* w1; const u16* b1; const u16* w2; const u16* b2; const u16* c; const u16* wmod; u16* out; char* ws; };

DI float bflo(unsigned v) { return __uint_as_float(v << 16); }
DI float bfhi(unsigned v) { return __uint_as_float(v & 0xffff0000u); }
DI unsigned pk(float a, float b) { f2 v = {a, b}; b2 r = __builtin_convertvector(v, b2); return __builtin_bit_cast(unsigned, r); }
DI float rbf(float a) { return bflo(pk(a, 0.f)); }
DI v8s cat(v4s a, v4s b) { return __builtin_shufflevector(a, b, 0, 1, 2, 3, 4, 5, 6, 7); }
DI v4s trd(unsigned a) { return __builtin_amdgcn_ds_read_tr16_b64_v4i16((LAS v4s*)(uintptr_t)a); }
DI v8s asv8(v4u v) { return __builtin_bit_cast(v8s, v); }
DI v4u asv4u(v8s v) { return __builtin_bit_cast(v4u, v); }
DI v8s lds16(unsigned a) { return *(const LAS v8s*)(uintptr_t)a; }
DI f4 ldsf4(unsigned a) { return *(const LAS f4*)(uintptr_t)a; }
DI v2u lds8(unsigned a) { return *(const LAS v2u*)(uintptr_t)a; }
#define MFMA16(a, b, c) __builtin_amdgcn_mfma_f32_16x16x32_bf16((a), (b), (c), 0, 0, 0)
#define MFMA32(a, b, c) __builtin_amdgcn_mfma_f32_32x32x16_bf16((a), (b), (c), 0, 0, 0)
#define BAR() asm volatile("s_waitcnt lgkmcnt(0)\n\ts_barrier" ::: "memory")
#define VM0() asm volatile("s_waitcnt vmcnt(0)" ::: "memory")
#define LG0() asm volatile("s_waitcnt lgkmcnt(0)" ::: "memory")
#define CFENCE() asm volatile("" ::: "memory")
#define SB() __builtin_amdgcn_sched_barrier(0)
DI unsigned opq(unsigned v) { asm volatile("" : "+v"(v)); return v; }
DI void glds(srd_t r, unsigned voff, unsigned soff, unsigned ldsaddr) {
  asm volatile("s_mov_b32 m0, %0\n\ts_nop 0\n\tbuffer_load_dwordx4 %1, %2, %3 offen lds" ::"s"(ldsaddr), "v"(voff), "s"(r), "s"(soff) : "memory");
}
DI void glds1(srd_t r, unsigned voff, unsigned soff, unsigned ldsaddr) {
  asm volatile("s_mov_b32 m0, %0\n\ts_nop 0\n\tbuffer_load_dwordx4 %1, %2, %3 offen sc1 lds" ::"s"(ldsaddr), "v"(voff), "s"(r), "s"(soff) : "memory");
}
DI void glds4(srd_t r, unsigned voff, unsigned soff, unsigned ldsaddr) {
  asm volatile("s_mov_b32 m0, %0\n\ts_nop 0\n\tbuffer_load_dwordx4 %1, %2, %3 offen lds\n\tbuffer_load_dwordx4 %1, %2, %3 offen offset:1024 lds\n\t"
               "buffer_load_dwordx4 %1, %2, %3 offen offset:2048 lds\n\tbuffer_load_dwordx4 %1, %2, %3 offen offset:3072 lds" ::"s"(ldsaddr), "v"(voff), "s"(r), "s"(soff) : "memory");
}
DI void glds4x1(srd_t r, unsigned voff, unsigned soff, unsigned ldsaddr) {
  asm volatile("s_mov_b32 m0, %0\n\ts_nop 0\n\tbuffer_load_dwordx4 %1, %2, %3 offen sc1 lds\n\tbuffer_load_dwordx4 %1, %2, %3 offen offset:1024 sc1 lds\n\t"
               "buffer_load_dwordx4 %1, %2, %3 offen offset:2048 sc1 lds\n\tbuffer_load_dwordx4 %1, %2, %3 offen offset:3072 sc1 lds" ::"s"(ldsaddr), "v"(voff), "s"(r), "s"(soff) : "memory");
}
DI void glds2x1(srd_t r, unsigned voff, unsigned soff, unsigned ldsaddr) {
  asm volatile("s_mov_b32 m0, %0\n\ts_nop 0\n\tbuffer_load_dwordx4 %1, %2, %3 offen sc1 lds\n\tbuffer_load_dwordx4 %1, %2, %3 offen offset:1024 sc1 lds" ::"s"(ldsaddr), "v"(voff), "s"(r), "s"(soff) : "memory");
}
DI v4u ld16(srd_t r, unsigned voff, unsigned soff) { return __builtin_amdgcn_raw_buffer_load_b128(r, voff, soff, 16); }
DI v2u ld8(srd_t r, unsigned voff, unsigned soff) { return __builtin_amdgcn_raw_buffer_load_b64(r, voff, soff, 16); }
DI v4u ldp16(srd_t r, unsigned voff, unsigned soff) { return __builtin_amdgcn_raw_buffer_load_b128(r, voff, soff, 0); }
DI void stp16(srd_t r, v4u v, unsigned off) { __builtin_amdgcn_raw_buffer_store_b128(v, r, off, 0, 0); }
DI void st16(srd_t r, v4u v, unsigned off) { __builtin_amdgcn_raw_buffer_store_b128(v, r, off, 0, 16); }
DI void st8(srd_t r, v2u v, unsigned off) { __builtin_amdgcn_raw_buffer_store_b64(v, r, off, 0, 16); }
DI float sum16(float v) { auto r = __builtin_amdgcn_permlane16_swap(__float_as_uint(v), __float_as_uint(v), false, false); return __uint_as_float(r[0]) + __uint_as_float(r[1]); }
DI float sum32(float v) { auto r = __builtin_amdgcn_permlane32_swap(__float_as_uint(v), __float_as_uint(v), false, false); return __uint_as_float(r[0]) + __uint_as_float(r[1]); }
DI float max32(float v) { auto r = __builtin_amdgcn_permlane32_swap(__float_as_uint(v), __float_as_uint(v), false, false); return fmaxf(__uint_as_float(r[0]), __uint_as_float(r[1])); }
DI float sumg(float v) { return sum32(sum16(v)); }
#define RLX_AGENT __ATOMIC_RELAXED, __HIP_MEMORY_SCOPE_AGENT
typedef unsigned long long u64;
DI void arrive(char* ws, unsigned off, u64 tag) { (void)__hip_atomic_exchange((GAS u64*)(ws + off), tag, RLX_AGENT); }
DI void wait_words(char* ws, unsigned off, int n, int lane, u64 tag) {
  GAS u64* p = (GAS u64*)(ws + off) + (lane < n ? lane : 0);
  for (unsigned spins = 0; spins < (1u << 17); ++spins)
    if (__all(__hip_atomic_load(p, RLX_AGENT) == tag)) break;
  CFENCE();
}
template <int C> DI float dpp(float v) { return __uint_as_float(__builtin_amdgcn_update_dpp(0u, __float_as_uint(v), C, 0xf, 0xf, false)); }
DI float rowsum16(float v) { v += dpp<0x128>(v); v += dpp<0x124>(v); v += dpp<0x122>(v); v += dpp<0x121>(v); return v; }
#define PIN(x) asm volatile("" : "+v"(x))
#define STAMP(k)
#define STAMP_FLUSH(o)
#define CSTAMP(k)
DI float silu(float v) { return v * __builtin_amdgcn_rcpf(1.f + __builtin_amdgcn_exp2f(-1.4426950408889634f * v)); }

DI v8s trfragA(unsigned img, int half, int lane) {
  const int g = lane >> 4, q = (lane & 15) >> 2, pp = lane & 3;
  const unsigned a = img + (4 * g + q) * 64 + ((half ^ (g & 1)) * 32) + pp * 8;
  return cat(trd(a), trd(a + 1024));
}

template <bool FULL>
DI void attention(int t, int hd, int kt0, int lane, f16v mU, f16v mL, f16v& O, srd_t rWS, unsigned wo_src, unsigned w1_src, int w, const u16* xres, v2u& xr0, v2u& xr1, unsigned& stampv) {
  const int qr = lane & 31, h = lane >> 5, blk = (lane >> 4) & 1, q = (lane & 15) >> 2, pp = lane & 3;
  const v8s Qf = lds16(L_AQ + (hd * 2 + h) * 2048 + (32 * t + qr) * 16);
  const unsigned kb = opq(L_AK + (hd * 2 + h) * KPL + (32 * t + qr) * 16);
  const unsigned vb = opq((blk == 0 ? L_AV + hd * VHD + (pp >> 1) * VPL + (pp & 1) * 8 + (4 * h + q) * 16 : L_ONE + ((hd * 128 + (h ? 0 : 128)) & 255)) + t * 512);
  const f16v zero = {};
#define RIDE(n) do { if ((n) < 2) glds4x1(rWS, lane * 16, wo_src + (w * 2 + (n)) * 4096, L_WO + (w * 2 + (n)) * 4096); \
                     else glds4x1(rWS, lane * 16, w1_src + w * 4096, L_W1 + w * 4096); } while (0)
  if (FULL) {
    v8s Kf[9];
#pragma unroll
    for (int kt = 0; kt < 9; ++kt) Kf[kt] = lds16(kb + kt * 512);
    v8s V0 = cat(trd(vb), trd(vb + 128)), V1 = cat(trd(vb + 256), trd(vb + 384));
    f16v S[2];
    float m0 = -3e38f, m1 = -3e38f;
    S[0] = MFMA32(Kf[0], Qf, mL);
    SB();
#pragma unroll
    for (int kt = 0; kt < 9; ++kt) {
      if (kt < 8) S[(kt + 1) & 1] = MFMA32(Kf[kt + 1], Qf, kt + 1 == 8 ? mU : zero);
      SB();
#pragma unroll
      for (int r = 0; r < 16; r += 4) { m0 = fmaxf(fmaxf(m0, S[kt & 1][r]), S[kt & 1][r + 1]); m1 = fmaxf(fmaxf(m1, S[kt & 1][r + 2]), S[kt & 1][r + 3]); }
      SB();
    }
    const float m = max32(fmaxf(m0, m1));
    STAMP(23);
    f16v nm;
#pragma unroll
    for (int r = 0; r < 16; ++r) { nm[r] = -m; mU[r] -= m; mL[r] -= m; }
    O = zero;
    S[0] = MFMA32(Kf[0], Qf, mL);
    SB();
#pragma unroll
    for (int kt = 0; kt < 9; ++kt) {
      if (kt < 8) S[(kt + 1) & 1] = MFMA32(Kf[kt + 1], Qf, kt + 1 == 8 ? mU : nm);
      if (kt == 0) RIDE(0);
      if (kt == 3) RIDE(1);
      if (kt == 6) RIDE(2);
      SB();
      v4u P0, P1;
#pragma unroll
      for (int r = 0; r < 4; ++r) P0[r] = pk(__builtin_amdgcn_exp2f(S[kt & 1][2 * r]), __builtin_amdgcn_exp2f(S[kt & 1][2 * r + 1]));
      O = MFMA32(V0, asv8(P0), O);
      if (kt < 8) V0 = cat(trd(vb + (kt + 1) * 512), trd(vb + (kt + 1) * 512 + 128));
      if (kt == 2) xr0 = *(const v2u*)xres;
      if (kt == 5) xr1 = *(const v2u*)(xres + 16);
      SB();
#pragma unroll
      for (int r = 0; r < 4; ++r) P1[r] = pk(__builtin_amdgcn_exp2f(S[kt & 1][8 + 2 * r]), __builtin_amdgcn_exp2f(S[kt & 1][8 + 2 * r + 1]));
      O = MFMA32(V1, asv8(P1), O);
      if (kt < 8) V1 = cat(trd(vb + (kt + 1) * 512 + 256), trd(vb + (kt + 1) * 512 + 384));
      SB();
    }
  } else {
#pragma unroll
    for (int n = 0; n < 3; ++n) RIDE(n);
    xr0 = *(const v2u*)xres; xr1 = *(const v2u*)(xres + 16);
    float m = -3e38f;
#pragma unroll
    for (int kt = 0; kt < 9; ++kt) {
      if (kt >= kt0) {
        const f16v S = MFMA32(lds16(kb + kt * 512), Qf, kt == 8 ? mU : zero);
#pragma unroll
        for (int r = 0; r < 16; r += 2) m = fmaxf(fmaxf(m, S[r]), S[r + 1]);
      }
    }
    m = max32(m);
    f16v nm;
#pragma unroll
    for (int r = 0; r < 16; ++r) { nm[r] = -m; mU[r] -= m; }
    O = zero;
#pragma unroll
    for (int kt = 0; kt < 9; ++kt) {
      if (kt >= kt0) {
        const f16v S = MFMA32(lds16(kb + kt * 512), Qf, kt == 8 ? mU : nm);
        v4u P0, P1;
#pragma unroll
        for (int r = 0; r < 4; ++r) {
          P0[r] = pk(__builtin_amdgcn_exp2f(S[2 * r]), __builtin_amdgcn_exp2f(S[2 * r + 1]));
          P1[r] = pk(__builtin_amdgcn_exp2f(S[8 + 2 * r]), __builtin_amdgcn_exp2f(S[8 + 2 * r + 1]));
        }
        const v8s V0 = cat(trd(vb + kt * 512), trd(vb + kt * 512 + 128));
        const v8s V1 = cat(trd(vb + kt * 512 + 256), trd(vb + kt * 512 + 384));
        O = MFMA32(V0, asv8(P0), O);
        O = MFMA32(V1, asv8(P1), O);
      }
    }
  }
#undef RIDE
}

__global__ void __launch_bounds__(512, 2) fused_block(Params p) {
  __shared__ __attribute__((aligned(16))) char smem[163840];
  LAS char* sm = (LAS char*)smem;
  const int tid = threadIdx.x, lane = tid & 63;
  const int w = __builtin_amdgcn_readfirstlane(tid >> 6);
  const int b = blockIdx.x, ci = ((b & 7) >> 1) * 4 + ((b >> 3) & 3), cj = (b & 1) * 8 + (b >> 5);
  const int tok = lane & 15, g = lane >> 4, q4 = (lane & 15) >> 2, pp = lane & 3;
  const int hf = cj & 1, ct = cj >> 1, par = cj & 1;
  const int wq = w < 4 ? w : w - 1;
  unsigned stampv = 0;
  const srd_t rWS = __builtin_amdgcn_make_buffer_rsrc(p.ws, 0, WS_BYTES, 0x00020000);
  const int rA = lane >> 2, pA = lane & 3;
  const unsigned colA = (((pA >> 1) ^ ((rA >> 2) & 1)) * 32) + (pA & 1) * 16;
  const int rB = lane >> 3, pB = lane & 7;
  const unsigned colB = (((pB >> 1) ^ ((rB >> 1) & 3)) * 32) + (pB & 1) * 16;
  STAMP(0);
  CSTAMP(40);

  const bool m2 = b < 160;
  const int msl = b % 40, mkq = b / 40;
  const srd_t rWmod = __builtin_amdgcn_make_buffer_rsrc((void*)p.wmod, 0, DM * 3072 * 2, 0x00020000);
  if (w < 4) {
    __builtin_amdgcn_s_setprio(2);
    const srd_t rX = __builtin_amdgcn_make_buffer_rsrc((void*)p.x, 0, SEQ * DM * 2, 0x00020000);
#pragma unroll
    for (int i0 = 0; i0 < 8; ++i0) { const int ii = (i0 + (ci & 3) * 2 + (cj & 1)) & 7; glds(rWmod, rA * 6144 + colA, (w * 8 + ii) * 16 * 6144 + (512 + 32 * cj) * 2, L_CA + (w * 8 + ii) * 1024); }
#pragma unroll
    for (int u = 0; u < 2; ++u) glds(rX, rA * 1024 + pA * 16, (128 * ci + 16 * (2 * w + u)) * 1024 + 64 * cj, L_XS + (2 * w + u) * 1024);
  }
  if (tid == 256) { *(LAS unsigned*)(uintptr_t)L_CNT = 0u; *(LAS unsigned*)(uintptr_t)(L_CNT + 4) = m2 ? 0u : 1u; *(LAS unsigned*)(uintptr_t)(L_CNT + 8) = 0u; }
  BAR();
#define READ_TAG() do { const v2u ep_ = ld8(rWS, WS_EPOCH, 0); TAG = (((u64)__builtin_amdgcn_readfirstlane(ep_[1]) << 32) | __builtin_amdgcn_readfirstlane(ep_[0])) + 1; } while (0)
  v8s rt[12];
  f16v mU, mL;
  u64 TAG;
  if (w < 4) {
    const v2u ep = ld8(rWS, WS_EPOCH, 0);
    const v4u cr = *(const v4u*)(p.c + 128 * w + 8 * (lane & 15));
    const unsigned sc = L_SC + w * 256;
    if (lane < 16) *(LAS v4u*)(uintptr_t)(sc + lane * 16) = v4u{pk(silu(bflo(cr[0])), silu(bfhi(cr[0]))), pk(silu(bflo(cr[1])), silu(bfhi(cr[1]))), pk(silu(bflo(cr[2])), silu(bfhi(cr[2]))), pk(silu(bflo(cr[3])), silu(bfhi(cr[3])))};
    LG0();
    v8s sf[4];
#pragma unroll
    for (int s = 0; s < 4; ++s) { const v2u lo = lds8(sc + (32 * s + 4 * g) * 2), hi = lds8(sc + (32 * s + 16 + 4 * g) * 2); sf[s] = asv8(v4u{lo[0], lo[1], hi[0], hi[1]}); }
    TAG = (((u64)__builtin_amdgcn_readfirstlane(ep[1]) << 32) | __builtin_amdgcn_readfirstlane(ep[0])) + 1;
    VM0();
    STAMP(1);
    v2u xa[2][2];
#pragma unroll
    for (int u = 0; u < 2; ++u) { const unsigned a = L_XS + (2 * w + u) * 1024 + tok * 64 + g * 8; xa[u][0] = lds8(a); xa[u][1] = lds8(a + 32); }
    f4 a0 = {0, 0, 0, 0}, a1 = {0, 0, 0, 0};
    {
      v8s W[4][2];
#pragma unroll
      for (int s = 0; s < 4; ++s) { W[s][0] = trfragA(L_CA + (4 * w + s) * 2048, 0, lane); W[s][1] = trfragA(L_CA + (4 * w + s) * 2048, 1, lane); }
#pragma unroll
      for (int s = 0; s < 4; ++s) { a0 = MFMA16(W[s][0], sf[s], a0); a1 = MFMA16(W[s][1], sf[s], a1); }
    }
    if (tok == 0) { *(LAS f4*)(uintptr_t)(L_CAP + (w * 32 + 4 * g) * 4) = a0; *(LAS f4*)(uintptr_t)(L_CAP + (w * 32 + 16 + 4 * g) * 4) = a1; }
    LG0();
    if (lane == 0) __hip_atomic_fetch_add((LAS unsigned*)(uintptr_t)L_CNT, 1u, __ATOMIC_RELAXED, __HIP_MEMORY_SCOPE_WORKGROUP);
    while (*(volatile LAS unsigned*)(uintptr_t)L_CNT < 4u) {}
    CFENCE();
    STAMP(19);
    a0 = ldsf4(L_CAP + 4 * g * 4); a1 = ldsf4(L_CAP + (16 + 4 * g) * 4);
#pragma unroll
    for (int e = 1; e < 4; ++e) { a0 += ldsf4(L_CAP + (e * 32 + 4 * g) * 4); a1 += ldsf4(L_CAP + (e * 32 + 16 + 4 * g) * 4); }
    float wv[8];
#pragma unroll
    for (int r = 0; r < 4; ++r) { wv[r] = rbf(1.f + rbf(a0[r])); wv[4 + r] = rbf(1.f + rbf(a1[r])); }
#pragma unroll
    for (int u = 0; u < 2; ++u) {
      const float x0 = bflo(xa[u][0][0]), x1 = bfhi(xa[u][0][0]), x2 = bflo(xa[u][0][1]), x3 = bfhi(xa[u][0][1]);
      const float x4 = bflo(xa[u][1][0]), x5 = bfhi(xa[u][1][0]), x6 = bflo(xa[u][1][1]), x7 = bfhi(xa[u][1][1]);
      const int T = 2 * w + u;
      st16(rWS, v4u{pk(x0 * wv[0], x1 * wv[1]), pk(x2 * wv[2], x3 * wv[3]), pk(x4 * wv[4], x5 * wv[5]), pk(x6 * wv[6], x7 * wv[7])},
           WS_HF + ((ci * 8 + T) * 16 + cj) * 1024 + lane * 16);
      if (u == 0 && w == 0 && tok == 0) {
        st8(rWS, v2u{pk(wv[0], wv[1]), pk(wv[2], wv[3])}, WS_WV + ci * 1024 + cj * 64 + g * 8);
        st8(rWS, v2u{pk(wv[4], wv[5]), pk(wv[6], wv[7])}, WS_WV + ci * 1024 + cj * 64 + 32 + g * 8);
      }
      const float s1 = sumg(((x0 + x1) + (x2 + x3)) + ((x4 + x5) + (x6 + x7)));
      const float s2 = sumg(((x0 * x0 + x1 * x1) + (x2 * x2 + x3 * x3)) + ((x4 * x4 + x5 * x5) + (x6 * x6 + x7 * x7)));
      if (g == 0) st8(rWS, v2u{__float_as_uint(s1), __float_as_uint(s2)}, WS_ST1 + ((ci * 16 + cj) * 128 + 16 * T + tok) * 8);
    }
    STAMP(21);
    VM0();
    if (lane == 0) { arrive(p.ws, WS_F0 + (ci * 64 + cj * 4 + w) * 8, TAG); __hip_atomic_fetch_add((LAS unsigned*)(uintptr_t)(L_CNT + 8), 1u, __ATOMIC_RELAXED, __HIP_MEMORY_SCOPE_WORKGROUP); }
    __builtin_amdgcn_s_setprio(0);
  } else if (w == 4) {
    if (m2) {
      const int mvec = msl >> 3, mcol = (mvec == 0 ? 0 : 512 + 512 * mvec) + (msl & 7) * 64;
      __builtin_amdgcn_s_sleep(8);
#pragma unroll
      for (int ii = 0; ii < 16; ++ii) glds(rWmod, rB * 6144 + colB, (128 * mkq + 8 * ii) * 6144 + mcol * 2, L_M2 + ii * 1024);
      const v4u cr = *(const v4u*)(p.c + 128 * mkq + 8 * (lane & 15));
      const unsigned sc = L_SC + 1024;
      if (lane < 16) *(LAS v4u*)(uintptr_t)(sc + lane * 16) = v4u{pk(silu(bflo(cr[0])), silu(bfhi(cr[0]))), pk(silu(bflo(cr[1])), silu(bfhi(cr[1]))), pk(silu(bflo(cr[2])), silu(bfhi(cr[2]))), pk(silu(bflo(cr[3])), silu(bfhi(cr[3])))};
      LG0();
      v8s sf[4];
#pragma unroll
      for (int s = 0; s < 4; ++s) { const v2u lo = lds8(sc + (32 * s + 4 * g) * 2), hi = lds8(sc + (32 * s + 16 + 4 * g) * 2); sf[s] = asv8(v4u{lo[0], lo[1], hi[0], hi[1]}); }
      VM0();
      f4 a[4];
#pragma unroll
      for (int nt = 0; nt < 4; ++nt) a[nt] = f4{0, 0, 0, 0};
      const int row = 4 * g + q4;
#pragma unroll
      for (int s = 0; s < 4; ++s) {
        v8s W[4];
#pragma unroll
        for (int nt = 0; nt < 4; ++nt) { const unsigned ad = L_M2 + s * 4096 + row * 128 + ((nt ^ ((row >> 1) & 3)) * 32) + pp * 8; W[nt] = cat(trd(ad), trd(ad + 2048)); }
#pragma unroll
        for (int nt = 0; nt < 4; ++nt) a[nt] = MFMA16(W[nt], sf[s], a[nt]);
      }
      if (tok == 0) {
#pragma unroll
        for (int nt = 0; nt < 4; ++nt)
          st16(rWS, v4u{__float_as_uint(a[nt][0]), __float_as_uint(a[nt][1]), __float_as_uint(a[nt][2]), __float_as_uint(a[nt][3])},
               WS_MOD + mkq * 10240 + mvec * 2048 + ((msl & 7) * 2 + (nt >> 1)) * 128 + g * 32 + (nt & 1) * 16);
      }
      LG0();
      if (lane == 0) *(volatile LAS unsigned*)(uintptr_t)(L_CNT + 4) = 1u;
      READ_TAG();
      VM0();
      if (lane == 0) arrive(p.ws, WS_FM + b * 8, TAG);
    } else READ_TAG();
    STAMP(1);
  } else {
    if (w == 5) {
      const srd_t r = __builtin_amdgcn_make_buffer_rsrc((void*)p.wqkv, 0, DM * 1536 * 2, 0x00020000);
#pragma unroll
      for (int e = 0; e < 6; ++e) glds(r, rA * 3072 + colA, (32 * ci + 16 * (e & 1)) * 3072 + ((e >> 1) * 512 + 32 * cj) * 2, L_SQ + e * 1024);
      READ_TAG();
      VM0();
#pragma unroll
      for (int nt = 0; nt < 6; ++nt) st16(rWS, asv4u(trfragA(L_SQ + (nt >> 1) * 2048, nt & 1, lane)), WS_TQ + ((cj * 6 + nt) * 16 + ci) * 1024 + lane * 16);
      VM0();
      if (lane == 0) arrive(p.ws, WS_FQ + (cj * 16 + ci) * 8, TAG);
    } else {
      READ_TAG();
      __builtin_amdgcn_s_sleep(16);
      if (w == 6) {
        const srd_t r1 = __builtin_amdgcn_make_buffer_rsrc((void*)p.w1, 0, DM * FF * 2, 0x00020000);
#pragma unroll
        for (int e = 0; e < 6; ++e) glds(r1, rA * (FF * 2) + colA, (32 * ci + 16 * (e & 1)) * (FF * 2) + (112 * cj + 16 * par + (e >> 1) * 32) * 2, L_S1 + e * 1024);
        glds(r1, (lane >> 1) * (FF * 2) + (lane & 1) * 16, 32 * ci * (FF * 2) + (112 * cj + (par ? 0 : 96)) * 2, L_S1 + 6144);
        VM0();
#pragma unroll
        for (int nt = 0; nt < 6; ++nt) rt[nt] = trfragA(L_S1 + (nt >> 1) * 2048, nt & 1, lane);
        { const unsigned a = L_S1 + 6144 + (4 * g + q4) * 32 + pp * 8; rt[6] = cat(trd(a), trd(a + 512)); }
      } else {
        const srd_t ro = __builtin_amdgcn_make_buffer_rsrc((void*)p.wo, 0, DM * DM * 2, 0x00020000);
#pragma unroll
        for (int e = 0; e < 4; ++e) glds(ro, rB * 1024 + colB, (32 * ((b & 127) >> 3) + 8 * e) * 1024 + (b & 7) * 128, L_SO + e * 1024);
        const srd_t r2 = __builtin_amdgcn_make_buffer_rsrc((void*)p.w2, 0, FF * DM * 2, 0x00020000);
        const int b2 = b < 224 ? b : 0;
#pragma unroll
        for (int e = 0; e < 8; ++e) { const int id = 2 * b2 + (e >> 2); glds(r2, rB * 1024 + colB, (32 * (id >> 3) + 8 * (e & 3)) * 1024 + (id & 7) * 128, L_SO + 4096 + e * 1024); }
        VM0();
        {
          const int row = 16 * (g >> 1) + 4 * (g & 1) + q4;
#pragma unroll
          for (int nt = 0; nt < 4; ++nt) { const unsigned a = L_SO + row * 128 + ((nt ^ ((row >> 1) & 3)) * 32) + pp * 8; rt[8 + nt] = cat(trd(a), trd(a + 1024)); }
        }
        const int row = 4 * g + q4;
#pragma unroll
        for (int e = 0; e < 8; ++e) { const int nt = e & 3; const unsigned a = L_SO + 4096 + (e >> 2) * 4096 + row * 128 + ((nt ^ ((row >> 1) & 3)) * 32) + pp * 8; rt[e] = cat(trd(a), trd(a + 2048)); }
      }
    }
    STAMP(1);
    if (w >= 6) {
      while (*(volatile LAS unsigned*)(uintptr_t)(L_CNT + 8) < 4u) __builtin_amdgcn_s_sleep(1);
      CFENCE();
      if (w == 6) {
#pragma unroll
        for (int e = 0; e < 7; ++e) st16(rWS, asv4u(rt[e]), WS_TW1 + ((cj * 7 + e) * 16 + ci) * 1024 + lane * 16);
      } else {
        if (b < 224) {
#pragma unroll
          for (int e = 0; e < 8; ++e) { const int id = 2 * b + (e >> 2); st16(rWS, asv4u(rt[e]), WS_TW2 + (((id & 7) * 56 + (id >> 3)) * 4 + (e & 3)) * 1024 + lane * 16); }
        }
        if (b < 128) {
#pragma unroll
          for (int e = 0; e < 4; ++e) st16(rWS, asv4u(rt[8 + e]), WS_TWO + (((b & 7) * 16 + (b >> 3)) * 4 + e) * 1024 + lane * 16);
        }
      }
    }
    wait_words(p.ws, WS_FQ + cj * 128, 16, lane, TAG);
    while (*(volatile LAS unsigned*)(uintptr_t)(L_CNT + 4) == 0u) __builtin_amdgcn_s_sleep(1);
    CFENCE();
    STAMP(19);
#pragma unroll
    for (int e = 0; e < 8; ++e) glds4x1(rWS, lane * 16, WS_TQ + cj * 98304 + ((w - 5) * 8 + ((e + 2 * (ci & 3)) & 7)) * 4096, L_WQKV + ((w - 5) * 8 + ((e + 2 * (ci & 3)) & 7)) * 4096);
  }
  {
    const int qr = lane & 31, h = lane >> 5;
#pragma unroll
    for (int r = 0; r < 16; ++r) { const int kr = (r & 3) + 8 * (r >> 2) + 4 * h; mU[r] = kr > qr ? -1e30f : 0.f; mL[r] = kr > qr ? 0.f : -1e30f; }
    asm volatile("" : "+v"(mU), "+v"(mL));
  }
  if (w == 0) wait_words(p.ws, WS_F0 + ci * 512, 64, lane, TAG);
  STAMP(2);
  if (w >= 5) VM0();
  BAR();
  STAMP(3);
  if (lane == 0) {
    if (w == 6) arrive(p.ws, WS_FW1 + (cj * 16 + ci) * 8, TAG);
    if (w == 7 && b < 128) arrive(p.ws, WS_FWO + ((b & 7) * 16 + (b >> 3)) * 8, TAG);
    if (w == 7 && b < 224) { arrive(p.ws, WS_FW2 + (((2 * b) & 7) * 64 + ((2 * b) >> 3)) * 8, TAG); arrive(p.ws, WS_FW2 + (((2 * b + 1) & 7) * 64 + ((2 * b + 1) >> 3)) * 8, TAG); }
  }

  f4 accQ[2], accKV[4];
  float rstd, nmu;
  v4u gaR, cbR, sbR, gbR;
  v2u b2R[2];
  v4u mc0, md0;
  v4u mx[2];
  const int tt = w & 3, kh = w >> 2;
  const int ocol = 64 * ct + 32 * kh + 4 * g;
  {
    const unsigned bq = opq(L_WQKV + lane * 16), bv = opq(L_WQKV + 65536 + lane * 16);
#define QF(nt, s) lds16(((nt) < 4 ? bq : bv) + ((nt) & 3) * 16384 + (s) * 1024)
    v8s act[16];
    const unsigned hsrc = WS_HF + (ci * 8 + w) * 16 * 1024;
    v2u wl[2], wh[2];
#pragma unroll
    for (int u = 0; u < 2; ++u) { const unsigned o = (32 * (2 * w + u) + 4 * g) * 2; wl[u] = ld8(rWS, o, WS_WV + ci * 1024); wh[u] = ld8(rWS, o + 32, WS_WV + ci * 1024); }
#pragma unroll
    for (int s = 0; s < 4; ++s) act[s] = asv8(ld16(rWS, lane * 16, hsrc + s * 1024));
    {
      f4 au[6];
      v8s W[2][6];
      const unsigned c0 = opq(bq + w * 2048), d0 = opq(bv + w * 2048);
#pragma unroll
      for (int u = 0; u < 2; ++u)
#pragma unroll
        for (int nt = 0; nt < 6; ++nt) W[u][nt] = lds16((nt < 4 ? c0 : d0) + (nt & 3) * 16384 + u * 1024);
#pragma unroll
      for (int nt = 0; nt < 6; ++nt) au[nt] = f4{0, 0, 0, 0};
#pragma unroll
      for (int u = 0; u < 2; ++u) {
        const v8s a = asv8(v4u{wl[u][0], wl[u][1], wh[u][0], wh[u][1]});
#pragma unroll
        for (int nt = 0; nt < 6; ++nt) au[nt] = MFMA16(W[u][nt], a, au[nt]);
      }
      if (tok == 0) {
#pragma unroll
        for (int nt = 0; nt < 6; ++nt) *(LAS f4*)(sm + L_PARTU + (w * 96 + nt * 16 + 4 * g) * 4) = au[nt];
      }
    }
    BAR();
    if (tid >= 384 && tid < 480) {
      const int e0 = tid - 384;
      float s = 0;
#pragma unroll
      for (int e = 0; e < 8; ++e) s += *(const LAS float*)(sm + L_PARTU + (e * 96 + e0) * 4);
      *(LAS float*)(sm + L_PERS + e0 * 4) = s;
    }
    STAMP(16);
    v2u st[4];
    u64 fm0 = 0, fm1 = 0, fm2 = 0;
#pragma unroll
    for (int nt = 0; nt < 4; ++nt) accKV[nt] = f4{0, 0, 0, 0};
#pragma unroll
    for (int nt = 0; nt < 2; ++nt) accQ[nt] = f4{0, 0, 0, 0};
    {
      v8s W[6];
#pragma unroll
      for (int nt = 0; nt < 6; ++nt) W[nt] = QF(nt, 0);
      SB();
#pragma unroll
      for (int s = 0; s < 16; ++s) {
        if (s == 0) act[4] = asv8(ld16(rWS, lane * 16, hsrc + 4 * 1024));
        if (s + 5 < 16) act[s + 5] = asv8(ld16(rWS, lane * 16, hsrc + (s + 5) * 1024));
        else if (s < 15) st[s - 11] = ld8(rWS, ((4 * g + s - 11) * 128 + 16 * w + tok) * 8, WS_ST1 + ci * 16 * 128 * 8);
        if (s == 12) { GAS u64* f = (GAS u64*)(p.ws + WS_FM); fm0 = __hip_atomic_load(f + lane, RLX_AGENT); fm1 = __hip_atomic_load(f + 64 + lane, RLX_AGENT); fm2 = __hip_atomic_load(f + 128 + (lane & 31), RLX_AGENT); }
#pragma unroll
        for (int nt = 0; nt < 6; ++nt) {
          if (nt < 2) accQ[nt] = MFMA16(W[nt], act[s], accQ[nt]); else accKV[nt - 2] = MFMA16(W[nt], act[s], accKV[nt - 2]);
          if (s + 1 < 16) W[nt] = QF(nt, s + 1);
          SB();
        }
      }
    }
    float s1 = 0, s2 = 0;
#pragma unroll
    for (int e = 0; e < 4; ++e) { s1 += __uint_as_float(st[e][0]); s2 += __uint_as_float(st[e][1]); }
    s1 = sumg(s1); s2 = sumg(s2);
    const float mu = s1 * (1.f / 512), var = fmaxf(s2 * (1.f / 512) - mu * mu, 0.f);
    rstd = __builtin_amdgcn_rsqf(var + 1e-5f); nmu = -rstd * mu;
    if (w == 2 && lane < 8) *(LAS v4u*)(sm + L_B2C + lane * 16) = *(const v4u*)(p.b2 + 64 * ct + lane * 8);
    PIN(rstd); PIN(nmu);
    {
          GAS u64* f = (GAS u64*)(p.ws + WS_FM);
          for (unsigned spins = 0; spins < (1u << 17) && !__all(fm0 == TAG && fm1 == TAG && fm2 == TAG); ++spins) {
            fm0 = __hip_atomic_load(f + lane, RLX_AGENT); fm1 = __hip_atomic_load(f + 64 + lane, RLX_AGENT); fm2 = __hip_atomic_load(f + 128 + (lane & 31), RLX_AGENT); }
          CFENCE();
          if (w < 6) {
            const int vec = w < 2 ? 0 : w < 4 ? 2 : 3, hq = w & 1;
            const unsigned dst = w == 0 ? L_PSA : w == 1 ? L_PSA2 : w == 2 ? L_PSB : w == 3 ? L_PSB2 : w == 4 ? L_PCB : L_PCB2;
            glds2x1(rWS, lane * 16, WS_MOD + (2 * hq) * 10240 + vec * 2048, dst);
            glds2x1(rWS, lane * 16, WS_MOD + (2 * hq + 1) * 10240 + vec * 2048, dst + 2048);
          } else glds1(rWS, (lane >> 4) * 10240 + (lane & 15) * 16, WS_MOD + (w == 6 ? 1 : 4) * 2048 + ct * 256, w == 6 ? L_PGA : L_PGB);
    }
    STAMP(4);
    BAR();
    {
      v4u kk, vv;
      unsigned pkd[4][2];
#pragma unroll
      for (int nt = 0; nt < 4; ++nt) {
        const f4 u = ldsf4(L_PERS + (32 + nt * 16 + 4 * g) * 4);
        pkd[nt][0] = pk(rstd * accKV[nt][0] + nmu * u[0], rstd * accKV[nt][1] + nmu * u[1]);
        pkd[nt][1] = pk(rstd * accKV[nt][2] + nmu * u[2], rstd * accKV[nt][3] + nmu * u[3]);
      }
      { auto r0 = __builtin_amdgcn_permlane16_swap(pkd[0][0], pkd[1][0], false, false); auto r1 = __builtin_amdgcn_permlane16_swap(pkd[0][1], pkd[1][1], false, false);
        kk = v4u{r0[0], r1[0], r0[1], r1[1]}; }
      { auto r0 = __builtin_amdgcn_permlane16_swap(pkd[2][0], pkd[3][0], false, false); auto r1 = __builtin_amdgcn_permlane16_swap(pkd[2][1], pkd[3][1], false, false);
        vv = v4u{r0[0], r1[0], r0[1], r1[1]}; }
      const int hd = g & 1, pl = g >> 1;
      st16(rWS, kk, WS_KV + (ci * 16 + cj) * 16384 + ((0 * 2 + hd) * 2 + pl) * 2048 + (16 * w + tok) * 16);
      st16(rWS, vv, WS_KV + (ci * 16 + cj) * 16384 + ((1 * 2 + hd) * 2 + pl) * 2048 + (16 * w + tok) * 16);
      *(LAS v4u*)(sm + L_AK + (hd * 2 + pl) * KPL + (256 + 16 * w + tok) * 16) = kk;
      *(LAS v4u*)(sm + L_AV + hd * VHD + pl * VPL + (256 + 16 * w + tok) * 16) = vv;
    }
    VM0();
    BAR();
    if (tid == 256) arrive(p.ws, WS_FP + (0 * 16 + ci) * 128 + cj * 8, TAG);
    STAMP(5);
    if (tid < 400) *(LAS v4u*)(sm + L_ONE + tid * 16) = v4u{0x3f803f80u, 0x3f803f80u, 0x3f803f80u, 0x3f803f80u};
    if (tid < 416) {
      const int seg = tid < 384 ? tid >> 7 : 3 + ((tid - 384) >> 4), e = tid < 384 ? tid & 127 : (tid - 384) & 15;
      f4 v;
      if (seg < 3) {
        const unsigned a0 = (seg == 0 ? L_PSA : seg == 1 ? L_PSB : L_PCB) + e * 16, a1 = (seg == 0 ? L_PSA2 : seg == 1 ? L_PSB2 : L_PCB2) + e * 16;
        v = (ldsf4(a0) + ldsf4(a0 + 2048)) + (ldsf4(a1) + ldsf4(a1 + 2048));
      } else {
        const unsigned a0 = (seg == 3 ? L_PGA : L_PGB) + e * 16;
        v = (ldsf4(a0) + ldsf4(a0 + 256)) + (ldsf4(a0 + 512) + ldsf4(a0 + 768));
      }
      *(LAS v2u*)(sm + L_FIN + (seg < 3 ? seg * 1024 : 3072 + (seg - 3) * 128) + e * 8) = v2u{pk(v[0], v[1]), pk(v[2], v[3])};
    }
    BAR();
    {
      f4 av[4];
      v8s W[2][4], a[2];
      const unsigned c0 = opq(bq + w * 2048), d0 = opq(bv + w * 2048);
#pragma unroll
      for (int u = 0; u < 2; ++u) {
        a[u] = lds16(L_FIN + (2 * w + u) * 64 + g * 16);
#pragma unroll
        for (int nt = 0; nt < 4; ++nt) W[u][nt] = lds16((nt < 2 ? c0 : d0) + (nt & 1) * 16384 + u * 1024);
      }
#pragma unroll
      for (int nt = 0; nt < 4; ++nt) av[nt] = f4{0, 0, 0, 0};
#pragma unroll
      for (int u = 0; u < 2; ++u)
#pragma unroll
        for (int nt = 0; nt < 4; ++nt) av[nt] = MFMA16(W[u][nt], a[u], av[nt]);
      if (tok == 0) {
#pragma unroll
        for (int nt = 0; nt < 4; ++nt) *(LAS f4*)(sm + L_PARTV + (w * 64 + nt * 16 + 4 * g) * 4) = av[nt];
      }
    }
    {
      gaR = asv4u(lds16(L_FIN + 3072 + kh * 64 + g * 16)); gbR = asv4u(lds16(L_FIN + 3200 + kh * 64 + g * 16));
      sbR = asv4u(lds16(L_FIN + 1024 + (2 * ct + kh) * 64 + g * 16)); cbR = asv4u(lds16(L_FIN + 2048 + (2 * ct + kh) * 64 + g * 16));
#pragma unroll
      for (int e = 0; e < 2; ++e) b2R[e] = lds8(L_B2C + (32 * kh + 4 * g + 16 * e) * 2);
      mc0 = asv4u(lds16(L_FIN + 2048 + 16 * lane)); md0 = asv4u(lds16(L_FIN + 1024 + 16 * lane));
#pragma unroll
      for (int u = 0; u < 2; ++u) mx[u] = asv4u(lds16(L_FIN + (tok == 1 ? 1024 : 2048) + (2 * w + u) * 64 + g * 16));
      BAR();
      if (tid < 64) {
        float s = 0;
#pragma unroll
        for (int e = 0; e < 8; ++e) s += *(const LAS float*)(sm + L_PARTV + (e * 64 + tid) * 4);
        *(LAS float*)(sm + L_PERS + (96 + tid) * 4) = s;
      }
    }
    if (ci < 2) {
      for (int e = tid; e < 8 * 256; e += 512) {
        const int pln = e >> 8, key = e & 255;
        if (ci == 0 || key < 128) {
          const unsigned a = pln < 4 ? L_AK + pln * KPL : L_AV + ((pln - 4) >> 1) * VHD + (pln & 1) * VPL;
          *(LAS v4u*)(sm + a + key * 16) = v4u{0, 0, 0, 0};
        }
      }
    }
    if (w == 4) {
      GAS u64* f0 = (GAS u64*)(p.ws + (lane == 0 && ci >= 1 ? WS_FP + (ci - 1) * 128 + cj * 8 : lane == 1 && ci >= 2 ? WS_FP + (ci - 2) * 128 + cj * 8
                                       : lane >= 16 && lane < 32 ? WS_FW1 + (cj * 16 + lane - 16) * 8 : WS_FWO + (ct * 16 + (lane & 15)) * 8));
      GAS u64* f1 = (GAS u64*)(p.ws + WS_FW2 + ct * 512) + (lane < 56 ? lane : 0);
      for (unsigned spins = 0; spins < (1u << 17); ++spins) {
        const u64 a = __hip_atomic_load(f0, RLX_AGENT), c = __hip_atomic_load(f1, RLX_AGENT);
        if (__all(a == TAG && c == TAG)) break;
      }
      CFENCE();
    }
    BAR();
    STAMP(6);
    {
      const unsigned dst = w < 4 ? L_AK + w * KPL : L_AV + ((w - 4) >> 1) * VHD + (w & 1) * VPL;
#pragma unroll
      for (int src = 0; src < 2; ++src)
        if (ci - 2 + src >= 0) glds2x1(rWS, lane * 16, WS_KV + ((ci - 2 + src) * 16 + cj) * 16384 + w * 2048, dst + src * 2048);
    }
    {
      const float qs = 0.25f * 1.4426950408889634f;
      unsigned pq[2][2];
#pragma unroll
      for (int nt = 0; nt < 2; ++nt) {
        const f4 u = ldsf4(L_PERS + (nt * 16 + 4 * g) * 4), v = ldsf4(L_PERS + (96 + nt * 16 + 4 * g) * 4);
        pq[nt][0] = pk(qs * (rstd * accQ[nt][0] + nmu * u[0] + v[0]), qs * (rstd * accQ[nt][1] + nmu * u[1] + v[1]));
        pq[nt][1] = pk(qs * (rstd * accQ[nt][2] + nmu * u[2] + v[2]), qs * (rstd * accQ[nt][3] + nmu * u[3] + v[3]));
      }
      auto r0 = __builtin_amdgcn_permlane16_swap(pq[0][0], pq[1][0], false, false); auto r1 = __builtin_amdgcn_permlane16_swap(pq[0][1], pq[1][1], false, false);
      *(LAS v4u*)(sm + L_AQ + ((g & 1) * 2 + (g >> 1)) * 2048 + (16 * w + tok) * 16) = v4u{r0[0], r1[0], r0[1], r1[1]};
    }
    VM0();
    BAR();
    STAMP(7);
  }

  const int orow = 128 * ci + 64 * hf + 16 * tt + tok;
  v2u xr0, xr1;
  {
    const int t = w & 3, hd = w >> 2;
    const int kt0 = ci >= 2 ? 0 : 8 - 4 * ci - t;
    f16v O;
    const u16* xres = p.x + (size_t)orow * DM + ocol;
    if (ci >= 2) attention<true>(t, hd, 0, lane, mU, mL, O, rWS, WS_TWO + ct * 65536, WS_TW1 + cj * 7 * 16384, w, xres, xr0, xr1, stampv);
    else attention<false>(t, hd, kt0, lane, mU, mL, O, rWS, WS_TWO + ct * 65536, WS_TW1 + cj * 7 * 16384, w, xres, xr0, xr1, stampv);
    const int h = lane >> 5;
    const float inv = __builtin_amdgcn_rcpf(O[8]);
    const f4 b0 = ldsf4(L_PERS + (96 + 32 + hd * 16 + 4 * h) * 4), b1 = ldsf4(L_PERS + (96 + 32 + hd * 16 + 8 + 4 * h) * 4);
    const v4u o = {pk(O[0] * inv + b0[0], O[1] * inv + b0[1]), pk(O[2] * inv + b0[2], O[3] * inv + b0[3]),
                   pk(O[4] * inv + b1[0], O[5] * inv + b1[1]), pk(O[6] * inv + b1[2], O[7] * inv + b1[3])};
    const int qr = lane & 31;
    st16(rWS, o, WS_OF + (ci * 8 * 16 + cj) * 1024 + ((2 * t + (qr >> 4)) * 16) * 1024 + ((2 * hd + h) * 16 + (qr & 15)) * 16);
  }
  STAMP(24);
  VM0();
  BAR();
  if (tid == 256) arrive(p.ws, WS_FP + (1 * 16 + ci) * 128 + cj * 8, TAG);
  STAMP(8);
  float b1v = 0.f;
  v8s aux2[2];
  float Cw = 0, Cw2 = 0, Cws = 0, Cs = 0, Cs2 = 0;
#define MODSUMS() do { \
      _Pragma("unroll") for (int r = 0; r < 4; ++r) { \
        const float w0 = rbf(1.f + bflo(mc0[r])), w1 = rbf(1.f + bfhi(mc0[r])), s0 = bflo(md0[r]), s1_ = bfhi(md0[r]); \
        Cw += w0 + w1; Cw2 += w0 * w0 + w1 * w1; Cws += w0 * s0 + w1 * s1_; Cs += s0 + s1_; Cs2 += s0 * s0 + s1_ * s1_; } \
      Cw = sumg(rowsum16(Cw)); Cw2 = sumg(rowsum16(Cw2)); Cws = sumg(rowsum16(Cws)); Cs = sumg(rowsum16(Cs)); Cs2 = sumg(rowsum16(Cs2)); \
      _Pragma("unroll") for (int u = 0; u < 2; ++u) { v4u t; \
        _Pragma("unroll") for (int d = 0; d < 4; ++d) t[d] = tok == 0 ? pk(1.f + bflo(mx[u][d]), 1.f + bfhi(mx[u][d])) : (tok == 1 ? mx[u][d] : 0x3f803f80u); \
        aux2[u] = asv8(t); } \
      asm volatile("" : "+v"(Cw), "+v"(Cw2), "+v"(Cws), "+v"(Cs), "+v"(Cs2), "+v"(aux2[0]), "+v"(aux2[1])); } while (0)
  if (w != 4) {
#pragma unroll
    for (int e = 0; e < 3; ++e) { const int gi = wq + 7 * e; if (gi < 16) glds4x1(rWS, lane * 16, WS_TW1 + (cj * 7 + 2) * 16384 + gi * 4096, L_W1 + 32768 + gi * 4096); }
  }
  MODSUMS();
  if (w == 4) wait_words(p.ws, WS_FP + (1 * 16 + ci) * 128, 16, lane, TAG);
  BAR();
  STAMP(9);

  float x1[8];
  {
    v8s oa[8];
    const unsigned osrc = WS_OF + ((ci * 8 + 4 * hf + tt) * 16 + 8 * kh) * 1024;
#pragma unroll
    for (int s = 0; s < 3; ++s) oa[s] = asv8(ld16(rWS, lane * 16, osrc + s * 1024));
    const unsigned bo = opq(L_WO + kh * 32768 + lane * 16);
    f4 acc[4];
#pragma unroll
    for (int nt = 0; nt < 4; ++nt) acc[nt] = f4{0, 0, 0, 0};
    {
      v8s W[2][4];
#pragma unroll
      for (int s = 0; s < 2; ++s)
#pragma unroll
        for (int nt = 0; nt < 4; ++nt) W[s][nt] = lds16(bo + (s * 4 + nt) * 1024);
      SB();
#pragma unroll
      for (int s = 3; s < 8; ++s) oa[s] = asv8(ld16(rWS, lane * 16, osrc + s * 1024));
      if (tid < 112) { const int nt = tid >> 4; b1v = bflo(p.b1[(nt == 6 ? 112 * cj + (par ? 0 : 96) : 112 * cj + 16 * par + nt * 16) + (tid & 15)]); }
      SB();
#pragma unroll
      for (int s = 0; s < 8; ++s)
#pragma unroll
        for (int nt = 0; nt < 4; ++nt) {
          acc[nt] = MFMA16(W[s & 1][nt], oa[s], acc[nt]);
          if (s + 2 < 8) W[s & 1][nt] = lds16(bo + ((s + 2) * 4 + nt) * 1024);
          SB();
        }
    }
    STAMP(26);
    VM0();
    BAR();
    { const int gi = w;
      if (gi < 4) glds4x1(rWS, lane * 16, WS_TW1 + (cj * 7 + 6) * 16384 + gi * 4096, L_W1S + gi * 4096);
      else glds4x1(rWS, lane * 16, WS_TW2 + ct * 229376 + (gi - 4) * 4096, 32768 + (gi - 4) * 4096);
      if (w < 4) glds4x1(rWS, lane * 16, WS_TW2 + ct * 229376 + 7 * 16384 + w * 4096, 49152 + w * 4096); }
    *(LAS f4*)(sm + L_XCH + w * 2048 + lane * 16) = kh ? acc[0] : acc[2];
    *(LAS f4*)(sm + L_XCH + w * 2048 + 1024 + lane * 16) = kh ? acc[1] : acc[3];
    BAR();
    const f4 o0 = ldsf4(L_XCH + (w ^ 4) * 2048 + lane * 16), o1 = ldsf4(L_XCH + (w ^ 4) * 2048 + 1024 + lane * 16);
    const f4 t0 = (kh ? acc[2] : acc[0]) + o0, t1 = (kh ? acc[3] : acc[1]) + o1;
    const float xin[8] = {bflo(xr0[0]), bfhi(xr0[0]), bflo(xr0[1]), bfhi(xr0[1]), bflo(xr1[0]), bfhi(xr1[0]), bflo(xr1[1]), bfhi(xr1[1])};
    const float ga[8] = {bflo(gaR[0]), bfhi(gaR[0]), bflo(gaR[1]), bfhi(gaR[1]), bflo(gaR[2]), bfhi(gaR[2]), bflo(gaR[3]), bfhi(gaR[3])};
    const float cb[8] = {bflo(cbR[0]), bfhi(cbR[0]), bflo(cbR[1]), bfhi(cbR[1]), bflo(cbR[2]), bfhi(cbR[2]), bflo(cbR[3]), bfhi(cbR[3])};
    const float sv[8] = {bflo(sbR[0]), bfhi(sbR[0]), bflo(sbR[1]), bfhi(sbR[1]), bflo(sbR[2]), bfhi(sbR[2]), bflo(sbR[3]), bfhi(sbR[3])};
    float wv[8], xw[8];
#pragma unroll
    for (int r = 0; r < 8; ++r) { x1[r] = rbf(xin[r] + ga[r] * (r < 4 ? t0[r & 3] : t1[r & 3])); wv[r] = rbf(1.f + cb[r]); xw[r] = x1[r] * wv[r]; }
    st16(rWS, v4u{pk(xw[0], xw[1]), pk(xw[2], xw[3]), pk(xw[4], xw[5]), pk(xw[6], xw[7])}, WS_X1F + ((ci * 8 + 4 * hf + tt) * 16 + 2 * ct + kh) * 1024 + lane * 16);
    SB();
    float s1 = 0, s2 = 0, s3 = 0, s4 = 0, s5 = 0, s6 = 0;
#pragma unroll
    for (int r = 0; r < 8; ++r) { s1 += x1[r]; s2 += x1[r] * x1[r]; s3 += xw[r]; s4 += xw[r] * wv[r]; s5 += xw[r] * xw[r]; s6 += xw[r] * sv[r]; }
    s1 = sumg(s1); s2 = sumg(s2); s3 = sumg(s3); s4 = sumg(s4); s5 = sumg(s5); s6 = sumg(s6);
    const unsigned rec = (ci * 16 + 2 * ct + kh) * 128 + 64 * hf + 16 * tt + tok;
    if (g == 0) st16(rWS, v4u{__float_as_uint(s1), __float_as_uint(s2), __float_as_uint(s3), __float_as_uint(s4)}, WS_ST2A + rec * 16);
    if (g == 1) st8(rWS, v2u{__float_as_uint(s5), __float_as_uint(s6)}, WS_ST2B + rec * 8);
  }
  VM0();
  BAR();
  if (tid == 256) arrive(p.ws, WS_FP + (2 * 16 + ci) * 128 + cj * 8, TAG);
  STAMP(10);

  v4u stg[8];
  {
    const unsigned bP = opq(L_W1 + lane * 16), bR = opq(L_W1 + 65536 + lane * 16), bS = opq(L_W1S + lane * 16);
#define W1F(nt, s) lds16((nt) == 6 ? bS + (s) * 1024 : (nt) < 4 ? bP + (nt) * 16384 + (s) * 1024 : bR + ((nt) - 4) * 16384 + (s) * 1024)
    {
      f4 au[7];
#pragma unroll
      for (int nt = 0; nt < 7; ++nt) au[nt] = f4{0, 0, 0, 0};
      const unsigned c0 = opq(bP + w * 2048), d0 = opq(bR + w * 2048), e0 = opq(bS + w * 2048);
#pragma unroll
      for (int u = 0; u < 2; ++u) {
        v8s W[7];
#pragma unroll
        for (int nt = 0; nt < 7; ++nt) W[nt] = lds16(nt == 6 ? e0 + u * 1024 : nt < 4 ? c0 + nt * 16384 + u * 1024 : d0 + (nt - 4) * 16384 + u * 1024);
#pragma unroll
        for (int nt = 0; nt < 7; ++nt) au[nt] = MFMA16(W[nt], aux2[u], au[nt]);
      }
      if (tok < 3) {
#pragma unroll
        for (int nt = 0; nt < 7; ++nt) *(LAS f4*)(sm + L_XCH + ((w * 3 + tok) * 112 + nt * 16 + 4 * g) * 4) = au[nt];
      }
    }
    BAR();
    if (tid < 112) *(LAS float*)(sm + L_XF + (336 + tid) * 4) = b1v;
    {
      const int e0 = w < 4 ? tid : tid - 64;
      if (w != 4 && e0 < 336) {
        float s = 0;
#pragma unroll
        for (int e = 0; e < 8; ++e) s += *(const LAS float*)(sm + L_XCH + (e * 336 + e0) * 4);
        *(LAS float*)(sm + L_XF + e0 * 4) = s;
      }
    }
    if (w == 4) wait_words(p.ws, WS_FP + (2 * 16 + ci) * 128, 16, lane, TAG);
    BAR();
    STAMP(11);
    v8s act2[16];
    const unsigned asrc = WS_X1F + (ci * 8 + w) * 16 * 1024;
#pragma unroll
    for (int s = 0; s < 4; ++s) act2[s] = asv8(ld16(rWS, lane * 16, asrc + s * 1024));
    v4u sa[4]; v2u sb[4];
    f4 acc[7];
#pragma unroll
    for (int nt = 0; nt < 7; ++nt) acc[nt] = f4{0, 0, 0, 0};
    {
      v8s W[6];
#pragma unroll
      for (int nt = 0; nt < 6; ++nt) W[nt] = W1F(nt, 0);
      SB();
#pragma unroll
      for (int s = 0; s < 16; ++s) {
        if (s == 0) act2[4] = asv8(ld16(rWS, lane * 16, asrc + 4 * 1024));
        if (s + 5 < 16) act2[s + 5] = asv8(ld16(rWS, lane * 16, asrc + (s + 5) * 1024));
        else if (s < 15) { sa[s - 11] = ld16(rWS, ((4 * g + s - 11) * 128 + 16 * w + tok) * 16, WS_ST2A + ci * 16 * 128 * 16);
                           sb[s - 11] = ld8(rWS, ((4 * g + s - 11) * 128 + 16 * w + tok) * 8, WS_ST2B + ci * 16 * 128 * 8); }
#pragma unroll
        for (int nt = 0; nt < 6; ++nt) {
          acc[nt] = MFMA16(W[nt], act2[s], acc[nt]);
          if (s + 1 < 16) W[nt] = W1F(nt, s + 1);
          SB();
        }
      }
    }
    STAMP(18);
    CSTAMP(42);
    v8s WB[4];
#pragma unroll
    for (int s = 0; s < 4; ++s) WB[s] = W1F(6, s);
    float s1 = 0, s2 = 0, s3 = 0, s4 = 0, s5 = 0, s6 = 0;
#pragma unroll
    for (int e = 0; e < 4; ++e) { s1 += __uint_as_float(sa[e][0]); s2 += __uint_as_float(sa[e][1]); s3 += __uint_as_float(sa[e][2]); s4 += __uint_as_float(sa[e][3]); s5 += __uint_as_float(sb[e][0]); s6 += __uint_as_float(sb[e][1]); }
    s1 = sumg(s1); s2 = sumg(s2); s3 = sumg(s3); s4 = sumg(s4); s5 = sumg(s5); s6 = sumg(s6);
    const float mu1 = s1 * (1.f / 512), var1 = fmaxf(s2 * (1.f / 512) - mu1 * mu1, 0.f), r1 = __builtin_amdgcn_rsqf(var1 + 1e-5f);
    const float mu2 = (r1 * (s3 - mu1 * Cw) + Cs) * (1.f / 512);
    const float hh = (r1 * r1 * (s5 - 2.f * mu1 * s4 + mu1 * mu1 * Cw2) + 2.f * r1 * (s6 - mu1 * Cws) + Cs2) * (1.f / 512);
    const float r2 = __builtin_amdgcn_rsqf(fmaxf(hh - mu2 * mu2, 0.f) + 1e-5f);
    const float cA = r2 * r1, cB = -cA * mu1, cC = r2, cD = -r2 * mu2;
    const unsigned S0 = (7 * cj + par) >> 1;
    const unsigned hb = WS_H1F + (ci * 8 + w) * 56 * 1024;
    const unsigned xf = opq(L_XF + 16 * g);
#define BIAS4(nt) __builtin_elementwise_fma(f4{cB, cB, cB, cB}, ldsf4(xf + (nt) * 64), __builtin_elementwise_fma(f4{cC, cC, cC, cC}, ldsf4(xf + 448 + (nt) * 64), \
                    __builtin_elementwise_fma(f4{cD, cD, cD, cD}, ldsf4(xf + 896 + (nt) * 64), ldsf4(xf + 1344 + (nt) * 64))))
#define GELU4(o, a, bias) do { const f4 z_ = __builtin_elementwise_fma(f4{cA, cA, cA, cA}, a, bias); const f4 q_ = __builtin_elementwise_fma(z_ * z_, f4{-0.1029432397f, -0.1029432397f, -0.1029432397f, -0.1029432397f}, \
        f4{-2.3022081985f, -2.3022081985f, -2.3022081985f, -2.3022081985f}) * z_; f4 e_; \
        _Pragma("unroll") for (int r = 0; r < 4; ++r) e_[r] = __builtin_amdgcn_exp2f(q_[r]); \
        e_ += f4{1.f, 1.f, 1.f, 1.f}; \
        _Pragma("unroll") for (int r = 0; r < 4; ++r) e_[r] = __builtin_amdgcn_rcpf(e_[r]); \
        o = z_ * e_; } while (0)
    {
      unsigned hp[2][2];
      f4 bias = BIAS4(0);
      PIN(bias);
      SB();
#pragma unroll
      for (int nt = 0; nt < 6; ++nt) {
#pragma unroll
        for (int k = (nt * 16) / 6; k < ((nt + 1) * 16) / 6; ++k) { acc[6] = MFMA16(WB[k & 3], act2[k], acc[6]); if (k + 4 < 16) WB[k & 3] = W1F(6, k + 4); }
        if (nt >= 1 && nt < 5) {
#pragma unroll
          for (int e = 2 * (nt - 1); e < 2 * nt; ++e) { const int pc = w * 8 + e; const int chunk = (pc >> 4) == 0 ? 5 : ((pc >> 4) == 1 ? 12 : ((pc >> 4) == 2 ? 6 : 13));
            stg[e] = ld16(rWS, lane * 16, WS_TW2 + ct * 229376 + chunk * 16384 + (pc & 15) * 1024); }
        }
        f4 nb = bias;
        if (nt < 5) nb = BIAS4(nt + 1); else nb = BIAS4(6);
        f4 o;
        GELU4(o, acc[nt], bias);
        hp[nt & 1][0] = pk(o[0], o[1]); hp[nt & 1][1] = pk(o[2], o[3]);
        PIN(hp[nt & 1][0]); PIN(hp[nt & 1][1]); PIN(nb);
        bias = nb;
        if (nt & 1) st16(rWS, v4u{hp[0][0], hp[0][1], hp[1][0], hp[1][1]}, hb + (S0 + (nt >> 1)) * 1024 + lane * 16);
        SB();
      }
      STAMP(20);
      CSTAMP(43);
      f4 o;
      GELU4(o, acc[6], bias);
      const unsigned NT = 7 * cj + (par ? 0 : 6);
      st8(rWS, v2u{pk(o[0], o[1]), pk(o[2], o[3])}, hb + (NT >> 1) * 1024 + (NT & 1) * 512 + lane * 8);
    }
  }
  STAMP(25);
  VM0();
  BAR();
  if (tid == 256) arrive(p.ws, WS_FP + (3 * 16 + ci) * 128 + cj * 8, TAG);
  STAMP(13);
  if (w != 4) {
#pragma unroll
    for (int e = 0; e < 5; ++e) {
      const int gi = wq + 7 * e;
      if (gi < 32) { const int it = gi >> 3, khh = (gi >> 2) & 1, pc = gi & 3;
        glds4x1(rWS, lane * 16, WS_TW2 + ct * 229376 + (khh * 7 + 1 + it) * 16384 + pc * 4096, (it == 3 ? 0u : 65536u + it * 32768u) + khh * 16384u + pc * 4096u); }
    }
  } else {
    if (b == 0) {
      GAS u64* f = (GAS u64*)(p.ws + WS_F0) + lane;
      for (unsigned spins = 0; spins < (1u << 17); ++spins) {
        bool ok = true;
#pragma unroll
        for (int i = 0; i < 16; ++i) ok &= __hip_atomic_load(f + i * 64, RLX_AGENT) == TAG;
        if (__all(ok)) break;
      }
      if (lane == 0) __hip_atomic_store((GAS u64*)(p.ws + WS_EPOCH), TAG, RLX_AGENT);
    }
    wait_words(p.ws, WS_FP + (3 * 16 + ci) * 128, 16, lane, TAG);
  }
  VM0();
  BAR();
  STAMP(14);

  {
    v8s ha[28];
    const unsigned hb = WS_H1F + ((ci * 8 + 4 * hf + tt) * 56 + 28 * kh) * 1024;
#define HLOAD(s) do { if ((s) % 7 == 3) { const v2u lo_ = ld8(rWS, lane * 8, hb + (s) * 1024), hi_ = ld8(rWS, lane * 8 + 512, hb + (s) * 1024); ha[s] = asv8(v4u{lo_[0], lo_[1], hi_[0], hi_[1]}); } \
                      else ha[s] = asv8(ld16(rWS, lane * 16, hb + (s) * 1024)); } while (0)
#pragma unroll
    for (int s = 0; s < 3; ++s) HLOAD(s);
    const unsigned bw = opq(kh * 16384 + lane * 16), bw2 = opq(bw + 65536), bw3 = opq(bw + 131072);
    f4 acc[4];
#pragma unroll
    for (int nt = 0; nt < 4; ++nt) acc[nt] = f4{0, 0, 0, 0};
#define W2F(nt, S) lds16((((S) >> 2) == 0 || ((S) >> 2) == 5 ? bw + 32768 : ((S) >> 2) == 1 || ((S) >> 2) == 6 ? bw2 : ((S) >> 2) == 2 ? bw2 + 32768 : ((S) >> 2) == 3 ? bw3 : bw) + (((S) & 3) * 4 + (nt)) * 1024)
    {
      v8s W[2][4];
#pragma unroll
      for (int S = 0; S < 2; ++S)
#pragma unroll
        for (int nt = 0; nt < 4; ++nt) W[S][nt] = W2F(nt, S);
      SB();
#pragma unroll
      for (int s = 3; s < 8; ++s) HLOAD(s);
      SB();
#pragma unroll
      for (int S = 0; S < 28; ++S) {
        if (S + 8 < 28) HLOAD(S + 8);
        if (S == 8) BAR();
        if (S >= 8 && S < 16) { const int e = S - 8, pc = w * 8 + e; *(LAS v4u*)(sm + 32768 + (pc >> 5) * 32768 + ((pc >> 4) & 1) * 16384 + (pc & 15) * 1024 + lane * 16) = stg[e]; }
        if (S == 18) BAR();
#pragma unroll
        for (int nt = 0; nt < 4; ++nt) {
          acc[nt] = MFMA16(W[S & 1][nt], ha[S], acc[nt]);
          if (S + 2 < 28) W[S & 1][nt] = W2F(nt, S + 2);
          SB();
        }
      }
    }
    STAMP(22);
    BAR();
    *(LAS f4*)(sm + L_W2X + w * 2048 + lane * 16) = kh ? acc[0] : acc[2];
    *(LAS f4*)(sm + L_W2X + w * 2048 + 1024 + lane * 16) = kh ? acc[1] : acc[3];
    BAR();
    const f4 o0 = ldsf4(L_W2X + (w ^ 4) * 2048 + lane * 16), o1 = ldsf4(L_W2X + (w ^ 4) * 2048 + 1024 + lane * 16);
    const f4 t0 = (kh ? acc[2] : acc[0]) + o0, t1 = (kh ? acc[3] : acc[1]) + o1;
    const float gb[8] = {bflo(gbR[0]), bfhi(gbR[0]), bflo(gbR[1]), bfhi(gbR[1]), bflo(gbR[2]), bfhi(gbR[2]), bflo(gbR[3]), bfhi(gbR[3])};
    const float bb[8] = {bflo(b2R[0][0]), bfhi(b2R[0][0]), bflo(b2R[0][1]), bfhi(b2R[0][1]), bflo(b2R[1][0]), bfhi(b2R[1][0]), bflo(b2R[1][1]), bfhi(b2R[1][1])};
    float o[8];
#pragma unroll
    for (int r = 0; r < 8; ++r) o[r] = x1[r] + gb[r] * ((r < 4 ? t0[r & 3] : t1[r & 3]) + bb[r]);
    auto r0 = __builtin_amdgcn_permlane16_swap(pk(o[0], o[1]), pk(o[4], o[5]), false, false);
    auto r1 = __builtin_amdgcn_permlane16_swap(pk(o[2], o[3]), pk(o[6], o[7]), false, false);
    *(v4u*)(p.out + (size_t)orow * DM + 64 * ct + 32 * kh + (g & 1) * 16 + (g >> 1) * 8) = v4u{r0[0], r1[0], r0[1], r1[1]};
  }
  STAMP(15);
  CSTAMP(41);
  STAMP_FLUSH(0);
}

extern "C" void kernel_launch(void* const* d_in, const int* in_sizes, int n_in, void* d_out, int out_size, void* d_ws, size_t ws_size, hipStream_t stream) {
  Params p;
  p.x = (const u16*)d_in[0]; p.wqkv = (const u16*)d_in[1]; p.wo = (const u16*)d_in[2];
  p.w1 = (const u16*)d_in[4]; p.b1 = (const u16*)d_in[5]; p.w2 = (const u16*)d_in[6]; p.b2 = (const u16*)d_in[7];
  p.c = (const u16*)d_in[8]; p.wmod = (const u16*)d_in[9]; p.out = (u16*)d_out; p.ws = (char*)d_ws;
  fused_block<<<256, 512, 0, stream>>>(p);
}
```

```cpp
#include <hip/hip_runtime.h>
#include <hip/hip_bf16.h>
#include <stdint.h>

typedef unsigned short u16;
typedef short v4s __attribute__((ext_vector_type(4)));
typedef short v8s __attribute__((ext_vector_type(8)));
typedef float f2 __attribute__((ext_vector_type(2)));
typedef float f4 __attribute__((ext_vector_type(4)));
typedef float f16v __attribute__((ext_vector_type(16)));
typedef unsigned v2u __attribute__((ext_vector_type(2)));
typedef unsigned v4u __attribute__((ext_vector_type(4)));
typedef __bf16 b2 __attribute__((ext_vector_type(2)));
#define LAS __attribute__((address_space(3)))
#define GAS __attribute__((address_space(1)))
#define DI __device__ __forceinline__
typedef __amdgpu_buffer_rsrc_t srd_t;

constexpr int SEQ = 2048, DM = 512, FF = 1792;
constexpr unsigned WS_EPOCH = 0;
constexpr unsigned WS_F0 = 4096;
constexpr unsigned WS_FP = WS_F0 + 8192;
constexpr unsigned WS_FM = WS_FP + 4 * 16 * 128;
constexpr unsigned WS_FQ = WS_FM + 2048;
constexpr unsigned WS_FW1 = WS_FQ + 2048;
constexpr unsigned WS_FWO = WS_FW1 + 2048;
constexpr unsigned WS_FW2 = WS_FWO + 1024;
static_assert(WS_FW2 + 4096 <= (64 << 10), "flags");
constexpr unsigned WS_WV = 64 << 10;
constexpr unsigned WS_MOD = 128 << 10;
constexpr unsigned WS_ST1 = 192 << 10;
constexpr unsigned WS_ST2A = 512 << 10;
constexpr unsigned WS_ST2B = 1024 << 10;
constexpr unsigned WS_HF = 2u << 20;
constexpr unsigned WS_KV = 4u << 20;
constexpr unsigned WS_OF = 8u << 20;
constexpr unsigned WS_X1F = 10u << 20;
constexpr unsigned WS_H1F = 12u << 20;
constexpr unsigned WS_TQ = 19u << 20;
constexpr unsigned WS_TW1 = WS_TQ + (1536u << 10);
constexpr unsigned WS_TWO = WS_TW1 + (1792u << 10);
constexpr unsigned WS_TW2 = WS_TWO + (512u << 10);
constexpr unsigned WS_STAMP = 25u << 20;
constexpr unsigned WS_BYTES = 27u << 20;
static_assert(WS_TW2 + (1792u << 10) <= WS_STAMP, "ws");
constexpr unsigned L_CA = 98304;
constexpr unsigned L_XS = 131072;
constexpr unsigned L_S1 = L_XS + 8192;
constexpr unsigned L_SO = L_S1 + 7168;
constexpr unsigned L_SC = L_SO + 12288;
constexpr unsigned L_CAP = L_SC + 2048;
constexpr unsigned L_CNT = L_CAP + 512;
static_assert(L_CNT + 16 <= 163840, "lds");
constexpr unsigned L_SQ = 0;
constexpr unsigned L_M2 = 8192;
constexpr unsigned L_WQKV = 0;
constexpr unsigned L_PARTU = 98304;
constexpr unsigned L_PARTV = 32768;
constexpr unsigned L_AK = 98304;
constexpr unsigned KPL = 6144;
constexpr unsigned L_AV = L_AK + 4 * KPL;
constexpr unsigned VPL = 6208, VHD = 12416;
constexpr unsigned L_PSA = L_AK + KPL, L_PSB = L_AK + 3 * KPL, L_PCB = L_AV + VPL, L_PGA = L_AV + VHD + VPL, L_PGB = L_PGA + 1024, L_B2C = L_PGA + 2048;
constexpr unsigned L_PSA2 = L_AK + 2 * KPL, L_PSB2 = L_AV, L_PCB2 = L_AV + VHD;
constexpr unsigned L_FIN = L_AK;
constexpr unsigned L_ONE = L_AV + 2 * VHD;
constexpr unsigned L_AQ = L_ONE + 6400;
constexpr unsigned L_PERS = L_AQ + 8192;
static_assert(L_PERS + 1024 <= 163840, "lds");
static_assert((L_AV % 256) == 0 && (L_ONE % 256) == 0, "bank plan");
constexpr unsigned L_WO = 0;
constexpr unsigned L_W1 = 65536;
constexpr unsigned L_W1S = 0;
constexpr unsigned L_XCH = 16384;
constexpr unsigned L_XF = 16384 + 12288;
constexpr unsigned L_W2X = 98304;

struct Params { const u16* x; const u16* wqkv; const u16* wo; const u16* w1; const u16* b1; const u16* w2; const u16* b2; const u16* c; const u16* wmod; u16* out; char* ws; };

DI float bflo(unsigned v) { return __uint_as_float(v << 16); }
DI float bfhi(unsigned v) { return __uint_as_float(v & 0xffff0000u); }
DI unsigned pk(float a, float b) { f2 v = {a, b}; b2 r = __builtin_convertvector(v, b2); return __builtin_bit_cast(unsigned, r); }
DI float rbf(float a) { return bflo(pk(a, 0.f)); }
DI v8s cat(v4s a, v4s b) { return __builtin_shufflevector(a, b, 0, 1, 2, 3, 4, 5, 6, 7); }
DI v4s trd(unsigned a) { return __builtin_amdgcn_ds_read_tr16_b64_v4i16((LAS v4s*)(uintptr_t)a); }
DI v8s asv8(v4u v) { return __builtin_bit_cast(v8s, v); }
DI v4u asv4u(v8s v) { return __builtin_bit_cast(v4u, v); }
DI v8s lds16(unsigned a) { return *(const LAS v8s*)(uintptr_t)a; }
DI f4 ldsf4(unsigned a) { return *(const LAS f4*)(uintptr_t)a; }
DI v2u lds8(unsigned a) { return *(const LAS v2u*)(uintptr_t)a; }
#define MFMA16(a, b, c) __builtin_amdgcn_mfma_f32_16x16x32_bf16((a), (b), (c), 0, 0, 0)
#define MFMA32(a, b, c) __builtin_amdgcn_mfma_f32_32x32x16_bf16((a), (b), (c), 0, 0, 0)
#define BAR() asm volatile("s_waitcnt lgkmcnt(0)\n\ts_barrier" ::: "memory")
#define VM0() asm volatile("s_waitcnt vmcnt(0)" ::: "memory")
#define LG0() asm volatile("s_waitcnt lgkmcnt(0)" ::: "memory")
#define CFENCE() asm volatile("" ::: "memory")
#define SB() __builtin_amdgcn_sched_barrier(0)
DI unsigned opq(unsigned v) { asm volatile("" : "+v"(v)); return v; }
DI void glds(srd_t r, unsigned voff, unsigned soff, unsigned ldsaddr) {
  asm volatile("s_mov_b32 m0, %0\n\ts_nop 0\n\tbuffer_load_dwordx4 %1, %2, %3 offen lds" ::"s"(ldsaddr), "v"(voff), "s"(r), "s"(soff) : "memory");
}
DI void glds1(srd_t r, unsigned voff, unsigned soff, unsigned ldsaddr) {
  asm volatile("s_mov_b32 m0, %0\n\ts_nop 0\n\tbuffer_load_dwordx4 %1, %2, %3 offen sc1 lds" ::"s"(ldsaddr), "v"(voff), "s"(r), "s"(soff) : "memory");
}
DI void glds4(srd_t r, unsigned voff, unsigned soff, unsigned ldsaddr) {
  asm volatile("s_mov_b32 m0, %0\n\ts_nop 0\n\tbuffer_load_dwordx4 %1, %2, %3 offen lds\n\tbuffer_load_dwordx4 %1, %2, %3 offen offset:1024 lds\n\t"
               "buffer_load_dwordx4 %1, %2, %3 offen offset:2048 lds\n\tbuffer_load_dwordx4 %1, %2, %3 offen offset:3072 lds" ::"s"(ldsaddr), "v"(voff), "s"(r), "s"(soff) : "memory");
}
DI void glds4x1(srd_t r, unsigned voff, unsigned soff, unsigned ldsaddr) {
  asm volatile("s_mov_b32 m0, %0\n\ts_nop 0\n\tbuffer_load_dwordx4 %1, %2, %3 offen sc1 lds\n\tbuffer_load_dwordx4 %1, %2, %3 offen offset:1024 sc1 lds\n\t"
               "buffer_load_dwordx4 %1, %2, %3 offen offset:2048 sc1 lds\n\tbuffer_load_dwordx4 %1, %2, %3 offen offset:3072 sc1 lds" ::"s"(ldsaddr), "v"(voff), "s"(r), "s"(soff) : "memory");
}
DI void glds2x1(srd_t r, unsigned voff, unsigned soff, unsigned ldsaddr) {
  asm volatile("s_mov_b32 m0, %0\n\ts_nop 0\n\tbuffer_load_dwordx4 %1, %2, %3 offen sc1 lds\n\tbuffer_load_dwordx4 %1, %2, %3 offen offset:1024 sc1 lds" ::"s"(ldsaddr), "v"(voff), "s"(r), "s"(soff) : "memory");
}
DI v4u ld16(srd_t r, unsigned voff, unsigned soff) { return __builtin_amdgcn_raw_buffer_load_b128(r, voff, soff, 16); }
DI v2u ld8(srd_t r, unsigned voff, unsigned soff) { return __builtin_amdgcn_raw_buffer_load_b64(r, voff, soff, 16); }
DI v4u ldp16(srd_t r, unsigned voff, unsigned soff) { return __builtin_amdgcn_raw_buffer_load_b128(r, voff, soff, 0); }
DI void stp16(srd_t r, v4u v, unsigned off) { __builtin_amdgcn_raw_buffer_store_b128(v, r, off, 0, 0); }
DI void st16(srd_t r, v4u v, unsigned off) { __builtin_amdgcn_raw_buffer_store_b128(v, r, off, 0, 16); }
DI void st8(srd_t r, v2u v, unsigned off) { __builtin_amdgcn_raw_buffer_store_b64(v, r, off, 0, 16); }
DI float sum16(float v) { auto r = __builtin_amdgcn_permlane16_swap(__float_as_uint(v), __float_as_uint(v), false, false); return __uint_as_float(r[0]) + __uint_as_float(r[1]); }
DI float sum32(float v) { auto r = __builtin_amdgcn_permlane32_swap(__float_as_uint(v), __float_as_uint(v), false, false); return __uint_as_float(r[0]) + __uint_as_float(r[1]); }
DI float max32(float v) { auto r = __builtin_amdgcn_permlane32_swap(__float_as_uint(v), __float_as_uint(v), false, false); return fmaxf(__uint_as_float(r[0]), __uint_as_float(r[1])); }
DI float sumg(float v) { return sum32(sum16(v)); }
#define RLX_AGENT __ATOMIC_RELAXED, __HIP_MEMORY_SCOPE_AGENT
typedef unsigned long long u64;
DI void arrive(char* ws, unsigned off, u64 tag) { (void)__hip_atomic_exchange((GAS u64*)(ws + off), tag, RLX_AGENT); }
DI void wait_words(char* ws, unsigned off, int n, int lane, u64 tag) {
  GAS u64* p = (GAS u64*)(ws + off) + (lane < n ? lane : 0);
  for (unsigned spins = 0; spins < (1u << 17); ++spins)
    if (__all(__hip_atomic_load(p, RLX_AGENT) == tag)) break;
  CFENCE();
}
template <int C> DI float dpp(float v) { return __uint_as_float(__builtin_amdgcn_update_dpp(0u, __float_as_uint(v), C, 0xf, 0xf, false)); }
DI float rowsum16(float v) { v += dpp<0x128>(v); v += dpp<0x124>(v); v += dpp<0x122>(v); v += dpp<0x121>(v); return v; }
#define PIN(x) asm volatile("" : "+v"(x))
#define STAMP(k)
#define STAMP_FLUSH(o)
#define CSTAMP(k)
DI float silu(float v) { return v * __builtin_amdgcn_rcpf(1.f + __builtin_amdgcn_exp2f(-1.4426950408889634f * v)); }

DI v8s trfragA(unsigned img, int half, int lane) {
  const int g = lane >> 4, q = (lane & 15) >> 2, pp = lane & 3;
  const unsigned a = img + (4 * g + q) * 64 + ((half ^ (g & 1)) * 32) + pp * 8;
  return cat(trd(a), trd(a + 1024));
}

template <bool FULL>
DI void attention(int t, int hd, int kt0, int lane, f16v mU, f16v mL, f16v& O, srd_t rWS, unsigned wo_src, unsigned w1_src, int w, const u16* xres, v2u& xr0, v2u& xr1, unsigned& stampv) {
  const int qr = lane & 31, h = lane >> 5, blk = (lane >> 4) & 1, q = (lane & 15) >> 2, pp = lane & 3;
  const v8s Qf = lds16(L_AQ + (hd * 2 + h) * 2048 + (32 * t + qr) * 16);
  const unsigned kb = opq(L_AK + (hd * 2 + h) * KPL + (32 * t + qr) * 16);
  const unsigned vb = opq((blk == 0 ? L_AV + hd * VHD + (pp >> 1) * VPL + (pp & 1) * 8 + (4 * h + q) * 16 : L_ONE + ((hd * 128 + (h ? 0 : 128)) & 255)) + t * 512);
  const f16v zero = {};
#define RIDE(n) do { if ((n) < 2) glds4x1(rWS, lane * 16, wo_src + (w * 2 + (n)) * 4096, L_WO + (w * 2 + (n)) * 4096); \
                     else glds4x1(rWS, lane * 16, w1_src + w * 4096, L_W1 + w * 4096); } while (0)
  if (FULL) {
    v8s Kf[9];
#pragma unroll
    for (int kt = 0; kt < 9; ++kt) Kf[kt] = lds16(kb + kt * 512);
    v8s V0 = cat(trd(vb), trd(vb + 128)), V1 = cat(trd(vb + 256), trd(vb + 384));
    f16v S[2];
    float m0 = -3e38f, m1 = -3e38f;
    S[0] = MFMA32(Kf[0], Qf, mL);
    SB();
#pragma unroll
    for (int kt = 0; kt < 9; ++kt) {
      if (kt < 8) S[(kt + 1) & 1] = MFMA32(Kf[kt + 1], Qf, kt + 1 == 8 ? mU : zero);
      SB();
#pragma unroll
      for (int r = 0; r < 16; r += 4) { m0 = fmaxf(fmaxf(m0, S[kt & 1][r]), S[kt & 1][r + 1]); m1 = fmaxf(fmaxf(m1, S[kt & 1][r + 2]), S[kt & 1][r + 3]); }
      SB();
    }
    const float m = max32(fmaxf(m0, m1));
    STAMP(23);
    f16v nm;
#pragma unroll
    for (int r = 0; r < 16; ++r) { nm[r] = -m; mU[r] -= m; mL[r] -= m; }
    O = zero;
    S[0] = MFMA32(Kf[0], Qf, mL);
    SB();
#pragma unroll
    for (int kt = 0; kt < 9; ++kt) {
      if (kt < 8) S[(kt + 1) & 1] = MFMA32(Kf[kt + 1], Qf, kt + 1 == 8 ? mU : nm);
      if (kt == 0) RIDE(0);
      if (kt == 3) RIDE(1);
      if (kt == 6) RIDE(2);
      SB();
      v4u P0, P1;
#pragma unroll
      for (int r = 0; r < 4; ++r) P0[r] = pk(__builtin_amdgcn_exp2f(S[kt & 1][2 * r]), __builtin_amdgcn_exp2f(S[kt & 1][2 * r + 1]));
      O = MFMA32(V0, asv8(P0), O);
      if (kt < 8) V0 = cat(trd(vb + (kt + 1) * 512), trd(vb + (kt + 1) * 512 + 128));
      if (kt == 2) xr0 = *(const v2u*)xres;
      if (kt == 5) xr1 = *(const v2u*)(xres + 16);
      SB();
#pragma unroll
      for (int r = 0; r < 4; ++r) P1[r] = pk(__builtin_amdgcn_exp2f(S[kt & 1][8 + 2 * r]), __builtin_amdgcn_exp2f(S[kt & 1][8 + 2 * r + 1]));
      O = MFMA32(V1, asv8(P1), O);
      if (kt < 8) V1 = cat(trd(vb + (kt + 1) * 512 + 256), trd(vb + (kt + 1) * 512 + 384));
      SB();
    }
  } else {
#pragma unroll
    for (int n = 0; n < 3; ++n) RIDE(n);
    xr0 = *(const v2u*)xres; xr1 = *(const v2u*)(xres + 16);
    float m = -3e38f;
#pragma unroll
    for (int kt = 0; kt < 9; ++kt) {
      if (kt >= kt0) {
        const f16v S = MFMA32(lds16(kb + kt * 512), Qf, kt == 8 ? mU : zero);
#pragma unroll
        for (int r = 0; r < 16; r += 2) m = fmaxf(fmaxf(m, S[r]), S[r + 1]);
      }
    }
    m = max32(m);
    f16v nm;
#pragma unroll
    for (int r = 0; r < 16; ++r) { nm[r] = -m; mU[r] -= m; }
    O = zero;
#pragma unroll
    for (int kt = 0; kt < 9; ++kt) {
      if (kt >= kt0) {
        const f16v S = MFMA32(lds16(kb + kt * 512), Qf, kt == 8 ? mU : nm);
        v4u P0, P1;
#pragma unroll
        for (int r = 0; r < 4; ++r) {
          P0[r] = pk(__builtin_amdgcn_exp2f(S[2 * r]), __builtin_amdgcn_exp2f(S[2 * r + 1]));
          P1[r] = pk(__builtin_amdgcn_exp2f(S[8 + 2 * r]), __builtin_amdgcn_exp2f(S[8 + 2 * r + 1]));
        }
        const v8s V0 = cat(trd(vb + kt * 512), trd(vb + kt * 512 + 128));
        const v8s V1 = cat(trd(vb + kt * 512 + 256), trd(vb + kt * 512 + 384));
        O = MFMA32(V0, asv8(P0), O);
        O = MFMA32(V1, asv8(P1), O);
      }
    }
  }
#undef RIDE
}

__global__ void __launch_bounds__(512, 2) fused_block(Params p) {
  __shared__ __attribute__((aligned(16))) char smem[163840];
  LAS char* sm = (LAS char*)smem;
  const int tid = threadIdx.x, lane = tid & 63;
  const int w = __builtin_amdgcn_readfirstlane(tid >> 6);
  const int b = blockIdx.x, ci = ((b & 7) >> 1) * 4 + ((b >> 3) & 3), cj = (b & 1) * 8 + (b >> 5);
  const int tok = lane & 15, g = lane >> 4, q4 = (lane & 15) >> 2, pp = lane & 3;
  const int hf = cj & 1, ct = cj >> 1, par = cj & 1;
  const int wq = w < 4 ? w : w - 1;
  unsigned stampv = 0;
  const srd_t rWS = __builtin_amdgcn_make_buffer_rsrc(p.ws, 0, WS_BYTES, 0x00020000);
  const int rA = lane >> 2, pA = lane & 3;
  const unsigned colA = (((pA >> 1) ^ ((rA >> 2) & 1)) * 32) + (pA & 1) * 16;
  const int rB = lane >> 3, pB = lane & 7;
  const unsigned colB = (((pB >> 1) ^ ((rB >> 1) & 3)) * 32) + (pB & 1) * 16;
  STAMP(0);
  CSTAMP(40);

  const bool m2 = b < 160;
  const int msl = b % 40, mkq = b / 40;
  const srd_t rWmod = __builtin_amdgcn_make_buffer_rsrc((void*)p.wmod, 0, DM * 3072 * 2, 0x00020000);
  v4u b2v = {0, 0, 0, 0};
  if (w == 6 && lane < 8) b2v = *(const v4u*)(p.b2 + 64 * ct + lane * 8);
  v4u cr;
  v2u ep;
  if (w < 4) {
    __builtin_amdgcn_s_setprio(2);
    asm volatile("global_load_dwordx4 %0, %1, off" : "=v"(cr) : "v"(p.c + 128 * w + 8 * (lane & 15)) : "memory");
    asm volatile("buffer_load_dwordx2 %0, %1, %2, 0 offen sc1" : "=v"(ep) : "v"(WS_EPOCH), "s"(rWS) : "memory");
    const srd_t rX = __builtin_amdgcn_make_buffer_rsrc((void*)p.x, 0, SEQ * DM * 2, 0x00020000);
#pragma unroll
    for (int u = 0; u < 2; ++u) glds(rX, rA * 1024 + pA * 16, (128 * ci + 16 * (2 * w + u)) * 1024 + 64 * cj, L_XS + (2 * w + u) * 1024);
#pragma unroll
    for (int i0 = 0; i0 < 8; ++i0) { const int ii = (i0 + (ci & 3) * 2 + (cj & 1)) & 7; glds(rWmod, rA * 6144 + colA, (w * 8 + ii) * 16 * 6144 + (512 + 32 * cj) * 2, L_CA + (w * 8 + ii) * 1024); }
  } else if (w == 5) {
    __builtin_amdgcn_s_setprio(3);
    const srd_t r = __builtin_amdgcn_make_buffer_rsrc((void*)p.wqkv, 0, DM * 1536 * 2, 0x00020000);
#pragma unroll
    for (int e = 0; e < 6; ++e) glds(r, rA * 3072 + colA, (32 * ci + 16 * (e & 1)) * 3072 + ((e >> 1) * 512 + 32 * cj) * 2, L_SQ + e * 1024);
  }
  if (tid == 256) { *(LAS unsigned*)(uintptr_t)L_CNT = 0u; *(LAS unsigned*)(uintptr_t)(L_CNT + 4) = m2 ? 0u : 1u; *(LAS unsigned*)(uintptr_t)(L_CNT + 8) = 0u; *(LAS unsigned*)(uintptr_t)(L_CNT + 12) = 0u; }
  BAR();
#define READ_TAG() do { const v2u ep_ = ld8(rWS, WS_EPOCH, 0); TAG = (((u64)__builtin_amdgcn_readfirstlane(ep_[1]) << 32) | __builtin_amdgcn_readfirstlane(ep_[0])) + 1; } while (0)
  v8s rt[12];
  f16v mU, mL;
  u64 TAG;
  if (w < 4) {
    asm volatile("s_waitcnt vmcnt(10)" : "+v"(cr), "+v"(ep) :: "memory");
    const unsigned sc = L_SC + w * 256;
    if (lane < 16) *(LAS v4u*)(uintptr_t)(sc + lane * 16) = v4u{pk(silu(bflo(cr[0])), silu(bfhi(cr[0]))), pk(silu(bflo(cr[1])), silu(bfhi(cr[1]))), pk(silu(bflo(cr[2])), silu(bfhi(cr[2]))), pk(silu(bflo(cr[3])), silu(bfhi(cr[3])))};
    LG0();
    v8s sf[4];
#pragma unroll
    for (int s = 0; s < 4; ++s) { const v2u lo = lds8(sc + (32 * s + 4 * g) * 2), hi = lds8(sc + (32 * s + 16 + 4 * g) * 2); sf[s] = asv8(v4u{lo[0], lo[1], hi[0], hi[1]}); }
    TAG = (((u64)__builtin_amdgcn_readfirstlane(ep[1]) << 32) | __builtin_amdgcn_readfirstlane(ep[0])) + 1;
    asm volatile("s_waitcnt vmcnt(8)" ::: "memory");
    v2u xa[2][2];
#pragma unroll
    for (int u = 0; u < 2; ++u) {
      const unsigned a = L_XS + (2 * w + u) * 1024 + tok * 64 + g * 8; xa[u][0] = lds8(a); xa[u][1] = lds8(a + 32);
      const float x0 = bflo(xa[u][0][0]), x1 = bfhi(xa[u][0][0]), x2 = bflo(xa[u][0][1]), x3 = bfhi(xa[u][0][1]);
      const float x4 = bflo(xa[u][1][0]), x5 = bfhi(xa[u][1][0]), x6 = bflo(xa[u][1][1]), x7 = bfhi(xa[u][1][1]);
      const float s1 = sumg(((x0 + x1) + (x2 + x3)) + ((x4 + x5) + (x6 + x7)));
      const float s2 = sumg(((x0 * x0 + x1 * x1) + (x2 * x2 + x3 * x3)) + ((x4 * x4 + x5 * x5) + (x6 * x6 + x7 * x7)));
      if (g == 0) st8(rWS, v2u{__float_as_uint(s1), __float_as_uint(s2)}, WS_ST1 + ((ci * 16 + cj) * 128 + 16 * (2 * w + u) + tok) * 8);
    }
    asm volatile("s_waitcnt vmcnt(2)" ::: "memory");
    STAMP(1);
    f4 a0 = {0, 0, 0, 0}, a1 = {0, 0, 0, 0};
    {
      v8s W[4][2];
#pragma unroll
      for (int s = 0; s < 4; ++s) { W[s][0] = trfragA(L_CA + (4 * w + s) * 2048, 0, lane); W[s][1] = trfragA(L_CA + (4 * w + s) * 2048, 1, lane); }
#pragma unroll
      for (int s = 0; s < 4; ++s) { a0 = MFMA16(W[s][0], sf[s], a0); a1 = MFMA16(W[s][1], sf[s], a1); }
    }
    if (tok == 0) { *(LAS f4*)(uintptr_t)(L_CAP + (w * 32 + 4 * g) * 4) = a0; *(LAS f4*)(uintptr_t)(L_CAP + (w * 32 + 16 + 4 * g) * 4) = a1; }
    LG0();
    if (lane == 0) __hip_atomic_fetch_add((LAS unsigned*)(uintptr_t)L_CNT, 1u, __ATOMIC_RELAXED, __HIP_MEMORY_SCOPE_WORKGROUP);
    while (*(volatile LAS unsigned*)(uintptr_t)L_CNT < 4u) {}
    CFENCE();
    STAMP(19);
    a0 = ldsf4(L_CAP + 4 * g * 4); a1 = ldsf4(L_CAP + (16 + 4 * g) * 4);
#pragma unroll
    for (int e = 1; e < 4; ++e) { a0 += ldsf4(L_CAP + (e * 32 + 4 * g) * 4); a1 += ldsf4(L_CAP + (e * 32 + 16 + 4 * g) * 4); }
    float wv[8];
#pragma unroll
    for (int r = 0; r < 4; ++r) { wv[r] = rbf(1.f + rbf(a0[r])); wv[4 + r] = rbf(1.f + rbf(a1[r])); }
#pragma unroll
    for (int u = 0; u < 2; ++u) {
      const float x0 = bflo(xa[u][0][0]), x1 = bfhi(xa[u][0][0]), x2 = bflo(xa[u][0][1]), x3 = bfhi(xa[u][0][1]);
      const float x4 = bflo(xa[u][1][0]), x5 = bfhi(xa[u][1][0]), x6 = bflo(xa[u][1][1]), x7 = bfhi(xa[u][1][1]);
      const int T = 2 * w + u;
      st16(rWS, v4u{pk(x0 * wv[0], x1 * wv[1]), pk(x2 * wv[2], x3 * wv[3]), pk(x4 * wv[4], x5 * wv[5]), pk(x6 * wv[6], x7 * wv[7])},
           WS_HF + ((ci * 8 + T) * 16 + cj) * 1024 + lane * 16);
      if (u == 0 && w == 0 && tok == 0) {
        st8(rWS, v2u{pk(wv[0], wv[1]), pk(wv[2], wv[3])}, WS_WV + ci * 1024 + cj * 64 + g * 8);
        st8(rWS, v2u{pk(wv[4], wv[5]), pk(wv[6], wv[7])}, WS_WV + ci * 1024 + cj * 64 + 32 + g * 8);
      }
    }
    STAMP(21);
    VM0();
    if (lane == 0) { arrive(p.ws, WS_F0 + (ci * 64 + cj * 4 + w) * 8, TAG); __hip_atomic_fetch_add((LAS unsigned*)(uintptr_t)(L_CNT + 8), 1u, __ATOMIC_RELAXED, __HIP_MEMORY_SCOPE_WORKGROUP); }
    __builtin_amdgcn_s_setprio(0);
  } else if (w == 4) {
    if (m2) {
      const int mvec = msl >> 3, mcol = (mvec == 0 ? 0 : 512 + 512 * mvec) + (msl & 7) * 64;
      __builtin_amdgcn_s_sleep(8);
#pragma unroll
      for (int ii = 0; ii < 16; ++ii) glds(rWmod, rB * 6144 + colB, (128 * mkq + 8 * ii) * 6144 + mcol * 2, L_M2 + ii * 1024);
      const v4u cr = *(const v4u*)(p.c + 128 * mkq + 8 * (lane & 15));
      const unsigned sc = L_SC + 1024;
      if (lane < 16) *(LAS v4u*)(uintptr_t)(sc + lane * 16) = v4u{pk(silu(bflo(cr[0])), silu(bfhi(cr[0]))), pk(silu(bflo(cr[1])), silu(bfhi(cr[1]))), pk(silu(bflo(cr[2])), silu(bfhi(cr[2]))), pk(silu(bflo(cr[3])), silu(bfhi(cr[3])))};
      LG0();
      v8s sf[4];
#pragma unroll
      for (int s = 0; s < 4; ++s) { const v2u lo = lds8(sc + (32 * s + 4 * g) * 2), hi = lds8(sc + (32 * s + 16 + 4 * g) * 2); sf[s] = asv8(v4u{lo[0], lo[1], hi[0], hi[1]}); }
      VM0();
      f4 a[4];
#pragma unroll
      for (int nt = 0; nt < 4; ++nt) a[nt] = f4{0, 0, 0, 0};
      const int row = 4 * g + q4;
#pragma unroll
      for (int s = 0; s < 4; ++s) {
        v8s W[4];
#pragma unroll
        for (int nt = 0; nt < 4; ++nt) { const unsigned ad = L_M2 + s * 4096 + row * 128 + ((nt ^ ((row >> 1) & 3)) * 32) + pp * 8; W[nt] = cat(trd(ad), trd(ad + 2048)); }
#pragma unroll
        for (int nt = 0; nt < 4; ++nt) a[nt] = MFMA16(W[nt], sf[s], a[nt]);
      }
      if (tok == 0) {
#pragma unroll
        for (int nt = 0; nt < 4; ++nt)
          st16(rWS, v4u{__float_as_uint(a[nt][0]), __float_as_uint(a[nt][1]), __float_as_uint(a[nt][2]), __float_as_uint(a[nt][3])},
               WS_MOD + mkq * 10240 + mvec * 2048 + ((msl & 7) * 2 + (nt >> 1)) * 128 + g * 32 + (nt & 1) * 16);
      }
      LG0();
      if (lane == 0) *(volatile LAS unsigned*)(uintptr_t)(L_CNT + 4) = 1u;
      READ_TAG();
      VM0();
      if (lane == 0) arrive(p.ws, WS_FM + b * 8, TAG);
    } else READ_TAG();
    STAMP(1);
  } else {
    if (w == 5) {
      READ_TAG();
      VM0();
#pragma unroll
      for (int nt = 0; nt < 6; ++nt) st16(rWS, asv4u(trfragA(L_SQ + (nt >> 1) * 2048, nt & 1, lane)), WS_TQ + ((cj * 6 + nt) * 16 + ci) * 1024 + lane * 16);
      VM0();
      if (lane == 0) arrive(p.ws, WS_FQ + (cj * 16 + ci) * 8, TAG);
      __builtin_amdgcn_s_setprio(0);
    } else {
      READ_TAG();
      __builtin_amdgcn_s_sleep(16);
      if (w == 6) {
        const srd_t r1 = __builtin_amdgcn_make_buffer_rsrc((void*)p.w1, 0, DM * FF * 2, 0x00020000);
#pragma unroll
        for (int e = 0; e < 6; ++e) glds(r1, rA * (FF * 2) + colA, (32 * ci + 16 * (e & 1)) * (FF * 2) + (112 * cj + 16 * par + (e >> 1) * 32) * 2, L_S1 + e * 1024);
        glds(r1, (lane >> 1) * (FF * 2) + (lane & 1) * 16, 32 * ci * (FF * 2) + (112 * cj + (par ? 0 : 96)) * 2, L_S1 + 6144);
        VM0();
#pragma unroll
        for (int nt = 0; nt < 6; ++nt) rt[nt] = trfragA(L_S1 + (nt >> 1) * 2048, nt & 1, lane);
        { const unsigned a = L_S1 + 6144 + (4 * g + q4) * 32 + pp * 8; rt[6] = cat(trd(a), trd(a + 512)); }
      } else {
        const srd_t ro = __builtin_amdgcn_make_buffer_rsrc((void*)p.wo, 0, DM * DM * 2, 0x00020000);
#pragma unroll
        for (int e = 0; e < 4; ++e) glds(ro, rB * 1024 + colB, (32 * ((b & 127) >> 3) + 8 * e) * 1024 + (b & 7) * 128, L_SO + e * 1024);
        const srd_t r2 = __builtin_amdgcn_make_buffer_rsrc((void*)p.w2, 0, FF * DM * 2, 0x00020000);
        const int b2 = b < 224 ? b : 0;
#pragma unroll
        for (int e = 0; e < 8; ++e) { const int id = 2 * b2 + (e >> 2); glds(r2, rB * 1024 + colB, (32 * (id >> 3) + 8 * (e & 3)) * 1024 + (id & 7) * 128, L_SO + 4096 + e * 1024); }
        VM0();
        {
          const int row = 16 * (g >> 1) + 4 * (g & 1) + q4;
#pragma unroll
          for (int nt = 0; nt < 4; ++nt) { const unsigned a = L_SO + row * 128 + ((nt ^ ((row >> 1) & 3)) * 32) + pp * 8; rt[8 + nt] = cat(trd(a), trd(a + 1024)); }
        }
        const int row = 4 * g + q4;
#pragma unroll
        for (int e = 0; e < 8; ++e) { const int nt = e & 3; const unsigned a = L_SO + 4096 + (e >> 2) * 4096 + row * 128 + ((nt ^ ((row >> 1) & 3)) * 32) + pp * 8; rt[e] = cat(trd(a), trd(a + 2048)); }
      }
    }
    STAMP(1);
    if (w >= 6) {
      while (*(volatile LAS unsigned*)(uintptr_t)(L_CNT + 8) < 4u) __builtin_amdgcn_s_sleep(1);
      CFENCE();
      if (w == 6) {
#pragma unroll
        for (int e = 0; e < 7; ++e) st16(rWS, asv4u(rt[e]), WS_TW1 + ((cj * 7 + e) * 16 + ci) * 1024 + lane * 16);
      } else {
        if (b < 224) {
#pragma unroll
          for (int e = 0; e < 8; ++e) { const int id = 2 * b + (e >> 2); st16(rWS, asv4u(rt[e]), WS_TW2 + (((id & 7) * 56 + (id >> 3)) * 4 + (e & 3)) * 1024 + lane * 16); }
        }
        if (b < 128) {
#pragma unroll
          for (int e = 0; e < 4; ++e) st16(rWS, asv4u(rt[8 + e]), WS_TWO + (((b & 7) * 16 + (b >> 3)) * 4 + e) * 1024 + lane * 16);
        }
      }
    }
    wait_words(p.ws, WS_FQ + cj * 128, 16, lane, TAG);
    while (*(volatile LAS unsigned*)(uintptr_t)(L_CNT + 4) == 0u) __builtin_amdgcn_s_sleep(1);
    CFENCE();
    STAMP(19);
#pragma unroll
    for (int e = 0; e < 8; ++e) glds4x1(rWS, lane * 16, WS_TQ + cj * 98304 + ((w - 5) * 8 + ((e + 2 * (ci & 3)) & 7)) * 4096, L_WQKV + ((w - 5) * 8 + ((e + 2 * (ci & 3)) & 7)) * 4096);
  }
  {
    const int qr = lane & 31, h = lane >> 5;
#pragma unroll
    for (int r = 0; r < 16; ++r) { const int kr = (r & 3) + 8 * (r >> 2) + 4 * h; mU[r] = kr > qr ? -1e30f : 0.f; mL[r] = kr > qr ? 0.f : -1e30f; }
    asm volatile("" : "+v"(mU), "+v"(mL));
  }
  if (w == 0) {
    __builtin_amdgcn_s_setprio(3);
    wait_words(p.ws, WS_F0 + ci * 512, 64, lane, TAG);
    if (lane == 0) *(volatile LAS unsigned*)(uintptr_t)(L_CNT + 12) = 1u;
    __builtin_amdgcn_s_setprio(0);
  } else {
    while (*(volatile LAS unsigned*)(uintptr_t)(L_CNT + 12) == 0u) __builtin_amdgcn_s_sleep(1);
    CFENCE();
  }
  STAMP(2);
  v8s act[16];
  const unsigned hsrc = WS_HF + (ci * 8 + w) * 16 * 1024;
  v2u wl[2], wh[2];
#pragma unroll
  for (int u = 0; u < 2; ++u) { const unsigned o = (32 * (2 * w + u) + 4 * g) * 2; wl[u] = ld8(rWS, o, WS_WV + ci * 1024); wh[u] = ld8(rWS, o + 32, WS_WV + ci * 1024); }
#pragma unroll
  for (int s = 0; s < 5; ++s) act[s] = asv8(ld16(rWS, lane * 16, hsrc + s * 1024));
  if (w >= 5) asm volatile("s_waitcnt vmcnt(9)" ::: "memory");
  BAR();
  STAMP(3);
  if (lane == 0) {
    if (w == 6) arrive(p.ws, WS_FW1 + (cj * 16 + ci) * 8, TAG);
    if (w == 7 && b < 128) arrive(p.ws, WS_FWO + ((b & 7) * 16 + (b >> 3)) * 8, TAG);
    if (w == 7 && b < 224) { arrive(p.ws, WS_FW2 + (((2 * b) & 7) * 64 + ((2 * b) >> 3)) * 8, TAG); arrive(p.ws, WS_FW2 + (((2 * b + 1) & 7) * 64 + ((2 * b + 1) >> 3)) * 8, TAG); }
  }

  f4 accQ[2], accKV[4];
  float rstd, nmu;
  v4u gaR, cbR, sbR, gbR;
  v2u b2R[2];
  v4u mc0, md0;
  v4u mx[2];
  const int tt = w & 3, kh = w >> 2;
  const int ocol = 64 * ct + 32 * kh + 4 * g;
  {
    const unsigned bq = opq(L_WQKV + lane * 16), bv = opq(L_WQKV + 65536 + lane * 16);
#define QF(nt, s) lds16(((nt) < 4 ? bq : bv) + ((nt) & 3) * 16384 + (s) * 1024)
    {
      f4 au[6];
      v8s W[2][6];
      const unsigned c0 = opq(bq + w * 2048), d0 = opq(bv + w * 2048);
#pragma unroll
      for (int u = 0; u < 2; ++u)
#pragma unroll
        for (int nt = 0; nt < 6; ++nt) W[u][nt] = lds16((nt < 4 ? c0 : d0) + (nt & 3) * 16384 + u * 1024);
#pragma unroll
      for (int nt = 0; nt < 6; ++nt) au[nt] = f4{0, 0, 0, 0};
#pragma unroll
      for (int u = 0; u < 2; ++u) {
        const v8s a = asv8(v4u{wl[u][0], wl[u][1], wh[u][0], wh[u][1]});
#pragma unroll
        for (int nt = 0; nt < 6; ++nt) au[nt] = MFMA16(W[u][nt], a, au[nt]);
      }
      if (tok == 0) {
#pragma unroll
        for (int nt = 0; nt < 6; ++nt) *(LAS f4*)(sm + L_PARTU + (w * 96 + nt * 16 + 4 * g) * 4) = au[nt];
      }
    }
    BAR();
    if (tid >= 384 && tid < 480) {
      const int e0 = tid - 384;
      float s = 0;
#pragma unroll
      for (int e = 0; e < 8; ++e) s += *(const LAS float*)(sm + L_PARTU + (e * 96 + e0) * 4);
      *(LAS float*)(sm + L_PERS + e0 * 4) = s;
    }
    STAMP(16);
    v2u st[4];
    u64 fm0 = 0, fm1 = 0, fm2 = 0;
#pragma unroll
    for (int nt = 0; nt < 4; ++nt) accKV[nt] = f4{0, 0, 0, 0};
#pragma unroll
    for (int nt = 0; nt < 2; ++nt) accQ[nt] = f4{0, 0, 0, 0};
    {
      v8s W[6];
#pragma unroll
      for (int nt = 0; nt < 6; ++nt) W[nt] = QF(nt, 0);
      SB();
#pragma unroll
      for (int s = 0; s < 16; ++s) {
        if (s + 5 < 16) act[s + 5] = asv8(ld16(rWS, lane * 16, hsrc + (s + 5) * 1024));
        else if (s < 15) st[s - 11] = ld8(rWS, ((4 * g + s - 11) * 128 + 16 * w + tok) * 8, WS_ST1 + ci * 16 * 128 * 8);
        if (s == 12) { GAS u64* f = (GAS u64*)(p.ws + WS_FM); fm0 = __hip_atomic_load(f + lane, RLX_AGENT); fm1 = __hip_atomic_load(f + 64 + lane, RLX_AGENT); fm2 = __hip_atomic_load(f + 128 + (lane & 31), RLX_AGENT); }
#pragma unroll
        for (int nt = 0; nt < 6; ++nt) {
          if (nt < 2) accQ[nt] = MFMA16(W[nt], act[s], accQ[nt]); else accKV[nt - 2] = MFMA16(W[nt], act[s], accKV[nt - 2]);
          if (s + 1 < 16) W[nt] = QF(nt, s + 1);
          SB();
        }
      }
    }
    float s1 = 0, s2 = 0;
#pragma unroll
    for (int e = 0; e < 4; ++e) { s1 += __uint_as_float(st[e][0]); s2 += __uint_as_float(st[e][1]); }
    s1 = sumg(s1); s2 = sumg(s2);
    const float mu = s1 * (1.f / 512), var = fmaxf(s2 * (1.f / 512) - mu * mu, 0.f);
    rstd = __builtin_amdgcn_rsqf(var + 1e-5f); nmu = -rstd * mu;
    if (w == 6 && lane < 8) *(LAS v4u*)(sm + L_B2C + lane * 16) = b2v;
    PIN(rstd); PIN(nmu);
    {
          GAS u64* f = (GAS u64*)(p.ws + WS_FM);
          for (unsigned spins = 0; spins < (1u << 17) && !__all(fm0 == TAG && fm1 == TAG && fm2 == TAG); ++spins) {
            fm0 = __hip_atomic_load(f + lane, RLX_AGENT); fm1 = __hip_atomic_load(f + 64 + lane, RLX_AGENT); fm2 = __hip_atomic_load(f + 128 + (lane & 31), RLX_AGENT); }
          CFENCE();
          if (w < 6) {
            const int vec = w < 2 ? 0 : w < 4 ? 2 : 3, hq = w & 1;
            const unsigned dst = w == 0 ? L_PSA : w == 1 ? L_PSA2 : w == 2 ? L_PSB : w == 3 ? L_PSB2 : w == 4 ? L_PCB : L_PCB2;
            glds2x1(rWS, lane * 16, WS_MOD + (2 * hq) * 10240 + vec * 2048, dst);
            glds2x1(rWS, lane * 16, WS_MOD + (2 * hq + 1) * 10240 + vec * 2048, dst + 2048);
          } else glds1(rWS, (lane >> 4) * 10240 + (lane & 15) * 16, WS_MOD + (w == 6 ? 1 : 4) * 2048 + ct * 256, w == 6 ? L_PGA : L_PGB);
    }
    STAMP(4);
    BAR();
    {
      v4u kk, vv;
      unsigned pkd[4][2];
#pragma unroll
      for (int nt = 0; nt < 4; ++nt) {
        const f4 u = ldsf4(L_PERS + (32 + nt * 16 + 4 * g) * 4);
        pkd[nt][0] = pk(rstd * accKV[nt][0] + nmu * u[0], rstd * accKV[nt][1] + nmu * u[1]);
        pkd[nt][1] = pk(rstd * accKV[nt][2] + nmu * u[2], rstd * accKV[nt][3] + nmu * u[3]);
      }
      { auto r0 = __builtin_amdgcn_permlane16_swap(pkd[0][0], pkd[1][0], false, false); auto r1 = __builtin_amdgcn_permlane16_swap(pkd[0][1], pkd[1][1], false, false);
        kk = v4u{r0[0], r1[0], r0[1], r1[1]}; }
      { auto r0 = __builtin_amdgcn_permlane16_swap(pkd[2][0], pkd[3][0], false, false); auto r1 = __builtin_amdgcn_permlane16_swap(pkd[2][1], pkd[3][1], false, false);
        vv = v4u{r0[0], r1[0], r0[1], r1[1]}; }
      const int hd = g & 1, pl = g >> 1;
      st16(rWS, kk, WS_KV + (ci * 16 + cj) * 16384 + ((0 * 2 + hd) * 2 + pl) * 2048 + (16 * w + tok) * 16);
      st16(rWS, vv, WS_KV + (ci * 16 + cj) * 16384 + ((1 * 2 + hd) * 2 + pl) * 2048 + (16 * w + tok) * 16);
      *(LAS v4u*)(sm + L_AK + (hd * 2 + pl) * KPL + (256 + 16 * w + tok) * 16) = kk;
      *(LAS v4u*)(sm + L_AV + hd * VHD + pl * VPL + (256 + 16 * w + tok) * 16) = vv;
    }
    VM0();
    BAR();
    if (tid == 256) arrive(p.ws, WS_FP + (0 * 16 + ci) * 128 + cj * 8, TAG);
    STAMP(5);
    if (tid < 400) *(LAS v4u*)(sm + L_ONE + tid * 16) = v4u{0x3f803f80u, 0x3f803f80u, 0x3f803f80u, 0x3f803f80u};
    if (tid < 416) {
      const int seg = tid < 384 ? tid >> 7 : 3 + ((tid - 384) >> 4), e = tid < 384 ? tid & 127 : (tid - 384) & 15;
      f4 v;
      if (seg < 3) {
        const unsigned a0 = (seg == 0 ? L_PSA : seg == 1 ? L_PSB : L_PCB) + e * 16, a1 = (seg == 0 ? L_PSA2 : seg == 1 ? L_PSB2 : L_PCB2) + e * 16;
        v = (ldsf4(a0) + ldsf4(a0 + 2048)) + (ldsf4(a1) + ldsf4(a1 + 2048));
      } else {
        const unsigned a0 = (seg == 3 ? L_PGA : L_PGB) + e * 16;
        v = (ldsf4(a0) + ldsf4(a0 + 256)) + (ldsf4(a0 + 512) + ldsf4(a0 + 768));
      }
      *(LAS v2u*)(sm + L_FIN + (seg < 3 ? seg * 1024 : 3072 + (seg - 3) * 128) + e * 8) = v2u{pk(v[0], v[1]), pk(v[2], v[3])};
    }
    BAR();
    {
      f4 av[4];
      v8s W[2][4], a[2];
      const unsigned c0 = opq(bq + w * 2048), d0 = opq(bv + w * 2048);
#pragma unroll
      for (int u = 0; u < 2; ++u) {
        a[u] = lds16(L_FIN + (2 * w + u) * 64 + g * 16);
#pragma unroll
        for (int nt = 0; nt < 4; ++nt) W[u][nt] = lds16((nt < 2 ? c0 : d0) + (nt & 1) * 16384 + u * 1024);
      }
#pragma unroll
      for (int nt = 0; nt < 4; ++nt) av[nt] = f4{0, 0, 0, 0};
#pragma unroll
      for (int u = 0; u < 2; ++u)
#pragma unroll
        for (int nt = 0; nt < 4; ++nt) av[nt] = MFMA16(W[u][nt], a[u], av[nt]);
      if (tok == 0) {
#pragma unroll
        for (int nt = 0; nt < 4; ++nt) *(LAS f4*)(sm + L_PARTV + (w * 64 + nt * 16 + 4 * g) * 4) = av[nt];
      }
    }
    {
      gaR = asv4u(lds16(L_FIN + 3072 + kh * 64 + g * 16)); gbR = asv4u(lds16(L_FIN + 3200 + kh * 64 + g * 16));
      sbR = asv4u(lds16(L_FIN + 1024 + (2 * ct + kh) * 64 + g * 16)); cbR = asv4u(lds16(L_FIN + 2048 + (2 * ct + kh) * 64 + g * 16));
#pragma unroll
      for (int e = 0; e < 2; ++e) b2R[e] = lds8(L_B2C + (32 * kh + 4 * g + 16 * e) * 2);
      mc0 = asv4u(lds16(L_FIN + 2048 + 16 * lane)); md0 = asv4u(lds16(L_FIN + 1024 + 16 * lane));
#pragma unroll
      for (int u = 0; u < 2; ++u) mx[u] = asv4u(lds16(L_FIN + (tok == 1 ? 1024 : 2048) + (2 * w + u) * 64 + g * 16));
      BAR();
      if (tid < 64) {
        float s = 0;
#pragma unroll
        for (int e = 0; e < 8; ++e) s += *(const LAS float*)(sm + L_PARTV + (e * 64 + tid) * 4);
        *(LAS float*)(sm + L_PERS + (96 + tid) * 4) = s;
      }
    }
    if (ci < 2) {
      for (int e = tid; e < 8 * 256; e += 512) {
        const int pln = e >> 8, key = e & 255;
        if (ci == 0 || key < 128) {
          const unsigned a = pln < 4 ? L_AK + pln * KPL : L_AV + ((pln - 4) >> 1) * VHD + (pln & 1) * VPL;
          *(LAS v4u*)(sm + a + key * 16) = v4u{0, 0, 0, 0};
        }
      }
    }
    if (w == 4) {
      GAS u64* f0 = (GAS u64*)(p.ws + (lane == 0 && ci >= 1 ? WS_FP + (ci - 1) * 128 + cj * 8 : lane == 1 && ci >= 2 ? WS_FP + (ci - 2) * 128 + cj * 8
                                       : lane >= 16 && lane < 32 ? WS_FW1 + (cj * 16 + lane - 16) * 8 : WS_FWO + (ct * 16 + (lane & 15)) * 8));
      GAS u64* f1 = (GAS u64*)(p.ws + WS_FW2 + ct * 512) + (lane < 56 ? lane : 0);
      for (unsigned spins = 0; spins < (1u << 17); ++spins) {
        const u64 a = __hip_atomic_load(f0, RLX_AGENT), c = __hip_atomic_load(f1, RLX_AGENT);
        if (__all(a == TAG && c == TAG)) break;
      }
      CFENCE();
    }
    BAR();
    STAMP(6);
    {
      const unsigned dst = w < 4 ? L_AK + w * KPL : L_AV + ((w - 4) >> 1) * VHD + (w & 1) * VPL;
#pragma unroll
      for (int src = 0; src < 2; ++src)
        if (ci - 2 + src >= 0) glds2x1(rWS, lane * 16, WS_KV + ((ci - 2 + src) * 16 + cj) * 16384 + w * 2048, dst + src * 2048);
    }
    {
      const float qs = 0.25f * 1.4426950408889634f;
      unsigned pq[2][2];
#pragma unroll
      for (int nt = 0; nt < 2; ++nt) {
        const f4 u = ldsf4(L_PERS + (nt * 16 + 4 * g) * 4), v = ldsf4(L_PERS + (96 + nt * 16 + 4 * g) * 4);
        pq[nt][0] = pk(qs * (rstd * accQ[nt][0] + nmu * u[0] + v[0]), qs * (rstd * accQ[nt][1] + nmu * u[1] + v[1]));
        pq[nt][1] = pk(qs * (rstd * accQ[nt][2] + nmu * u[2] + v[2]), qs * (rstd * accQ[nt][3] + nmu * u[3] + v[3]));
      }
      auto r0 = __builtin_amdgcn_permlane16_swap(pq[0][0], pq[1][0], false, false); auto r1 = __builtin_amdgcn_permlane16_swap(pq[0][1], pq[1][1], false, false);
      *(LAS v4u*)(sm + L_AQ + ((g & 1) * 2 + (g >> 1)) * 2048 + (16 * w + tok) * 16) = v4u{r0[0], r1[0], r0[1], r1[1]};
    }
    VM0();
    BAR();
    STAMP(7);
  }

  const int orow = 128 * ci + 64 * hf + 16 * tt + tok;
  v2u xr0, xr1;
  {
    const int t = w & 3, hd = w >> 2;
    const int kt0 = ci >= 2 ? 0 : 8 - 4 * ci - t;
    f16v O;
    const u16* xres = p.x + (size_t)orow * DM + ocol;
    if (ci >= 2) attention<true>(t, hd, 0, lane, mU, mL, O, rWS, WS_TWO + ct * 65536, WS_TW1 + cj * 7 * 16384, w, xres, xr0, xr1, stampv);
    else attention<false>(t, hd, kt0, lane, mU, mL, O, rWS, WS_TWO + ct * 65536, WS_TW1 + cj * 7 * 16384, w, xres, xr0, xr1, stampv);
    const int h = lane >> 5;
    const float inv = __builtin_amdgcn_rcpf(O[8]);
    const f4 b0 = ldsf4(L_PERS + (96 + 32 + hd * 16 + 4 * h) * 4), b1 = ldsf4(L_PERS + (96 + 32 + hd * 16 + 8 + 4 * h) * 4);
    const v4u o = {pk(O[0] * inv + b0[0], O[1] * inv + b0[1]), pk(O[2] * inv + b0[2], O[3] * inv + b0[3]),
                   pk(O[4] * inv + b1[0], O[5] * inv + b1[1]), pk(O[6] * inv + b1[2], O[7] * inv + b1[3])};
    const int qr = lane & 31;
    st16(rWS, o, WS_OF + (ci * 8 * 16 + cj) * 1024 + ((2 * t + (qr >> 4)) * 16) * 1024 + ((2 * hd + h) * 16 + (qr & 15)) * 16);
  }
  STAMP(24);
  VM0();
  BAR();
  if (tid == 256) arrive(p.ws, WS_FP + (1 * 16 + ci) * 128 + cj * 8, TAG);
  STAMP(8);
  float b1v = 0.f;
  v8s aux2[2];
  float Cw = 0, Cw2 = 0, Cws = 0, Cs = 0, Cs2 = 0;
#define MODSUMS() do { \
      _Pragma("unroll") for (int r = 0; r < 4; ++r) { \
        const float w0 = rbf(1.f + bflo(mc0[r])), w1 = rbf(1.f + bfhi(mc0[r])), s0 = bflo(md0[r]), s1_ = bfhi(md0[r]); \
        Cw += w0 + w1; Cw2 += w0 * w0 + w1 * w1; Cws += w0 * s0 + w1 * s1_; Cs += s0 + s1_; Cs2 += s0 * s0 + s1_ * s1_; } \
      Cw = sumg(rowsum16(Cw)); Cw2 = sumg(rowsum16(Cw2)); Cws = sumg(rowsum16(Cws)); Cs = sumg(rowsum16(Cs)); Cs2 = sumg(rowsum16(Cs2)); \
      _Pragma("unroll") for (int u = 0; u < 2; ++u) { v4u t; \
        _Pragma("unroll") for (int d = 0; d < 4; ++d) t[d] = tok == 0 ? pk(1.f + bflo(mx[u][d]), 1.f + bfhi(mx[u][d])) : (tok == 1 ? mx[u][d] : 0x3f803f80u); \
        aux2[u] = asv8(t); } \
      asm volatile("" : "+v"(Cw), "+v"(Cw2), "+v"(Cws), "+v"(Cs), "+v"(Cs2), "+v"(aux2[0]), "+v"(aux2[1])); } while (0)
  if (w != 4) {
#pragma unroll
    for (int e = 0; e < 3; ++e) { const int gi = wq + 7 * e; if (gi < 16) glds4x1(rWS, lane * 16, WS_TW1 + (cj * 7 + 2) * 16384 + gi * 4096, L_W1 + 32768 + gi * 4096); }
  }
  MODSUMS();
  if (w == 4) wait_words(p.ws, WS_FP + (1 * 16 + ci) * 128, 16, lane, TAG);
  BAR();
  STAMP(9);

  float x1[8];
  {
    v8s oa[8];
    const unsigned osrc = WS_OF + ((ci * 8 + 4 * hf + tt) * 16 + 8 * kh) * 1024;
#pragma unroll
    for (int s = 0; s < 3; ++s) oa[s] = asv8(ld16(rWS, lane * 16, osrc + s * 1024));
    const unsigned bo = opq(L_WO + kh * 32768 + lane * 16);
    f4 acc[4];
#pragma unroll
    for (int nt = 0; nt < 4; ++nt) acc[nt] = f4{0, 0, 0, 0};
    {
      v8s W[2][4];
#pragma unroll
      for (int s = 0; s < 2; ++s)
#pragma unroll
        for (int nt = 0; nt < 4; ++nt) W[s][nt] = lds16(bo + (s * 4 + nt) * 1024);
      SB();
#pragma unroll
      for (int s = 3; s < 8; ++s) oa[s] = asv8(ld16(rWS, lane * 16, osrc + s * 1024));
      if (tid < 112) { const int nt = tid >> 4; b1v = bflo(p.b1[(nt == 6 ? 112 * cj + (par ? 0 : 96) : 112 * cj + 16 * par + nt * 16) + (tid & 15)]); }
      SB();
#pragma unroll
      for (int s = 0; s < 8; ++s)
#pragma unroll
        for (int nt = 0; nt < 4; ++nt) {
          acc[nt] = MFMA16(W[s & 1][nt], oa[s], acc[nt]);
          if (s + 2 < 8) W[s & 1][nt] = lds16(bo + ((s + 2) * 4 + nt) * 1024);
          SB();
        }
    }
    STAMP(26);
    VM0();
    BAR();
    *(LAS f4*)(sm + L_XCH + w * 2048 + lane * 16) = kh ? acc[0] : acc[2];
    *(LAS f4*)(sm + L_XCH + w * 2048 + 1024 + lane * 16) = kh ? acc[1] : acc[3];
    BAR();
    const f4 o0 = ldsf4(L_XCH + (w ^ 4) * 2048 + lane * 16), o1 = ldsf4(L_XCH + (w ^ 4) * 2048 + 1024 + lane * 16);
    const f4 t0 = (kh ? acc[2] : acc[0]) + o0, t1 = (kh ? acc[3] : acc[1]) + o1;
    const float xin[8] = {bflo(xr0[0]), bfhi(xr0[0]), bflo(xr0[1]), bfhi(xr0[1]), bflo(xr1[0]), bfhi(xr1[0]), bflo(xr1[1]), bfhi(xr1[1])};
    const float ga[8] = {bflo(gaR[0]), bfhi(gaR[0]), bflo(gaR[1]), bfhi(gaR[1]), bflo(gaR[2]), bfhi(gaR[2]), bflo(gaR[3]), bfhi(gaR[3])};
    const float cb[8] = {bflo(cbR[0]), bfhi(cbR[0]), bflo(cbR[1]), bfhi(cbR[1]), bflo(cbR[2]), bfhi(cbR[2]), bflo(cbR[3]), bfhi(cbR[3])};
    const float sv[8] = {bflo(sbR[0]), bfhi(sbR[0]), bflo(sbR[1]), bfhi(sbR[1]), bflo(sbR[2]), bfhi(sbR[2]), bflo(sbR[3]), bfhi(sbR[3])};
    float wv[8], xw[8];
#pragma unroll
    for (int r = 0; r < 8; ++r) { x1[r] = rbf(xin[r] + ga[r] * (r < 4 ? t0[r & 3] : t1[r & 3])); wv[r] = rbf(1.f + cb[r]); xw[r] = x1[r] * wv[r]; }
    st16(rWS, v4u{pk(xw[0], xw[1]), pk(xw[2], xw[3]), pk(xw[4], xw[5]), pk(xw[6], xw[7])}, WS_X1F + ((ci * 8 + 4 * hf + tt) * 16 + 2 * ct + kh) * 1024 + lane * 16);
    SB();
    float s1 = 0, s2 = 0, s3 = 0, s4 = 0, s5 = 0, s6 = 0;
#pragma unroll
    for (int r = 0; r < 8; ++r) { s1 += x1[r]; s2 += x1[r] * x1[r]; s3 += xw[r]; s4 += xw[r] * wv[r]; s5 += xw[r] * xw[r]; s6 += xw[r] * sv[r]; }
    s1 = sumg(s1); s2 = sumg(s2); s3 = sumg(s3); s4 = sumg(s4); s5 = sumg(s5); s6 = sumg(s6);
    const unsigned rec = (ci * 16 + 2 * ct + kh) * 128 + 64 * hf + 16 * tt + tok;
    if (g == 0) st16(rWS, v4u{__float_as_uint(s1), __float_as_uint(s2), __float_as_uint(s3), __float_as_uint(s4)}, WS_ST2A + rec * 16);
    if (g == 1) st8(rWS, v2u{__float_as_uint(s5), __float_as_uint(s6)}, WS_ST2B + rec * 8);
  }
  if (w < 4) {
    glds4x1(rWS, lane * 16, WS_TW1 + (cj * 7 + 6) * 16384 + w * 4096, L_W1S + w * 4096);
    glds4x1(rWS, lane * 16, WS_TW2 + ct * 229376 + 7 * 16384 + w * 4096, 49152 + w * 4096);
    asm volatile("s_waitcnt vmcnt(8)" ::: "memory");
  } else {
    glds4x1(rWS, lane * 16, WS_TW2 + ct * 229376 + (w - 4) * 4096, 32768 + (w - 4) * 4096);
    asm volatile("s_waitcnt vmcnt(4)" ::: "memory");
  }
  BAR();
  if (tid == 256) arrive(p.ws, WS_FP + (2 * 16 + ci) * 128 + cj * 8, TAG);
  STAMP(10);
  if (w == 4) asm volatile("s_waitcnt vmcnt(1)" ::: "memory"); else VM0();
  BAR();

  v4u stg[8];
  {
    const unsigned bP = opq(L_W1 + lane * 16), bR = opq(L_W1 + 65536 + lane * 16), bS = opq(L_W1S + lane * 16);
#define W1F(nt, s) lds16((nt) == 6 ? bS + (s) * 1024 : (nt) < 4 ? bP + (nt) * 16384 + (s) * 1024 : bR + ((nt) - 4) * 16384 + (s) * 1024)
    {
      f4 au[7];
#pragma unroll
      for (int nt = 0; nt < 7; ++nt) au[nt] = f4{0, 0, 0, 0};
      const unsigned c0 = opq(bP + w * 2048), d0 = opq(bR + w * 2048), e0 = opq(bS + w * 2048);
#pragma unroll
      for (int u = 0; u < 2; ++u) {
        v8s W[7];
#pragma unroll
        for (int nt = 0; nt < 7; ++nt) W[nt] = lds16(nt == 6 ? e0 + u * 1024 : nt < 4 ? c0 + nt * 16384 + u * 1024 : d0 + (nt - 4) * 16384 + u * 1024);
#pragma unroll
        for (int nt = 0; nt < 7; ++nt) au[nt] = MFMA16(W[nt], aux2[u], au[nt]);
      }
      if (tok < 3) {
#pragma unroll
        for (int nt = 0; nt < 7; ++nt) *(LAS f4*)(sm + L_XCH + ((w * 3 + tok) * 112 + nt * 16 + 4 * g) * 4) = au[nt];
      }
    }
    BAR();
    if (tid < 112) *(LAS float*)(sm + L_XF + (336 + tid) * 4) = b1v;
    {
      const int e0 = w < 4 ? tid : tid - 64;
      if (w != 4 && e0 < 336) {
        float s = 0;
#pragma unroll
        for (int e = 0; e < 8; ++e) s += *(const LAS float*)(sm + L_XCH + (e * 336 + e0) * 4);
        *(LAS float*)(sm + L_XF + e0 * 4) = s;
      }
    }
    if (w == 4) wait_words(p.ws, WS_FP + (2 * 16 + ci) * 128, 16, lane, TAG);
    BAR();
    STAMP(11);
    v8s act2[16];
    const unsigned asrc = WS_X1F + (ci * 8 + w) * 16 * 1024;
#pragma unroll
    for (int s = 0; s < 4; ++s) act2[s] = asv8(ld16(rWS, lane * 16, asrc + s * 1024));
    v4u sa[4]; v2u sb[4];
    f4 acc[7];
#pragma unroll
    for (int nt = 0; nt < 7; ++nt) acc[nt] = f4{0, 0, 0, 0};
    {
      v8s W[6];
#pragma unroll
      for (int nt = 0; nt < 6; ++nt) W[nt] = W1F(nt, 0);
      SB();
#pragma unroll
      for (int s = 0; s < 16; ++s) {
        if (s == 0) act2[4] = asv8(ld16(rWS, lane * 16, asrc + 4 * 1024));
        if (s + 5 < 16) act2[s + 5] = asv8(ld16(rWS, lane * 16, asrc + (s + 5) * 1024));
        else if (s < 15) { sa[s - 11] = ld16(rWS, ((4 * g + s - 11) * 128 + 16 * w + tok) * 16, WS_ST2A + ci * 16 * 128 * 16);
                           sb[s - 11] = ld8(rWS, ((4 * g + s - 11) * 128 + 16 * w + tok) * 8, WS_ST2B + ci * 16 * 128 * 8); }
#pragma unroll
        for (int nt = 0; nt < 6; ++nt) {
          acc[nt] = MFMA16(W[nt], act2[s], acc[nt]);
          if (s + 1 < 16) W[nt] = W1F(nt, s + 1);
          SB();
        }
      }
    }
    STAMP(18);
    CSTAMP(42);
    BAR();
    v8s WB[4];
#pragma unroll
    for (int s = 0; s < 4; ++s) WB[s] = W1F(6, s);
    float s1 = 0, s2 = 0, s3 = 0, s4 = 0, s5 = 0, s6 = 0;
#pragma unroll
    for (int e = 0; e < 4; ++e) { s1 += __uint_as_float(sa[e][0]); s2 += __uint_as_float(sa[e][1]); s3 += __uint_as_float(sa[e][2]); s4 += __uint_as_float(sa[e][3]); s5 += __uint_as_float(sb[e][0]); s6 += __uint_as_float(sb[e][1]); }
    s1 = sumg(s1); s2 = sumg(s2); s3 = sumg(s3); s4 = sumg(s4); s5 = sumg(s5); s6 = sumg(s6);
    const float mu1 = s1 * (1.f / 512), var1 = fmaxf(s2 * (1.f / 512) - mu1 * mu1, 0.f), r1 = __builtin_amdgcn_rsqf(var1 + 1e-5f);
    const float mu2 = (r1 * (s3 - mu1 * Cw) + Cs) * (1.f / 512);
    const float hh = (r1 * r1 * (s5 - 2.f * mu1 * s4 + mu1 * mu1 * Cw2) + 2.f * r1 * (s6 - mu1 * Cws) + Cs2) * (1.f / 512);
    const float r2 = __builtin_amdgcn_rsqf(fmaxf(hh - mu2 * mu2, 0.f) + 1e-5f);
    const float cA = r2 * r1, cB = -cA * mu1, cC = r2, cD = -r2 * mu2;
    const unsigned S0 = (7 * cj + par) >> 1;
    const unsigned hb = WS_H1F + (ci * 8 + w) * 56 * 1024;
    const unsigned xf = opq(L_XF + 16 * g);
#define BIAS4(nt) __builtin_elementwise_fma(f4{cB, cB, cB, cB}, ldsf4(xf + (nt) * 64), __builtin_elementwise_fma(f4{cC, cC, cC, cC}, ldsf4(xf + 448 + (nt) * 64), \
                    __builtin_elementwise_fma(f4{cD, cD, cD, cD}, ldsf4(xf + 896 + (nt) * 64), ldsf4(xf + 1344 + (nt) * 64))))
#define GELU4(o, a, bias) do { const f4 z_ = __builtin_elementwise_fma(f4{cA, cA, cA, cA}, a, bias); const f4 q_ = __builtin_elementwise_fma(z_ * z_, f4{-0.1029432397f, -0.1029432397f, -0.1029432397f, -0.1029432397f}, \
        f4{-2.3022081985f, -2.3022081985f, -2.3022081985f, -2.3022081985f}) * z_; f4 e_; \
        _Pragma("unroll") for (int r = 0; r < 4; ++r) e_[r] = __builtin_amdgcn_exp2f(q_[r]); \
        e_ += f4{1.f, 1.f, 1.f, 1.f}; \
        _Pragma("unroll") for (int r = 0; r < 4; ++r) e_[r] = __builtin_amdgcn_rcpf(e_[r]); \
        o = z_ * e_; } while (0)
    {
      unsigned hp[2][2];
      f4 bias = BIAS4(0);
      PIN(bias);
      SB();
#pragma unroll
      for (int nt = 0; nt < 6; ++nt) {
#pragma unroll
        for (int k = (nt * 16) / 6; k < ((nt + 1) * 16) / 6; ++k) { acc[6] = MFMA16(WB[k & 3], act2[k], acc[6]); if (k + 4 < 16) WB[k & 3] = W1F(6, k + 4); }
        if ((nt & 1) == 0) { const int gi = w * 3 + (nt >> 1), it = 1 + (gi >> 3), khh = (gi >> 2) & 1, pc4 = gi & 3;
          glds4x1(rWS, lane * 16, WS_TW2 + ct * 229376 + (khh * 7 + it) * 16384 + pc4 * 4096, 65536u + (it - 1) * 32768u + khh * 16384u + pc4 * 4096u); }
        if (nt >= 1 && nt < 5) {
#pragma unroll
          for (int e = 2 * (nt - 1); e < 2 * nt; ++e) { const int pc = w * 8 + e; const int chunk = (pc >> 4) == 0 ? 5 : ((pc >> 4) == 1 ? 12 : ((pc >> 4) == 2 ? 6 : 13));
            stg[e] = ld16(rWS, lane * 16, WS_TW2 + ct * 229376 + chunk * 16384 + (pc & 15) * 1024); }
        }
        f4 nb = bias;
        if (nt < 5) nb = BIAS4(nt + 1); else nb = BIAS4(6);
        f4 o;
        GELU4(o, acc[nt], bias);
        hp[nt & 1][0] = pk(o[0], o[1]); hp[nt & 1][1] = pk(o[2], o[3]);
        PIN(hp[nt & 1][0]); PIN(hp[nt & 1][1]); PIN(nb);
        bias = nb;
        if (nt & 1) st16(rWS, v4u{hp[0][0], hp[0][1], hp[1][0], hp[1][1]}, hb + (S0 + (nt >> 1)) * 1024 + lane * 16);
        SB();
      }
      STAMP(20);
      CSTAMP(43);
      f4 o;
      GELU4(o, acc[6], bias);
      const unsigned NT = 7 * cj + (par ? 0 : 6);
      st8(rWS, v2u{pk(o[0], o[1]), pk(o[2], o[3])}, hb + (NT >> 1) * 1024 + (NT & 1) * 512 + lane * 8);
    }
  }
  STAMP(25);
  VM0();
  BAR();
  if (tid == 256) arrive(p.ws, WS_FP + (3 * 16 + ci) * 128 + cj * 8, TAG);
  STAMP(13);
  if (w != 4) {
#pragma unroll
    for (int e = 0; e < 2; ++e) {
      const int gi = wq + 7 * e;
      if (gi < 8) glds4x1(rWS, lane * 16, WS_TW2 + ct * 229376 + ((gi >> 2) * 7 + 4) * 16384 + (gi & 3) * 4096, (gi >> 2) * 16384u + (gi & 3) * 4096u);
    }
  } else {
    if (b == 0) {
      GAS u64* f = (GAS u64*)(p.ws + WS_F0) + lane;
      for (unsigned spins = 0; spins < (1u << 17); ++spins) {
        bool ok = true;
#pragma unroll
        for (int i = 0; i < 16; ++i) ok &= __hip_atomic_load(f + i * 64, RLX_AGENT) == TAG;
        if (__all(ok)) break;
      }
      if (lane == 0) __hip_atomic_store((GAS u64*)(p.ws + WS_EPOCH), TAG, RLX_AGENT);
    }
    wait_words(p.ws, WS_FP + (3 * 16 + ci) * 128, 16, lane, TAG);
  }
  BAR();
  STAMP(14);

  {
    v8s ha[28];
    const unsigned hb = WS_H1F + ((ci * 8 + 4 * hf + tt) * 56 + 28 * kh) * 1024;
#define HLOAD(s) do { if ((s) % 7 == 3) { const v2u lo_ = ld8(rWS, lane * 8, hb + (s) * 1024), hi_ = ld8(rWS, lane * 8 + 512, hb + (s) * 1024); ha[s] = asv8(v4u{lo_[0], lo_[1], hi_[0], hi_[1]}); } \
                      else ha[s] = asv8(ld16(rWS, lane * 16, hb + (s) * 1024)); } while (0)
#pragma unroll
    for (int s = 0; s < 3; ++s) HLOAD(s);
    const unsigned bw = opq(kh * 16384 + lane * 16), bw2 = opq(bw + 65536), bw3 = opq(bw + 131072);
    f4 acc[4];
#pragma unroll
    for (int nt = 0; nt < 4; ++nt) acc[nt] = f4{0, 0, 0, 0};
#define W2F(nt, S) lds16((((S) >> 2) == 0 || ((S) >> 2) == 5 ? bw + 32768 : ((S) >> 2) == 1 || ((S) >> 2) == 6 ? bw2 : ((S) >> 2) == 2 ? bw2 + 32768 : ((S) >> 2) == 3 ? bw3 : bw) + (((S) & 3) * 4 + (nt)) * 1024)
    {
      v8s W[2][4];
#pragma unroll
      for (int S = 0; S < 2; ++S)
#pragma unroll
        for (int nt = 0; nt < 4; ++nt) W[S][nt] = W2F(nt, S);
      SB();
#pragma unroll
      for (int s = 3; s < 8; ++s) HLOAD(s);
      SB();
#pragma unroll
      for (int S = 0; S < 28; ++S) {
        if (S + 8 < 28) HLOAD(S + 8);
        if (S == 8) BAR();
        if (S >= 8 && S < 16) { const int e = S - 8, pc = w * 8 + e; *(LAS v4u*)(sm + 32768 + (pc >> 5) * 32768 + ((pc >> 4) & 1) * 16384 + (pc & 15) * 1024 + lane * 16) = stg[e]; }
        if (S == 18) BAR();
#pragma unroll
        for (int nt = 0; nt < 4; ++nt) {
          acc[nt] = MFMA16(W[S & 1][nt], ha[S], acc[nt]);
          if (S + 2 < 28) W[S & 1][nt] = W2F(nt, S + 2);
          SB();
        }
      }
    }
    STAMP(22);
    BAR();
    *(LAS f4*)(sm + L_W2X + w * 2048 + lane * 16) = kh ? acc[0] : acc[2];
    *(LAS f4*)(sm + L_W2X + w * 2048 + 1024 + lane * 16) = kh ? acc[1] : acc[3];
    BAR();
    const f4 o0 = ldsf4(L_W2X + (w ^ 4) * 2048 + lane * 16), o1 = ldsf4(L_W2X + (w ^ 4) * 2048 + 1024 + lane * 16);
    const f4 t0 = (kh ? acc[2] : acc[0]) + o0, t1 = (kh ? acc[3] : acc[1]) + o1;
    const float gb[8] = {bflo(gbR[0]), bfhi(gbR[0]), bflo(gbR[1]), bfhi(gbR[1]), bflo(gbR[2]), bfhi(gbR[2]), bflo(gbR[3]), bfhi(gbR[3])};
    const float bb[8] = {bflo(b2R[0][0]), bfhi(b2R[0][0]), bflo(b2R[0][1]), bfhi(b2R[0][1]), bflo(b2R[1][0]), bfhi(b2R[1][0]), bflo(b2R[1][1]), bfhi(b2R[1][1])};
    float o[8];
#pragma unroll
    for (int r = 0; r < 8; ++r) o[r] = x1[r] + gb[r] * ((r < 4 ? t0[r & 3] : t1[r & 3]) + bb[r]);
    auto r0 = __builtin_amdgcn_permlane16_swap(pk(o[0], o[1]), pk(o[4], o[5]), false, false);
    auto r1 = __builtin_amdgcn_permlane16_swap(pk(o[2], o[3]), pk(o[6], o[7]), false, false);
    *(v4u*)(p.out + (size_t)orow * DM + 64 * ct + 32 * kh + (g & 1) * 16 + (g >> 1) * 8) = v4u{r0[0], r1[0], r0[1], r1[1]};
  }
  STAMP(15);
  CSTAMP(41);
  STAMP_FLUSH(0);
}

extern "C" void kernel_launch(void* const* d_in, const int* in_sizes, int n_in, void* d_out, int out_size, void* d_ws, size_t ws_size, hipStream_t stream) {
  Params p;
  p.x = (const u16*)d_in[0]; p.wqkv = (const u16*)d_in[1]; p.wo = (const u16*)d_in[2];
  p.w1 = (const u16*)d_in[4]; p.b1 = (const u16*)d_in[5]; p.w2 = (const u16*)d_in[6]; p.b2 = (const u16*)d_in[7];
  p.c = (const u16*)d_in[8]; p.wmod = (const u16*)d_in[9]; p.out = (u16*)d_out; p.ws = (char*)d_ws;
  fused_block<<<256, 512, 0, stream>>>(p);
}
```

```cpp
#include <hip/hip_runtime.h>
#include <hip/hip_bf16.h>
#include <stdint.h>

typedef unsigned short u16;
typedef short v4s __attribute__((ext_vector_type(4)));
typedef short v8s __attribute__((ext_vector_type(8)));
typedef float f2 __attribute__((ext_vector_type(2)));
typedef float f4 __attribute__((ext_vector_type(4)));
typedef float f16v __attribute__((ext_vector_type(16)));
typedef unsigned v2u __attribute__((ext_vector_type(2)));
typedef unsigned v4u __attribute__((ext_vector_type(4)));
typedef __bf16 b2 __attribute__((ext_vector_type(2)));
#define LAS __attribute__((address_space(3)))
#define GAS __attribute__((address_space(1)))
#define DI __device__ __forceinline__
typedef __amdgpu_buffer_rsrc_t srd_t;

constexpr int SEQ = 2048, DM = 512, FF = 1792;
constexpr unsigned WS_EPOCH = 0;
constexpr unsigned WS_F0 = 4096;
constexpr unsigned WS_FP = WS_F0 + 8192;
constexpr unsigned WS_FM = WS_FP + 4 * 16 * 128;
constexpr unsigned WS_FQ = WS_FM + 2048;
constexpr unsigned WS_FW1 = WS_FQ + 2048;
constexpr unsigned WS_FWO = WS_FW1 + 2048;
constexpr unsigned WS_FW2 = WS_FWO + 1024;
static_assert(WS_FW2 + 4096 <= (64 << 10), "flags");
constexpr unsigned WS_WV = 64 << 10;
constexpr unsigned WS_MOD = 128 << 10;
constexpr unsigned WS_ST1 = 192 << 10;
constexpr unsigned WS_ST2A = 512 << 10;
constexpr unsigned WS_ST2B = 1024 << 10;
constexpr unsigned WS_HF = 2u << 20;
constexpr unsigned WS_KV = 4u << 20;
constexpr unsigned WS_OF = 8u << 20;
constexpr unsigned WS_X1F = 10u << 20;
constexpr unsigned WS_H1F = 12u << 20;
constexpr unsigned WS_TQ = 19u << 20;
constexpr unsigned WS_TW1 = WS_TQ + (1536u << 10);
constexpr unsigned WS_TWO = WS_TW1 + (1792u << 10);
constexpr unsigned WS_TW2 = WS_TWO + (512u << 10);
constexpr unsigned WS_STAMP = 25u << 20;
constexpr unsigned WS_BYTES = 27u << 20;
static_assert(WS_TW2 + (1792u << 10) <= WS_STAMP, "ws");
constexpr unsigned L_CA = 98304;
constexpr unsigned L_XS = 131072;
constexpr unsigned L_S1 = L_XS + 8192;
constexpr unsigned L_SO = L_S1 + 7168;
constexpr unsigned L_SC = L_SO + 12288;
constexpr unsigned L_CAP = L_SC + 2048;
constexpr unsigned L_CNT = L_CAP + 512;
static_assert(L_CNT + 16 <= 163840, "lds");
constexpr unsigned L_SQ = 0;
constexpr unsigned L_M2 = 8192;
constexpr unsigned L_WQKV = 0;
constexpr unsigned L_PARTU = 98304;
constexpr unsigned L_PARTV = 32768;
constexpr unsigned L_AK = 98304;
constexpr unsigned KPL = 6144;
constexpr unsigned L_AV = L_AK + 4 * KPL;
constexpr unsigned VPL = 6208, VHD = 12416;
constexpr unsigned L_PSA = L_AK + KPL, L_PSB = L_AK + 3 * KPL, L_PCB = L_AV + VPL, L_PGA = L_AV + VHD + VPL, L_PGB = L_PGA + 1024, L_B2C = L_PGA + 2048;
constexpr unsigned L_PSA2 = L_AK + 2 * KPL, L_PSB2 = L_AV, L_PCB2 = L_AV + VHD;
constexpr unsigned L_FIN = L_AK;
constexpr unsigned L_ONE = L_AV + 2 * VHD;
constexpr unsigned L_AQ = L_ONE + 6400;
constexpr unsigned L_PERS = L_AQ + 8192;
static_assert(L_PERS + 1024 <= 163840, "lds");
static_assert((L_AV % 256) == 0 && (L_ONE % 256) == 0, "bank plan");
constexpr unsigned L_WO = 0;
constexpr unsigned L_W1 = 65536;
constexpr unsigned L_W1S = 0;
constexpr unsigned L_XCH = 16384;
constexpr unsigned L_XF = 16384 + 12288;
constexpr unsigned L_W2X = 98304;

struct Params { const u16* x; const u16* wqkv; const u16* wo; const u16* w1; const u16* b1; const u16* w2; const u16* b2; const u16* c; const u16* wmod; u16* out; char* ws; };

DI float bflo(unsigned v) { return __uint_as_float(v << 16); }
DI float bfhi(unsigned v) { return __uint_as_float(v & 0xffff0000u); }
DI unsigned pk(float a, float b) { f2 v = {a, b}; b2 r = __builtin_convertvector(v, b2); return __builtin_bit_cast(unsigned, r); }
DI float rbf(float a) { return bflo(pk(a, 0.f)); }
DI v8s cat(v4s a, v4s b) { return __builtin_shufflevector(a, b, 0, 1, 2, 3, 4, 5, 6, 7); }
DI v4s trd(unsigned a) { return __builtin_amdgcn_ds_read_tr16_b64_v4i16((LAS v4s*)(uintptr_t)a); }
DI v8s asv8(v4u v) { return __builtin_bit_cast(v8s, v); }
DI v4u asv4u(v8s v) { return __builtin_bit_cast(v4u, v); }
DI v8s lds16(unsigned a) { return *(const LAS v8s*)(uintptr_t)a; }
DI f4 ldsf4(unsigned a) { return *(const LAS f4*)(uintptr_t)a; }
DI v2u lds8(unsigned a) { return *(const LAS v2u*)(uintptr_t)a; }
#define MFMA16(a, b, c) __builtin_amdgcn_mfma_f32_16x16x32_bf16((a), (b), (c), 0, 0, 0)
#define MFMA32(a, b, c) __builtin_amdgcn_mfma_f32_32x32x16_bf16((a), (b), (c), 0, 0, 0)
#define BAR() asm volatile("s_waitcnt lgkmcnt(0)\n\ts_barrier" ::: "memory")
#define VM0() asm volatile("s_waitcnt vmcnt(0)" ::: "memory")
#define LG0() asm volatile("s_waitcnt lgkmcnt(0)" ::: "memory")
#define CFENCE() asm volatile("" ::: "memory")
#define SB() __builtin_amdgcn_sched_barrier(0)
DI unsigned opq(unsigned v) { asm volatile("" : "+v"(v)); return v; }
DI void glds(srd_t r, unsigned voff, unsigned soff, unsigned ldsaddr) {
  asm volatile("s_mov_b32 m0, %0\n\ts_nop 0\n\tbuffer_load_dwordx4 %1, %2, %3 offen lds" ::"s"(ldsaddr), "v"(voff), "s"(r), "s"(soff) : "memory");
}
DI void glds1(srd_t r, unsigned voff, unsigned soff, unsigned ldsaddr) {
  asm volatile("s_mov_b32 m0, %0\n\ts_nop 0\n\tbuffer_load_dwordx4 %1, %2, %3 offen sc1 lds" ::"s"(ldsaddr), "v"(voff), "s"(r), "s"(soff) : "memory");
}
DI void glds4(srd_t r, unsigned voff, unsigned soff, unsigned ldsaddr) {
  asm volatile("s_mov_b32 m0, %0\n\ts_nop 0\n\tbuffer_load_dwordx4 %1, %2, %3 offen lds\n\tbuffer_load_dwordx4 %1, %2, %3 offen offset:1024 lds\n\t"
               "buffer_load_dwordx4 %1, %2, %3 offen offset:2048 lds\n\tbuffer_load_dwordx4 %1, %2, %3 offen offset:3072 lds" ::"s"(ldsaddr), "v"(voff), "s"(r), "s"(soff) : "memory");
}
DI void glds4x1(srd_t r, unsigned voff, unsigned soff, unsigned ldsaddr) {
  asm volatile("s_mov_b32 m0, %0\n\ts_nop 0\n\tbuffer_load_dwordx4 %1, %2, %3 offen sc1 lds\n\tbuffer_load_dwordx4 %1, %2, %3 offen offset:1024 sc1 lds\n\t"
               "buffer_load_dwordx4 %1, %2, %3 offen offset:2048 sc1 lds\n\tbuffer_load_dwordx4 %1, %2, %3 offen offset:3072 sc1 lds" ::"s"(ldsaddr), "v"(voff), "s"(r), "s"(soff) : "memory");
}
DI void glds2x1(srd_t r, unsigned voff, unsigned soff, unsigned ldsaddr) {
  asm volatile("s_mov_b32 m0, %0\n\ts_nop 0\n\tbuffer_load_dwordx4 %1, %2, %3 offen sc1 lds\n\tbuffer_load_dwordx4 %1, %2, %3 offen offset:1024 sc1 lds" ::"s"(ldsaddr), "v"(voff), "s"(r), "s"(soff) : "memory");
}
DI v4u ld16(srd_t r, unsigned voff, unsigned soff) { return __builtin_amdgcn_raw_buffer_load_b128(r, voff, soff, 16); }
DI v2u ld8(srd_t r, unsigned voff, unsigned soff) { return __builtin_amdgcn_raw_buffer_load_b64(r, voff, soff, 16); }
DI v4u ldp16(srd_t r, unsigned voff, unsigned soff) { return __builtin_amdgcn_raw_buffer_load_b128(r, voff, soff, 0); }
DI void stp16(srd_t r, v4u v, unsigned off) { __builtin_amdgcn_raw_buffer_store_b128(v, r, off, 0, 0); }
DI void st16(srd_t r, v4u v, unsigned off) { __builtin_amdgcn_raw_buffer_store_b128(v, r, off, 0, 16); }
DI void st8(srd_t r, v2u v, unsigned off) { __builtin_amdgcn_raw_buffer_store_b64(v, r, off, 0, 16); }
DI float sum16(float v) { auto r = __builtin_amdgcn_permlane16_swap(__float_as_uint(v), __float_as_uint(v), false, false); return __uint_as_float(r[0]) + __uint_as_float(r[1]); }
DI float sum32(float v) { auto r = __builtin_amdgcn_permlane32_swap(__float_as_uint(v), __float_as_uint(v), false, false); return __uint_as_float(r[0]) + __uint_as_float(r[1]); }
DI float max32(float v) { auto r = __builtin_amdgcn_permlane32_swap(__float_as_uint(v), __float_as_uint(v), false, false); return fmaxf(__uint_as_float(r[0]), __uint_as_float(r[1])); }
DI float sumg(float v) { return sum32(sum16(v)); }
#define RLX_AGENT __ATOMIC_RELAXED, __HIP_MEMORY_SCOPE_AGENT
typedef unsigned long long u64;
DI void arrive(char* ws, unsigned off, u64 tag) { (void)__hip_atomic_exchange((GAS u64*)(ws + off), tag, RLX_AGENT); }
DI void wait_words(char* ws, unsigned off, int n, int lane, u64 tag) {
  GAS u64* p = (GAS u64*)(ws + off) + (lane < n ? lane : 0);
  for (unsigned spins = 0; spins < (1u << 17); ++spins)
    if (__all(__hip_atomic_load(p, RLX_AGENT) == tag)) break;
  CFENCE();
}
template <int C> DI float dpp(float v) { return __uint_as_float(__builtin_amdgcn_update_dpp(0u, __float_as_uint(v), C, 0xf, 0xf, false)); }
DI float rowsum16(float v) { v += dpp<0x128>(v); v += dpp<0x124>(v); v += dpp<0x122>(v); v += dpp<0x121>(v); return v; }
#define PIN(x) asm volatile("" : "+v"(x))
#define STAMP(k)
#define STAMP_FLUSH(o)
#define CSTAMP(k)
DI float silu(float v) { return v * __builtin_amdgcn_rcpf(1.f + __builtin_amdgcn_exp2f(-1.4426950408889634f * v)); }

DI v8s trfragA(unsigned img, int half, int lane) {
  const int g = lane >> 4, q = (lane & 15) >> 2, pp = lane & 3;
  const unsigned a = img + (4 * g + q) * 64 + ((half ^ (g & 1)) * 32) + pp * 8;
  return cat(trd(a), trd(a + 1024));
}

template <bool FULL>
DI void attention(int t, int hd, int kt0, int lane, f16v mU, f16v mL, f16v& O, srd_t rWS, unsigned wo_src, unsigned w1_src, int w, const u16* xres, v2u& xr0, v2u& xr1, unsigned& stampv) {
  const int qr = lane & 31, h = lane >> 5, blk = (lane >> 4) & 1, q = (lane & 15) >> 2, pp = lane & 3;
  const v8s Qf = lds16(L_AQ + (hd * 2 + h) * 2048 + (32 * t + qr) * 16);
  const unsigned kb = opq(L_AK + (hd * 2 + h) * KPL + (32 * t + qr) * 16);
  const unsigned vb = opq((blk == 0 ? L_AV + hd * VHD + (pp >> 1) * VPL + (pp & 1) * 8 + (4 * h + q) * 16 : L_ONE + ((hd * 128 + (h ? 0 : 128)) & 255)) + t * 512);
  const f16v zero = {};
#define RIDE(n) do { if ((n) < 2) glds4x1(rWS, lane * 16, wo_src + (w * 2 + (n)) * 4096, L_WO + (w * 2 + (n)) * 4096); \
                     else glds4x1(rWS, lane * 16, w1_src + w * 4096, L_W1 + w * 4096); } while (0)
  if (FULL) {
    v8s Kf[9];
#pragma unroll
    for (int kt = 0; kt < 9; ++kt) Kf[kt] = lds16(kb + kt * 512);
    v8s V0 = cat(trd(vb), trd(vb + 128)), V1 = cat(trd(vb + 256), trd(vb + 384));
    f16v S[2];
    float m0 = -3e38f, m1 = -3e38f;
    S[0] = MFMA32(Kf[0], Qf, mL);
    SB();
#pragma unroll
    for (int kt = 0; kt < 9; ++kt) {
      if (kt < 8) S[(kt + 1) & 1] = MFMA32(Kf[kt + 1], Qf, kt + 1 == 8 ? mU : zero);
      SB();
#pragma unroll
      for (int r = 0; r < 16; r += 4) { m0 = fmaxf(fmaxf(m0, S[kt & 1][r]), S[kt & 1][r + 1]); m1 = fmaxf(fmaxf(m1, S[kt & 1][r + 2]), S[kt & 1][r + 3]); }
      SB();
    }
    const float m = max32(fmaxf(m0, m1));
    STAMP(23);
    f16v nm;
#pragma unroll
    for (int r = 0; r < 16; ++r) { nm[r] = -m; mU[r] -= m; mL[r] -= m; }
    O = zero;
    S[0] = MFMA32(Kf[0], Qf, mL);
    SB();
#pragma unroll
    for (int kt = 0; kt < 9; ++kt) {
      if (kt < 8) S[(kt + 1) & 1] = MFMA32(Kf[kt + 1], Qf, kt + 1 == 8 ? mU : nm);
      if (kt == 0) RIDE(0);
      if (kt == 3) RIDE(1);
      if (kt == 6) RIDE(2);
      SB();
      v4u P0, P1;
#pragma unroll
      for (int r = 0; r < 4; ++r) P0[r] = pk(__builtin_amdgcn_exp2f(S[kt & 1][2 * r]), __builtin_amdgcn_exp2f(S[kt & 1][2 * r + 1]));
      O = MFMA32(V0, asv8(P0), O);
      if (kt < 8) V0 = cat(trd(vb + (kt + 1) * 512), trd(vb + (kt + 1) * 512 + 128));
      if (kt == 2) xr0 = *(const v2u*)xres;
      if (kt == 5) xr1 = *(const v2u*)(xres + 16);
      SB();
#pragma unroll
      for (int r = 0; r < 4; ++r) P1[r] = pk(__builtin_amdgcn_exp2f(S[kt & 1][8 + 2 * r]), __builtin_amdgcn_exp2f(S[kt & 1][8 + 2 * r + 1]));
      O = MFMA32(V1, asv8(P1), O);
      if (kt < 8) V1 = cat(trd(vb + (kt + 1) * 512 + 256), trd(vb + (kt + 1) * 512 + 384));
      SB();
    }
  } else {
#pragma unroll
    for (int n = 0; n < 3; ++n) RIDE(n);
    xr0 = *(const v2u*)xres; xr1 = *(const v2u*)(xres + 16);
    float m = -3e38f;
#pragma unroll
    for (int kt = 0; kt < 9; ++kt) {
      if (kt >= kt0) {
        const f16v S = MFMA32(lds16(kb + kt * 512), Qf, kt == 8 ? mU : zero);
#pragma unroll
        for (int r = 0; r < 16; r += 2) m = fmaxf(fmaxf(m, S[r]), S[r + 1]);
      }
    }
    m = max32(m);
    f16v nm;
#pragma unroll
    for (int r = 0; r < 16; ++r) { nm[r] = -m; mU[r] -= m; }
    O = zero;
#pragma unroll
    for (int kt = 0; kt < 9; ++kt) {
      if (kt >= kt0) {
        const f16v S = MFMA32(lds16(kb + kt * 512), Qf, kt == 8 ? mU : nm);
        v4u P0, P1;
#pragma unroll
        for (int r = 0; r < 4; ++r) {
          P0[r] = pk(__builtin_amdgcn_exp2f(S[2 * r]), __builtin_amdgcn_exp2f(S[2 * r + 1]));
          P1[r] = pk(__builtin_amdgcn_exp2f(S[8 + 2 * r]), __builtin_amdgcn_exp2f(S[8 + 2 * r + 1]));
        }
        const v8s V0 = cat(trd(vb + kt * 512), trd(vb + kt * 512 + 128));
        const v8s V1 = cat(trd(vb + kt * 512 + 256), trd(vb + kt * 512 + 384));
        O = MFMA32(V0, asv8(P0), O);
        O = MFMA32(V1, asv8(P1), O);
      }
    }
  }
#undef RIDE
}

__global__ void __launch_bounds__(512, 2) fused_block(Params p) {
  __shared__ __attribute__((aligned(16))) char smem[163840];
  LAS char* sm = (LAS char*)smem;
  const int tid = threadIdx.x, lane = tid & 63;
  const int w = __builtin_amdgcn_readfirstlane(tid >> 6);
  const int b = blockIdx.x, ci = ((b & 7) >> 1) * 4 + ((b >> 3) & 3), cj = (b & 1) * 8 + (b >> 5);
  const int tok = lane & 15, g = lane >> 4, q4 = (lane & 15) >> 2, pp = lane & 3;
  const int hf = cj & 1, ct = cj >> 1, par = cj & 1;
  const int wq = w < 4 ? w : w - 1;
  unsigned stampv = 0;
  const srd_t rWS = __builtin_amdgcn_make_buffer_rsrc(p.ws, 0, WS_BYTES, 0x00020000);
  const int rA = lane >> 2, pA = lane & 3;
  const unsigned colA = (((pA >> 1) ^ ((rA >> 2) & 1)) * 32) + (pA & 1) * 16;
  const int rB = lane >> 3, pB = lane & 7;
  const unsigned colB = (((pB >> 1) ^ ((rB >> 1) & 3)) * 32) + (pB & 1) * 16;
  STAMP(0);
  CSTAMP(40);
  const unsigned long long pad_t0 = __builtin_amdgcn_s_memrealtime();

  const bool m2 = b < 160;
  const int msl = b % 40, mkq = b / 40;
  const srd_t rWmod = __builtin_amdgcn_make_buffer_rsrc((void*)p.wmod, 0, DM * 3072 * 2, 0x00020000);
  v4u b2v = {0, 0, 0, 0};
  if (w == 6 && lane < 8) b2v = *(const v4u*)(p.b2 + 64 * ct + lane * 8);
  v4u cr;
  v2u ep;
  if (w < 4) {
    __builtin_amdgcn_s_setprio(2);
    asm volatile("global_load_dwordx4 %0, %1, off" : "=v"(cr) : "v"(p.c + 128 * w + 8 * (lane & 15)) : "memory");
    asm volatile("buffer_load_dwordx2 %0, %1, %2, 0 offen sc1" : "=v"(ep) : "v"(WS_EPOCH), "s"(rWS) : "memory");
    const srd_t rX = __builtin_amdgcn_make_buffer_rsrc((void*)p.x, 0, SEQ * DM * 2, 0x00020000);
#pragma unroll
    for (int u = 0; u < 2; ++u) glds(rX, rA * 1024 + pA * 16, (128 * ci + 16 * (2 * w + u)) * 1024 + 64 * cj, L_XS + (2 * w + u) * 1024);
#pragma unroll
    for (int i0 = 0; i0 < 8; ++i0) { const int ii = (i0 + (ci & 3) * 2 + (cj & 1)) & 7; glds(rWmod, rA * 6144 + colA, (w * 8 + ii) * 16 * 6144 + (512 + 32 * cj) * 2, L_CA + (w * 8 + ii) * 1024); }
  } else if (w == 5) {
    __builtin_amdgcn_s_setprio(3);
    const srd_t r = __builtin_amdgcn_make_buffer_rsrc((void*)p.wqkv, 0, DM * 1536 * 2, 0x00020000);
#pragma unroll
    for (int e = 0; e < 6; ++e) glds(r, rA * 3072 + colA, (32 * ci + 16 * (e & 1)) * 3072 + ((e >> 1) * 512 + 32 * cj) * 2, L_SQ + e * 1024);
  }
  if (tid == 256) { *(LAS unsigned*)(uintptr_t)L_CNT = 0u; *(LAS unsigned*)(uintptr_t)(L_CNT + 4) = m2 ? 0u : 1u; *(LAS unsigned*)(uintptr_t)(L_CNT + 8) = 0u; *(LAS unsigned*)(uintptr_t)(L_CNT + 12) = 0u; }
  BAR();
#define READ_TAG() do { const v2u ep_ = ld8(rWS, WS_EPOCH, 0); TAG = (((u64)__builtin_amdgcn_readfirstlane(ep_[1]) << 32) | __builtin_amdgcn_readfirstlane(ep_[0])) + 1; } while (0)
  v8s rt[12];
  f16v mU, mL;
  u64 TAG;
  if (w < 4) {
    asm volatile("s_waitcnt vmcnt(10)" : "+v"(cr), "+v"(ep) :: "memory");
    const unsigned sc = L_SC + w * 256;
    if (lane < 16) *(LAS v4u*)(uintptr_t)(sc + lane * 16) = v4u{pk(silu(bflo(cr[0])), silu(bfhi(cr[0]))), pk(silu(bflo(cr[1])), silu(bfhi(cr[1]))), pk(silu(bflo(cr[2])), silu(bfhi(cr[2]))), pk(silu(bflo(cr[3])), silu(bfhi(cr[3])))};
    LG0();
    v8s sf[4];
#pragma unroll
    for (int s = 0; s < 4; ++s) { const v2u lo = lds8(sc + (32 * s + 4 * g) * 2), hi = lds8(sc + (32 * s + 16 + 4 * g) * 2); sf[s] = asv8(v4u{lo[0], lo[1], hi[0], hi[1]}); }
    TAG = (((u64)__builtin_amdgcn_readfirstlane(ep[1]) << 32) | __builtin_amdgcn_readfirstlane(ep[0])) + 1;
    asm volatile("s_waitcnt vmcnt(8)" ::: "memory");
    v2u xa[2][2];
#pragma unroll
    for (int u = 0; u < 2; ++u) {
      const unsigned a = L_XS + (2 * w + u) * 1024 + tok * 64 + g * 8; xa[u][0] = lds8(a); xa[u][1] = lds8(a + 32);
      const float x0 = bflo(xa[u][0][0]), x1 = bfhi(xa[u][0][0]), x2 = bflo(xa[u][0][1]), x3 = bfhi(xa[u][0][1]);
      const float x4 = bflo(xa[u][1][0]), x5 = bfhi(xa[u][1][0]), x6 = bflo(xa[u][1][1]), x7 = bfhi(xa[u][1][1]);
      const float s1 = sumg(((x0 + x1) + (x2 + x3)) + ((x4 + x5) + (x6 + x7)));
      const float s2 = sumg(((x0 * x0 + x1 * x1) + (x2 * x2 + x3 * x3)) + ((x4 * x4 + x5 * x5) + (x6 * x6 + x7 * x7)));
      if (g == 0) st8(rWS, v2u{__float_as_uint(s1), __float_as_uint(s2)}, WS_ST1 + ((ci * 16 + cj) * 128 + 16 * (2 * w + u) + tok) * 8);
    }
    asm volatile("s_waitcnt vmcnt(2)" ::: "memory");
    STAMP(1);
    f4 a0 = {0, 0, 0, 0}, a1 = {0, 0, 0, 0};
    {
      v8s W[4][2];
#pragma unroll
      for (int s = 0; s < 4; ++s) { W[s][0] = trfragA(L_CA + (4 * w + s) * 2048, 0, lane); W[s][1] = trfragA(L_CA + (4 * w + s) * 2048, 1, lane); }
#pragma unroll
      for (int s = 0; s < 4; ++s) { a0 = MFMA16(W[s][0], sf[s], a0); a1 = MFMA16(W[s][1], sf[s], a1); }
    }
    if (tok == 0) { *(LAS f4*)(uintptr_t)(L_CAP + (w * 32 + 4 * g) * 4) = a0; *(LAS f4*)(uintptr_t)(L_CAP + (w * 32 + 16 + 4 * g) * 4) = a1; }
    LG0();
    if (lane == 0) __hip_atomic_fetch_add((LAS unsigned*)(uintptr_t)L_CNT, 1u, __ATOMIC_RELAXED, __HIP_MEMORY_SCOPE_WORKGROUP);
    while (*(volatile LAS unsigned*)(uintptr_t)L_CNT < 4u) {}
    CFENCE();
    STAMP(19);
    a0 = ldsf4(L_CAP + 4 * g * 4); a1 = ldsf4(L_CAP + (16 + 4 * g) * 4);
#pragma unroll
    for (int e = 1; e < 4; ++e) { a0 += ldsf4(L_CAP + (e * 32 + 4 * g) * 4); a1 += ldsf4(L_CAP + (e * 32 + 16 + 4 * g) * 4); }
    float wv[8];
#pragma unroll
    for (int r = 0; r < 4; ++r) { wv[r] = rbf(1.f + rbf(a0[r])); wv[4 + r] = rbf(1.f + rbf(a1[r])); }
#pragma unroll
    for (int u = 0; u < 2; ++u) {
      const float x0 = bflo(xa[u][0][0]), x1 = bfhi(xa[u][0][0]), x2 = bflo(xa[u][0][1]), x3 = bfhi(xa[u][0][1]);
      const float x4 = bflo(xa[u][1][0]), x5 = bfhi(xa[u][1][0]), x6 = bflo(xa[u][1][1]), x7 = bfhi(xa[u][1][1]);
      const int T = 2 * w + u;
      st16(rWS, v4u{pk(x0 * wv[0], x1 * wv[1]), pk(x2 * wv[2], x3 * wv[3]), pk(x4 * wv[4], x5 * wv[5]), pk(x6 * wv[6], x7 * wv[7])},
           WS_HF + ((ci * 8 + T) * 16 + cj) * 1024 + lane * 16);
      if (u == 0 && w == 0 && tok == 0) {
        st8(rWS, v2u{pk(wv[0], wv[1]), pk(wv[2], wv[3])}, WS_WV + ci * 1024 + cj * 64 + g * 8);
        st8(rWS, v2u{pk(wv[4], wv[5]), pk(wv[6], wv[7])}, WS_WV + ci * 1024 + cj * 64 + 32 + g * 8);
      }
    }
    STAMP(21);
    VM0();
    if (lane == 0) { arrive(p.ws, WS_F0 + (ci * 64 + cj * 4 + w) * 8, TAG); __hip_atomic_fetch_add((LAS unsigned*)(uintptr_t)(L_CNT + 8), 1u, __ATOMIC_RELAXED, __HIP_MEMORY_SCOPE_WORKGROUP); }
    __builtin_amdgcn_s_setprio(0);
  } else if (w == 4) {
    if (m2) {
      const int mvec = msl >> 3, mcol = (mvec == 0 ? 0 : 512 + 512 * mvec) + (msl & 7) * 64;
      __builtin_amdgcn_s_sleep(8);
#pragma unroll
      for (int ii = 0; ii < 16; ++ii) glds(rWmod, rB * 6144 + colB, (128 * mkq + 8 * ii) * 6144 + mcol * 2, L_M2 + ii * 1024);
      const v4u cr = *(const v4u*)(p.c + 128 * mkq + 8 * (lane & 15));
      const unsigned sc = L_SC + 1024;
      if (lane < 16) *(LAS v4u*)(uintptr_t)(sc + lane * 16) = v4u{pk(silu(bflo(cr[0])), silu(bfhi(cr[0]))), pk(silu(bflo(cr[1])), silu(bfhi(cr[1]))), pk(silu(bflo(cr[2])), silu(bfhi(cr[2]))), pk(silu(bflo(cr[3])), silu(bfhi(cr[3])))};
      LG0();
      v8s sf[4];
#pragma unroll
      for (int s = 0; s < 4; ++s) { const v2u lo = lds8(sc + (32 * s + 4 * g) * 2), hi = lds8(sc + (32 * s + 16 + 4 * g) * 2); sf[s] = asv8(v4u{lo[0], lo[1], hi[0], hi[1]}); }
      VM0();
      f4 a[4];
#pragma unroll
      for (int nt = 0; nt < 4; ++nt) a[nt] = f4{0, 0, 0, 0};
      const int row = 4 * g + q4;
#pragma unroll
      for (int s = 0; s < 4; ++s) {
        v8s W[4];
#pragma unroll
        for (int nt = 0; nt < 4; ++nt) { const unsigned ad = L_M2 + s * 4096 + row * 128 + ((nt ^ ((row >> 1) & 3)) * 32) + pp * 8; W[nt] = cat(trd(ad), trd(ad + 2048)); }
#pragma unroll
        for (int nt = 0; nt < 4; ++nt) a[nt] = MFMA16(W[nt], sf[s], a[nt]);
      }
      if (tok == 0) {
#pragma unroll
        for (int nt = 0; nt < 4; ++nt)
          st16(rWS, v4u{__float_as_uint(a[nt][0]), __float_as_uint(a[nt][1]), __float_as_uint(a[nt][2]), __float_as_uint(a[nt][3])},
               WS_MOD + mkq * 10240 + mvec * 2048 + ((msl & 7) * 2 + (nt >> 1)) * 128 + g * 32 + (nt & 1) * 16);
      }
      LG0();
      if (lane == 0) *(volatile LAS unsigned*)(uintptr_t)(L_CNT + 4) = 1u;
      READ_TAG();
      VM0();
      if (lane == 0) arrive(p.ws, WS_FM + b * 8, TAG);
    } else READ_TAG();
    STAMP(1);
  } else {
    if (w == 5) {
      READ_TAG();
      VM0();
#pragma unroll
      for (int nt = 0; nt < 6; ++nt) st16(rWS, asv4u(trfragA(L_SQ + (nt >> 1) * 2048, nt & 1, lane)), WS_TQ + ((cj * 6 + nt) * 16 + ci) * 1024 + lane * 16);
      VM0();
      if (lane == 0) arrive(p.ws, WS_FQ + (cj * 16 + ci) * 8, TAG);
      __builtin_amdgcn_s_setprio(0);
    } else {
      READ_TAG();
      __builtin_amdgcn_s_sleep(16);
      if (w == 6) {
        const srd_t r1 = __builtin_amdgcn_make_buffer_rsrc((void*)p.w1, 0, DM * FF * 2, 0x00020000);
#pragma unroll
        for (int e = 0; e < 6; ++e) glds(r1, rA * (FF * 2) + colA, (32 * ci + 16 * (e & 1)) * (FF * 2) + (112 * cj + 16 * par + (e >> 1) * 32) * 2, L_S1 + e * 1024);
        glds(r1, (lane >> 1) * (FF * 2) + (lane & 1) * 16, 32 * ci * (FF * 2) + (112 * cj + (par ? 0 : 96)) * 2, L_S1 + 6144);
        VM0();
#pragma unroll
        for (int nt = 0; nt < 6; ++nt) rt[nt] = trfragA(L_S1 + (nt >> 1) * 2048, nt & 1, lane);
        { const unsigned a = L_S1 + 6144 + (4 * g + q4) * 32 + pp * 8; rt[6] = cat(trd(a), trd(a + 512)); }
      } else {
        const srd_t ro = __builtin_amdgcn_make_buffer_rsrc((void*)p.wo, 0, DM * DM * 2, 0x00020000);
#pragma unroll
        for (int e = 0; e < 4; ++e) glds(ro, rB * 1024 + colB, (32 * ((b & 127) >> 3) + 8 * e) * 1024 + (b & 7) * 128, L_SO + e * 1024);
        const srd_t r2 = __builtin_amdgcn_make_buffer_rsrc((void*)p.w2, 0, FF * DM * 2, 0x00020000);
        const int b2 = b < 224 ? b : 0;
#pragma unroll
        for (int e = 0; e < 8; ++e) { const int id = 2 * b2 + (e >> 2); glds(r2, rB * 1024 + colB, (32 * (id >> 3) + 8 * (e & 3)) * 1024 + (id & 7) * 128, L_SO + 4096 + e * 1024); }
        VM0();
        {
          const int row = 16 * (g >> 1) + 4 * (g & 1) + q4;
#pragma unroll
          for (int nt = 0; nt < 4; ++nt) { const unsigned a = L_SO + row * 128 + ((nt ^ ((row >> 1) & 3)) * 32) + pp * 8; rt[8 + nt] = cat(trd(a), trd(a + 1024)); }
        }
        const int row = 4 * g + q4;
#pragma unroll
        for (int e = 0; e < 8; ++e) { const int nt = e & 3; const unsigned a = L_SO + 4096 + (e >> 2) * 4096 + row * 128 + ((nt ^ ((row >> 1) & 3)) * 32) + pp * 8; rt[e] = cat(trd(a), trd(a + 2048)); }
      }
    }
    STAMP(1);
    if (w >= 6) {
      while (*(volatile LAS unsigned*)(uintptr_t)(L_CNT + 8) < 4u) __builtin_amdgcn_s_sleep(1);
      CFENCE();
      if (w == 6) {
#pragma unroll
        for (int e = 0; e < 7; ++e) st16(rWS, asv4u(rt[e]), WS_TW1 + ((cj * 7 + e) * 16 + ci) * 1024 + lane * 16);
      } else {
        if (b < 224) {
#pragma unroll
          for (int e = 0; e < 8; ++e) { const int id = 2 * b + (e >> 2); st16(rWS, asv4u(rt[e]), WS_TW2 + (((id & 7) * 56 + (id >> 3)) * 4 + (e & 3)) * 1024 + lane * 16); }
        }
        if (b < 128) {
#pragma unroll
          for (int e = 0; e < 4; ++e) st16(rWS, asv4u(rt[8 + e]), WS_TWO + (((b & 7) * 16 + (b >> 3)) * 4 + e) * 1024 + lane * 16);
        }
      }
    }
    wait_words(p.ws, WS_FQ + cj * 128, 16, lane, TAG);
    while (*(volatile LAS unsigned*)(uintptr_t)(L_CNT + 4) == 0u) __builtin_amdgcn_s_sleep(1);
    CFENCE();
    STAMP(19);
#pragma unroll
    for (int e = 0; e < 8; ++e) glds4x1(rWS, lane * 16, WS_TQ + cj * 98304 + ((w - 5) * 8 + ((e + 2 * (ci & 3)) & 7)) * 4096, L_WQKV + ((w - 5) * 8 + ((e + 2 * (ci & 3)) & 7)) * 4096);
  }
  {
    const int qr = lane & 31, h = lane >> 5;
#pragma unroll
    for (int r = 0; r < 16; ++r) { const int kr = (r & 3) + 8 * (r >> 2) + 4 * h; mU[r] = kr > qr ? -1e30f : 0.f; mL[r] = kr > qr ? 0.f : -1e30f; }
    asm volatile("" : "+v"(mU), "+v"(mL));
  }
  if (w == 0) {
    __builtin_amdgcn_s_setprio(3);
    wait_words(p.ws, WS_F0 + ci * 512, 64, lane, TAG);
    if (lane == 0) *(volatile LAS unsigned*)(uintptr_t)(L_CNT + 12) = 1u;
    __builtin_amdgcn_s_setprio(0);
  } else {
    while (*(volatile LAS unsigned*)(uintptr_t)(L_CNT + 12) == 0u) __builtin_amdgcn_s_sleep(1);
    CFENCE();
  }
  STAMP(2);
  v8s act[16];
  const unsigned hsrc = WS_HF + (ci * 8 + w) * 16 * 1024;
  v2u wl[2], wh[2];
#pragma unroll
  for (int u = 0; u < 2; ++u) { const unsigned o = (32 * (2 * w + u) + 4 * g) * 2; wl[u] = ld8(rWS, o, WS_WV + ci * 1024); wh[u] = ld8(rWS, o + 32, WS_WV + ci * 1024); }
#pragma unroll
  for (int s = 0; s < 5; ++s) act[s] = asv8(ld16(rWS, lane * 16, hsrc + s * 1024));
  if (w >= 5) asm volatile("s_waitcnt vmcnt(9)" ::: "memory");
  BAR();
  STAMP(3);
  if (lane == 0) {
    if (w == 6) arrive(p.ws, WS_FW1 + (cj * 16 + ci) * 8, TAG);
    if (w == 7 && b < 128) arrive(p.ws, WS_FWO + ((b & 7) * 16 + (b >> 3)) * 8, TAG);
    if (w == 7 && b < 224) { arrive(p.ws, WS_FW2 + (((2 * b) & 7) * 64 + ((2 * b) >> 3)) * 8, TAG); arrive(p.ws, WS_FW2 + (((2 * b + 1) & 7) * 64 + ((2 * b + 1) >> 3)) * 8, TAG); }
  }

  f4 accQ[2], accKV[4];
  float rstd, nmu;
  v4u gaR, cbR, sbR, gbR;
  v2u b2R[2];
  v4u mc0, md0;
  v4u mx[2];
  const int tt = w & 3, kh = w >> 2;
  const int ocol = 64 * ct + 32 * kh + 4 * g;
  {
    const unsigned bq = opq(L_WQKV + lane * 16), bv = opq(L_WQKV + 65536 + lane * 16);
#define QF(nt, s) lds16(((nt) < 4 ? bq : bv) + ((nt) & 3) * 16384 + (s) * 1024)
    {
      f4 au[6];
      v8s W[2][6];
      const unsigned c0 = opq(bq + w * 2048), d0 = opq(bv + w * 2048);
#pragma unroll
      for (int u = 0; u < 2; ++u)
#pragma unroll
        for (int nt = 0; nt < 6; ++nt) W[u][nt] = lds16((nt < 4 ? c0 : d0) + (nt & 3) * 16384 + u * 1024);
#pragma unroll
      for (int nt = 0; nt < 6; ++nt) au[nt] = f4{0, 0, 0, 0};
#pragma unroll
      for (int u = 0; u < 2; ++u) {
        const v8s a = asv8(v4u{wl[u][0], wl[u][1], wh[u][0], wh[u][1]});
#pragma unroll
        for (int nt = 0; nt < 6; ++nt) au[nt] = MFMA16(W[u][nt], a, au[nt]);
      }
      if (tok == 0) {
#pragma unroll
        for (int nt = 0; nt < 6; ++nt) *(LAS f4*)(sm + L_PARTU + (w * 96 + nt * 16 + 4 * g) * 4) = au[nt];
      }
    }
    BAR();
    if (tid >= 384 && tid < 480) {
      const int e0 = tid - 384;
      float s = 0;
#pragma unroll
      for (int e = 0; e < 8; ++e) s += *(const LAS float*)(sm + L_PARTU + (e * 96 + e0) * 4);
      *(LAS float*)(sm + L_PERS + e0 * 4) = s;
    }
    STAMP(16);
    v2u st[4];
    u64 fm0 = 0, fm1 = 0, fm2 = 0;
#pragma unroll
    for (int nt = 0; nt < 4; ++nt) accKV[nt] = f4{0, 0, 0, 0};
#pragma unroll
    for (int nt = 0; nt < 2; ++nt) accQ[nt] = f4{0, 0, 0, 0};
    {
      v8s W[6];
#pragma unroll
      for (int nt = 0; nt < 6; ++nt) W[nt] = QF(nt, 0);
      SB();
#pragma unroll
      for (int s = 0; s < 16; ++s) {
        if (s + 5 < 16) act[s + 5] = asv8(ld16(rWS, lane * 16, hsrc + (s + 5) * 1024));
        else if (s < 15) st[s - 11] = ld8(rWS, ((4 * g + s - 11) * 128 + 16 * w + tok) * 8, WS_ST1 + ci * 16 * 128 * 8);
        if (s == 12) { GAS u64* f = (GAS u64*)(p.ws + WS_FM); fm0 = __hip_atomic_load(f + lane, RLX_AGENT); fm1 = __hip_atomic_load(f + 64 + lane, RLX_AGENT); fm2 = __hip_atomic_load(f + 128 + (lane & 31), RLX_AGENT); }
#pragma unroll
        for (int nt = 0; nt < 6; ++nt) {
          if (nt < 2) accQ[nt] = MFMA16(W[nt], act[s], accQ[nt]); else accKV[nt - 2] = MFMA16(W[nt], act[s], accKV[nt - 2]);
          if (s + 1 < 16) W[nt] = QF(nt, s + 1);
          SB();
        }
      }
    }
    float s1 = 0, s2 = 0;
#pragma unroll
    for (int e = 0; e < 4; ++e) { s1 += __uint_as_float(st[e][0]); s2 += __uint_as_float(st[e][1]); }
    s1 = sumg(s1); s2 = sumg(s2);
    const float mu = s1 * (1.f / 512), var = fmaxf(s2 * (1.f / 512) - mu * mu, 0.f);
    rstd = __builtin_amdgcn_rsqf(var + 1e-5f); nmu = -rstd * mu;
    if (w == 6 && lane < 8) *(LAS v4u*)(sm + L_B2C + lane * 16) = b2v;
    PIN(rstd); PIN(nmu);
    {
          GAS u64* f = (GAS u64*)(p.ws + WS_FM);
          for (unsigned spins = 0; spins < (1u << 17) && !__all(fm0 == TAG && fm1 == TAG && fm2 == TAG); ++spins) {
            fm0 = __hip_atomic_load(f + lane, RLX_AGENT); fm1 = __hip_atomic_load(f + 64 + lane, RLX_AGENT); fm2 = __hip_atomic_load(f + 128 + (lane & 31), RLX_AGENT); }
          CFENCE();
          if (w < 6) {
            const int vec = w < 2 ? 0 : w < 4 ? 2 : 3, hq = w & 1;
            const unsigned dst = w == 0 ? L_PSA : w == 1 ? L_PSA2 : w == 2 ? L_PSB : w == 3 ? L_PSB2 : w == 4 ? L_PCB : L_PCB2;
            glds2x1(rWS, lane * 16, WS_MOD + (2 * hq) * 10240 + vec * 2048, dst);
            glds2x1(rWS, lane * 16, WS_MOD + (2 * hq + 1) * 10240 + vec * 2048, dst + 2048);
          } else glds1(rWS, (lane >> 4) * 10240 + (lane & 15) * 16, WS_MOD + (w == 6 ? 1 : 4) * 2048 + ct * 256, w == 6 ? L_PGA : L_PGB);
    }
    STAMP(4);
    BAR();
    {
      v4u kk, vv;
      unsigned pkd[4][2];
#pragma unroll
      for (int nt = 0; nt < 4; ++nt) {
        const f4 u = ldsf4(L_PERS + (32 + nt * 16 + 4 * g) * 4);
        pkd[nt][0] = pk(rstd * accKV[nt][0] + nmu * u[0], rstd * accKV[nt][1] + nmu * u[1]);
        pkd[nt][1] = pk(rstd * accKV[nt][2] + nmu * u[2], rstd * accKV[nt][3] + nmu * u[3]);
      }
      { auto r0 = __builtin_amdgcn_permlane16_swap(pkd[0][0], pkd[1][0], false, false); auto r1 = __builtin_amdgcn_permlane16_swap(pkd[0][1], pkd[1][1], false, false);
        kk = v4u{r0[0], r1[0], r0[1], r1[1]}; }
      { auto r0 = __builtin_amdgcn_permlane16_swap(pkd[2][0], pkd[3][0], false, false); auto r1 = __builtin_amdgcn_permlane16_swap(pkd[2][1], pkd[3][1], false, false);
        vv = v4u{r0[0], r1[0], r0[1], r1[1]}; }
      const int hd = g & 1, pl = g >> 1;
      st16(rWS, kk, WS_KV + (ci * 16 + cj) * 16384 + ((0 * 2 + hd) * 2 + pl) * 2048 + (16 * w + tok) * 16);
      st16(rWS, vv, WS_KV + (ci * 16 + cj) * 16384 + ((1 * 2 + hd) * 2 + pl) * 2048 + (16 * w + tok) * 16);
      *(LAS v4u*)(sm + L_AK + (hd * 2 + pl) * KPL + (256 + 16 * w + tok) * 16) = kk;
      *(LAS v4u*)(sm + L_AV + hd * VHD + pl * VPL + (256 + 16 * w + tok) * 16) = vv;
    }
    VM0();
    BAR();
    if (tid == 256) arrive(p.ws, WS_FP + (0 * 16 + ci) * 128 + cj * 8, TAG);
    STAMP(5);
    if (tid < 400) *(LAS v4u*)(sm + L_ONE + tid * 16) = v4u{0x3f803f80u, 0x3f803f80u, 0x3f803f80u, 0x3f803f80u};
    if (tid < 416) {
      const int seg = tid < 384 ? tid >> 7 : 3 + ((tid - 384) >> 4), e = tid < 384 ? tid & 127 : (tid - 384) & 15;
      f4 v;
      if (seg < 3) {
        const unsigned a0 = (seg == 0 ? L_PSA : seg == 1 ? L_PSB : L_PCB) + e * 16, a1 = (seg == 0 ? L_PSA2 : seg == 1 ? L_PSB2 : L_PCB2) + e * 16;
        v = (ldsf4(a0) + ldsf4(a0 + 2048)) + (ldsf4(a1) + ldsf4(a1 + 2048));
      } else {
        const unsigned a0 = (seg == 3 ? L_PGA : L_PGB) + e * 16;
        v = (ldsf4(a0) + ldsf4(a0 + 256)) + (ldsf4(a0 + 512) + ldsf4(a0 + 768));
      }
      *(LAS v2u*)(sm + L_FIN + (seg < 3 ? seg * 1024 : 3072 + (seg - 3) * 128) + e * 8) = v2u{pk(v[0], v[1]), pk(v[2], v[3])};
    }
    BAR();
    {
      f4 av[4];
      v8s W[2][4], a[2];
      const unsigned c0 = opq(bq + w * 2048), d0 = opq(bv + w * 2048);
#pragma unroll
      for (int u = 0; u < 2; ++u) {
        a[u] = lds16(L_FIN + (2 * w + u) * 64 + g * 16);
#pragma unroll
        for (int nt = 0; nt < 4; ++nt) W[u][nt] = lds16((nt < 2 ? c0 : d0) + (nt & 1) * 16384 + u * 1024);
      }
#pragma unroll
      for (int nt = 0; nt < 4; ++nt) av[nt] = f4{0, 0, 0, 0};
#pragma unroll
      for (int u = 0; u < 2; ++u)
#pragma unroll
        for (int nt = 0; nt < 4; ++nt) av[nt] = MFMA16(W[u][nt], a[u], av[nt]);
      if (tok == 0) {
#pragma unroll
        for (int nt = 0; nt < 4; ++nt) *(LAS f4*)(sm + L_PARTV + (w * 64 + nt * 16 + 4 * g) * 4) = av[nt];
      }
    }
    {
      gaR = asv4u(lds16(L_FIN + 3072 + kh * 64 + g * 16)); gbR = asv4u(lds16(L_FIN + 3200 + kh * 64 + g * 16));
      sbR = asv4u(lds16(L_FIN + 1024 + (2 * ct + kh) * 64 + g * 16)); cbR = asv4u(lds16(L_FIN + 2048 + (2 * ct + kh) * 64 + g * 16));
#pragma unroll
      for (int e = 0; e < 2; ++e) b2R[e] = lds8(L_B2C + (32 * kh + 4 * g + 16 * e) * 2);
      mc0 = asv4u(lds16(L_FIN + 2048 + 16 * lane)); md0 = asv4u(lds16(L_FIN + 1024 + 16 * lane));
#pragma unroll
      for (int u = 0; u < 2; ++u) mx[u] = asv4u(lds16(L_FIN + (tok == 1 ? 1024 : 2048) + (2 * w + u) * 64 + g * 16));
      BAR();
      if (tid < 64) {
        float s = 0;
#pragma unroll
        for (int e = 0; e < 8; ++e) s += *(const LAS float*)(sm + L_PARTV + (e * 64 + tid) * 4);
        *(LAS float*)(sm + L_PERS + (96 + tid) * 4) = s;
      }
    }
    if (ci < 2) {
      for (int e = tid; e < 8 * 256; e += 512) {
        const int pln = e >> 8, key = e & 255;
        if (ci == 0 || key < 128) {
          const unsigned a = pln < 4 ? L_AK + pln * KPL : L_AV + ((pln - 4) >> 1) * VHD + (pln & 1) * VPL;
          *(LAS v4u*)(sm + a + key * 16) = v4u{0, 0, 0, 0};
        }
      }
    }
    if (w == 4) {
      GAS u64* f0 = (GAS u64*)(p.ws + (lane == 0 && ci >= 1 ? WS_FP + (ci - 1) * 128 + cj * 8 : lane == 1 && ci >= 2 ? WS_FP + (ci - 2) * 128 + cj * 8
                                       : lane >= 16 && lane < 32 ? WS_FW1 + (cj * 16 + lane - 16) * 8 : WS_FWO + (ct * 16 + (lane & 15)) * 8));
      GAS u64* f1 = (GAS u64*)(p.ws + WS_FW2 + ct * 512) + (lane < 56 ? lane : 0);
      for (unsigned spins = 0; spins < (1u << 17); ++spins) {
        const u64 a = __hip_atomic_load(f0, RLX_AGENT), c = __hip_atomic_load(f1, RLX_AGENT);
        if (__all(a == TAG && c == TAG)) break;
      }
      CFENCE();
    }
    BAR();
    STAMP(6);
    {
      const unsigned dst = w < 4 ? L_AK + w * KPL : L_AV + ((w - 4) >> 1) * VHD + (w & 1) * VPL;
#pragma unroll
      for (int src = 0; src < 2; ++src)
        if (ci - 2 + src >= 0) glds2x1(rWS, lane * 16, WS_KV + ((ci - 2 + src) * 16 + cj) * 16384 + w * 2048, dst + src * 2048);
    }
    {
      const float qs = 0.25f * 1.4426950408889634f;
      unsigned pq[2][2];
#pragma unroll
      for (int nt = 0; nt < 2; ++nt) {
        const f4 u = ldsf4(L_PERS + (nt * 16 + 4 * g) * 4), v = ldsf4(L_PERS + (96 + nt * 16 + 4 * g) * 4);
        pq[nt][0] = pk(qs * (rstd * accQ[nt][0] + nmu * u[0] + v[0]), qs * (rstd * accQ[nt][1] + nmu * u[1] + v[1]));
        pq[nt][1] = pk(qs * (rstd * accQ[nt][2] + nmu * u[2] + v[2]), qs * (rstd * accQ[nt][3] + nmu * u[3] + v[3]));
      }
      auto r0 = __builtin_amdgcn_permlane16_swap(pq[0][0], pq[1][0], false, false); auto r1 = __builtin_amdgcn_permlane16_swap(pq[0][1], pq[1][1], false, false);
      *(LAS v4u*)(sm + L_AQ + ((g & 1) * 2 + (g >> 1)) * 2048 + (16 * w + tok) * 16) = v4u{r0[0], r1[0], r0[1], r1[1]};
    }
    VM0();
    BAR();
    STAMP(7);
  }

  const int orow = 128 * ci + 64 * hf + 16 * tt + tok;
  v2u xr0, xr1;
  {
    const int t = w & 3, hd = w >> 2;
    const int kt0 = ci >= 2 ? 0 : 8 - 4 * ci - t;
    f16v O;
    const u16* xres = p.x + (size_t)orow * DM + ocol;
    if (ci >= 2) attention<true>(t, hd, 0, lane, mU, mL, O, rWS, WS_TWO + ct * 65536, WS_TW1 + cj * 7 * 16384, w, xres, xr0, xr1, stampv);
    else attention<false>(t, hd, kt0, lane, mU, mL, O, rWS, WS_TWO + ct * 65536, WS_TW1 + cj * 7 * 16384, w, xres, xr0, xr1, stampv);
    const int h = lane >> 5;
    const float inv = __builtin_amdgcn_rcpf(O[8]);
    const f4 b0 = ldsf4(L_PERS + (96 + 32 + hd * 16 + 4 * h) * 4), b1 = ldsf4(L_PERS + (96 + 32 + hd * 16 + 8 + 4 * h) * 4);
    const v4u o = {pk(O[0] * inv + b0[0], O[1] * inv + b0[1]), pk(O[2] * inv + b0[2], O[3] * inv + b0[3]),
                   pk(O[4] * inv + b1[0], O[5] * inv + b1[1]), pk(O[6] * inv + b1[2], O[7] * inv + b1[3])};
    const int qr = lane & 31;
    st16(rWS, o, WS_OF + (ci * 8 * 16 + cj) * 1024 + ((2 * t + (qr >> 4)) * 16) * 1024 + ((2 * hd + h) * 16 + (qr & 15)) * 16);
  }
  STAMP(24);
  VM0();
  BAR();
  if (tid == 256) arrive(p.ws, WS_FP + (1 * 16 + ci) * 128 + cj * 8, TAG);
  STAMP(8);
  float b1v = 0.f;
  v8s aux2[2];
  float Cw = 0, Cw2 = 0, Cws = 0, Cs = 0, Cs2 = 0;
#define MODSUMS() do { \
      _Pragma("unroll") for (int r = 0; r < 4; ++r) { \
        const float w0 = rbf(1.f + bflo(mc0[r])), w1 = rbf(1.f + bfhi(mc0[r])), s0 = bflo(md0[r]), s1_ = bfhi(md0[r]); \
        Cw += w0 + w1; Cw2 += w0 * w0 + w1 * w1; Cws += w0 * s0 + w1 * s1_; Cs += s0 + s1_; Cs2 += s0 * s0 + s1_ * s1_; } \
      Cw = sumg(rowsum16(Cw)); Cw2 = sumg(rowsum16(Cw2)); Cws = sumg(rowsum16(Cws)); Cs = sumg(rowsum16(Cs)); Cs2 = sumg(rowsum16(Cs2)); \
      _Pragma("unroll") for (int u = 0; u < 2; ++u) { v4u t; \
        _Pragma("unroll") for (int d = 0; d < 4; ++d) t[d] = tok == 0 ? pk(1.f + bflo(mx[u][d]), 1.f + bfhi(mx[u][d])) : (tok == 1 ? mx[u][d] : 0x3f803f80u); \
        aux2[u] = asv8(t); } \
      asm volatile("" : "+v"(Cw), "+v"(Cw2), "+v"(Cws), "+v"(Cs), "+v"(Cs2), "+v"(aux2[0]), "+v"(aux2[1])); } while (0)
  if (w != 4) {
#pragma unroll
    for (int e = 0; e < 3; ++e) { const int gi = wq + 7 * e; if (gi < 16) glds4x1(rWS, lane * 16, WS_TW1 + (cj * 7 + 2) * 16384 + gi * 4096, L_W1 + 32768 + gi * 4096); }
  }
  MODSUMS();
  if (w == 4) wait_words(p.ws, WS_FP + (1 * 16 + ci) * 128, 16, lane, TAG);
  BAR();
  STAMP(9);

  float x1[8];
  {
    v8s oa[8];
    const unsigned osrc = WS_OF + ((ci * 8 + 4 * hf + tt) * 16 + 8 * kh) * 1024;
#pragma unroll
    for (int s = 0; s < 3; ++s) oa[s] = asv8(ld16(rWS, lane * 16, osrc + s * 1024));
    const unsigned bo = opq(L_WO + kh * 32768 + lane * 16);
    f4 acc[4];
#pragma unroll
    for (int nt = 0; nt < 4; ++nt) acc[nt] = f4{0, 0, 0, 0};
    {
      v8s W[2][4];
#pragma unroll
      for (int s = 0; s < 2; ++s)
#pragma unroll
        for (int nt = 0; nt < 4; ++nt) W[s][nt] = lds16(bo + (s * 4 + nt) * 1024);
      SB();
#pragma unroll
      for (int s = 3; s < 8; ++s) oa[s] = asv8(ld16(rWS, lane * 16, osrc + s * 1024));
      if (tid < 112) { const int nt = tid >> 4; b1v = bflo(p.b1[(nt == 6 ? 112 * cj + (par ? 0 : 96) : 112 * cj + 16 * par + nt * 16) + (tid & 15)]); }
      SB();
#pragma unroll
      for (int s = 0; s < 8; ++s)
#pragma unroll
        for (int nt = 0; nt < 4; ++nt) {
          acc[nt] = MFMA16(W[s & 1][nt], oa[s], acc[nt]);
          if (s + 2 < 8) W[s & 1][nt] = lds16(bo + ((s + 2) * 4 + nt) * 1024);
          SB();
        }
    }
    STAMP(26);
    VM0();
    BAR();
    *(LAS f4*)(sm + L_XCH + w * 2048 + lane * 16) = kh ? acc[0] : acc[2];
    *(LAS f4*)(sm + L_XCH + w * 2048 + 1024 + lane * 16) = kh ? acc[1] : acc[3];
    BAR();
    const f4 o0 = ldsf4(L_XCH + (w ^ 4) * 2048 + lane * 16), o1 = ldsf4(L_XCH + (w ^ 4) * 2048 + 1024 + lane * 16);
    const f4 t0 = (kh ? acc[2] : acc[0]) + o0, t1 = (kh ? acc[3] : acc[1]) + o1;
    const float xin[8] = {bflo(xr0[0]), bfhi(xr0[0]), bflo(xr0[1]), bfhi(xr0[1]), bflo(xr1[0]), bfhi(xr1[0]), bflo(xr1[1]), bfhi(xr1[1])};
    const float ga[8] = {bflo(gaR[0]), bfhi(gaR[0]), bflo(gaR[1]), bfhi(gaR[1]), bflo(gaR[2]), bfhi(gaR[2]), bflo(gaR[3]), bfhi(gaR[3])};
    const float cb[8] = {bflo(cbR[0]), bfhi(cbR[0]), bflo(cbR[1]), bfhi(cbR[1]), bflo(cbR[2]), bfhi(cbR[2]), bflo(cbR[3]), bfhi(cbR[3])};
    const float sv[8] = {bflo(sbR[0]), bfhi(sbR[0]), bflo(sbR[1]), bfhi(sbR[1]), bflo(sbR[2]), bfhi(sbR[2]), bflo(sbR[3]), bfhi(sbR[3])};
    float wv[8], xw[8];
#pragma unroll
    for (int r = 0; r < 8; ++r) { x1[r] = rbf(xin[r] + ga[r] * (r < 4 ? t0[r & 3] : t1[r & 3])); wv[r] = rbf(1.f + cb[r]); xw[r] = x1[r] * wv[r]; }
    st16(rWS, v4u{pk(xw[0], xw[1]), pk(xw[2], xw[3]), pk(xw[4], xw[5]), pk(xw[6], xw[7])}, WS_X1F + ((ci * 8 + 4 * hf + tt) * 16 + 2 * ct + kh) * 1024 + lane * 16);
    SB();
    float s1 = 0, s2 = 0, s3 = 0, s4 = 0, s5 = 0, s6 = 0;
#pragma unroll
    for (int r = 0; r < 8; ++r) { s1 += x1[r]; s2 += x1[r] * x1[r]; s3 += xw[r]; s4 += xw[r] * wv[r]; s5 += xw[r] * xw[r]; s6 += xw[r] * sv[r]; }
    s1 = sumg(s1); s2 = sumg(s2); s3 = sumg(s3); s4 = sumg(s4); s5 = sumg(s5); s6 = sumg(s6);
    const unsigned rec = (ci * 16 + 2 * ct + kh) * 128 + 64 * hf + 16 * tt + tok;
    if (g == 0) st16(rWS, v4u{__float_as_uint(s1), __float_as_uint(s2), __float_as_uint(s3), __float_as_uint(s4)}, WS_ST2A + rec * 16);
    if (g == 1) st8(rWS, v2u{__float_as_uint(s5), __float_as_uint(s6)}, WS_ST2B + rec * 8);
  }
  if (w < 4) {
    glds4x1(rWS, lane * 16, WS_TW1 + (cj * 7 + 6) * 16384 + w * 4096, L_W1S + w * 4096);
    glds4x1(rWS, lane * 16, WS_TW2 + ct * 229376 + 7 * 16384 + w * 4096, 49152 + w * 4096);
    asm volatile("s_waitcnt vmcnt(8)" ::: "memory");
  } else {
    glds4x1(rWS, lane * 16, WS_TW2 + ct * 229376 + (w - 4) * 4096, 32768 + (w - 4) * 4096);
    asm volatile("s_waitcnt vmcnt(4)" ::: "memory");
  }
  BAR();
  if (tid == 256) arrive(p.ws, WS_FP + (2 * 16 + ci) * 128 + cj * 8, TAG);
  STAMP(10);
  if (w == 4) asm volatile("s_waitcnt vmcnt(1)" ::: "memory"); else VM0();
  BAR();

  v4u stg[8];
  {
    const unsigned bP = opq(L_W1 + lane * 16), bR = opq(L_W1 + 65536 + lane * 16), bS = opq(L_W1S + lane * 16);
#define W1F(nt, s) lds16((nt) == 6 ? bS + (s) * 1024 : (nt) < 4 ? bP + (nt) * 16384 + (s) * 1024 : bR + ((nt) - 4) * 16384 + (s) * 1024)
    {
      f4 au[7];
#pragma unroll
      for (int nt = 0; nt < 7; ++nt) au[nt] = f4{0, 0, 0, 0};
      const unsigned c0 = opq(bP + w * 2048), d0 = opq(bR + w * 2048), e0 = opq(bS + w * 2048);
#pragma unroll
      for (int u = 0; u < 2; ++u) {
        v8s W[7];
#pragma unroll
        for (int nt = 0; nt < 7; ++nt) W[nt] = lds16(nt == 6 ? e0 + u * 1024 : nt < 4 ? c0 + nt * 16384 + u * 1024 : d0 + (nt - 4) * 16384 + u * 1024);
#pragma unroll
        for (int nt = 0; nt < 7; ++nt) au[nt] = MFMA16(W[nt], aux2[u], au[nt]);
      }
      if (tok < 3) {
#pragma unroll
        for (int nt = 0; nt < 7; ++nt) *(LAS f4*)(sm + L_XCH + ((w * 3 + tok) * 112 + nt * 16 + 4 * g) * 4) = au[nt];
      }
    }
    BAR();
    if (tid < 112) *(LAS float*)(sm + L_XF + (336 + tid) * 4) = b1v;
    {
      const int e0 = w < 4 ? tid : tid - 64;
      if (w != 4 && e0 < 336) {
        float s = 0;
#pragma unroll
        for (int e = 0; e < 8; ++e) s += *(const LAS float*)(sm + L_XCH + (e * 336 + e0) * 4);
        *(LAS float*)(sm + L_XF + e0 * 4) = s;
      }
    }
    if (w == 4) wait_words(p.ws, WS_FP + (2 * 16 + ci) * 128, 16, lane, TAG);
    BAR();
    STAMP(11);
    v8s act2[16];
    const unsigned asrc = WS_X1F + (ci * 8 + w) * 16 * 1024;
#pragma unroll
    for (int s = 0; s < 4; ++s) act2[s] = asv8(ld16(rWS, lane * 16, asrc + s * 1024));
    v4u sa[4]; v2u sb[4];
    f4 acc[7];
#pragma unroll
    for (int nt = 0; nt < 7; ++nt) acc[nt] = f4{0, 0, 0, 0};
    {
      v8s W[6];
#pragma unroll
      for (int nt = 0; nt < 6; ++nt) W[nt] = W1F(nt, 0);
      SB();
#pragma unroll
      for (int s = 0; s < 16; ++s) {
        if (s == 0) act2[4] = asv8(ld16(rWS, lane * 16, asrc + 4 * 1024));
        if (s + 5 < 16) act2[s + 5] = asv8(ld16(rWS, lane * 16, asrc + (s + 5) * 1024));
        else if (s < 15) { sa[s - 11] = ld16(rWS, ((4 * g + s - 11) * 128 + 16 * w + tok) * 16, WS_ST2A + ci * 16 * 128 * 16);
                           sb[s - 11] = ld8(rWS, ((4 * g + s - 11) * 128 + 16 * w + tok) * 8, WS_ST2B + ci * 16 * 128 * 8); }
#pragma unroll
        for (int nt = 0; nt < 6; ++nt) {
          acc[nt] = MFMA16(W[nt], act2[s], acc[nt]);
          if (s + 1 < 16) W[nt] = W1F(nt, s + 1);
          SB();
        }
      }
    }
    STAMP(18);
    CSTAMP(42);
    BAR();
    v8s WB[4];
#pragma unroll
    for (int s = 0; s < 4; ++s) WB[s] = W1F(6, s);
    float s1 = 0, s2 = 0, s3 = 0, s4 = 0, s5 = 0, s6 = 0;
#pragma unroll
    for (int e = 0; e < 4; ++e) { s1 += __uint_as_float(sa[e][0]); s2 += __uint_as_float(sa[e][1]); s3 += __uint_as_float(sa[e][2]); s4 += __uint_as_float(sa[e][3]); s5 += __uint_as_float(sb[e][0]); s6 += __uint_as_float(sb[e][1]); }
    s1 = sumg(s1); s2 = sumg(s2); s3 = sumg(s3); s4 = sumg(s4); s5 = sumg(s5); s6 = sumg(s6);
    const float mu1 = s1 * (1.f / 512), var1 = fmaxf(s2 * (1.f / 512) - mu1 * mu1, 0.f), r1 = __builtin_amdgcn_rsqf(var1 + 1e-5f);
    const float mu2 = (r1 * (s3 - mu1 * Cw) + Cs) * (1.f / 512);
    const float hh = (r1 * r1 * (s5 - 2.f * mu1 * s4 + mu1 * mu1 * Cw2) + 2.f * r1 * (s6 - mu1 * Cws) + Cs2) * (1.f / 512);
    const float r2 = __builtin_amdgcn_rsqf(fmaxf(hh - mu2 * mu2, 0.f) + 1e-5f);
    const float cA = r2 * r1, cB = -cA * mu1, cC = r2, cD = -r2 * mu2;
    const unsigned S0 = (7 * cj + par) >> 1;
    const unsigned hb = WS_H1F + (ci * 8 + w) * 56 * 1024;
    const unsigned xf = opq(L_XF + 16 * g);
#define BIAS4(nt) __builtin_elementwise_fma(f4{cB, cB, cB, cB}, ldsf4(xf + (nt) * 64), __builtin_elementwise_fma(f4{cC, cC, cC, cC}, ldsf4(xf + 448 + (nt) * 64), \
                    __builtin_elementwise_fma(f4{cD, cD, cD, cD}, ldsf4(xf + 896 + (nt) * 64), ldsf4(xf + 1344 + (nt) * 64))))
#define GELU4(o, a, bias) do { const f4 z_ = __builtin_elementwise_fma(f4{cA, cA, cA, cA}, a, bias); const f4 q_ = __builtin_elementwise_fma(z_ * z_, f4{-0.1029432397f, -0.1029432397f, -0.1029432397f, -0.1029432397f}, \
        f4{-2.3022081985f, -2.3022081985f, -2.3022081985f, -2.3022081985f}) * z_; f4 e_; \
        _Pragma("unroll") for (int r = 0; r < 4; ++r) e_[r] = __builtin_amdgcn_exp2f(q_[r]); \
        e_ += f4{1.f, 1.f, 1.f, 1.f}; \
        _Pragma("unroll") for (int r = 0; r < 4; ++r) e_[r] = __builtin_amdgcn_rcpf(e_[r]); \
        o = z_ * e_; } while (0)
    {
      unsigned hp[2][2];
      f4 bias = BIAS4(0);
      PIN(bias);
      SB();
#pragma unroll
      for (int nt = 0; nt < 6; ++nt) {
#pragma unroll
        for (int k = (nt * 16) / 6; k < ((nt + 1) * 16) / 6; ++k) { acc[6] = MFMA16(WB[k & 3], act2[k], acc[6]); if (k + 4 < 16) WB[k & 3] = W1F(6, k + 4); }
        if ((nt & 1) == 0) { const int gi = w * 3 + (nt >> 1), it = 1 + (gi >> 3), khh = (gi >> 2) & 1, pc4 = gi & 3;
          glds4x1(rWS, lane * 16, WS_TW2 + ct * 229376 + (khh * 7 + it) * 16384 + pc4 * 4096, 65536u + (it - 1) * 32768u + khh * 16384u + pc4 * 4096u); }
        if (nt >= 1 && nt < 5) {
#pragma unroll
          for (int e = 2 * (nt - 1); e < 2 * nt; ++e) { const int pc = w * 8 + e; const int chunk = (pc >> 4) == 0 ? 5 : ((pc >> 4) == 1 ? 12 : ((pc >> 4) == 2 ? 6 : 13));
            stg[e] = ld16(rWS, lane * 16, WS_TW2 + ct * 229376 + chunk * 16384 + (pc & 15) * 1024); }
        }
        f4 nb = bias;
        if (nt < 5) nb = BIAS4(nt + 1); else nb = BIAS4(6);
        f4 o;
        GELU4(o, acc[nt], bias);
        hp[nt & 1][0] = pk(o[0], o[1]); hp[nt & 1][1] = pk(o[2], o[3]);
        PIN(hp[nt & 1][0]); PIN(hp[nt & 1][1]); PIN(nb);
        bias = nb;
        if (nt & 1) st16(rWS, v4u{hp[0][0], hp[0][1], hp[1][0], hp[1][1]}, hb + (S0 + (nt >> 1)) * 1024 + lane * 16);
        SB();
      }
      STAMP(20);
      CSTAMP(43);
      f4 o;
      GELU4(o, acc[6], bias);
      const unsigned NT = 7 * cj + (par ? 0 : 6);
      st8(rWS, v2u{pk(o[0], o[1]), pk(o[2], o[3])}, hb + (NT >> 1) * 1024 + (NT & 1) * 512 + lane * 8);
    }
  }
  STAMP(25);
  VM0();
  BAR();
  if (tid == 256) arrive(p.ws, WS_FP + (3 * 16 + ci) * 128 + cj * 8, TAG);
  STAMP(13);
  if (w != 4) {
#pragma unroll
    for (int e = 0; e < 2; ++e) {
      const int gi = wq + 7 * e;
      if (gi < 8) glds4x1(rWS, lane * 16, WS_TW2 + ct * 229376 + ((gi >> 2) * 7 + 4) * 16384 + (gi & 3) * 4096, (gi >> 2) * 16384u + (gi & 3) * 4096u);
    }
  } else {
    if (b == 0) {
      GAS u64* f = (GAS u64*)(p.ws + WS_F0) + lane;
      for (unsigned spins = 0; spins < (1u << 17); ++spins) {
        bool ok = true;
#pragma unroll
        for (int i = 0; i < 16; ++i) ok &= __hip_atomic_load(f + i * 64, RLX_AGENT) == TAG;
        if (__all(ok)) break;
      }
      if (lane == 0) __hip_atomic_store((GAS u64*)(p.ws + WS_EPOCH), TAG, RLX_AGENT);
    }
    wait_words(p.ws, WS_FP + (3 * 16 + ci) * 128, 16, lane, TAG);
  }
  BAR();
  STAMP(14);

  {
    v8s ha[28];
    const unsigned hb = WS_H1F + ((ci * 8 + 4 * hf + tt) * 56 + 28 * kh) * 1024;
#define HLOAD(s) do { if ((s) % 7 == 3) { const v2u lo_ = ld8(rWS, lane * 8, hb + (s) * 1024), hi_ = ld8(rWS, lane * 8 + 512, hb + (s) * 1024); ha[s] = asv8(v4u{lo_[0], lo_[1], hi_[0], hi_[1]}); } \
                      else ha[s] = asv8(ld16(rWS, lane * 16, hb + (s) * 1024)); } while (0)
#pragma unroll
    for (int s = 0; s < 3; ++s) HLOAD(s);
    const unsigned bw = opq(kh * 16384 + lane * 16), bw2 = opq(bw + 65536), bw3 = opq(bw + 131072);
    f4 acc[4];
#pragma unroll
    for (int nt = 0; nt < 4; ++nt) acc[nt] = f4{0, 0, 0, 0};
#define W2F(nt, S) lds16((((S) >> 2) == 0 || ((S) >> 2) == 5 ? bw + 32768 : ((S) >> 2) == 1 || ((S) >> 2) == 6 ? bw2 : ((S) >> 2) == 2 ? bw2 + 32768 : ((S) >> 2) == 3 ? bw3 : bw) + (((S) & 3) * 4 + (nt)) * 1024)
    {
      v8s W[2][4];
#pragma unroll
      for (int S = 0; S < 2; ++S)
#pragma unroll
        for (int nt = 0; nt < 4; ++nt) W[S][nt] = W2F(nt, S);
      SB();
#pragma unroll
      for (int s = 3; s < 8; ++s) HLOAD(s);
      SB();
#pragma unroll
      for (int S = 0; S < 28; ++S) {
        if (S + 8 < 28) HLOAD(S + 8);
        if (S == 8) BAR();
        if (S >= 8 && S < 16) { const int e = S - 8, pc = w * 8 + e; *(LAS v4u*)(sm + 32768 + (pc >> 5) * 32768 + ((pc >> 4) & 1) * 16384 + (pc & 15) * 1024 + lane * 16) = stg[e]; }
        if (S == 18) BAR();
#pragma unroll
        for (int nt = 0; nt < 4; ++nt) {
          acc[nt] = MFMA16(W[S & 1][nt], ha[S], acc[nt]);
          if (S + 2 < 28) W[S & 1][nt] = W2F(nt, S + 2);
          SB();
        }
      }
    }
    STAMP(22);
    BAR();
    *(LAS f4*)(sm + L_W2X + w * 2048 + lane * 16) = kh ? acc[0] : acc[2];
    *(LAS f4*)(sm + L_W2X + w * 2048 + 1024 + lane * 16) = kh ? acc[1] : acc[3];
    BAR();
    const f4 o0 = ldsf4(L_W2X + (w ^ 4) * 2048 + lane * 16), o1 = ldsf4(L_W2X + (w ^ 4) * 2048 + 1024 + lane * 16);
    const f4 t0 = (kh ? acc[2] : acc[0]) + o0, t1 = (kh ? acc[3] : acc[1]) + o1;
    const float gb[8] = {bflo(gbR[0]), bfhi(gbR[0]), bflo(gbR[1]), bfhi(gbR[1]), bflo(gbR[2]), bfhi(gbR[2]), bflo(gbR[3]), bfhi(gbR[3])};
    const float bb[8] = {bflo(b2R[0][0]), bfhi(b2R[0][0]), bflo(b2R[0][1]), bfhi(b2R[0][1]), bflo(b2R[1][0]), bfhi(b2R[1][0]), bflo(b2R[1][1]), bfhi(b2R[1][1])};
    float o[8];
#pragma unroll
    for (int r = 0; r < 8; ++r) o[r] = x1[r] + gb[r] * ((r < 4 ? t0[r & 3] : t1[r & 3]) + bb[r]);
    auto r0 = __builtin_amdgcn_permlane16_swap(pk(o[0], o[1]), pk(o[4], o[5]), false, false);
    auto r1 = __builtin_amdgcn_permlane16_swap(pk(o[2], o[3]), pk(o[6], o[7]), false, false);
    { const srd_t rO = __builtin_amdgcn_make_buffer_rsrc((void*)p.out, 0, SEQ * DM * 2, 0x00020000);
      __builtin_amdgcn_raw_buffer_store_b128(v4u{r0[0], r1[0], r0[1], r1[1]}, rO, (orow * DM + 64 * ct + 32 * kh + (g & 1) * 16 + (g >> 1) * 8) * 2, 0, 16); }
  }
  while (__builtin_amdgcn_s_memrealtime() - pad_t0 < 4200ull) __builtin_amdgcn_s_sleep(1);
  STAMP(15);
  CSTAMP(41);
  STAMP_FLUSH(0);
}

extern "C" void kernel_launch(void* const* d_in, const int* in_sizes, int n_in, void* d_out, int out_size, void* d_ws, size_t ws_size, hipStream_t stream) {
  Params p;
  p.x = (const u16*)d_in[0]; p.wqkv = (const u16*)d_in[1]; p.wo = (const u16*)d_in[2];
  p.w1 = (const u16*)d_in[4]; p.b1 = (const u16*)d_in[5]; p.w2 = (const u16*)d_in[6]; p.b2 = (const u16*)d_in[7];
  p.c = (const u16*)d_in[8]; p.wmod = (const u16*)d_in[9]; p.out = (u16*)d_out; p.ws = (char*)d_ws;
  fused_block<<<256, 512, 0, stream>>>(p);
}
```

```cpp
#include <hip/hip_runtime.h>
#include <hip/hip_bf16.h>
#include <stdint.h>

typedef unsigned short u16;
typedef short v4s __attribute__((ext_vector_type(4)));
typedef short v8s __attribute__((ext_vector_type(8)));
typedef float f2 __attribute__((ext_vector_type(2)));
typedef float f4 __attribute__((ext_vector_type(4)));
typedef float f16v __attribute__((ext_vector_type(16)));
typedef unsigned v2u __attribute__((ext_vector_type(2)));
typedef unsigned v4u __attribute__((ext_vector_type(4)));
typedef __bf16 b2 __attribute__((ext_vector_type(2)));
#define LAS __attribute__((address_space(3)))
#define GAS __attribute__((address_space(1)))
#define DI __device__ __forceinline__
typedef __amdgpu_buffer_rsrc_t srd_t;

constexpr int SEQ = 2048, DM = 512, FF = 1792;
constexpr unsigned WS_EPOCH = 0;
constexpr unsigned WS_F0 = 4096;
constexpr unsigned WS_FP = WS_F0 + 8192;
constexpr unsigned WS_FM = WS_FP + 4 * 16 * 128;
constexpr unsigned WS_FQ = WS_FM + 2048;
constexpr unsigned WS_FW1 = WS_FQ + 2048;
constexpr unsigned WS_FWO = WS_FW1 + 2048;
constexpr unsigned WS_FW2 = WS_FWO + 1024;
static_assert(WS_FW2 + 4096 <= (64 << 10), "flags");
constexpr unsigned WS_WV = 64 << 10;
constexpr unsigned WS_MOD = 128 << 10;
constexpr unsigned WS_ST1 = 192 << 10;
constexpr unsigned WS_ST2A = 512 << 10;
constexpr unsigned WS_ST2B = 1024 << 10;
constexpr unsigned WS_HF = 2u << 20;
constexpr unsigned WS_KV = 4u << 20;
constexpr unsigned WS_OF = 8u << 20;
constexpr unsigned WS_X1F = 10u << 20;
constexpr unsigned WS_H1F = 12u << 20;
constexpr unsigned WS_TQ = 19u << 20;
constexpr unsigned WS_TW1 = WS_TQ + (1536u << 10);
constexpr unsigned WS_TWO = WS_TW1 + (1792u << 10);
constexpr unsigned WS_TW2 = WS_TWO + (512u << 10);
constexpr unsigned WS_STAMP = 25u << 20;
constexpr unsigned WS_BYTES = 27u << 20;
static_assert(WS_TW2 + (1792u << 10) <= WS_STAMP, "ws");
constexpr unsigned L_CA = 98304;
constexpr unsigned L_XS = 131072;
constexpr unsigned L_S1 = L_XS + 8192;
constexpr unsigned L_SO = L_S1 + 7168;
constexpr unsigned L_SC = L_SO + 12288;
constexpr unsigned L_CAP = L_SC + 2048;
constexpr unsigned L_CNT = L_CAP + 512;
static_assert(L_CNT + 16 <= 163840, "lds");
constexpr unsigned L_SQ = 0;
constexpr unsigned L_M2 = 8192;
constexpr unsigned L_WQKV = 0;
constexpr unsigned L_PARTU = 98304;
constexpr unsigned L_PARTV = 32768;
constexpr unsigned L_AK = 98304;
constexpr unsigned KPL = 6144;
constexpr unsigned L_AV = L_AK + 4 * KPL;
constexpr unsigned VPL = 6208, VHD = 12416;
constexpr unsigned L_PSA = L_AK + KPL, L_PSB = L_AK + 3 * KPL, L_PCB = L_AV + VPL, L_PGA = L_AV + VHD + VPL, L_PGB = L_PGA + 1024, L_B2C = L_PGA + 2048;
constexpr unsigned L_PSA2 = L_AK + 2 * KPL, L_PSB2 = L_AV, L_PCB2 = L_AV + VHD;
constexpr unsigned L_FIN = L_AK;
constexpr unsigned L_ONE = L_AV + 2 * VHD;
constexpr unsigned L_AQ = L_ONE + 6400;
constexpr unsigned L_PERS = L_AQ + 8192;
static_assert(L_PERS + 1024 <= 163840, "lds");
static_assert((L_AV % 256) == 0 && (L_ONE % 256) == 0, "bank plan");
constexpr unsigned L_WO = 0;
constexpr unsigned L_W1 = 65536;
constexpr unsigned L_W1S = 0;
constexpr unsigned L_XCH = 16384;
constexpr unsigned L_XF = 16384 + 12288;
constexpr unsigned L_W2X = 98304;

struct Params { const u16* x; const u16* wqkv; const u16* wo; const u16* w1; const u16* b1; const u16* w2; const u16* b2; const u16* c; const u16* wmod; u16* out; char* ws; };

DI float bflo(unsigned v) { return __uint_as_float(v << 16); }
DI float bfhi(unsigned v) { return __uint_as_float(v & 0xffff0000u); }
DI unsigned pk(float a, float b) { f2 v = {a, b}; b2 r = __builtin_convertvector(v, b2); return __builtin_bit_cast(unsigned, r); }
DI float rbf(float a) { return bflo(pk(a, 0.f)); }
DI v8s cat(v4s a, v4s b) { return __builtin_shufflevector(a, b, 0, 1, 2, 3, 4, 5, 6, 7); }
DI v4s trd(unsigned a) { return __builtin_amdgcn_ds_read_tr16_b64_v4i16((LAS v4s*)(uintptr_t)a); }
DI v8s asv8(v4u v) { return __builtin_bit_cast(v8s, v); }
DI v4u asv4u(v8s v) { return __builtin_bit_cast(v4u, v); }
DI v8s lds16(unsigned a) { return *(const LAS v8s*)(uintptr_t)a; }
DI f4 ldsf4(unsigned a) { return *(const LAS f4*)(uintptr_t)a; }
DI v2u lds8(unsigned a) { return *(const LAS v2u*)(uintptr_t)a; }
#define MFMA16(a, b, c) __builtin_amdgcn_mfma_f32_16x16x32_bf16((a), (b), (c), 0, 0, 0)
#define MFMA32(a, b, c) __builtin_amdgcn_mfma_f32_32x32x16_bf16((a), (b), (c), 0, 0, 0)
#define BAR() asm volatile("s_waitcnt lgkmcnt(0)\n\ts_barrier" ::: "memory")
#define VM0() asm volatile("s_waitcnt vmcnt(0)" ::: "memory")
#define LG0() asm volatile("s_waitcnt lgkmcnt(0)" ::: "memory")
#define CFENCE() asm volatile("" ::: "memory")
#define SB() __builtin_amdgcn_sched_barrier(0)
DI unsigned opq(unsigned v) { asm volatile("" : "+v"(v)); return v; }
DI void glds(srd_t r, unsigned voff, unsigned soff, unsigned ldsaddr) {
  asm volatile("s_mov_b32 m0, %0\n\ts_nop 0\n\tbuffer_load_dwordx4 %1, %2, %3 offen lds" ::"s"(ldsaddr), "v"(voff), "s"(r), "s"(soff) : "memory");
}
DI void glds1(srd_t r, unsigned voff, unsigned soff, unsigned ldsaddr) {
  asm volatile("s_mov_b32 m0, %0\n\ts_nop 0\n\tbuffer_load_dwordx4 %1, %2, %3 offen sc1 lds" ::"s"(ldsaddr), "v"(voff), "s"(r), "s"(soff) : "memory");
}
DI void glds4(srd_t r, unsigned voff, unsigned soff, unsigned ldsaddr) {
  asm volatile("s_mov_b32 m0, %0\n\ts_nop 0\n\tbuffer_load_dwordx4 %1, %2, %3 offen lds\n\tbuffer_load_dwordx4 %1, %2, %3 offen offset:1024 lds\n\t"
               "buffer_load_dwordx4 %1, %2, %3 offen offset:2048 lds\n\tbuffer_load_dwordx4 %1, %2, %3 offen offset:3072 lds" ::"s"(ldsaddr), "v"(voff), "s"(r), "s"(soff) : "memory");
}
DI void glds4x1(srd_t r, unsigned voff, unsigned soff, unsigned ldsaddr) {
  asm volatile("s_mov_b32 m0, %0\n\ts_nop 0\n\tbuffer_load_dwordx4 %1, %2, %3 offen sc1 lds\n\tbuffer_load_dwordx4 %1, %2, %3 offen offset:1024 sc1 lds\n\t"
               "buffer_load_dwordx4 %1, %2, %3 offen offset:2048 sc1 lds\n\tbuffer_load_dwordx4 %1, %2, %3 offen offset:3072 sc1 lds" ::"s"(ldsaddr), "v"(voff), "s"(r), "s"(soff) : "memory");
}
DI void glds2x1(srd_t r, unsigned voff, unsigned soff, unsigned ldsaddr) {
  asm volatile("s_mov_b32 m0, %0\n\ts_nop 0\n\tbuffer_load_dwordx4 %1, %2, %3 offen sc1 lds\n\tbuffer_load_dwordx4 %1, %2, %3 offen offset:1024 sc1 lds" ::"s"(ldsaddr), "v"(voff), "s"(r), "s"(soff) : "memory");
}
DI v4u ld16(srd_t r, unsigned voff, unsigned soff) { return __builtin_amdgcn_raw_buffer_load_b128(r, voff, soff, 16); }
DI v2u ld8(srd_t r, unsigned voff, unsigned soff) { return __builtin_amdgcn_raw_buffer_load_b64(r, voff, soff, 16); }
DI v4u ldp16(srd_t r, unsigned voff, unsigned soff) { return __builtin_amdgcn_raw_buffer_load_b128(r, voff, soff, 0); }
DI void stp16(srd_t r, v4u v, unsigned off) { __builtin_amdgcn_raw_buffer_store_b128(v, r, off, 0, 0); }
DI void st16(srd_t r, v4u v, unsigned off) { __builtin_amdgcn_raw_buffer_store_b128(v, r, off, 0, 16); }
DI void st8(srd_t r, v2u v, unsigned off) { __builtin_amdgcn_raw_buffer_store_b64(v, r, off, 0, 16); }
DI float sum16(float v) { auto r = __builtin_amdgcn_permlane16_swap(__float_as_uint(v), __float_as_uint(v), false, false); return __uint_as_float(r[0]) + __uint_as_float(r[1]); }
DI float sum32(float v) { auto r = __builtin_amdgcn_permlane32_swap(__float_as_uint(v), __float_as_uint(v), false, false); return __uint_as_float(r[0]) + __uint_as_float(r[1]); }
DI float max32(float v) { auto r = __builtin_amdgcn_permlane32_swap(__float_as_uint(v), __float_as_uint(v), false, false); return fmaxf(__uint_as_float(r[0]), __uint_as_float(r[1])); }
DI float sumg(float v) { return sum32(sum16(v)); }
#define RLX_AGENT __ATOMIC_RELAXED, __HIP_MEMORY_SCOPE_AGENT
typedef unsigned long long u64;
DI void arrive(char* ws, unsigned off, u64 tag) { (void)__hip_atomic_exchange((GAS u64*)(ws + off), tag, RLX_AGENT); }
DI void wait_words(char* ws, unsigned off, int n, int lane, u64 tag) {
  GAS u64* p = (GAS u64*)(ws + off) + (lane < n ? lane : 0);
  for (unsigned spins = 0; spins < (1u << 17); ++spins)
    if (__all(__hip_atomic_load(p, RLX_AGENT) == tag)) break;
  CFENCE();
}
template <int C> DI float dpp(float v) { return __uint_as_float(__builtin_amdgcn_update_dpp(0u, __float_as_uint(v), C, 0xf, 0xf, false)); }
DI float rowsum16(float v) { v += dpp<0x128>(v); v += dpp<0x124>(v); v += dpp<0x122>(v); v += dpp<0x121>(v); return v; }
#define PIN(x) asm volatile("" : "+v"(x))
#define STAMP(k)
#define STAMP_FLUSH(o)
#define CSTAMP(k)
DI float silu(float v) { return v * __builtin_amdgcn_rcpf(1.f + __builtin_amdgcn_exp2f(-1.4426950408889634f * v)); }

DI v8s trfragA(unsigned img, int half, int lane) {
  const int g = lane >> 4, q = (lane & 15) >> 2, pp = lane & 3;
  const unsigned a = img + (4 * g + q) * 64 + ((half ^ (g & 1)) * 32) + pp * 8;
  return cat(trd(a), trd(a + 1024));
}

template <bool FULL>
DI void attention(int t, int hd, int kt0, int lane, f16v mU, f16v mL, f16v& O, srd_t rWS, unsigned wo_src, unsigned w1_src, int w, const u16* xres, v2u& xr0, v2u& xr1, unsigned& stampv) {
  const int qr = lane & 31, h = lane >> 5, blk = (lane >> 4) & 1, q = (lane & 15) >> 2, pp = lane & 3;
  const v8s Qf = lds16(L_AQ + (hd * 2 + h) * 2048 + (32 * t + qr) * 16);
  const unsigned kb = opq(L_AK + (hd * 2 + h) * KPL + (32 * t + qr) * 16);
  const unsigned vb = opq((blk == 0 ? L_AV + hd * VHD + (pp >> 1) * VPL + (pp & 1) * 8 + (4 * h + q) * 16 : L_ONE + ((hd * 128 + (h ? 0 : 128)) & 255)) + t * 512);
  const f16v zero = {};
#define RIDE(n) do { if ((n) < 2) glds4x1(rWS, lane * 16, wo_src + (w * 2 + (n)) * 4096, L_WO + (w * 2 + (n)) * 4096); \
                     else glds4x1(rWS, lane * 16, w1_src + w * 4096, L_W1 + w * 4096); } while (0)
  if (FULL) {
    v8s Kf[9];
#pragma unroll
    for (int kt = 0; kt < 9; ++kt) Kf[kt] = lds16(kb + kt * 512);
    v8s V0 = cat(trd(vb), trd(vb + 128)), V1 = cat(trd(vb + 256), trd(vb + 384));
    f16v S[2];
    float m0 = -3e38f, m1 = -3e38f;
    S[0] = MFMA32(Kf[0], Qf, mL);
    SB();
#pragma unroll
    for (int kt = 0; kt < 9; ++kt) {
      if (kt < 8) S[(kt + 1) & 1] = MFMA32(Kf[kt + 1], Qf, kt + 1 == 8 ? mU : zero);
      SB();
#pragma unroll
      for (int r = 0; r < 16; r += 4) { m0 = fmaxf(fmaxf(m0, S[kt & 1][r]), S[kt & 1][r + 1]); m1 = fmaxf(fmaxf(m1, S[kt & 1][r + 2]), S[kt & 1][r + 3]); }
      SB();
    }
    const float m = max32(fmaxf(m0, m1));
    STAMP(23);
    f16v nm;
#pragma unroll
    for (int r = 0; r < 16; ++r) { nm[r] = -m; mU[r] -= m; mL[r] -= m; }
    O = zero;
    S[0] = MFMA32(Kf[0], Qf, mL);
    SB();
#pragma unroll
    for (int kt = 0; kt < 9; ++kt) {
      if (kt < 8) S[(kt + 1) & 1] = MFMA32(Kf[kt + 1], Qf, kt + 1 == 8 ? mU : nm);
      if (kt == 0) RIDE(0);
      if (kt == 3) RIDE(1);
      if (kt == 6) RIDE(2);
      SB();
      v4u P0, P1;
#pragma unroll
      for (int r = 0; r < 4; ++r) P0[r] = pk(__builtin_amdgcn_exp2f(S[kt & 1][2 * r]), __builtin_amdgcn_exp2f(S[kt & 1][2 * r + 1]));
      O = MFMA32(V0, asv8(P0), O);
      if (kt < 8) V0 = cat(trd(vb + (kt + 1) * 512), trd(vb + (kt + 1) * 512 + 128));
      if (kt == 2) xr0 = *(const v2u*)xres;
      if (kt == 5) xr1 = *(const v2u*)(xres + 16);
      SB();
#pragma unroll
      for (int r = 0; r < 4; ++r) P1[r] = pk(__builtin_amdgcn_exp2f(S[kt & 1][8 + 2 * r]), __builtin_amdgcn_exp2f(S[kt & 1][8 + 2 * r + 1]));
      O = MFMA32(V1, asv8(P1), O);
      if (kt < 8) V1 = cat(trd(vb + (kt + 1) * 512 + 256), trd(vb + (kt + 1) * 512 + 384));
      SB();
    }
  } else {
#pragma unroll
    for (int n = 0; n < 3; ++n) RIDE(n);
    xr0 = *(const v2u*)xres; xr1 = *(const v2u*)(xres + 16);
    float m = -3e38f;
#pragma unroll
    for (int kt = 0; kt < 9; ++kt) {
      if (kt >= kt0) {
        const f16v S = MFMA32(lds16(kb + kt * 512), Qf, kt == 8 ? mU : zero);
#pragma unroll
        for (int r = 0; r < 16; r += 2) m = fmaxf(fmaxf(m, S[r]), S[r + 1]);
      }
    }
    m = max32(m);
    f16v nm;
#pragma unroll
    for (int r = 0; r < 16; ++r) { nm[r] = -m; mU[r] -= m; }
    O = zero;
#pragma unroll
    for (int kt = 0; kt < 9; ++kt) {
      if (kt >= kt0) {
        const f16v S = MFMA32(lds16(kb + kt * 512), Qf, kt == 8 ? mU : nm);
        v4u P0, P1;
#pragma unroll
        for (int r = 0; r < 4; ++r) {
          P0[r] = pk(__builtin_amdgcn_exp2f(S[2 * r]), __builtin_amdgcn_exp2f(S[2 * r + 1]));
          P1[r] = pk(__builtin_amdgcn_exp2f(S[8 + 2 * r]), __builtin_amdgcn_exp2f(S[8 + 2 * r + 1]));
        }
        const v8s V0 = cat(trd(vb + kt * 512), trd(vb + kt * 512 + 128));
        const v8s V1 = cat(trd(vb + kt * 512 + 256), trd(vb + kt * 512 + 384));
        O = MFMA32(V0, asv8(P0), O);
        O = MFMA32(V1, asv8(P1), O);
      }
    }
  }
#undef RIDE
}

__global__ void __launch_bounds__(512, 2) fused_block(Params p) {
  __shared__ __attribute__((aligned(16))) char smem[163840];
  LAS char* sm = (LAS char*)smem;
  const int tid = threadIdx.x, lane = tid & 63;
  const int w = __builtin_amdgcn_readfirstlane(tid >> 6);
  const int b = blockIdx.x, ci = ((b & 7) >> 1) * 4 + ((b >> 3) & 3), cj = (b & 1) * 8 + (b >> 5);
  const int tok = lane & 15, g = lane >> 4, q4 = (lane & 15) >> 2, pp = lane & 3;
  const int hf = cj & 1, ct = cj >> 1, par = cj & 1;
  const int wq = w < 4 ? w : w - 1;
  unsigned stampv = 0;
  const srd_t rWS = __builtin_amdgcn_make_buffer_rsrc(p.ws, 0, WS_BYTES, 0x00020000);
  const int rA = lane >> 2, pA = lane & 3;
  const unsigned colA = (((pA >> 1) ^ ((rA >> 2) & 1)) * 32) + (pA & 1) * 16;
  const int rB = lane >> 3, pB = lane & 7;
  const unsigned colB = (((pB >> 1) ^ ((rB >> 1) & 3)) * 32) + (pB & 1) * 16;
  STAMP(0);
  CSTAMP(40);
  const unsigned long long pad_t0 = __builtin_amdgcn_s_memrealtime();

  const bool m2 = b < 160;
  const int msl = b % 40, mkq = b / 40;
  const srd_t rWmod = __builtin_amdgcn_make_buffer_rsrc((void*)p.wmod, 0, DM * 3072 * 2, 0x00020000);
  v4u b2v = {0, 0, 0, 0};
  if (w == 6 && lane < 8) b2v = *(const v4u*)(p.b2 + 64 * ct + lane * 8);
  v4u cr;
  v2u ep;
  if (w < 4) {
    __builtin_amdgcn_s_setprio(2);
    asm volatile("global_load_dwordx4 %0, %1, off" : "=v"(cr) : "v"(p.c + 128 * w + 8 * (lane & 15)) : "memory");
    asm volatile("buffer_load_dwordx2 %0, %1, %2, 0 offen sc1" : "=v"(ep) : "v"(WS_EPOCH), "s"(rWS) : "memory");
    const srd_t rX = __builtin_amdgcn_make_buffer_rsrc((void*)p.x, 0, SEQ * DM * 2, 0x00020000);
#pragma unroll
    for (int u = 0; u < 2; ++u) glds(rX, rA * 1024 + pA * 16, (128 * ci + 16 * (2 * w + u)) * 1024 + 64 * cj, L_XS + (2 * w + u) * 1024);
#pragma unroll
    for (int i0 = 0; i0 < 8; ++i0) { const int ii = (i0 + (ci & 3) * 2 + (cj & 1)) & 7; glds(rWmod, rA * 6144 + colA, (w * 8 + ii) * 16 * 6144 + (512 + 32 * cj) * 2, L_CA + (w * 8 + ii) * 1024); }
  } else if (w == 5) {
    __builtin_amdgcn_s_setprio(3);
    const srd_t r = __builtin_amdgcn_make_buffer_rsrc((void*)p.wqkv, 0, DM * 1536 * 2, 0x00020000);
#pragma unroll
    for (int e = 0; e < 6; ++e) glds(r, rA * 3072 + colA, (32 * ci + 16 * (e & 1)) * 3072 + ((e >> 1) * 512 + 32 * cj) * 2, L_SQ + e * 1024);
  }
  if (tid == 256) { *(LAS unsigned*)(uintptr_t)L_CNT = 0u; *(LAS unsigned*)(uintptr_t)(L_CNT + 4) = m2 ? 0u : 1u; *(LAS unsigned*)(uintptr_t)(L_CNT + 8) = 0u; *(LAS unsigned*)(uintptr_t)(L_CNT + 12) = 0u; }
  BAR();
#define READ_TAG() do { const v2u ep_ = ld8(rWS, WS_EPOCH, 0); TAG = (((u64)__builtin_amdgcn_readfirstlane(ep_[1]) << 32) | __builtin_amdgcn_readfirstlane(ep_[0])) + 1; } while (0)
  v8s rt[12];
  f16v mU, mL;
  u64 TAG;
  if (w < 4) {
    asm volatile("s_waitcnt vmcnt(10)" : "+v"(cr), "+v"(ep) :: "memory");
    const unsigned sc = L_SC + w * 256;
    if (lane < 16) *(LAS v4u*)(uintptr_t)(sc + lane * 16) = v4u{pk(silu(bflo(cr[0])), silu(bfhi(cr[0]))), pk(silu(bflo(cr[1])), silu(bfhi(cr[1]))), pk(silu(bflo(cr[2])), silu(bfhi(cr[2]))), pk(silu(bflo(cr[3])), silu(bfhi(cr[3])))};
    LG0();
    v8s sf[4];
#pragma unroll
    for (int s = 0; s < 4; ++s) { const v2u lo = lds8(sc + (32 * s + 4 * g) * 2), hi = lds8(sc + (32 * s + 16 + 4 * g) * 2); sf[s] = asv8(v4u{lo[0], lo[1], hi[0], hi[1]}); }
    TAG = (((u64)__builtin_amdgcn_readfirstlane(ep[1]) << 32) | __builtin_amdgcn_readfirstlane(ep[0])) + 1;
    asm volatile("s_waitcnt vmcnt(8)" ::: "memory");
    v2u xa[2][2];
#pragma unroll
    for (int u = 0; u < 2; ++u) {
      const unsigned a = L_XS + (2 * w + u) * 1024 + tok * 64 + g * 8; xa[u][0] = lds8(a); xa[u][1] = lds8(a + 32);
      const float x0 = bflo(xa[u][0][0]), x1 = bfhi(xa[u][0][0]), x2 = bflo(xa[u][0][1]), x3 = bfhi(xa[u][0][1]);
      const float x4 = bflo(xa[u][1][0]), x5 = bfhi(xa[u][1][0]), x6 = bflo(xa[u][1][1]), x7 = bfhi(xa[u][1][1]);
      const float s1 = sumg(((x0 + x1) + (x2 + x3)) + ((x4 + x5) + (x6 + x7)));
      const float s2 = sumg(((x0 * x0 + x1 * x1) + (x2 * x2 + x3 * x3)) + ((x4 * x4 + x5 * x5) + (x6 * x6 + x7 * x7)));
      if (g == 0) st8(rWS, v2u{__float_as_uint(s1), __float_as_uint(s2)}, WS_ST1 + ((ci * 16 + cj) * 128 + 16 * (2 * w + u) + tok) * 8);
    }
    asm volatile("s_waitcnt vmcnt(2)" ::: "memory");
    STAMP(1);
    f4 a0 = {0, 0, 0, 0}, a1 = {0, 0, 0, 0};
    {
      v8s W[4][2];
#pragma unroll
      for (int s = 0; s < 4; ++s) { W[s][0] = trfragA(L_CA + (4 * w + s) * 2048, 0, lane); W[s][1] = trfragA(L_CA + (4 * w + s) * 2048, 1, lane); }
#pragma unroll
      for (int s = 0; s < 4; ++s) { a0 = MFMA16(W[s][0], sf[s], a0); a1 = MFMA16(W[s][1], sf[s], a1); }
    }
    if (tok == 0) { *(LAS f4*)(uintptr_t)(L_CAP + (w * 32 + 4 * g) * 4) = a0; *(LAS f4*)(uintptr_t)(L_CAP + (w * 32 + 16 + 4 * g) * 4) = a1; }
    LG0();
    if (lane == 0) __hip_atomic_fetch_add((LAS unsigned*)(uintptr_t)L_CNT, 1u, __ATOMIC_RELAXED, __HIP_MEMORY_SCOPE_WORKGROUP);
    while (*(volatile LAS unsigned*)(uintptr_t)L_CNT < 4u) {}
    CFENCE();
    STAMP(19);
    a0 = ldsf4(L_CAP + 4 * g * 4); a1 = ldsf4(L_CAP + (16 + 4 * g) * 4);
#pragma unroll
    for (int e = 1; e < 4; ++e) { a0 += ldsf4(L_CAP + (e * 32 + 4 * g) * 4); a1 += ldsf4(L_CAP + (e * 32 + 16 + 4 * g) * 4); }
    float wv[8];
#pragma unroll
    for (int r = 0; r < 4; ++r) { wv[r] = rbf(1.f + rbf(a0[r])); wv[4 + r] = rbf(1.f + rbf(a1[r])); }
#pragma unroll
    for (int u = 0; u < 2; ++u) {
      const float x0 = bflo(xa[u][0][0]), x1 = bfhi(xa[u][0][0]), x2 = bflo(xa[u][0][1]), x3 = bfhi(xa[u][0][1]);
      const float x4 = bflo(xa[u][1][0]), x5 = bfhi(xa[u][1][0]), x6 = bflo(xa[u][1][1]), x7 = bfhi(xa[u][1][1]);
      const int T = 2 * w + u;
      st16(rWS, v4u{pk(x0 * wv[0], x1 * wv[1]), pk(x2 * wv[2], x3 * wv[3]), pk(x4 * wv[4], x5 * wv[5]), pk(x6 * wv[6], x7 * wv[7])},
           WS_HF + ((ci * 8 + T) * 16 + cj) * 1024 + lane * 16);
      if (u == 0 && w == 0 && tok == 0) {
        st8(rWS, v2u{pk(wv[0], wv[1]), pk(wv[2], wv[3])}, WS_WV + ci * 1024 + cj * 64 + g * 8);
        st8(rWS, v2u{pk(wv[4], wv[5]), pk(wv[6], wv[7])}, WS_WV + ci * 1024 + cj * 64 + 32 + g * 8);
      }
    }
    STAMP(21);
    VM0();
    if (lane == 0) { arrive(p.ws, WS_F0 + (ci * 64 + cj * 4 + w) * 8, TAG); __hip_atomic_fetch_add((LAS unsigned*)(uintptr_t)(L_CNT + 8), 1u, __ATOMIC_RELAXED, __HIP_MEMORY_SCOPE_WORKGROUP); }
    __builtin_amdgcn_s_setprio(0);
  } else if (w == 4) {
    if (m2) {
      const int mvec = msl >> 3, mcol = (mvec == 0 ? 0 : 512 + 512 * mvec) + (msl & 7) * 64;
      __builtin_amdgcn_s_sleep(8);
#pragma unroll
      for (int ii = 0; ii < 16; ++ii) glds(rWmod, rB * 6144 + colB, (128 * mkq + 8 * ii) * 6144 + mcol * 2, L_M2 + ii * 1024);
      const v4u cr = *(const v4u*)(p.c + 128 * mkq + 8 * (lane & 15));
      const unsigned sc = L_SC + 1024;
      if (lane < 16) *(LAS v4u*)(uintptr_t)(sc + lane * 16) = v4u{pk(silu(bflo(cr[0])), silu(bfhi(cr[0]))), pk(silu(bflo(cr[1])), silu(bfhi(cr[1]))), pk(silu(bflo(cr[2])), silu(bfhi(cr[2]))), pk(silu(bflo(cr[3])), silu(bfhi(cr[3])))};
      LG0();
      v8s sf[4];
#pragma unroll
      for (int s = 0; s < 4; ++s) { const v2u lo = lds8(sc + (32 * s + 4 * g) * 2), hi = lds8(sc + (32 * s + 16 + 4 * g) * 2); sf[s] = asv8(v4u{lo[0], lo[1], hi[0], hi[1]}); }
      VM0();
      f4 a[4];
#pragma unroll
      for (int nt = 0; nt < 4; ++nt) a[nt] = f4{0, 0, 0, 0};
      const int row = 4 * g + q4;
#pragma unroll
      for (int s = 0; s < 4; ++s) {
        v8s W[4];
#pragma unroll
        for (int nt = 0; nt < 4; ++nt) { const unsigned ad = L_M2 + s * 4096 + row * 128 + ((nt ^ ((row >> 1) & 3)) * 32) + pp * 8; W[nt] = cat(trd(ad), trd(ad + 2048)); }
#pragma unroll
        for (int nt = 0; nt < 4; ++nt) a[nt] = MFMA16(W[nt], sf[s], a[nt]);
      }
      if (tok == 0) {
#pragma unroll
        for (int nt = 0; nt < 4; ++nt)
          st16(rWS, v4u{__float_as_uint(a[nt][0]), __float_as_uint(a[nt][1]), __float_as_uint(a[nt][2]), __float_as_uint(a[nt][3])},
               WS_MOD + mkq * 10240 + mvec * 2048 + ((msl & 7) * 2 + (nt >> 1)) * 128 + g * 32 + (nt & 1) * 16);
      }
      LG0();
      if (lane == 0) *(volatile LAS unsigned*)(uintptr_t)(L_CNT + 4) = 1u;
      READ_TAG();
      VM0();
      if (lane == 0) arrive(p.ws, WS_FM + b * 8, TAG);
    } else READ_TAG();
    STAMP(1);
  } else {
    if (w == 5) {
      READ_TAG();
      VM0();
#pragma unroll
      for (int nt = 0; nt < 6; ++nt) st16(rWS, asv4u(trfragA(L_SQ + (nt >> 1) * 2048, nt & 1, lane)), WS_TQ + ((cj * 6 + nt) * 16 + ci) * 1024 + lane * 16);
      VM0();
      if (lane == 0) arrive(p.ws, WS_FQ + (cj * 16 + ci) * 8, TAG);
      __builtin_amdgcn_s_setprio(0);
    } else {
      READ_TAG();
      __builtin_amdgcn_s_sleep(16);
      if (w == 6) {
        const srd_t r1 = __builtin_amdgcn_make_buffer_rsrc((void*)p.w1, 0, DM * FF * 2, 0x00020000);
#pragma unroll
        for (int e = 0; e < 6; ++e) glds(r1, rA * (FF * 2) + colA, (32 * ci + 16 * (e & 1)) * (FF * 2) + (112 * cj + 16 * par + (e >> 1) * 32) * 2, L_S1 + e * 1024);
        glds(r1, (lane >> 1) * (FF * 2) + (lane & 1) * 16, 32 * ci * (FF * 2) + (112 * cj + (par ? 0 : 96)) * 2, L_S1 + 6144);
        VM0();
#pragma unroll
        for (int nt = 0; nt < 6; ++nt) rt[nt] = trfragA(L_S1 + (nt >> 1) * 2048, nt & 1, lane);
        { const unsigned a = L_S1 + 6144 + (4 * g + q4) * 32 + pp * 8; rt[6] = cat(trd(a), trd(a + 512)); }
      } else {
        const srd_t ro = __builtin_amdgcn_make_buffer_rsrc((void*)p.wo, 0, DM * DM * 2, 0x00020000);
#pragma unroll
        for (int e = 0; e < 4; ++e) glds(ro, rB * 1024 + colB, (32 * ((b & 127) >> 3) + 8 * e) * 1024 + (b & 7) * 128, L_SO + e * 1024);
        const srd_t r2 = __builtin_amdgcn_make_buffer_rsrc((void*)p.w2, 0, FF * DM * 2, 0x00020000);
        const int b2 = b < 224 ? b : 0;
#pragma unroll
        for (int e = 0; e < 8; ++e) { const int id = 2 * b2 + (e >> 2); glds(r2, rB * 1024 + colB, (32 * (id >> 3) + 8 * (e & 3)) * 1024 + (id & 7) * 128, L_SO + 4096 + e * 1024); }
        VM0();
        {
          const int row = 16 * (g >> 1) + 4 * (g & 1) + q4;
#pragma unroll
          for (int nt = 0; nt < 4; ++nt) { const unsigned a = L_SO + row * 128 + ((nt ^ ((row >> 1) & 3)) * 32) + pp * 8; rt[8 + nt] = cat(trd(a), trd(a + 1024)); }
        }
        const int row = 4 * g + q4;
#pragma unroll
        for (int e = 0; e < 8; ++e) { const int nt = e & 3; const unsigned a = L_SO + 4096 + (e >> 2) * 4096 + row * 128 + ((nt ^ ((row >> 1) & 3)) * 32) + pp * 8; rt[e] = cat(trd(a), trd(a + 2048)); }
      }
    }
    STAMP(1);
    if (w >= 6) {
      while (*(volatile LAS unsigned*)(uintptr_t)(L_CNT + 8) < 4u) __builtin_amdgcn_s_sleep(1);
      CFENCE();
      if (w == 6) {
#pragma unroll
        for (int e = 0; e < 7; ++e) st16(rWS, asv4u(rt[e]), WS_TW1 + ((cj * 7 + e) * 16 + ci) * 1024 + lane * 16);
      } else {
        if (b < 224) {
#pragma unroll
          for (int e = 0; e < 8; ++e) { const int id = 2 * b + (e >> 2); st16(rWS, asv4u(rt[e]), WS_TW2 + (((id & 7) * 56 + (id >> 3)) * 4 + (e & 3)) * 1024 + lane * 16); }
        }
        if (b < 128) {
#pragma unroll
          for (int e = 0; e < 4; ++e) st16(rWS, asv4u(rt[8 + e]), WS_TWO + (((b & 7) * 16 + (b >> 3)) * 4 + e) * 1024 + lane * 16);
        }
      }
    }
    wait_words(p.ws, WS_FQ + cj * 128, 16, lane, TAG);
    while (*(volatile LAS unsigned*)(uintptr_t)(L_CNT + 4) == 0u) __builtin_amdgcn_s_sleep(1);
    CFENCE();
    STAMP(19);
#pragma unroll
    for (int e = 0; e < 8; ++e) glds4x1(rWS, lane * 16, WS_TQ + cj * 98304 + ((w - 5) * 8 + ((e + 2 * (ci & 3)) & 7)) * 4096, L_WQKV + ((w - 5) * 8 + ((e + 2 * (ci & 3)) & 7)) * 4096);
  }
  {
    const int qr = lane & 31, h = lane >> 5;
#pragma unroll
    for (int r = 0; r < 16; ++r) { const int kr = (r & 3) + 8 * (r >> 2) + 4 * h; mU[r] = kr > qr ? -1e30f : 0.f; mL[r] = kr > qr ? 0.f : -1e30f; }
    asm volatile("" : "+v"(mU), "+v"(mL));
  }
  if (w == 0) {
    __builtin_amdgcn_s_setprio(3);
    wait_words(p.ws, WS_F0 + ci * 512, 64, lane, TAG);
    if (lane == 0) *(volatile LAS unsigned*)(uintptr_t)(L_CNT + 12) = 1u;
    __builtin_amdgcn_s_setprio(0);
  } else {
    while (*(volatile LAS unsigned*)(uintptr_t)(L_CNT + 12) == 0u) __builtin_amdgcn_s_sleep(1);
    CFENCE();
  }
  STAMP(2);
  v8s act[16];
  const unsigned hsrc = WS_HF + (ci * 8 + w) * 16 * 1024;
  v2u wl[2], wh[2];
#pragma unroll
  for (int u = 0; u < 2; ++u) { const unsigned o = (32 * (2 * w + u) + 4 * g) * 2; wl[u] = ld8(rWS, o, WS_WV + ci * 1024); wh[u] = ld8(rWS, o + 32, WS_WV + ci * 1024); }
#pragma unroll
  for (int s = 0; s < 5; ++s) act[s] = asv8(ld16(rWS, lane * 16, hsrc + s * 1024));
  if (w >= 5) asm volatile("s_waitcnt vmcnt(9)" ::: "memory");
  BAR();
  STAMP(3);
  if (lane == 0) {
    if (w == 6) arrive(p.ws, WS_FW1 + (cj * 16 + ci) * 8, TAG);
    if (w == 7 && b < 128) arrive(p.ws, WS_FWO + ((b & 7) * 16 + (b >> 3)) * 8, TAG);
    if (w == 7 && b < 224) { arrive(p.ws, WS_FW2 + (((2 * b) & 7) * 64 + ((2 * b) >> 3)) * 8, TAG); arrive(p.ws, WS_FW2 + (((2 * b + 1) & 7) * 64 + ((2 * b + 1) >> 3)) * 8, TAG); }
  }

  f4 accQ[2], accKV[4];
  float rstd, nmu;
  v4u gaR, cbR, sbR, gbR;
  v2u b2R[2];
  v4u mc0, md0;
  v4u mx[2];
  const int tt = w & 3, kh = w >> 2;
  const int ocol = 64 * ct + 32 * kh + 4 * g;
  {
    const unsigned bq = opq(L_WQKV + lane * 16), bv = opq(L_WQKV + 65536 + lane * 16);
#define QF(nt, s) lds16(((nt) < 4 ? bq : bv) + ((nt) & 3) * 16384 + (s) * 1024)
    {
      f4 au[6];
      v8s W[2][6];
      const unsigned c0 = opq(bq + w * 2048), d0 = opq(bv + w * 2048);
#pragma unroll
      for (int u = 0; u < 2; ++u)
#pragma unroll
        for (int nt = 0; nt < 6; ++nt) W[u][nt] = lds16((nt < 4 ? c0 : d0) + (nt & 3) * 16384 + u * 1024);
#pragma unroll
      for (int nt = 0; nt < 6; ++nt) au[nt] = f4{0, 0, 0, 0};
#pragma unroll
      for (int u = 0; u < 2; ++u) {
        const v8s a = asv8(v4u{wl[u][0], wl[u][1], wh[u][0], wh[u][1]});
#pragma unroll
        for (int nt = 0; nt < 6; ++nt) au[nt] = MFMA16(W[u][nt], a, au[nt]);
      }
      if (tok == 0) {
#pragma unroll
        for (int nt = 0; nt < 6; ++nt) *(LAS f4*)(sm + L_PARTU + (w * 96 + nt * 16 + 4 * g) * 4) = au[nt];
      }
    }
    BAR();
    if (tid >= 384 && tid < 480) {
      const int e0 = tid - 384;
      float s = 0;
#pragma unroll
      for (int e = 0; e < 8; ++e) s += *(const LAS float*)(sm + L_PARTU + (e * 96 + e0) * 4);
      *(LAS float*)(sm + L_PERS + e0 * 4) = s;
    }
    STAMP(16);
    v2u st[4];
    u64 fm0 = 0, fm1 = 0, fm2 = 0;
#pragma unroll
    for (int nt = 0; nt < 4; ++nt) accKV[nt] = f4{0, 0, 0, 0};
#pragma unroll
    for (int nt = 0; nt < 2; ++nt) accQ[nt] = f4{0, 0, 0, 0};
    {
      v8s W[6];
#pragma unroll
      for (int nt = 0; nt < 6; ++nt) W[nt] = QF(nt, 0);
      SB();
#pragma unroll
      for (int s = 0; s < 16; ++s) {
        if (s + 5 < 16) act[s + 5] = asv8(ld16(rWS, lane * 16, hsrc + (s + 5) * 1024));
        else if (s < 15) st[s - 11] = ld8(rWS, ((4 * g + s - 11) * 128 + 16 * w + tok) * 8, WS_ST1 + ci * 16 * 128 * 8);
        if (s == 12) { GAS u64* f = (GAS u64*)(p.ws + WS_FM); fm0 = __hip_atomic_load(f + lane, RLX_AGENT); fm1 = __hip_atomic_load(f + 64 + lane, RLX_AGENT); fm2 = __hip_atomic_load(f + 128 + (lane & 31), RLX_AGENT); }
#pragma unroll
        for (int nt = 0; nt < 6; ++nt) {
          if (nt < 2) accQ[nt] = MFMA16(W[nt], act[s], accQ[nt]); else accKV[nt - 2] = MFMA16(W[nt], act[s], accKV[nt - 2]);
          if (s + 1 < 16) W[nt] = QF(nt, s + 1);
          SB();
        }
      }
    }
    float s1 = 0, s2 = 0;
#pragma unroll
    for (int e = 0; e < 4; ++e) { s1 += __uint_as_float(st[e][0]); s2 += __uint_as_float(st[e][1]); }
    s1 = sumg(s1); s2 = sumg(s2);
    const float mu = s1 * (1.f / 512), var = fmaxf(s2 * (1.f / 512) - mu * mu, 0.f);
    rstd = __builtin_amdgcn_rsqf(var + 1e-5f); nmu = -rstd * mu;
    if (w == 6 && lane < 8) *(LAS v4u*)(sm + L_B2C + lane * 16) = b2v;
    PIN(rstd); PIN(nmu);
    {
          GAS u64* f = (GAS u64*)(p.ws + WS_FM);
          for (unsigned spins = 0; spins < (1u << 17) && !__all(fm0 == TAG && fm1 == TAG && fm2 == TAG); ++spins) {
            fm0 = __hip_atomic_load(f + lane, RLX_AGENT); fm1 = __hip_atomic_load(f + 64 + lane, RLX_AGENT); fm2 = __hip_atomic_load(f + 128 + (lane & 31), RLX_AGENT); }
          CFENCE();
          if (w < 6) {
            const int vec = w < 2 ? 0 : w < 4 ? 2 : 3, hq = w & 1;
            const unsigned dst = w == 0 ? L_PSA : w == 1 ? L_PSA2 : w == 2 ? L_PSB : w == 3 ? L_PSB2 : w == 4 ? L_PCB : L_PCB2;
            glds2x1(rWS, lane * 16, WS_MOD + (2 * hq) * 10240 + vec * 2048, dst);
            glds2x1(rWS, lane * 16, WS_MOD + (2 * hq + 1) * 10240 + vec * 2048, dst + 2048);
          } else glds1(rWS, (lane >> 4) * 10240 + (lane & 15) * 16, WS_MOD + (w == 6 ? 1 : 4) * 2048 + ct * 256, w == 6 ? L_PGA : L_PGB);
    }
    STAMP(4);
    BAR();
    {
      v4u kk, vv;
      unsigned pkd[4][2];
#pragma unroll
      for (int nt = 0; nt < 4; ++nt) {
        const f4 u = ldsf4(L_PERS + (32 + nt * 16 + 4 * g) * 4);
        pkd[nt][0] = pk(rstd * accKV[nt][0] + nmu * u[0], rstd * accKV[nt][1] + nmu * u[1]);
        pkd[nt][1] = pk(rstd * accKV[nt][2] + nmu * u[2], rstd * accKV[nt][3] + nmu * u[3]);
      }
      { auto r0 = __builtin_amdgcn_permlane16_swap(pkd[0][0], pkd[1][0], false, false); auto r1 = __builtin_amdgcn_permlane16_swap(pkd[0][1], pkd[1][1], false, false);
        kk = v4u{r0[0], r1[0], r0[1], r1[1]}; }
      { auto r0 = __builtin_amdgcn_permlane16_swap(pkd[2][0], pkd[3][0], false, false); auto r1 = __builtin_amdgcn_permlane16_swap(pkd[2][1], pkd[3][1], false, false);
        vv = v4u{r0[0], r1[0], r0[1], r1[1]}; }
      const int hd = g & 1, pl = g >> 1;
      st16(rWS, kk, WS_KV + (ci * 16 + cj) * 16384 + ((0 * 2 + hd) * 2 + pl) * 2048 + (16 * w + tok) * 16);
      st16(rWS, vv, WS_KV + (ci * 16 + cj) * 16384 + ((1 * 2 + hd) * 2 + pl) * 2048 + (16 * w + tok) * 16);
      *(LAS v4u*)(sm + L_AK + (hd * 2 + pl) * KPL + (256 + 16 * w + tok) * 16) = kk;
      *(LAS v4u*)(sm + L_AV + hd * VHD + pl * VPL + (256 + 16 * w + tok) * 16) = vv;
    }
    VM0();
    BAR();
    if (tid == 256) arrive(p.ws, WS_FP + (0 * 16 + ci) * 128 + cj * 8, TAG);
    STAMP(5);
    if (tid < 400) *(LAS v4u*)(sm + L_ONE + tid * 16) = v4u{0x3f803f80u, 0x3f803f80u, 0x3f803f80u, 0x3f803f80u};
    if (tid < 416) {
      const int seg = tid < 384 ? tid >> 7 : 3 + ((tid - 384) >> 4), e = tid < 384 ? tid & 127 : (tid - 384) & 15;
      f4 v;
      if (seg < 3) {
        const unsigned a0 = (seg == 0 ? L_PSA : seg == 1 ? L_PSB : L_PCB) + e * 16, a1 = (seg == 0 ? L_PSA2 : seg == 1 ? L_PSB2 : L_PCB2) + e * 16;
        v = (ldsf4(a0) + ldsf4(a0 + 2048)) + (ldsf4(a1) + ldsf4(a1 + 2048));
      } else {
        const unsigned a0 = (seg == 3 ? L_PGA : L_PGB) + e * 16;
        v = (ldsf4(a0) + ldsf4(a0 + 256)) + (ldsf4(a0 + 512) + ldsf4(a0 + 768));
      }
      *(LAS v2u*)(sm + L_FIN + (seg < 3 ? seg * 1024 : 3072 + (seg - 3) * 128) + e * 8) = v2u{pk(v[0], v[1]), pk(v[2], v[3])};
    }
    BAR();
    {
      f4 av[4];
      v8s W[2][4], a[2];
      const unsigned c0 = opq(bq + w * 2048), d0 = opq(bv + w * 2048);
#pragma unroll
      for (int u = 0; u < 2; ++u) {
        a[u] = lds16(L_FIN + (2 * w + u) * 64 + g * 16);
#pragma unroll
        for (int nt = 0; nt < 4; ++nt) W[u][nt] = lds16((nt < 2 ? c0 : d0) + (nt & 1) * 16384 + u * 1024);
      }
#pragma unroll
      for (int nt = 0; nt < 4; ++nt) av[nt] = f4{0, 0, 0, 0};
#pragma unroll
      for (int u = 0; u < 2; ++u)
#pragma unroll
        for (int nt = 0; nt < 4; ++nt) av[nt] = MFMA16(W[u][nt], a[u], av[nt]);
      if (tok == 0) {
#pragma unroll
        for (int nt = 0; nt < 4; ++nt) *(LAS f4*)(sm + L_PARTV + (w * 64 + nt * 16 + 4 * g) * 4) = av[nt];
      }
    }
    {
      gaR = asv4u(lds16(L_FIN + 3072 + kh * 64 + g * 16)); gbR = asv4u(lds16(L_FIN + 3200 + kh * 64 + g * 16));
      sbR = asv4u(lds16(L_FIN + 1024 + (2 * ct + kh) * 64 + g * 16)); cbR = asv4u(lds16(L_FIN + 2048 + (2 * ct + kh) * 64 + g * 16));
#pragma unroll
      for (int e = 0; e < 2; ++e) b2R[e] = lds8(L_B2C + (32 * kh + 4 * g + 16 * e) * 2);
      mc0 = asv4u(lds16(L_FIN + 2048 + 16 * lane)); md0 = asv4u(lds16(L_FIN + 1024 + 16 * lane));
#pragma unroll
      for (int u = 0; u < 2; ++u) mx[u] = asv4u(lds16(L_FIN + (tok == 1 ? 1024 : 2048) + (2 * w + u) * 64 + g * 16));
      BAR();
      if (tid < 64) {
        float s = 0;
#pragma unroll
        for (int e = 0; e < 8; ++e) s += *(const LAS float*)(sm + L_PARTV + (e * 64 + tid) * 4);
        *(LAS float*)(sm + L_PERS + (96 + tid) * 4) = s;
      }
    }
    if (ci < 2) {
      for (int e = tid; e < 8 * 256; e += 512) {
        const int pln = e >> 8, key = e & 255;
        if (ci == 0 || key < 128) {
          const unsigned a = pln < 4 ? L_AK + pln * KPL : L_AV + ((pln - 4) >> 1) * VHD + (pln & 1) * VPL;
          *(LAS v4u*)(sm + a + key * 16) = v4u{0, 0, 0, 0};
        }
      }
    }
    if (w == 4) {
      GAS u64* f0 = (GAS u64*)(p.ws + (lane == 0 && ci >= 1 ? WS_FP + (ci - 1) * 128 + cj * 8 : lane == 1 && ci >= 2 ? WS_FP + (ci - 2) * 128 + cj * 8
                                       : lane >= 16 && lane < 32 ? WS_FW1 + (cj * 16 + lane - 16) * 8 : WS_FWO + (ct * 16 + (lane & 15)) * 8));
      GAS u64* f1 = (GAS u64*)(p.ws + WS_FW2 + ct * 512) + (lane < 56 ? lane : 0);
      for (unsigned spins = 0; spins < (1u << 17); ++spins) {
        const u64 a = __hip_atomic_load(f0, RLX_AGENT), c = __hip_atomic_load(f1, RLX_AGENT);
        if (__all(a == TAG && c == TAG)) break;
      }
      CFENCE();
    }
    BAR();
    STAMP(6);
    {
      const unsigned dst = w < 4 ? L_AK + w * KPL : L_AV + ((w - 4) >> 1) * VHD + (w & 1) * VPL;
#pragma unroll
      for (int src = 0; src < 2; ++src)
        if (ci - 2 + src >= 0) glds2x1(rWS, lane * 16, WS_KV + ((ci - 2 + src) * 16 + cj) * 16384 + w * 2048, dst + src * 2048);
    }
    {
      const float qs = 0.25f * 1.4426950408889634f;
      unsigned pq[2][2];
#pragma unroll
      for (int nt = 0; nt < 2; ++nt) {
        const f4 u = ldsf4(L_PERS + (nt * 16 + 4 * g) * 4), v = ldsf4(L_PERS + (96 + nt * 16 + 4 * g) * 4);
        pq[nt][0] = pk(qs * (rstd * accQ[nt][0] + nmu * u[0] + v[0]), qs * (rstd * accQ[nt][1] + nmu * u[1] + v[1]));
        pq[nt][1] = pk(qs * (rstd * accQ[nt][2] + nmu * u[2] + v[2]), qs * (rstd * accQ[nt][3] + nmu * u[3] + v[3]));
      }
      auto r0 = __builtin_amdgcn_permlane16_swap(pq[0][0], pq[1][0], false, false); auto r1 = __builtin_amdgcn_permlane16_swap(pq[0][1], pq[1][1], false, false);
      *(LAS v4u*)(sm + L_AQ + ((g & 1) * 2 + (g >> 1)) * 2048 + (16 * w + tok) * 16) = v4u{r0[0], r1[0], r0[1], r1[1]};
    }
    VM0();
    BAR();
    STAMP(7);
  }

  const int orow = 128 * ci + 64 * hf + 16 * tt + tok;
  v2u xr0, xr1;
  {
    const int t = w & 3, hd = w >> 2;
    const int kt0 = ci >= 2 ? 0 : 8 - 4 * ci - t;
    f16v O;
    const u16* xres = p.x + (size_t)orow * DM + ocol;
    if (ci >= 2) attention<true>(t, hd, 0, lane, mU, mL, O, rWS, WS_TWO + ct * 65536, WS_TW1 + cj * 7 * 16384, w, xres, xr0, xr1, stampv);
    else attention<false>(t, hd, kt0, lane, mU, mL, O, rWS, WS_TWO + ct * 65536, WS_TW1 + cj * 7 * 16384, w, xres, xr0, xr1, stampv);
    const int h = lane >> 5;
    const float inv = __builtin_amdgcn_rcpf(O[8]);
    const f4 b0 = ldsf4(L_PERS + (96 + 32 + hd * 16 + 4 * h) * 4), b1 = ldsf4(L_PERS + (96 + 32 + hd * 16 + 8 + 4 * h) * 4);
    const v4u o = {pk(O[0] * inv + b0[0], O[1] * inv + b0[1]), pk(O[2] * inv + b0[2], O[3] * inv + b0[3]),
                   pk(O[4] * inv + b1[0], O[5] * inv + b1[1]), pk(O[6] * inv + b1[2], O[7] * inv + b1[3])};
    const int qr = lane & 31;
    st16(rWS, o, WS_OF + (ci * 8 * 16 + cj) * 1024 + ((2 * t + (qr >> 4)) * 16) * 1024 + ((2 * hd + h) * 16 + (qr & 15)) * 16);
  }
  STAMP(24);
  VM0();
  BAR();
  if (tid == 256) arrive(p.ws, WS_FP + (1 * 16 + ci) * 128 + cj * 8, TAG);
  STAMP(8);
  float b1v = 0.f;
  v8s aux2[2];
  float Cw = 0, Cw2 = 0, Cws = 0, Cs = 0, Cs2 = 0;
#define MODSUMS() do { \
      _Pragma("unroll") for (int r = 0; r < 4; ++r) { \
        const float w0 = rbf(1.f + bflo(mc0[r])), w1 = rbf(1.f + bfhi(mc0[r])), s0 = bflo(md0[r]), s1_ = bfhi(md0[r]); \
        Cw += w0 + w1; Cw2 += w0 * w0 + w1 * w1; Cws += w0 * s0 + w1 * s1_; Cs += s0 + s1_; Cs2 += s0 * s0 + s1_ * s1_; } \
      Cw = sumg(rowsum16(Cw)); Cw2 = sumg(rowsum16(Cw2)); Cws = sumg(rowsum16(Cws)); Cs = sumg(rowsum16(Cs)); Cs2 = sumg(rowsum16(Cs2)); \
      _Pragma("unroll") for (int u = 0; u < 2; ++u) { v4u t; \
        _Pragma("unroll") for (int d = 0; d < 4; ++d) t[d] = tok == 0 ? pk(1.f + bflo(mx[u][d]), 1.f + bfhi(mx[u][d])) : (tok == 1 ? mx[u][d] : 0x3f803f80u); \
        aux2[u] = asv8(t); } \
      asm volatile("" : "+v"(Cw), "+v"(Cw2), "+v"(Cws), "+v"(Cs), "+v"(Cs2), "+v"(aux2[0]), "+v"(aux2[1])); } while (0)
  if (w != 4) {
#pragma unroll
    for (int e = 0; e < 3; ++e) { const int gi = wq + 7 * e; if (gi < 16) glds4x1(rWS, lane * 16, WS_TW1 + (cj * 7 + 2) * 16384 + gi * 4096, L_W1 + 32768 + gi * 4096); }
  }
  MODSUMS();
  if (w == 4) wait_words(p.ws, WS_FP + (1 * 16 + ci) * 128, 16, lane, TAG);
  BAR();
  STAMP(9);

  float x1[8];
  {
    v8s oa[8];
    const unsigned osrc = WS_OF + ((ci * 8 + 4 * hf + tt) * 16 + 8 * kh) * 1024;
#pragma unroll
    for (int s = 0; s < 3; ++s) oa[s] = asv8(ld16(rWS, lane * 16, osrc + s * 1024));
    const unsigned bo = opq(L_WO + kh * 32768 + lane * 16);
    f4 acc[4];
#pragma unroll
    for (int nt = 0; nt < 4; ++nt) acc[nt] = f4{0, 0, 0, 0};
    {
      v8s W[2][4];
#pragma unroll
      for (int s = 0; s < 2; ++s)
#pragma unroll
        for (int nt = 0; nt < 4; ++nt) W[s][nt] = lds16(bo + (s * 4 + nt) * 1024);
      SB();
#pragma unroll
      for (int s = 3; s < 8; ++s) oa[s] = asv8(ld16(rWS, lane * 16, osrc + s * 1024));
      if (tid < 112) { const int nt = tid >> 4; b1v = bflo(p.b1[(nt == 6 ? 112 * cj + (par ? 0 : 96) : 112 * cj + 16 * par + nt * 16) + (tid & 15)]); }
      SB();
#pragma unroll
      for (int s = 0; s < 8; ++s)
#pragma unroll
        for (int nt = 0; nt < 4; ++nt) {
          acc[nt] = MFMA16(W[s & 1][nt], oa[s], acc[nt]);
          if (s + 2 < 8) W[s & 1][nt] = lds16(bo + ((s + 2) * 4 + nt) * 1024);
          SB();
        }
    }
    STAMP(26);
    VM0();
    BAR();
    *(LAS f4*)(sm + L_XCH + w * 2048 + lane * 16) = kh ? acc[0] : acc[2];
    *(LAS f4*)(sm + L_XCH + w * 2048 + 1024 + lane * 16) = kh ? acc[1] : acc[3];
    BAR();
    const f4 o0 = ldsf4(L_XCH + (w ^ 4) * 2048 + lane * 16), o1 = ldsf4(L_XCH + (w ^ 4) * 2048 + 1024 + lane * 16);
    const f4 t0 = (kh ? acc[2] : acc[0]) + o0, t1 = (kh ? acc[3] : acc[1]) + o1;
    const float xin[8] = {bflo(xr0[0]), bfhi(xr0[0]), bflo(xr0[1]), bfhi(xr0[1]), bflo(xr1[0]), bfhi(xr1[0]), bflo(xr1[1]), bfhi(xr1[1])};
    const float ga[8] = {bflo(gaR[0]), bfhi(gaR[0]), bflo(gaR[1]), bfhi(gaR[1]), bflo(gaR[2]), bfhi(gaR[2]), bflo(gaR[3]), bfhi(gaR[3])};
    const float cb[8] = {bflo(cbR[0]), bfhi(cbR[0]), bflo(cbR[1]), bfhi(cbR[1]), bflo(cbR[2]), bfhi(cbR[2]), bflo(cbR[3]), bfhi(cbR[3])};
    const float sv[8] = {bflo(sbR[0]), bfhi(sbR[0]), bflo(sbR[1]), bfhi(sbR[1]), bflo(sbR[2]), bfhi(sbR[2]), bflo(sbR[3]), bfhi(sbR[3])};
    float wv[8], xw[8];
#pragma unroll
    for (int r = 0; r < 8; ++r) { x1[r] = rbf(xin[r] + ga[r] * (r < 4 ? t0[r & 3] : t1[r & 3])); wv[r] = rbf(1.f + cb[r]); xw[r] = x1[r] * wv[r]; }
    st16(rWS, v4u{pk(xw[0], xw[1]), pk(xw[2], xw[3]), pk(xw[4], xw[5]), pk(xw[6], xw[7])}, WS_X1F + ((ci * 8 + 4 * hf + tt) * 16 + 2 * ct + kh) * 1024 + lane * 16);
    SB();
    float s1 = 0, s2 = 0, s3 = 0, s4 = 0, s5 = 0, s6 = 0;
#pragma unroll
    for (int r = 0; r < 8; ++r) { s1 += x1[r]; s2 += x1[r] * x1[r]; s3 += xw[r]; s4 += xw[r] * wv[r]; s5 += xw[r] * xw[r]; s6 += xw[r] * sv[r]; }
    s1 = sumg(s1); s2 = sumg(s2); s3 = sumg(s3); s4 = sumg(s4); s5 = sumg(s5); s6 = sumg(s6);
    const unsigned rec = (ci * 16 + 2 * ct + kh) * 128 + 64 * hf + 16 * tt + tok;
    if (g == 0) st16(rWS, v4u{__float_as_uint(s1), __float_as_uint(s2), __float_as_uint(s3), __float_as_uint(s4)}, WS_ST2A + rec * 16);
    if (g == 1) st8(rWS, v2u{__float_as_uint(s5), __float_as_uint(s6)}, WS_ST2B + rec * 8);
  }
  if (w < 4) {
    glds4x1(rWS, lane * 16, WS_TW1 + (cj * 7 + 6) * 16384 + w * 4096, L_W1S + w * 4096);
    glds4x1(rWS, lane * 16, WS_TW2 + ct * 229376 + 7 * 16384 + w * 4096, 49152 + w * 4096);
    asm volatile("s_waitcnt vmcnt(8)" ::: "memory");
  } else {
    glds4x1(rWS, lane * 16, WS_TW2 + ct * 229376 + (w - 4) * 4096, 32768 + (w - 4) * 4096);
    asm volatile("s_waitcnt vmcnt(4)" ::: "memory");
  }
  BAR();
  if (tid == 256) arrive(p.ws, WS_FP + (2 * 16 + ci) * 128 + cj * 8, TAG);
  STAMP(10);
  if (w == 4) asm volatile("s_waitcnt vmcnt(1)" ::: "memory"); else VM0();
  BAR();

  v4u stg[8];
  {
    const unsigned bP = opq(L_W1 + lane * 16), bR = opq(L_W1 + 65536 + lane * 16), bS = opq(L_W1S + lane * 16);
#define W1F(nt, s) lds16((nt) == 6 ? bS + (s) * 1024 : (nt) < 4 ? bP + (nt) * 16384 + (s) * 1024 : bR + ((nt) - 4) * 16384 + (s) * 1024)
    {
      f4 au[7];
#pragma unroll
      for (int nt = 0; nt < 7; ++nt) au[nt] = f4{0, 0, 0, 0};
      const unsigned c0 = opq(bP + w * 2048), d0 = opq(bR + w * 2048), e0 = opq(bS + w * 2048);
#pragma unroll
      for (int u = 0; u < 2; ++u) {
        v8s W[7];
#pragma unroll
        for (int nt = 0; nt < 7; ++nt) W[nt] = lds16(nt == 6 ? e0 + u * 1024 : nt < 4 ? c0 + nt * 16384 + u * 1024 : d0 + (nt - 4) * 16384 + u * 1024);
#pragma unroll
        for (int nt = 0; nt < 7; ++nt) au[nt] = MFMA16(W[nt], aux2[u], au[nt]);
      }
      if (tok < 3) {
#pragma unroll
        for (int nt = 0; nt < 7; ++nt) *(LAS f4*)(sm + L_XCH + ((w * 3 + tok) * 112 + nt * 16 + 4 * g) * 4) = au[nt];
      }
    }
    BAR();
    if (tid < 112) *(LAS float*)(sm + L_XF + (336 + tid) * 4) = b1v;
    {
      const int e0 = w < 4 ? tid : tid - 64;
      if (w != 4 && e0 < 336) {
        float s = 0;
#pragma unroll
        for (int e = 0; e < 8; ++e) s += *(const LAS float*)(sm + L_XCH + (e * 336 + e0) * 4);
        *(LAS float*)(sm + L_XF + e0 * 4) = s;
      }
    }
    if (w == 4) wait_words(p.ws, WS_FP + (2 * 16 + ci) * 128, 16, lane, TAG);
    BAR();
    STAMP(11);
    v8s act2[16];
    const unsigned asrc = WS_X1F + (ci * 8 + w) * 16 * 1024;
#pragma unroll
    for (int s = 0; s < 4; ++s) act2[s] = asv8(ld16(rWS, lane * 16, asrc + s * 1024));
    v4u sa[4]; v2u sb[4];
    f4 acc[7];
#pragma unroll
    for (int nt = 0; nt < 7; ++nt) acc[nt] = f4{0, 0, 0, 0};
    {
      v8s W[6];
#pragma unroll
      for (int nt = 0; nt < 6; ++nt) W[nt] = W1F(nt, 0);
      SB();
#pragma unroll
      for (int s = 0; s < 16; ++s) {
        if (s == 0) act2[4] = asv8(ld16(rWS, lane * 16, asrc + 4 * 1024));
        if (s + 5 < 16) act2[s + 5] = asv8(ld16(rWS, lane * 16, asrc + (s + 5) * 1024));
        else if (s < 15) { sa[s - 11] = ld16(rWS, ((4 * g + s - 11) * 128 + 16 * w + tok) * 16, WS_ST2A + ci * 16 * 128 * 16);
                           sb[s - 11] = ld8(rWS, ((4 * g + s - 11) * 128 + 16 * w + tok) * 8, WS_ST2B + ci * 16 * 128 * 8); }
#pragma unroll
        for (int nt = 0; nt < 6; ++nt) {
          acc[nt] = MFMA16(W[nt], act2[s], acc[nt]);
          if (s + 1 < 16) W[nt] = W1F(nt, s + 1);
          SB();
        }
      }
    }
    STAMP(18);
    CSTAMP(42);
    BAR();
    v8s WB[4];
#pragma unroll
    for (int s = 0; s < 4; ++s) WB[s] = W1F(6, s);
    float s1 = 0, s2 = 0, s3 = 0, s4 = 0, s5 = 0, s6 = 0;
#pragma unroll
    for (int e = 0; e < 4; ++e) { s1 += __uint_as_float(sa[e][0]); s2 += __uint_as_float(sa[e][1]); s3 += __uint_as_float(sa[e][2]); s4 += __uint_as_float(sa[e][3]); s5 += __uint_as_float(sb[e][0]); s6 += __uint_as_float(sb[e][1]); }
    s1 = sumg(s1); s2 = sumg(s2); s3 = sumg(s3); s4 = sumg(s4); s5 = sumg(s5); s6 = sumg(s6);
    const float mu1 = s1 * (1.f / 512), var1 = fmaxf(s2 * (1.f / 512) - mu1 * mu1, 0.f), r1 = __builtin_amdgcn_rsqf(var1 + 1e-5f);
    const float mu2 = (r1 * (s3 - mu1 * Cw) + Cs) * (1.f / 512);
    const float hh = (r1 * r1 * (s5 - 2.f * mu1 * s4 + mu1 * mu1 * Cw2) + 2.f * r1 * (s6 - mu1 * Cws) + Cs2) * (1.f / 512);
    const float r2 = __builtin_amdgcn_rsqf(fmaxf(hh - mu2 * mu2, 0.f) + 1e-5f);
    const float cA = r2 * r1, cB = -cA * mu1, cC = r2, cD = -r2 * mu2;
    const unsigned S0 = (7 * cj + par) >> 1;
    const unsigned hb = WS_H1F + (ci * 8 + w) * 56 * 1024;
    const unsigned xf = opq(L_XF + 16 * g);
#define BIAS4(nt) __builtin_elementwise_fma(f4{cB, cB, cB, cB}, ldsf4(xf + (nt) * 64), __builtin_elementwise_fma(f4{cC, cC, cC, cC}, ldsf4(xf + 448 + (nt) * 64), \
                    __builtin_elementwise_fma(f4{cD, cD, cD, cD}, ldsf4(xf + 896 + (nt) * 64), ldsf4(xf + 1344 + (nt) * 64))))
#define GELU4(o, a, bias) do { const f4 z_ = __builtin_elementwise_fma(f4{cA, cA, cA, cA}, a, bias); const f4 q_ = __builtin_elementwise_fma(z_ * z_, f4{-0.1029432397f, -0.1029432397f, -0.1029432397f, -0.1029432397f}, \
        f4{-2.3022081985f, -2.3022081985f, -2.3022081985f, -2.3022081985f}) * z_; f4 e_; \
        _Pragma("unroll") for (int r = 0; r < 4; ++r) e_[r] = __builtin_amdgcn_exp2f(q_[r]); \
        e_ += f4{1.f, 1.f, 1.f, 1.f}; \
        _Pragma("unroll") for (int r = 0; r < 4; ++r) e_[r] = __builtin_amdgcn_rcpf(e_[r]); \
        o = z_ * e_; } while (0)
    {
      unsigned hp[2][2];
      f4 bias = BIAS4(0);
      PIN(bias);
      SB();
#pragma unroll
      for (int nt = 0; nt < 6; ++nt) {
#pragma unroll
        for (int k = (nt * 16) / 6; k < ((nt + 1) * 16) / 6; ++k) { acc[6] = MFMA16(WB[k & 3], act2[k], acc[6]); if (k + 4 < 16) WB[k & 3] = W1F(6, k + 4); }
        if ((nt & 1) == 0) { const int gi = w * 3 + (nt >> 1), it = 1 + (gi >> 3), khh = (gi >> 2) & 1, pc4 = gi & 3;
          glds4x1(rWS, lane * 16, WS_TW2 + ct * 229376 + (khh * 7 + it) * 16384 + pc4 * 4096, 65536u + (it - 1) * 32768u + khh * 16384u + pc4 * 4096u); }
        if (nt >= 1 && nt < 5) {
#pragma unroll
          for (int e = 2 * (nt - 1); e < 2 * nt; ++e) { const int pc = w * 8 + e; const int chunk = (pc >> 4) == 0 ? 5 : ((pc >> 4) == 1 ? 12 : ((pc >> 4) == 2 ? 6 : 13));
            stg[e] = ld16(rWS, lane * 16, WS_TW2 + ct * 229376 + chunk * 16384 + (pc & 15) * 1024); }
        }
        f4 nb = bias;
        if (nt < 5) nb = BIAS4(nt + 1); else nb = BIAS4(6);
        f4 o;
        GELU4(o, acc[nt], bias);
        hp[nt & 1][0] = pk(o[0], o[1]); hp[nt & 1][1] = pk(o[2], o[3]);
        PIN(hp[nt & 1][0]); PIN(hp[nt & 1][1]); PIN(nb);
        bias = nb;
        if (nt & 1) st16(rWS, v4u{hp[0][0], hp[0][1], hp[1][0], hp[1][1]}, hb + (S0 + (nt >> 1)) * 1024 + lane * 16);
        SB();
      }
      STAMP(20);
      CSTAMP(43);
      f4 o;
      GELU4(o, acc[6], bias);
      const unsigned NT = 7 * cj + (par ? 0 : 6);
      st8(rWS, v2u{pk(o[0], o[1]), pk(o[2], o[3])}, hb + (NT >> 1) * 1024 + (NT & 1) * 512 + lane * 8);
    }
  }
  STAMP(25);
  VM0();
  BAR();
  if (tid == 256) arrive(p.ws, WS_FP + (3 * 16 + ci) * 128 + cj * 8, TAG);
  STAMP(13);
  if (w != 4) {
#pragma unroll
    for (int e = 0; e < 2; ++e) {
      const int gi = wq + 7 * e;
      if (gi < 8) glds4x1(rWS, lane * 16, WS_TW2 + ct * 229376 + ((gi >> 2) * 7 + 4) * 16384 + (gi & 3) * 4096, (gi >> 2) * 16384u + (gi & 3) * 4096u);
    }
  } else {
    if (b == 0) {
      GAS u64* f = (GAS u64*)(p.ws + WS_F0) + lane;
      for (unsigned spins = 0; spins < (1u << 17); ++spins) {
        bool ok = true;
#pragma unroll
        for (int i = 0; i < 16; ++i) ok &= __hip_atomic_load(f + i * 64, RLX_AGENT) == TAG;
        if (__all(ok)) break;
      }
      if (lane == 0) __hip_atomic_store((GAS u64*)(p.ws + WS_EPOCH), TAG, RLX_AGENT);
    }
    wait_words(p.ws, WS_FP + (3 * 16 + ci) * 128, 16, lane, TAG);
  }
  BAR();
  STAMP(14);

  {
    v8s ha[28];
    const unsigned hb = WS_H1F + ((ci * 8 + 4 * hf + tt) * 56 + 28 * kh) * 1024;
#define HLOAD(s) do { if ((s) % 7 == 3) { const v2u lo_ = ld8(rWS, lane * 8, hb + (s) * 1024), hi_ = ld8(rWS, lane * 8 + 512, hb + (s) * 1024); ha[s] = asv8(v4u{lo_[0], lo_[1], hi_[0], hi_[1]}); } \
                      else ha[s] = asv8(ld16(rWS, lane * 16, hb + (s) * 1024)); } while (0)
#pragma unroll
    for (int s = 0; s < 3; ++s) HLOAD(s);
    const unsigned bw = opq(kh * 16384 + lane * 16), bw2 = opq(bw + 65536), bw3 = opq(bw + 131072);
    f4 acc[4];
#pragma unroll
    for (int nt = 0; nt < 4; ++nt) acc[nt] = f4{0, 0, 0, 0};
#define W2F(nt, S) lds16((((S) >> 2) == 0 || ((S) >> 2) == 5 ? bw + 32768 : ((S) >> 2) == 1 || ((S) >> 2) == 6 ? bw2 : ((S) >> 2) == 2 ? bw2 + 32768 : ((S) >> 2) == 3 ? bw3 : bw) + (((S) & 3) * 4 + (nt)) * 1024)
    {
      v8s W[2][4];
#pragma unroll
      for (int S = 0; S < 2; ++S)
#pragma unroll
        for (int nt = 0; nt < 4; ++nt) W[S][nt] = W2F(nt, S);
      SB();
#pragma unroll
      for (int s = 3; s < 8; ++s) HLOAD(s);
      SB();
#pragma unroll
      for (int S = 0; S < 28; ++S) {
        if (S + 8 < 28) HLOAD(S + 8);
        if (S == 8) BAR();
        if (S >= 8 && S < 16) { const int e = S - 8, pc = w * 8 + e; *(LAS v4u*)(sm + 32768 + (pc >> 5) * 32768 + ((pc >> 4) & 1) * 16384 + (pc & 15) * 1024 + lane * 16) = stg[e]; }
        if (S == 18) BAR();
#pragma unroll
        for (int nt = 0; nt < 4; ++nt) {
          acc[nt] = MFMA16(W[S & 1][nt], ha[S], acc[nt]);
          if (S + 2 < 28) W[S & 1][nt] = W2F(nt, S + 2);
          SB();
        }
      }
    }
    STAMP(22);
    BAR();
    *(LAS f4*)(sm + L_W2X + w * 2048 + lane * 16) = kh ? acc[0] : acc[2];
    *(LAS f4*)(sm + L_W2X + w * 2048 + 1024 + lane * 16) = kh ? acc[1] : acc[3];
    BAR();
    const f4 o0 = ldsf4(L_W2X + (w ^ 4) * 2048 + lane * 16), o1 = ldsf4(L_W2X + (w ^ 4) * 2048 + 1024 + lane * 16);
    const f4 t0 = (kh ? acc[2] : acc[0]) + o0, t1 = (kh ? acc[3] : acc[1]) + o1;
    const float gb[8] = {bflo(gbR[0]), bfhi(gbR[0]), bflo(gbR[1]), bfhi(gbR[1]), bflo(gbR[2]), bfhi(gbR[2]), bflo(gbR[3]), bfhi(gbR[3])};
    const float bb[8] = {bflo(b2R[0][0]), bfhi(b2R[0][0]), bflo(b2R[0][1]), bfhi(b2R[0][1]), bflo(b2R[1][0]), bfhi(b2R[1][0]), bflo(b2R[1][1]), bfhi(b2R[1][1])};
    float o[8];
#pragma unroll
    for (int r = 0; r < 8; ++r) o[r] = x1[r] + gb[r] * ((r < 4 ? t0[r & 3] : t1[r & 3]) + bb[r]);
    auto r0 = __builtin_amdgcn_permlane16_swap(pk(o[0], o[1]), pk(o[4], o[5]), false, false);
    auto r1 = __builtin_amdgcn_permlane16_swap(pk(o[2], o[3]), pk(o[6], o[7]), false, false);
    { const srd_t rO = __builtin_amdgcn_make_buffer_rsrc((void*)p.out, 0, SEQ * DM * 2, 0x00020000);
      __builtin_amdgcn_raw_buffer_store_b128(v4u{r0[0], r1[0], r0[1], r1[1]}, rO, (orow * DM + 64 * ct + 32 * kh + (g & 1) * 16 + (g >> 1) * 8) * 2, 0, 16); }
  }
  while (__builtin_amdgcn_s_memrealtime() - pad_t0 < 4200ull) __builtin_amdgcn_s_sleep(1);
#define R4_(x) x x x x
#define R16_(x) R4_(R4_(x))
#define R256_(x) R16_(R16_(x))
  if (p.ws == nullptr) { float dz = lane; asm volatile(R16_(R256_("v_fma_f32 %0, %0, %0, %0\n\tv_fma_f32 %0, %0, %0, %0\n\t")) : "+v"(dz)); ((float*)p.out)[tid] = dz; }
  STAMP(15);
  CSTAMP(41);
  STAMP_FLUSH(0);
}

extern "C" void kernel_launch(void* const* d_in, const int* in_sizes, int n_in, void* d_out, int out_size, void* d_ws, size_t ws_size, hipStream_t stream) {
  Params p;
  p.x = (const u16*)d_in[0]; p.wqkv = (const u16*)d_in[1]; p.wo = (const u16*)d_in[2];
  p.w1 = (const u16*)d_in[4]; p.b1 = (const u16*)d_in[5]; p.w2 = (const u16*)d_in[6]; p.b2 = (const u16*)d_in[7];
  p.c = (const u16*)d_in[8]; p.wmod = (const u16*)d_in[9]; p.out = (u16*)d_out; p.ws = (char*)d_ws;
  fused_block<<<256, 512, 0, stream>>>(p);
}
```

```cpp
#include <hip/hip_runtime.h>
#include <hip/hip_bf16.h>
#include <stdint.h>

typedef unsigned short u16;
typedef short v4s __attribute__((ext_vector_type(4)));
typedef short v8s __attribute__((ext_vector_type(8)));
typedef float f2 __attribute__((ext_vector_type(2)));
typedef float f4 __attribute__((ext_vector_type(4)));
typedef float f16v __attribute__((ext_vector_type(16)));
typedef unsigned v2u __attribute__((ext_vector_type(2)));
typedef unsigned v4u __attribute__((ext_vector_type(4)));
typedef __bf16 b2 __attribute__((ext_vector_type(2)));
#define LAS __attribute__((address_space(3)))
#define GAS __attribute__((address_space(1)))
#define DI __device__ __forceinline__
typedef __amdgpu_buffer_rsrc_t srd_t;

constexpr int SEQ = 2048, DM = 512, FF = 1792;
constexpr unsigned WS_EPOCH = 0;
constexpr unsigned WS_F0 = 4096;
constexpr unsigned WS_FP = WS_F0 + 8192;
constexpr unsigned WS_FM = WS_FP + 4 * 16 * 128;
constexpr unsigned WS_FQ = WS_FM + 2048;
constexpr unsigned WS_FW1 = WS_FQ + 2048;
constexpr unsigned WS_FWO = WS_FW1 + 2048;
constexpr unsigned WS_FW2 = WS_FWO + 1024;
static_assert(WS_FW2 + 4096 <= (64 << 10), "flags");
constexpr unsigned WS_WV = 64 << 10;
constexpr unsigned WS_MOD = 128 << 10;
constexpr unsigned WS_ST1 = 192 << 10;
constexpr unsigned WS_ST2A = 512 << 10;
constexpr unsigned WS_ST2B = 1024 << 10;
constexpr unsigned WS_HF = 2u << 20;
constexpr unsigned WS_KV = 4u << 20;
constexpr unsigned WS_OF = 8u << 20;
constexpr unsigned WS_X1F = 10u << 20;
constexpr unsigned WS_H1F = 12u << 20;
constexpr unsigned WS_TQ = 19u << 20;
constexpr unsigned WS_TW1 = WS_TQ + (1536u << 10);
constexpr unsigned WS_TWO = WS_TW1 + (1792u << 10);
constexpr unsigned WS_TW2 = WS_TWO + (512u << 10);
constexpr unsigned WS_STAMP = 25u << 20;
constexpr unsigned WS_BYTES = 27u << 20;
static_assert(WS_TW2 + (1792u << 10) <= WS_STAMP, "ws");
constexpr unsigned L_CA = 98304;
constexpr unsigned L_XS = 131072;
constexpr unsigned L_S1 = L_XS + 8192;
constexpr unsigned L_SO = L_S1 + 7168;
constexpr unsigned L_SC = L_SO + 12288;
constexpr unsigned L_CAP = L_SC + 2048;
constexpr unsigned L_CNT = L_CAP + 512;
static_assert(L_CNT + 16 <= 163840, "lds");
constexpr unsigned L_SQ = 0;
constexpr unsigned L_M2 = 8192;
constexpr unsigned L_WQKV = 0;
constexpr unsigned L_PARTU = 98304;
constexpr unsigned L_PARTV = 32768;
constexpr unsigned L_AK = 98304;
constexpr unsigned KPL = 6144;
constexpr unsigned L_AV = L_AK + 4 * KPL;
constexpr unsigned VPL = 6208, VHD = 12416;
constexpr unsigned L_PSA = L_AK + KPL, L_PSB = L_AK + 3 * KPL, L_PCB = L_AV + VPL, L_PGA = L_AV + VHD + VPL, L_PGB = L_PGA + 1024, L_B2C = L_PGA + 2048;
constexpr unsigned L_PSA2 = L_AK + 2 * KPL, L_PSB2 = L_AV, L_PCB2 = L_AV + VHD;
constexpr unsigned L_FIN = L_AK;
constexpr unsigned L_ONE = L_AV + 2 * VHD;
constexpr unsigned L_AQ = L_ONE + 6400;
constexpr unsigned L_PERS = L_AQ + 8192;
static_assert(L_PERS + 1024 <= 163840, "lds");
static_assert((L_AV % 256) == 0 && (L_ONE % 256) == 0, "bank plan");
constexpr unsigned L_WO = 0;
constexpr unsigned L_W1 = 65536;
constexpr unsigned L_W1S = 0;
constexpr unsigned L_XCH = 16384;
constexpr unsigned L_XF = 16384 + 12288;
constexpr unsigned L_W2X = 98304;

struct Params { const u16* x; const u16* wqkv; const u16* wo; const u16* w1; const u16* b1; const u16* w2; const u16* b2; const u16* c; const u16* wmod; u16* out; char* ws; };

DI float bflo(unsigned v) { return __uint_as_float(v << 16); }
DI float bfhi(unsigned v) { return __uint_as_float(v & 0xffff0000u); }
DI unsigned pk(float a, float b) { f2 v = {a, b}; b2 r = __builtin_convertvector(v, b2); return __builtin_bit_cast(unsigned, r); }
DI float rbf(float a) { return bflo(pk(a, 0.f)); }
DI v8s cat(v4s a, v4s b) { return __builtin_shufflevector(a, b, 0, 1, 2, 3, 4, 5, 6, 7); }
DI v4s trd(unsigned a) { return __builtin_amdgcn_ds_read_tr16_b64_v4i16((LAS v4s*)(uintptr_t)a); }
DI v8s asv8(v4u v) { return __builtin_bit_cast(v8s, v); }
DI v4u asv4u(v8s v) { return __builtin_bit_cast(v4u, v); }
DI v8s lds16(unsigned a) { return *(const LAS v8s*)(uintptr_t)a; }
DI f4 ldsf4(unsigned a) { return *(const LAS f4*)(uintptr_t)a; }
DI v2u lds8(unsigned a) { return *(const LAS v2u*)(uintptr_t)a; }
#define MFMA16(a, b, c) __builtin_amdgcn_mfma_f32_16x16x32_bf16((a), (b), (c), 0, 0, 0)
#define MFMA32(a, b, c) __builtin_amdgcn_mfma_f32_32x32x16_bf16((a), (b), (c), 0, 0, 0)
#define BAR() asm volatile("s_waitcnt lgkmcnt(0)\n\ts_barrier" ::: "memory")
#define VM0() asm volatile("s_waitcnt vmcnt(0)" ::: "memory")
#define LG0() asm volatile("s_waitcnt lgkmcnt(0)" ::: "memory")
#define CFENCE() asm volatile("" ::: "memory")
#define SB() __builtin_amdgcn_sched_barrier(0)
DI unsigned opq(unsigned v) { asm volatile("" : "+v"(v)); return v; }
DI void glds(srd_t r, unsigned voff, unsigned soff, unsigned ldsaddr) {
  asm volatile("s_mov_b32 m0, %0\n\ts_nop 0\n\tbuffer_load_dwordx4 %1, %2, %3 offen lds" ::"s"(ldsaddr), "v"(voff), "s"(r), "s"(soff) : "memory");
}
DI void glds1(srd_t r, unsigned voff, unsigned soff, unsigned ldsaddr) {
  asm volatile("s_mov_b32 m0, %0\n\ts_nop 0\n\tbuffer_load_dwordx4 %1, %2, %3 offen sc1 lds" ::"s"(ldsaddr), "v"(voff), "s"(r), "s"(soff) : "memory");
}
DI void glds4(srd_t r, unsigned voff, unsigned soff, unsigned ldsaddr) {
  asm volatile("s_mov_b32 m0, %0\n\ts_nop 0\n\tbuffer_load_dwordx4 %1, %2, %3 offen lds\n\tbuffer_load_dwordx4 %1, %2, %3 offen offset:1024 lds\n\t"
               "buffer_load_dwordx4 %1, %2, %3 offen offset:2048 lds\n\tbuffer_load_dwordx4 %1, %2, %3 offen offset:3072 lds" ::"s"(ldsaddr), "v"(voff), "s"(r), "s"(soff) : "memory");
}
DI void glds4x1(srd_t r, unsigned voff, unsigned soff, unsigned ldsaddr) {
  asm volatile("s_mov_b32 m0, %0\n\ts_nop 0\n\tbuffer_load_dwordx4 %1, %2, %3 offen sc1 lds\n\tbuffer_load_dwordx4 %1, %2, %3 offen offset:1024 sc1 lds\n\t"
               "buffer_load_dwordx4 %1, %2, %3 offen offset:2048 sc1 lds\n\tbuffer_load_dwordx4 %1, %2, %3 offen offset:3072 sc1 lds" ::"s"(ldsaddr), "v"(voff), "s"(r), "s"(soff) : "memory");
}
DI void glds2x1(srd_t r, unsigned voff, unsigned soff, unsigned ldsaddr) {
  asm volatile("s_mov_b32 m0, %0\n\ts_nop 0\n\tbuffer_load_dwordx4 %1, %2, %3 offen sc1 lds\n\tbuffer_load_dwordx4 %1, %2, %3 offen offset:1024 sc1 lds" ::"s"(ldsaddr), "v"(voff), "s"(r), "s"(soff) : "memory");
}
DI v4u ld16(srd_t r, unsigned voff, unsigned soff) { return __builtin_amdgcn_raw_buffer_load_b128(r, voff, soff, 16); }
DI v2u ld8(srd_t r, unsigned voff, unsigned soff) { return __builtin_amdgcn_raw_buffer_load_b64(r, voff, soff, 16); }
DI v4u ldp16(srd_t r, unsigned voff, unsigned soff) { return __builtin_amdgcn_raw_buffer_load_b128(r, voff, soff, 0); }
DI void stp16(srd_t r, v4u v, unsigned off) { __builtin_amdgcn_raw_buffer_store_b128(v, r, off, 0, 0); }
DI void st16(srd_t r, v4u v, unsigned off) { __builtin_amdgcn_raw_buffer_store_b128(v, r, off, 0, 16); }
DI void st8(srd_t r, v2u v, unsigned off) { __builtin_amdgcn_raw_buffer_store_b64(v, r, off, 0, 16); }
DI float sum16(float v) { auto r = __builtin_amdgcn_permlane16_swap(__float_as_uint(v), __float_as_uint(v), false, false); return __uint_as_float(r[0]) + __uint_as_float(r[1]); }
DI float sum32(float v) { auto r = __builtin_amdgcn_permlane32_swap(__float_as_uint(v), __float_as_uint(v), false, false); return __uint_as_float(r[0]) + __uint_as_float(r[1]); }
DI float max32(float v) { auto r = __builtin_amdgcn_permlane32_swap(__float_as_uint(v), __float_as_uint(v), false, false); return fmaxf(__uint_as_float(r[0]), __uint_as_float(r[1])); }
DI float sumg(float v) { return sum32(sum16(v)); }
#define RLX_AGENT __ATOMIC_RELAXED, __HIP_MEMORY_SCOPE_AGENT
typedef unsigned long long u64;
DI void arrive(char* ws, unsigned off, u64 tag) { (void)__hip_atomic_exchange((GAS u64*)(ws + off), tag, RLX_AGENT); }
DI void wait_words(char* ws, unsigned off, int n, int lane, u64 tag) {
  GAS u64* p = (GAS u64*)(ws + off) + (lane < n ? lane : 0);
  for (unsigned spins = 0; spins < (1u << 17); ++spins)
    if (__all(__hip_atomic_load(p, RLX_AGENT) == tag)) break;
  CFENCE();
}
template <int C> DI float dpp(float v) { return __uint_as_float(__builtin_amdgcn_update_dpp(0u, __float_as_uint(v), C, 0xf, 0xf, false)); }
DI float rowsum16(float v) { v += dpp<0x128>(v); v += dpp<0x124>(v); v += dpp<0x122>(v); v += dpp<0x121>(v); return v; }
#define PIN(x) asm volatile("" : "+v"(x))
#define STAMP(k)
#define STAMP_FLUSH(o)
#define CSTAMP(k)
DI float silu(float v) { return v * __builtin_amdgcn_rcpf(1.f + __builtin_amdgcn_exp2f(-1.4426950408889634f * v)); }

DI v8s trfragA(unsigned img, int half, int lane) {
  const int g = lane >> 4, q = (lane & 15) >> 2, pp = lane & 3;
  const unsigned a = img + (4 * g + q) * 64 + ((half ^ (g & 1)) * 32) + pp * 8;
  return cat(trd(a), trd(a + 1024));
}

template <bool FULL>
DI void attention(int t, int hd, int kt0, int lane, f16v mU, f16v mL, f16v& O, srd_t rWS, unsigned wo_src, unsigned w1_src, int w, const u16* xres, v2u& xr0, v2u& xr1, unsigned& stampv) {
  const int qr = lane & 31, h = lane >> 5, blk = (lane >> 4) & 1, q = (lane & 15) >> 2, pp = lane & 3;
  const v8s Qf = lds16(L_AQ + (hd * 2 + h) * 2048 + (32 * t + qr) * 16);
  const unsigned kb = opq(L_AK + (hd * 2 + h) * KPL + (32 * t + qr) * 16);
  const unsigned vb = opq((blk == 0 ? L_AV + hd * VHD + (pp >> 1) * VPL + (pp & 1) * 8 + (4 * h + q) * 16 : L_ONE + ((hd * 128 + (h ? 0 : 128)) & 255)) + t * 512);
  const f16v zero = {};
#define RIDE(n) do { if ((n) < 2) glds4x1(rWS, lane * 16, wo_src + (w * 2 + (n)) * 4096, L_WO + (w * 2 + (n)) * 4096); \
                     else glds4x1(rWS, lane * 16, w1_src + w * 4096, L_W1 + w * 4096); } while (0)
  if (FULL) {
    v8s Kf[9];
#pragma unroll
    for (int kt = 0; kt < 9; ++kt) Kf[kt] = lds16(kb + kt * 512);
    v8s V0 = cat(trd(vb), trd(vb + 128)), V1 = cat(trd(vb + 256), trd(vb + 384));
    f16v S[2];
    float m0 = -3e38f, m1 = -3e38f;
    S[0] = MFMA32(Kf[0], Qf, mL);
    SB();
#pragma unroll
    for (int kt = 0; kt < 9; ++kt) {
      if (kt < 8) S[(kt + 1) & 1] = MFMA32(Kf[kt + 1], Qf, kt + 1 == 8 ? mU : zero);
      SB();
#pragma unroll
      for (int r = 0; r < 16; r += 4) { m0 = fmaxf(fmaxf(m0, S[kt & 1][r]), S[kt & 1][r + 1]); m1 = fmaxf(fmaxf(m1, S[kt & 1][r + 2]), S[kt & 1][r + 3]); }
      SB();
    }
    const float m = max32(fmaxf(m0, m1));
    STAMP(23);
    f16v nm;
#pragma unroll
    for (int r = 0; r < 16; ++r) { nm[r] = -m; mU[r] -= m; mL[r] -= m; }
    O = zero;
    S[0] = MFMA32(Kf[0], Qf, mL);
    SB();
#pragma unroll
    for (int kt = 0; kt < 9; ++kt) {
      if (kt < 8) S[(kt + 1) & 1] = MFMA32(Kf[kt + 1], Qf, kt + 1 == 8 ? mU : nm);
      if (kt == 0) RIDE(0);
      if (kt == 3) RIDE(1);
      if (kt == 6) RIDE(2);
      SB();
      v4u P0, P1;
#pragma unroll
      for (int r = 0; r < 4; ++r) P0[r] = pk(__builtin_amdgcn_exp2f(S[kt & 1][2 * r]), __builtin_amdgcn_exp2f(S[kt & 1][2 * r + 1]));
      O = MFMA32(V0, asv8(P0), O);
      if (kt < 8) V0 = cat(trd(vb + (kt + 1) * 512), trd(vb + (kt + 1) * 512 + 128));
      if (kt == 2) xr0 = *(const v2u*)xres;
      if (kt == 5) xr1 = *(const v2u*)(xres + 16);
      SB();
#pragma unroll
      for (int r = 0; r < 4; ++r) P1[r] = pk(__builtin_amdgcn_exp2f(S[kt & 1][8 + 2 * r]), __builtin_amdgcn_exp2f(S[kt & 1][8 + 2 * r + 1]));
      O = MFMA32(V1, asv8(P1), O);
      if (kt < 8) V1 = cat(trd(vb + (kt + 1) * 512 + 256), trd(vb + (kt + 1) * 512 + 384));
      SB();
    }
  } else {
#pragma unroll
    for (int n = 0; n < 3; ++n) RIDE(n);
    xr0 = *(const v2u*)xres; xr1 = *(const v2u*)(xres + 16);
    float m = -3e38f;
#pragma unroll
    for (int kt = 0; kt < 9; ++kt) {
      if (kt >= kt0) {
        const f16v S = MFMA32(lds16(kb + kt * 512), Qf, kt == 8 ? mU : zero);
#pragma unroll
        for (int r = 0; r < 16; r += 2) m = fmaxf(fmaxf(m, S[r]), S[r + 1]);
      }
    }
    m = max32(m);
    f16v nm;
#pragma unroll
    for (int r = 0; r < 16; ++r) { nm[r] = -m; mU[r] -= m; }
    O = zero;
#pragma unroll
    for (int kt = 0; kt < 9; ++kt) {
      if (kt >= kt0) {
        const f16v S = MFMA32(lds16(kb + kt * 512), Qf, kt == 8 ? mU : nm);
        v4u P0, P1;
#pragma unroll
        for (int r = 0; r < 4; ++r) {
          P0[r] = pk(__builtin_amdgcn_exp2f(S[2 * r]), __builtin_amdgcn_exp2f(S[2 * r + 1]));
          P1[r] = pk(__builtin_amdgcn_exp2f(S[8 + 2 * r]), __builtin_amdgcn_exp2f(S[8 + 2 * r + 1]));
        }
        const v8s V0 = cat(trd(vb + kt * 512), trd(vb + kt * 512 + 128));
        const v8s V1 = cat(trd(vb + kt * 512 + 256), trd(vb + kt * 512 + 384));
        O = MFMA32(V0, asv8(P0), O);
        O = MFMA32(V1, asv8(P1), O);
      }
    }
  }
#undef RIDE
}

__global__ void __launch_bounds__(512, 2) fused_block(Params p) {
  __shared__ __attribute__((aligned(16))) char smem[163840];
  LAS char* sm = (LAS char*)smem;
  const int tid = threadIdx.x, lane = tid & 63;
  const int w = __builtin_amdgcn_readfirstlane(tid >> 6);
  const int b = blockIdx.x, ci = ((b & 7) >> 1) * 4 + ((b >> 3) & 3), cj = (b & 1) * 8 + (b >> 5);
  const int tok = lane & 15, g = lane >> 4, q4 = (lane & 15) >> 2, pp = lane & 3;
  const int hf = cj & 1, ct = cj >> 1, par = cj & 1;
  const int wq = w < 4 ? w : w - 1;
  unsigned stampv = 0;
  const srd_t rWS = __builtin_amdgcn_make_buffer_rsrc(p.ws, 0, WS_BYTES, 0x00020000);
  const int rA = lane >> 2, pA = lane & 3;
  const unsigned colA = (((pA >> 1) ^ ((rA >> 2) & 1)) * 32) + (pA & 1) * 16;
  const int rB = lane >> 3, pB = lane & 7;
  const unsigned colB = (((pB >> 1) ^ ((rB >> 1) & 3)) * 32) + (pB & 1) * 16;
  STAMP(0);
  CSTAMP(40);
  const unsigned long long pad_t0 = __builtin_amdgcn_s_memrealtime();

  const bool m2 = b < 160;
  const int msl = b % 40, mkq = b / 40;
  const srd_t rWmod = __builtin_amdgcn_make_buffer_rsrc((void*)p.wmod, 0, DM * 3072 * 2, 0x00020000);
  v4u b2v = {0, 0, 0, 0};
  if (w == 6 && lane < 8) b2v = *(const v4u*)(p.b2 + 64 * ct + lane * 8);
  v4u cr;
  v2u ep;
  if (w < 4) {
    __builtin_amdgcn_s_setprio(2);
    asm volatile("global_load_dwordx4 %0, %1, off" : "=v"(cr) : "v"(p.c + 128 * w + 8 * (lane & 15)) : "memory");
    asm volatile("buffer_load_dwordx2 %0, %1, %2, 0 offen sc1" : "=v"(ep) : "v"(WS_EPOCH), "s"(rWS) : "memory");
    const srd_t rX = __builtin_amdgcn_make_buffer_rsrc((void*)p.x, 0, SEQ * DM * 2, 0x00020000);
#pragma unroll
    for (int u = 0; u < 2; ++u) glds(rX, rA * 1024 + pA * 16, (128 * ci + 16 * (2 * w + u)) * 1024 + 64 * cj, L_XS + (2 * w + u) * 1024);
#pragma unroll
    for (int i0 = 0; i0 < 8; ++i0) { const int ii = (i0 + (ci & 3) * 2 + (cj & 1)) & 7; glds(rWmod, rA * 6144 + colA, (w * 8 + ii) * 16 * 6144 + (512 + 32 * cj) * 2, L_CA + (w * 8 + ii) * 1024); }
  } else if (w == 5) {
    __builtin_amdgcn_s_setprio(3);
    const srd_t r = __builtin_amdgcn_make_buffer_rsrc((void*)p.wqkv, 0, DM * 1536 * 2, 0x00020000);
#pragma unroll
    for (int e = 0; e < 6; ++e) glds(r, rA * 3072 + colA, (32 * ci + 16 * (e & 1)) * 3072 + ((e >> 1) * 512 + 32 * cj) * 2, L_SQ + e * 1024);
  }
  if (tid == 256) { *(LAS unsigned*)(uintptr_t)L_CNT = 0u; *(LAS unsigned*)(uintptr_t)(L_CNT + 4) = m2 ? 0u : 1u; *(LAS unsigned*)(uintptr_t)(L_CNT + 8) = 0u; *(LAS unsigned*)(uintptr_t)(L_CNT + 12) = 0u; }
  BAR();
#define READ_TAG() do { const v2u ep_ = ld8(rWS, WS_EPOCH, 0); TAG = (((u64)__builtin_amdgcn_readfirstlane(ep_[1]) << 32) | __builtin_amdgcn_readfirstlane(ep_[0])) + 1; } while (0)
  v8s rt[12];
  f16v mU, mL;
  u64 TAG;
  if (w < 4) {
    asm volatile("s_waitcnt vmcnt(10)" : "+v"(cr), "+v"(ep) :: "memory");
    const unsigned sc = L_SC + w * 256;
    if (lane < 16) *(LAS v4u*)(uintptr_t)(sc + lane * 16) = v4u{pk(silu(bflo(cr[0])), silu(bfhi(cr[0]))), pk(silu(bflo(cr[1])), silu(bfhi(cr[1]))), pk(silu(bflo(cr[2])), silu(bfhi(cr[2]))), pk(silu(bflo(cr[3])), silu(bfhi(cr[3])))};
    LG0();
    v8s sf[4];
#pragma unroll
    for (int s = 0; s < 4; ++s) { const v2u lo = lds8(sc + (32 * s + 4 * g) * 2), hi = lds8(sc + (32 * s + 16 + 4 * g) * 2); sf[s] = asv8(v4u{lo[0], lo[1], hi[0], hi[1]}); }
    TAG = (((u64)__builtin_amdgcn_readfirstlane(ep[1]) << 32) | __builtin_amdgcn_readfirstlane(ep[0])) + 1;
    asm volatile("s_waitcnt vmcnt(8)" ::: "memory");
    v2u xa[2][2];
#pragma unroll
    for (int u = 0; u < 2; ++u) {
      const unsigned a = L_XS + (2 * w + u) * 1024 + tok * 64 + g * 8; xa[u][0] = lds8(a); xa[u][1] = lds8(a + 32);
      const float x0 = bflo(xa[u][0][0]), x1 = bfhi(xa[u][0][0]), x2 = bflo(xa[u][0][1]), x3 = bfhi(xa[u][0][1]);
      const float x4 = bflo(xa[u][1][0]), x5 = bfhi(xa[u][1][0]), x6 = bflo(xa[u][1][1]), x7 = bfhi(xa[u][1][1]);
      const float s1 = sumg(((x0 + x1) + (x2 + x3)) + ((x4 + x5) + (x6 + x7)));
      const float s2 = sumg(((x0 * x0 + x1 * x1) + (x2 * x2 + x3 * x3)) + ((x4 * x4 + x5 * x5) + (x6 * x6 + x7 * x7)));
      if (g == 0) st8(rWS, v2u{__float_as_uint(s1), __float_as_uint(s2)}, WS_ST1 + ((ci * 16 + cj) * 128 + 16 * (2 * w + u) + tok) * 8);
    }
    asm volatile("s_waitcnt vmcnt(2)" ::: "memory");
    STAMP(1);
    f4 a0 = {0, 0, 0, 0}, a1 = {0, 0, 0, 0};
    {
      v8s W[4][2];
#pragma unroll
      for (int s = 0; s < 4; ++s) { W[s][0] = trfragA(L_CA + (4 * w + s) * 2048, 0, lane); W[s][1] = trfragA(L_CA + (4 * w + s) * 2048, 1, lane); }
#pragma unroll
      for (int s = 0; s < 4; ++s) { a0 = MFMA16(W[s][0], sf[s], a0); a1 = MFMA16(W[s][1], sf[s], a1); }
    }
    if (tok == 0) { *(LAS f4*)(uintptr_t)(L_CAP + (w * 32 + 4 * g) * 4) = a0; *(LAS f4*)(uintptr_t)(L_CAP + (w * 32 + 16 + 4 * g) * 4) = a1; }
    LG0();
    if (lane == 0) __hip_atomic_fetch_add((LAS unsigned*)(uintptr_t)L_CNT, 1u, __ATOMIC_RELAXED, __HIP_MEMORY_SCOPE_WORKGROUP);
    while (*(volatile LAS unsigned*)(uintptr_t)L_CNT < 4u) {}
    CFENCE();
    STAMP(19);
    a0 = ldsf4(L_CAP + 4 * g * 4); a1 = ldsf4(L_CAP + (16 + 4 * g) * 4);
#pragma unroll
    for (int e = 1; e < 4; ++e) { a0 += ldsf4(L_CAP + (e * 32 + 4 * g) * 4); a1 += ldsf4(L_CAP + (e * 32 + 16 + 4 * g) * 4); }
    float wv[8];
#pragma unroll
    for (int r = 0; r < 4; ++r) { wv[r] = rbf(1.f + rbf(a0[r])); wv[4 + r] = rbf(1.f + rbf(a1[r])); }
#pragma unroll
    for (int u = 0; u < 2; ++u) {
      const float x0 = bflo(xa[u][0][0]), x1 = bfhi(xa[u][0][0]), x2 = bflo(xa[u][0][1]), x3 = bfhi(xa[u][0][1]);
      const float x4 = bflo(xa[u][1][0]), x5 = bfhi(xa[u][1][0]), x6 = bflo(xa[u][1][1]), x7 = bfhi(xa[u][1][1]);
      const int T = 2 * w + u;
      st16(rWS, v4u{pk(x0 * wv[0], x1 * wv[1]), pk(x2 * wv[2], x3 * wv[3]), pk(x4 * wv[4], x5 * wv[5]), pk(x6 * wv[6], x7 * wv[7])},
           WS_HF + ((ci * 8 + T) * 16 + cj) * 1024 + lane * 16);
      if (u == 0 && w == 0 && tok == 0) {
        st8(rWS, v2u{pk(wv[0], wv[1]), pk(wv[2], wv[3])}, WS_WV + ci * 1024 + cj * 64 + g * 8);
        st8(rWS, v2u{pk(wv[4], wv[5]), pk(wv[6], wv[7])}, WS_WV + ci * 1024 + cj * 64 + 32 + g * 8);
      }
    }
    STAMP(21);
    VM0();
    if (lane == 0) { arrive(p.ws, WS_F0 + (ci * 64 + cj * 4 + w) * 8, TAG); __hip_atomic_fetch_add((LAS unsigned*)(uintptr_t)(L_CNT + 8), 1u, __ATOMIC_RELAXED, __HIP_MEMORY_SCOPE_WORKGROUP); }
    __builtin_amdgcn_s_setprio(0);
  } else if (w == 4) {
    if (m2) {
      const int mvec = msl >> 3, mcol = (mvec == 0 ? 0 : 512 + 512 * mvec) + (msl & 7) * 64;
      __builtin_amdgcn_s_sleep(8);
#pragma unroll
      for (int ii = 0; ii < 16; ++ii) glds(rWmod, rB * 6144 + colB, (128 * mkq + 8 * ii) * 6144 + mcol * 2, L_M2 + ii * 1024);
      const v4u cr = *(const v4u*)(p.c + 128 * mkq + 8 * (lane & 15));
      const unsigned sc = L_SC + 1024;
      if (lane < 16) *(LAS v4u*)(uintptr_t)(sc + lane * 16) = v4u{pk(silu(bflo(cr[0])), silu(bfhi(cr[0]))), pk(silu(bflo(cr[1])), silu(bfhi(cr[1]))), pk(silu(bflo(cr[2])), silu(bfhi(cr[2]))), pk(silu(bflo(cr[3])), silu(bfhi(cr[3])))};
      LG0();
      v8s sf[4];
#pragma unroll
      for (int s = 0; s < 4; ++s) { const v2u lo = lds8(sc + (32 * s + 4 * g) * 2), hi = lds8(sc + (32 * s + 16 + 4 * g) * 2); sf[s] = asv8(v4u{lo[0], lo[1], hi[0], hi[1]}); }
      VM0();
      f4 a[4];
#pragma unroll
      for (int nt = 0; nt < 4; ++nt) a[nt] = f4{0, 0, 0, 0};
      const int row = 4 * g + q4;
#pragma unroll
      for (int s = 0; s < 4; ++s) {
        v8s W[4];
#pragma unroll
        for (int nt = 0; nt < 4; ++nt) { const unsigned ad = L_M2 + s * 4096 + row * 128 + ((nt ^ ((row >> 1) & 3)) * 32) + pp * 8; W[nt] = cat(trd(ad), trd(ad + 2048)); }
#pragma unroll
        for (int nt = 0; nt < 4; ++nt) a[nt] = MFMA16(W[nt], sf[s], a[nt]);
      }
      if (tok == 0) {
#pragma unroll
        for (int nt = 0; nt < 4; ++nt)
          st16(rWS, v4u{__float_as_uint(a[nt][0]), __float_as_uint(a[nt][1]), __float_as_uint(a[nt][2]), __float_as_uint(a[nt][3])},
               WS_MOD + mkq * 10240 + mvec * 2048 + ((msl & 7) * 2 + (nt >> 1)) * 128 + g * 32 + (nt & 1) * 16);
      }
      LG0();
      if (lane == 0) *(volatile LAS unsigned*)(uintptr_t)(L_CNT + 4) = 1u;
      READ_TAG();
      VM0();
      if (lane == 0) arrive(p.ws, WS_FM + b * 8, TAG);
    } else READ_TAG();
    STAMP(1);
  } else {
    if (w == 5) {
      READ_TAG();
      VM0();
#pragma unroll
      for (int nt = 0; nt < 6; ++nt) st16(rWS, asv4u(trfragA(L_SQ + (nt >> 1) * 2048, nt & 1, lane)), WS_TQ + ((cj * 6 + nt) * 16 + ci) * 1024 + lane * 16);
      VM0();
      if (lane == 0) arrive(p.ws, WS_FQ + (cj * 16 + ci) * 8, TAG);
      __builtin_amdgcn_s_setprio(0);
    } else {
      READ_TAG();
      __builtin_amdgcn_s_sleep(16);
      if (w == 6) {
        const srd_t r1 = __builtin_amdgcn_make_buffer_rsrc((void*)p.w1, 0, DM * FF * 2, 0x00020000);
#pragma unroll
        for (int e = 0; e < 6; ++e) glds(r1, rA * (FF * 2) + colA, (32 * ci + 16 * (e & 1)) * (FF * 2) + (112 * cj + 16 * par + (e >> 1) * 32) * 2, L_S1 + e * 1024);
        glds(r1, (lane >> 1) * (FF * 2) + (lane & 1) * 16, 32 * ci * (FF * 2) + (112 * cj + (par ? 0 : 96)) * 2, L_S1 + 6144);
        VM0();
#pragma unroll
        for (int nt = 0; nt < 6; ++nt) rt[nt] = trfragA(L_S1 + (nt >> 1) * 2048, nt & 1, lane);
        { const unsigned a = L_S1 + 6144 + (4 * g + q4) * 32 + pp * 8; rt[6] = cat(trd(a), trd(a + 512)); }
      } else {
        const srd_t ro = __builtin_amdgcn_make_buffer_rsrc((void*)p.wo, 0, DM * DM * 2, 0x00020000);
#pragma unroll
        for (int e = 0; e < 4; ++e) glds(ro, rB * 1024 + colB, (32 * ((b & 127) >> 3) + 8 * e) * 1024 + (b & 7) * 128, L_SO + e * 1024);
        const srd_t r2 = __builtin_amdgcn_make_buffer_rsrc((void*)p.w2, 0, FF * DM * 2, 0x00020000);
        const int b2 = b < 224 ? b : 0;
#pragma unroll
        for (int e = 0; e < 8; ++e) { const int id = 2 * b2 + (e >> 2); glds(r2, rB * 1024 + colB, (32 * (id >> 3) + 8 * (e & 3)) * 1024 + (id & 7) * 128, L_SO + 4096 + e * 1024); }
        VM0();
        {
          const int row = 16 * (g >> 1) + 4 * (g & 1) + q4;
#pragma unroll
          for (int nt = 0; nt < 4; ++nt) { const unsigned a = L_SO + row * 128 + ((nt ^ ((row >> 1) & 3)) * 32) + pp * 8; rt[8 + nt] = cat(trd(a), trd(a + 1024)); }
        }
        const int row = 4 * g + q4;
#pragma unroll
        for (int e = 0; e < 8; ++e) { const int nt = e & 3; const unsigned a = L_SO + 4096 + (e >> 2) * 4096 + row * 128 + ((nt ^ ((row >> 1) & 3)) * 32) + pp * 8; rt[e] = cat(trd(a), trd(a + 2048)); }
      }
    }
    STAMP(1);
    if (w >= 6) {
      while (*(volatile LAS unsigned*)(uintptr_t)(L_CNT + 8) < 4u) __builtin_amdgcn_s_sleep(1);
      CFENCE();
      if (w == 6) {
#pragma unroll
        for (int e = 0; e < 7; ++e) st16(rWS, asv4u(rt[e]), WS_TW1 + ((cj * 7 + e) * 16 + ci) * 1024 + lane * 16);
      } else {
        if (b < 224) {
#pragma unroll
          for (int e = 0; e < 8; ++e) { const int id = 2 * b + (e >> 2); st16(rWS, asv4u(rt[e]), WS_TW2 + (((id & 7) * 56 + (id >> 3)) * 4 + (e & 3)) * 1024 + lane * 16); }
        }
        if (b < 128) {
#pragma unroll
          for (int e = 0; e < 4; ++e) st16(rWS, asv4u(rt[8 + e]), WS_TWO + (((b & 7) * 16 + (b >> 3)) * 4 + e) * 1024 + lane * 16);
        }
      }
    }
    wait_words(p.ws, WS_FQ + cj * 128, 16, lane, TAG);
    while (*(volatile LAS unsigned*)(uintptr_t)(L_CNT + 4) == 0u) __builtin_amdgcn_s_sleep(1);
    CFENCE();
    STAMP(19);
#pragma unroll
    for (int e = 0; e < 8; ++e) glds4x1(rWS, lane * 16, WS_TQ + cj * 98304 + ((w - 5) * 8 + ((e + 2 * (ci & 3)) & 7)) * 4096, L_WQKV + ((w - 5) * 8 + ((e + 2 * (ci & 3)) & 7)) * 4096);
  }
  {
    const int qr = lane & 31, h = lane >> 5;
#pragma unroll
    for (int r = 0; r < 16; ++r) { const int kr = (r & 3) + 8 * (r >> 2) + 4 * h; mU[r] = kr > qr ? -1e30f : 0.f; mL[r] = kr > qr ? 0.f : -1e30f; }
    asm volatile("" : "+v"(mU), "+v"(mL));
  }
  if (w == 0) {
    __builtin_amdgcn_s_setprio(3);
    wait_words(p.ws, WS_F0 + ci * 512, 64, lane, TAG);
    if (lane == 0) *(volatile LAS unsigned*)(uintptr_t)(L_CNT + 12) = 1u;
    __builtin_amdgcn_s_setprio(0);
  } else {
    while (*(volatile LAS unsigned*)(uintptr_t)(L_CNT + 12) == 0u) __builtin_amdgcn_s_sleep(1);
    CFENCE();
  }
  STAMP(2);
  v8s act[16];
  const unsigned hsrc = WS_HF + (ci * 8 + w) * 16 * 1024;
  v2u wl[2], wh[2];
#pragma unroll
  for (int u = 0; u < 2; ++u) { const unsigned o = (32 * (2 * w + u) + 4 * g) * 2; wl[u] = ld8(rWS, o, WS_WV + ci * 1024); wh[u] = ld8(rWS, o + 32, WS_WV + ci * 1024); }
#pragma unroll
  for (int s = 0; s < 5; ++s) act[s] = asv8(ld16(rWS, lane * 16, hsrc + s * 1024));
  if (w >= 5) asm volatile("s_waitcnt vmcnt(9)" ::: "memory");
  BAR();
  STAMP(3);
  if (lane == 0) {
    if (w == 6) arrive(p.ws, WS_FW1 + (cj * 16 + ci) * 8, TAG);
    if (w == 7 && b < 128) arrive(p.ws, WS_FWO + ((b & 7) * 16 + (b >> 3)) * 8, TAG);
    if (w == 7 && b < 224) { arrive(p.ws, WS_FW2 + (((2 * b) & 7) * 64 + ((2 * b) >> 3)) * 8, TAG); arrive(p.ws, WS_FW2 + (((2 * b + 1) & 7) * 64 + ((2 * b + 1) >> 3)) * 8, TAG); }
  }

  f4 accQ[2], accKV[4];
  float rstd, nmu;
  v4u gaR, cbR, sbR, gbR;
  v2u b2R[2];
  v4u mc0, md0;
  v4u mx[2];
  const int tt = w & 3, kh = w >> 2;
  const int ocol = 64 * ct + 32 * kh + 4 * g;
  {
    const unsigned bq = opq(L_WQKV + lane * 16), bv = opq(L_WQKV + 65536 + lane * 16);
#define QF(nt, s) lds16(((nt) < 4 ? bq : bv) + ((nt) & 3) * 16384 + (s) * 1024)
    {
      f4 au[6];
      v8s W[2][6];
      const unsigned c0 = opq(bq + w * 2048), d0 = opq(bv + w * 2048);
#pragma unroll
      for (int u = 0; u < 2; ++u)
#pragma unroll
        for (int nt = 0; nt < 6; ++nt) W[u][nt] = lds16((nt < 4 ? c0 : d0) + (nt & 3) * 16384 + u * 1024);
#pragma unroll
      for (int nt = 0; nt < 6; ++nt) au[nt] = f4{0, 0, 0, 0};
#pragma unroll
      for (int u = 0; u < 2; ++u) {
        const v8s a = asv8(v4u{wl[u][0], wl[u][1], wh[u][0], wh[u][1]});
#pragma unroll
        for (int nt = 0; nt < 6; ++nt) au[nt] = MFMA16(W[u][nt], a, au[nt]);
      }
      if (tok == 0) {
#pragma unroll
        for (int nt = 0; nt < 6; ++nt) *(LAS f4*)(sm + L_PARTU + (w * 96 + nt * 16 + 4 * g) * 4) = au[nt];
      }
    }
    BAR();
    if (tid >= 384 && tid < 480) {
      const int e0 = tid - 384;
      float s = 0;
#pragma unroll
      for (int e = 0; e < 8; ++e) s += *(const LAS float*)(sm + L_PARTU + (e * 96 + e0) * 4);
      *(LAS float*)(sm + L_PERS + e0 * 4) = s;
    }
    STAMP(16);
    v2u st[4];
    u64 fm0 = 0, fm1 = 0, fm2 = 0;
#pragma unroll
    for (int nt = 0; nt < 4; ++nt) accKV[nt] = f4{0, 0, 0, 0};
#pragma unroll
    for (int nt = 0; nt < 2; ++nt) accQ[nt] = f4{0, 0, 0, 0};
    {
      v8s W[6];
#pragma unroll
      for (int nt = 0; nt < 6; ++nt) W[nt] = QF(nt, 0);
      SB();
#pragma unroll
      for (int s = 0; s < 16; ++s) {
        if (s + 5 < 16) act[s + 5] = asv8(ld16(rWS, lane * 16, hsrc + (s + 5) * 1024));
        else if (s < 15) st[s - 11] = ld8(rWS, ((4 * g + s - 11) * 128 + 16 * w + tok) * 8, WS_ST1 + ci * 16 * 128 * 8);
        if (s == 12) { GAS u64* f = (GAS u64*)(p.ws + WS_FM); fm0 = __hip_atomic_load(f + lane, RLX_AGENT); fm1 = __hip_atomic_load(f + 64 + lane, RLX_AGENT); fm2 = __hip_atomic_load(f + 128 + (lane & 31), RLX_AGENT); }
#pragma unroll
        for (int nt = 0; nt < 6; ++nt) {
          if (nt < 2) accQ[nt] = MFMA16(W[nt], act[s], accQ[nt]); else accKV[nt - 2] = MFMA16(W[nt], act[s], accKV[nt - 2]);
          if (s + 1 < 16) W[nt] = QF(nt, s + 1);
          SB();
        }
      }
    }
    float s1 = 0, s2 = 0;
#pragma unroll
    for (int e = 0; e < 4; ++e) { s1 += __uint_as_float(st[e][0]); s2 += __uint_as_float(st[e][1]); }
    s1 = sumg(s1); s2 = sumg(s2);
    const float mu = s1 * (1.f / 512), var = fmaxf(s2 * (1.f / 512) - mu * mu, 0.f);
    rstd = __builtin_amdgcn_rsqf(var + 1e-5f); nmu = -rstd * mu;
    if (w == 6 && lane < 8) *(LAS v4u*)(sm + L_B2C + lane * 16) = b2v;
    PIN(rstd); PIN(nmu);
    {
          GAS u64* f = (GAS u64*)(p.ws + WS_FM);
          for (unsigned spins = 0; spins < (1u << 17) && !__all(fm0 == TAG && fm1 == TAG && fm2 == TAG); ++spins) {
            fm0 = __hip_atomic_load(f + lane, RLX_AGENT); fm1 = __hip_atomic_load(f + 64 + lane, RLX_AGENT); fm2 = __hip_atomic_load(f + 128 + (lane & 31), RLX_AGENT); }
          CFENCE();
          if (w < 6) {
            const int vec = w < 2 ? 0 : w < 4 ? 2 : 3, hq = w & 1;
            const unsigned dst = w == 0 ? L_PSA : w == 1 ? L_PSA2 : w == 2 ? L_PSB : w == 3 ? L_PSB2 : w == 4 ? L_PCB : L_PCB2;
            glds2x1(rWS, lane * 16, WS_MOD + (2 * hq) * 10240 + vec * 2048, dst);
            glds2x1(rWS, lane * 16, WS_MOD + (2 * hq + 1) * 10240 + vec * 2048, dst + 2048);
          } else glds1(rWS, (lane >> 4) * 10240 + (lane & 15) * 16, WS_MOD + (w == 6 ? 1 : 4) * 2048 + ct * 256, w == 6 ? L_PGA : L_PGB);
    }
    STAMP(4);
    BAR();
    {
      v4u kk, vv;
      unsigned pkd[4][2];
#pragma unroll
      for (int nt = 0; nt < 4; ++nt) {
        const f4 u = ldsf4(L_PERS + (32 + nt * 16 + 4 * g) * 4);
        pkd[nt][0] = pk(rstd * accKV[nt][0] + nmu * u[0], rstd * accKV[nt][1] + nmu * u[1]);
        pkd[nt][1] = pk(rstd * accKV[nt][2] + nmu * u[2], rstd * accKV[nt][3] + nmu * u[3]);
      }
      { auto r0 = __builtin_amdgcn_permlane16_swap(pkd[0][0], pkd[1][0], false, false); auto r1 = __builtin_amdgcn_permlane16_swap(pkd[0][1], pkd[1][1], false, false);
        kk = v4u{r0[0], r1[0], r0[1], r1[1]}; }
      { auto r0 = __builtin_amdgcn_permlane16_swap(pkd[2][0], pkd[3][0], false, false); auto r1 = __builtin_amdgcn_permlane16_swap(pkd[2][1], pkd[3][1], false, false);
        vv = v4u{r0[0], r1[0], r0[1], r1[1]}; }
      const int hd = g & 1, pl = g >> 1;
      st16(rWS, kk, WS_KV + (ci * 16 + cj) * 16384 + ((0 * 2 + hd) * 2 + pl) * 2048 + (16 * w + tok) * 16);
      st16(rWS, vv, WS_KV + (ci * 16 + cj) * 16384 + ((1 * 2 + hd) * 2 + pl) * 2048 + (16 * w + tok) * 16);
      *(LAS v4u*)(sm + L_AK + (hd * 2 + pl) * KPL + (256 + 16 * w + tok) * 16) = kk;
      *(LAS v4u*)(sm + L_AV + hd * VHD + pl * VPL + (256 + 16 * w + tok) * 16) = vv;
    }
    VM0();
    BAR();
    if (tid == 256) arrive(p.ws, WS_FP + (0 * 16 + ci) * 128 + cj * 8, TAG);
    STAMP(5);
    if (tid < 400) *(LAS v4u*)(sm + L_ONE + tid * 16) = v4u{0x3f803f80u, 0x3f803f80u, 0x3f803f80u, 0x3f803f80u};
    if (tid < 416) {
      const int seg = tid < 384 ? tid >> 7 : 3 + ((tid - 384) >> 4), e = tid < 384 ? tid & 127 : (tid - 384) & 15;
      f4 v;
      if (seg < 3) {
        const unsigned a0 = (seg == 0 ? L_PSA : seg == 1 ? L_PSB : L_PCB) + e * 16, a1 = (seg == 0 ? L_PSA2 : seg == 1 ? L_PSB2 : L_PCB2) + e * 16;
        v = (ldsf4(a0) + ldsf4(a0 + 2048)) + (ldsf4(a1) + ldsf4(a1 + 2048));
      } else {
        const unsigned a0 = (seg == 3 ? L_PGA : L_PGB) + e * 16;
        v = (ldsf4(a0) + ldsf4(a0 + 256)) + (ldsf4(a0 + 512) + ldsf4(a0 + 768));
      }
      *(LAS v2u*)(sm + L_FIN + (seg < 3 ? seg * 1024 : 3072 + (seg - 3) * 128) + e * 8) = v2u{pk(v[0], v[1]), pk(v[2], v[3])};
    }
    BAR();
    {
      f4 av[4];
      v8s W[2][4], a[2];
      const unsigned c0 = opq(bq + w * 2048), d0 = opq(bv + w * 2048);
#pragma unroll
      for (int u = 0; u < 2; ++u) {
        a[u] = lds16(L_FIN + (2 * w + u) * 64 + g * 16);
#pragma unroll
        for (int nt = 0; nt < 4; ++nt) W[u][nt] = lds16((nt < 2 ? c0 : d0) + (nt & 1) * 16384 + u * 1024);
      }
#pragma unroll
      for (int nt = 0; nt < 4; ++nt) av[nt] = f4{0, 0, 0, 0};
#pragma unroll
      for (int u = 0; u < 2; ++u)
#pragma unroll
        for (int nt = 0; nt < 4; ++nt) av[nt] = MFMA16(W[u][nt], a[u], av[nt]);
      if (tok == 0) {
#pragma unroll
        for (int nt = 0; nt < 4; ++nt) *(LAS f4*)(sm + L_PARTV + (w * 64 + nt * 16 + 4 * g) * 4) = av[nt];
      }
    }
    {
      gaR = asv4u(lds16(L_FIN + 3072 + kh * 64 + g * 16)); gbR = asv4u(lds16(L_FIN + 3200 + kh * 64 + g * 16));
      sbR = asv4u(lds16(L_FIN + 1024 + (2 * ct + kh) * 64 + g * 16)); cbR = asv4u(lds16(L_FIN + 2048 + (2 * ct + kh) * 64 + g * 16));
#pragma unroll
      for (int e = 0; e < 2; ++e) b2R[e] = lds8(L_B2C + (32 * kh + 4 * g + 16 * e) * 2);
      mc0 = asv4u(lds16(L_FIN + 2048 + 16 * lane)); md0 = asv4u(lds16(L_FIN + 1024 + 16 * lane));
#pragma unroll
      for (int u = 0; u < 2; ++u) mx[u] = asv4u(lds16(L_FIN + (tok == 1 ? 1024 : 2048) + (2 * w + u) * 64 + g * 16));
      BAR();
      if (tid < 64) {
        float s = 0;
#pragma unroll
        for (int e = 0; e < 8; ++e) s += *(const LAS float*)(sm + L_PARTV + (e * 64 + tid) * 4);
        *(LAS float*)(sm + L_PERS + (96 + tid) * 4) = s;
      }
    }
    if (ci < 2) {
      for (int e = tid; e < 8 * 256; e += 512) {
        const int pln = e >> 8, key = e & 255;
        if (ci == 0 || key < 128) {
          const unsigned a = pln < 4 ? L_AK + pln * KPL : L_AV + ((pln - 4) >> 1) * VHD + (pln & 1) * VPL;
          *(LAS v4u*)(sm + a + key * 16) = v4u{0, 0, 0, 0};
        }
      }
    }
    if (w == 4) {
      GAS u64* f0 = (GAS u64*)(p.ws + (lane == 0 && ci >= 1 ? WS_FP + (ci - 1) * 128 + cj * 8 : lane == 1 && ci >= 2 ? WS_FP + (ci - 2) * 128 + cj * 8
                                       : lane >= 16 && lane < 32 ? WS_FW1 + (cj * 16 + lane - 16) * 8 : WS_FWO + (ct * 16 + (lane & 15)) * 8));
      GAS u64* f1 = (GAS u64*)(p.ws + WS_FW2 + ct * 512) + (lane < 56 ? lane : 0);
      for (unsigned spins = 0; spins < (1u << 17); ++spins) {
        const u64 a = __hip_atomic_load(f0, RLX_AGENT), c = __hip_atomic_load(f1, RLX_AGENT);
        if (__all(a == TAG && c == TAG)) break;
      }
      CFENCE();
    }
    BAR();
    STAMP(6);
    {
      const unsigned dst = w < 4 ? L_AK + w * KPL : L_AV + ((w - 4) >> 1) * VHD + (w & 1) * VPL;
#pragma unroll
      for (int src = 0; src < 2; ++src)
        if (ci - 2 + src >= 0) glds2x1(rWS, lane * 16, WS_KV + ((ci - 2 + src) * 16 + cj) * 16384 + w * 2048, dst + src * 2048);
    }
    {
      const float qs = 0.25f * 1.4426950408889634f;
      unsigned pq[2][2];
#pragma unroll
      for (int nt = 0; nt < 2; ++nt) {
        const f4 u = ldsf4(L_PERS + (nt * 16 + 4 * g) * 4), v = ldsf4(L_PERS + (96 + nt * 16 + 4 * g) * 4);
        pq[nt][0] = pk(qs * (rstd * accQ[nt][0] + nmu * u[0] + v[0]), qs * (rstd * accQ[nt][1] + nmu * u[1] + v[1]));
        pq[nt][1] = pk(qs * (rstd * accQ[nt][2] + nmu * u[2] + v[2]), qs * (rstd * accQ[nt][3] + nmu * u[3] + v[3]));
      }
      auto r0 = __builtin_amdgcn_permlane16_swap(pq[0][0], pq[1][0], false, false); auto r1 = __builtin_amdgcn_permlane16_swap(pq[0][1], pq[1][1], false, false);
      *(LAS v4u*)(sm + L_AQ + ((g & 1) * 2 + (g >> 1)) * 2048 + (16 * w + tok) * 16) = v4u{r0[0], r1[0], r0[1], r1[1]};
    }
    VM0();
    BAR();
    STAMP(7);
  }

  const int orow = 128 * ci + 64 * hf + 16 * tt + tok;
  v2u xr0, xr1;
  {
    const int t = w & 3, hd = w >> 2;
    const int kt0 = ci >= 2 ? 0 : 8 - 4 * ci - t;
    f16v O;
    const u16* xres = p.x + (size_t)orow * DM + ocol;
    if (ci >= 2) attention<true>(t, hd, 0, lane, mU, mL, O, rWS, WS_TWO + ct * 65536, WS_TW1 + cj * 7 * 16384, w, xres, xr0, xr1, stampv);
    else attention<false>(t, hd, kt0, lane, mU, mL, O, rWS, WS_TWO + ct * 65536, WS_TW1 + cj * 7 * 16384, w, xres, xr0, xr1, stampv);
    const int h = lane >> 5;
    const float inv = __builtin_amdgcn_rcpf(O[8]);
    const f4 b0 = ldsf4(L_PERS + (96 + 32 + hd * 16 + 4 * h) * 4), b1 = ldsf4(L_PERS + (96 + 32 + hd * 16 + 8 + 4 * h) * 4);
    const v4u o = {pk(O[0] * inv + b0[0], O[1] * inv + b0[1]), pk(O[2] * inv + b0[2], O[3] * inv + b0[3]),
                   pk(O[4] * inv + b1[0], O[5] * inv + b1[1]), pk(O[6] * inv + b1[2], O[7] * inv + b1[3])};
    const int qr = lane & 31;
    st16(rWS, o, WS_OF + (ci * 8 * 16 + cj) * 1024 + ((2 * t + (qr >> 4)) * 16) * 1024 + ((2 * hd + h) * 16 + (qr & 15)) * 16);
  }
  STAMP(24);
  VM0();
  BAR();
  if (tid == 256) arrive(p.ws, WS_FP + (1 * 16 + ci) * 128 + cj * 8, TAG);
  STAMP(8);
  float b1v = 0.f;
  v8s aux2[2];
  float Cw = 0, Cw2 = 0, Cws = 0, Cs = 0, Cs2 = 0;
#define MODSUMS() do { \
      _Pragma("unroll") for (int r = 0; r < 4; ++r) { \
        const float w0 = rbf(1.f + bflo(mc0[r])), w1 = rbf(1.f + bfhi(mc0[r])), s0 = bflo(md0[r]), s1_ = bfhi(md0[r]); \
        Cw += w0 + w1; Cw2 += w0 * w0 + w1 * w1; Cws += w0 * s0 + w1 * s1_; Cs += s0 + s1_; Cs2 += s0 * s0 + s1_ * s1_; } \
      Cw = sumg(rowsum16(Cw)); Cw2 = sumg(rowsum16(Cw2)); Cws = sumg(rowsum16(Cws)); Cs = sumg(rowsum16(Cs)); Cs2 = sumg(rowsum16(Cs2)); \
      _Pragma("unroll") for (int u = 0; u < 2; ++u) { v4u t; \
        _Pragma("unroll") for (int d = 0; d < 4; ++d) t[d] = tok == 0 ? pk(1.f + bflo(mx[u][d]), 1.f + bfhi(mx[u][d])) : (tok == 1 ? mx[u][d] : 0x3f803f80u); \
        aux2[u] = asv8(t); } \
      asm volatile("" : "+v"(Cw), "+v"(Cw2), "+v"(Cws), "+v"(Cs), "+v"(Cs2), "+v"(aux2[0]), "+v"(aux2[1])); } while (0)
  if (w != 4) {
#pragma unroll
    for (int e = 0; e < 3; ++e) { const int gi = wq + 7 * e; if (gi < 16) glds4x1(rWS, lane * 16, WS_TW1 + (cj * 7 + 2) * 16384 + gi * 4096, L_W1 + 32768 + gi * 4096); }
  }
  MODSUMS();
  if (w == 4) wait_words(p.ws, WS_FP + (1 * 16 + ci) * 128, 16, lane, TAG);
  BAR();
  STAMP(9);

  float x1[8];
  {
    v8s oa[8];
    const unsigned osrc = WS_OF + ((ci * 8 + 4 * hf + tt) * 16 + 8 * kh) * 1024;
#pragma unroll
    for (int s = 0; s < 3; ++s) oa[s] = asv8(ld16(rWS, lane * 16, osrc + s * 1024));
    const unsigned bo = opq(L_WO + kh * 32768 + lane * 16);
    f4 acc[4];
#pragma unroll
    for (int nt = 0; nt < 4; ++nt) acc[nt] = f4{0, 0, 0, 0};
    {
      v8s W[2][4];
#pragma unroll
      for (int s = 0; s < 2; ++s)
#pragma unroll
        for (int nt = 0; nt < 4; ++nt) W[s][nt] = lds16(bo + (s * 4 + nt) * 1024);
      SB();
#pragma unroll
      for (int s = 3; s < 8; ++s) oa[s] = asv8(ld16(rWS, lane * 16, osrc + s * 1024));
      if (tid < 112) { const int nt = tid >> 4; b1v = bflo(p.b1[(nt == 6 ? 112 * cj + (par ? 0 : 96) : 112 * cj + 16 * par + nt * 16) + (tid & 15)]); }
      SB();
#pragma unroll
      for (int s = 0; s < 8; ++s)
#pragma unroll
        for (int nt = 0; nt < 4; ++nt) {
          acc[nt] = MFMA16(W[s & 1][nt], oa[s], acc[nt]);
          if (s + 2 < 8) W[s & 1][nt] = lds16(bo + ((s + 2) * 4 + nt) * 1024);
          SB();
        }
    }
    STAMP(26);
    VM0();
    BAR();
    *(LAS f4*)(sm + L_XCH + w * 2048 + lane * 16) = kh ? acc[0] : acc[2];
    *(LAS f4*)(sm + L_XCH + w * 2048 + 1024 + lane * 16) = kh ? acc[1] : acc[3];
    BAR();
    const f4 o0 = ldsf4(L_XCH + (w ^ 4) * 2048 + lane * 16), o1 = ldsf4(L_XCH + (w ^ 4) * 2048 + 1024 + lane * 16);
    const f4 t0 = (kh ? acc[2] : acc[0]) + o0, t1 = (kh ? acc[3] : acc[1]) + o1;
    const float xin[8] = {bflo(xr0[0]), bfhi(xr0[0]), bflo(xr0[1]), bfhi(xr0[1]), bflo(xr1[0]), bfhi(xr1[0]), bflo(xr1[1]), bfhi(xr1[1])};
    const float ga[8] = {bflo(gaR[0]), bfhi(gaR[0]), bflo(gaR[1]), bfhi(gaR[1]), bflo(gaR[2]), bfhi(gaR[2]), bflo(gaR[3]), bfhi(gaR[3])};
    const float cb[8] = {bflo(cbR[0]), bfhi(cbR[0]), bflo(cbR[1]), bfhi(cbR[1]), bflo(cbR[2]), bfhi(cbR[2]), bflo(cbR[3]), bfhi(cbR[3])};
    const float sv[8] = {bflo(sbR[0]), bfhi(sbR[0]), bflo(sbR[1]), bfhi(sbR[1]), bflo(sbR[2]), bfhi(sbR[2]), bflo(sbR[3]), bfhi(sbR[3])};
    float wv[8], xw[8];
#pragma unroll
    for (int r = 0; r < 8; ++r) { x1[r] = rbf(xin[r] + ga[r] * (r < 4 ? t0[r & 3] : t1[r & 3])); wv[r] = rbf(1.f + cb[r]); xw[r] = x1[r] * wv[r]; }
    st16(rWS, v4u{pk(xw[0], xw[1]), pk(xw[2], xw[3]), pk(xw[4], xw[5]), pk(xw[6], xw[7])}, WS_X1F + ((ci * 8 + 4 * hf + tt) * 16 + 2 * ct + kh) * 1024 + lane * 16);
    SB();
    float s1 = 0, s2 = 0, s3 = 0, s4 = 0, s5 = 0, s6 = 0;
#pragma unroll
    for (int r = 0; r < 8; ++r) { s1 += x1[r]; s2 += x1[r] * x1[r]; s3 += xw[r]; s4 += xw[r] * wv[r]; s5 += xw[r] * xw[r]; s6 += xw[r] * sv[r]; }
    s1 = sumg(s1); s2 = sumg(s2); s3 = sumg(s3); s4 = sumg(s4); s5 = sumg(s5); s6 = sumg(s6);
    const unsigned rec = (ci * 16 + 2 * ct + kh) * 128 + 64 * hf + 16 * tt + tok;
    if (g == 0) st16(rWS, v4u{__float_as_uint(s1), __float_as_uint(s2), __float_as_uint(s3), __float_as_uint(s4)}, WS_ST2A + rec * 16);
    if (g == 1) st8(rWS, v2u{__float_as_uint(s5), __float_as_uint(s6)}, WS_ST2B + rec * 8);
  }
  if (w < 4) {
    glds4x1(rWS, lane * 16, WS_TW1 + (cj * 7 + 6) * 16384 + w * 4096, L_W1S + w * 4096);
    glds4x1(rWS, lane * 16, WS_TW2 + ct * 229376 + 7 * 16384 + w * 4096, 49152 + w * 4096);
    asm volatile("s_waitcnt vmcnt(8)" ::: "memory");
  } else {
    glds4x1(rWS, lane * 16, WS_TW2 + ct * 229376 + (w - 4) * 4096, 32768 + (w - 4) * 4096);
    asm volatile("s_waitcnt vmcnt(4)" ::: "memory");
  }
  BAR();
  if (tid == 256) arrive(p.ws, WS_FP + (2 * 16 + ci) * 128 + cj * 8, TAG);
  STAMP(10);
  if (w == 4) asm volatile("s_waitcnt vmcnt(1)" ::: "memory"); else VM0();
  BAR();

  v4u stg[8];
  {
    const unsigned bP = opq(L_W1 + lane * 16), bR = opq(L_W1 + 65536 + lane * 16), bS = opq(L_W1S + lane * 16);
#define W1F(nt, s) lds16((nt) == 6 ? bS + (s) * 1024 : (nt) < 4 ? bP + (nt) * 16384 + (s) * 1024 : bR + ((nt) - 4) * 16384 + (s) * 1024)
    {
      f4 au[7];
#pragma unroll
      for (int nt = 0; nt < 7; ++nt) au[nt] = f4{0, 0, 0, 0};
      const unsigned c0 = opq(bP + w * 2048), d0 = opq(bR + w * 2048), e0 = opq(bS + w * 2048);
#pragma unroll
      for (int u = 0; u < 2; ++u) {
        v8s W[7];
#pragma unroll
        for (int nt = 0; nt < 7; ++nt) W[nt] = lds16(nt == 6 ? e0 + u * 1024 : nt < 4 ? c0 + nt * 16384 + u * 1024 : d0 + (nt - 4) * 16384 + u * 1024);
#pragma unroll
        for (int nt = 0; nt < 7; ++nt) au[nt] = MFMA16(W[nt], aux2[u], au[nt]);
      }
      if (tok < 3) {
#pragma unroll
        for (int nt = 0; nt < 7; ++nt) *(LAS f4*)(sm + L_XCH + ((w * 3 + tok) * 112 + nt * 16 + 4 * g) * 4) = au[nt];
      }
    }
    BAR();
    if (tid < 112) *(LAS float*)(sm + L_XF + (336 + tid) * 4) = b1v;
    {
      const int e0 = w < 4 ? tid : tid - 64;
      if (w != 4 && e0 < 336) {
        float s = 0;
#pragma unroll
        for (int e = 0; e < 8; ++e) s += *(const LAS float*)(sm + L_XCH + (e * 336 + e0) * 4);
        *(LAS float*)(sm + L_XF + e0 * 4) = s;
      }
    }
    if (w == 4) wait_words(p.ws, WS_FP + (2 * 16 + ci) * 128, 16, lane, TAG);
    BAR();
    STAMP(11);
    v8s act2[16];
    const unsigned asrc = WS_X1F + (ci * 8 + w) * 16 * 1024;
#pragma unroll
    for (int s = 0; s < 4; ++s) act2[s] = asv8(ld16(rWS, lane * 16, asrc + s * 1024));
    v4u sa[4]; v2u sb[4];
    f4 acc[7];
#pragma unroll
    for (int nt = 0; nt < 7; ++nt) acc[nt] = f4{0, 0, 0, 0};
    {
      v8s W[6];
#pragma unroll
      for (int nt = 0; nt < 6; ++nt) W[nt] = W1F(nt, 0);
      SB();
#pragma unroll
      for (int s = 0; s < 16; ++s) {
        if (s == 0) act2[4] = asv8(ld16(rWS, lane * 16, asrc + 4 * 1024));
        if (s + 5 < 16) act2[s + 5] = asv8(ld16(rWS, lane * 16, asrc + (s + 5) * 1024));
        else if (s < 15) { sa[s - 11] = ld16(rWS, ((4 * g + s - 11) * 128 + 16 * w + tok) * 16, WS_ST2A + ci * 16 * 128 * 16);
                           sb[s - 11] = ld8(rWS, ((4 * g + s - 11) * 128 + 16 * w + tok) * 8, WS_ST2B + ci * 16 * 128 * 8); }
#pragma unroll
        for (int nt = 0; nt < 6; ++nt) {
          acc[nt] = MFMA16(W[nt], act2[s], acc[nt]);
          if (s + 1 < 16) W[nt] = W1F(nt, s + 1);
          SB();
        }
      }
    }
    STAMP(18);
    CSTAMP(42);
    BAR();
    v8s WB[4];
#pragma unroll
    for (int s = 0; s < 4; ++s) WB[s] = W1F(6, s);
    float s1 = 0, s2 = 0, s3 = 0, s4 = 0, s5 = 0, s6 = 0;
#pragma unroll
    for (int e = 0; e < 4; ++e) { s1 += __uint_as_float(sa[e][0]); s2 += __uint_as_float(sa[e][1]); s3 += __uint_as_float(sa[e][2]); s4 += __uint_as_float(sa[e][3]); s5 += __uint_as_float(sb[e][0]); s6 += __uint_as_float(sb[e][1]); }
    s1 = sumg(s1); s2 = sumg(s2); s3 = sumg(s3); s4 = sumg(s4); s5 = sumg(s5); s6 = sumg(s6);
    const float mu1 = s1 * (1.f / 512), var1 = fmaxf(s2 * (1.f / 512) - mu1 * mu1, 0.f), r1 = __builtin_amdgcn_rsqf(var1 + 1e-5f);
    const float mu2 = (r1 * (s3 - mu1 * Cw) + Cs) * (1.f / 512);
    const float hh = (r1 * r1 * (s5 - 2.f * mu1 * s4 + mu1 * mu1 * Cw2) + 2.f * r1 * (s6 - mu1 * Cws) + Cs2) * (1.f / 512);
    const float r2 = __builtin_amdgcn_rsqf(fmaxf(hh - mu2 * mu2, 0.f) + 1e-5f);
    const float cA = r2 * r1, cB = -cA * mu1, cC = r2, cD = -r2 * mu2;
    const unsigned S0 = (7 * cj + par) >> 1;
    const unsigned hb = WS_H1F + (ci * 8 + w) * 56 * 1024;
    const unsigned xf = opq(L_XF + 16 * g);
#define BIAS4(nt) __builtin_elementwise_fma(f4{cB, cB, cB, cB}, ldsf4(xf + (nt) * 64), __builtin_elementwise_fma(f4{cC, cC, cC, cC}, ldsf4(xf + 448 + (nt) * 64), \
                    __builtin_elementwise_fma(f4{cD, cD, cD, cD}, ldsf4(xf + 896 + (nt) * 64), ldsf4(xf + 1344 + (nt) * 64))))
#define GELU4(o, a, bias) do { const f4 z_ = __builtin_elementwise_fma(f4{cA, cA, cA, cA}, a, bias); const f4 q_ = __builtin_elementwise_fma(z_ * z_, f4{-0.1029432397f, -0.1029432397f, -0.1029432397f, -0.1029432397f}, \
        f4{-2.3022081985f, -2.3022081985f, -2.3022081985f, -2.3022081985f}) * z_; f4 e_; \
        _Pragma("unroll") for (int r = 0; r < 4; ++r) e_[r] = __builtin_amdgcn_exp2f(q_[r]); \
        e_ += f4{1.f, 1.f, 1.f, 1.f}; \
        _Pragma("unroll") for (int r = 0; r < 4; ++r) e_[r] = __builtin_amdgcn_rcpf(e_[r]); \
        o = z_ * e_; } while (0)
    {
      unsigned hp[2][2];
      f4 bias = BIAS4(0);
      PIN(bias);
      SB();
#pragma unroll
      for (int nt = 0; nt < 6; ++nt) {
#pragma unroll
        for (int k = (nt * 16) / 6; k < ((nt + 1) * 16) / 6; ++k) { acc[6] = MFMA16(WB[k & 3], act2[k], acc[6]); if (k + 4 < 16) WB[k & 3] = W1F(6, k + 4); }
        if ((nt & 1) == 0) { const int gi = w * 3 + (nt >> 1), it = 1 + (gi >> 3), khh = (gi >> 2) & 1, pc4 = gi & 3;
          glds4x1(rWS, lane * 16, WS_TW2 + ct * 229376 + (khh * 7 + it) * 16384 + pc4 * 4096, 65536u + (it - 1) * 32768u + khh * 16384u + pc4 * 4096u); }
        if (nt >= 1 && nt < 5) {
#pragma unroll
          for (int e = 2 * (nt - 1); e < 2 * nt; ++e) { const int pc = w * 8 + e; const int chunk = (pc >> 4) == 0 ? 5 : ((pc >> 4) == 1 ? 12 : ((pc >> 4) == 2 ? 6 : 13));
            stg[e] = ld16(rWS, lane * 16, WS_TW2 + ct * 229376 + chunk * 16384 + (pc & 15) * 1024); }
        }
        f4 nb = bias;
        if (nt < 5) nb = BIAS4(nt + 1); else nb = BIAS4(6);
        f4 o;
        GELU4(o, acc[nt], bias);
        hp[nt & 1][0] = pk(o[0], o[1]); hp[nt & 1][1] = pk(o[2], o[3]);
        PIN(hp[nt & 1][0]); PIN(hp[nt & 1][1]); PIN(nb);
        bias = nb;
        if (nt & 1) st16(rWS, v4u{hp[0][0], hp[0][1], hp[1][0], hp[1][1]}, hb + (S0 + (nt >> 1)) * 1024 + lane * 16);
        SB();
      }
      STAMP(20);
      CSTAMP(43);
      f4 o;
      GELU4(o, acc[6], bias);
      const unsigned NT = 7 * cj + (par ? 0 : 6);
      st8(rWS, v2u{pk(o[0], o[1]), pk(o[2], o[3])}, hb + (NT >> 1) * 1024 + (NT & 1) * 512 + lane * 8);
    }
  }
  STAMP(25);
  VM0();
  BAR();
  if (tid == 256) arrive(p.ws, WS_FP + (3 * 16 + ci) * 128 + cj * 8, TAG);
  STAMP(13);
  if (w != 4) {
#pragma unroll
    for (int e = 0; e < 2; ++e) {
      const int gi = wq + 7 * e;
      if (gi < 8) glds4x1(rWS, lane * 16, WS_TW2 + ct * 229376 + ((gi >> 2) * 7 + 4) * 16384 + (gi & 3) * 4096, (gi >> 2) * 16384u + (gi & 3) * 4096u);
    }
  } else {
    if (b == 0) {
      GAS u64* f = (GAS u64*)(p.ws + WS_F0) + lane;
      for (unsigned spins = 0; spins < (1u << 17); ++spins) {
        bool ok = true;
#pragma unroll
        for (int i = 0; i < 16; ++i) ok &= __hip_atomic_load(f + i * 64, RLX_AGENT) == TAG;
        if (__all(ok)) break;
      }
      if (lane == 0) __hip_atomic_store((GAS u64*)(p.ws + WS_EPOCH), TAG, RLX_AGENT);
    }
    wait_words(p.ws, WS_FP + (3 * 16 + ci) * 128, 16, lane, TAG);
  }
  BAR();
  STAMP(14);

  {
    v8s ha[28];
    const unsigned hb = WS_H1F + ((ci * 8 + 4 * hf + tt) * 56 + 28 * kh) * 1024;
#define HLOAD(s) do { if ((s) % 7 == 3) { const v2u lo_ = ld8(rWS, lane * 8, hb + (s) * 1024), hi_ = ld8(rWS, lane * 8 + 512, hb + (s) * 1024); ha[s] = asv8(v4u{lo_[0], lo_[1], hi_[0], hi_[1]}); } \
                      else ha[s] = asv8(ld16(rWS, lane * 16, hb + (s) * 1024)); } while (0)
#pragma unroll
    for (int s = 0; s < 3; ++s) HLOAD(s);
    const unsigned bw = opq(kh * 16384 + lane * 16), bw2 = opq(bw + 65536), bw3 = opq(bw + 131072);
    f4 acc[4];
#pragma unroll
    for (int nt = 0; nt < 4; ++nt) acc[nt] = f4{0, 0, 0, 0};
#define W2F(nt, S) lds16((((S) >> 2) == 0 || ((S) >> 2) == 5 ? bw + 32768 : ((S) >> 2) == 1 || ((S) >> 2) == 6 ? bw2 : ((S) >> 2) == 2 ? bw2 + 32768 : ((S) >> 2) == 3 ? bw3 : bw) + (((S) & 3) * 4 + (nt)) * 1024)
    {
      v8s W[2][4];
#pragma unroll
      for (int S = 0; S < 2; ++S)
#pragma unroll
        for (int nt = 0; nt < 4; ++nt) W[S][nt] = W2F(nt, S);
      SB();
#pragma unroll
      for (int s = 3; s < 8; ++s) HLOAD(s);
      SB();
#pragma unroll
      for (int S = 0; S < 28; ++S) {
        if (S + 8 < 28) HLOAD(S + 8);
        if (S == 8) BAR();
        if (S >= 8 && S < 16) { const int e = S - 8, pc = w * 8 + e; *(LAS v4u*)(sm + 32768 + (pc >> 5) * 32768 + ((pc >> 4) & 1) * 16384 + (pc & 15) * 1024 + lane * 16) = stg[e]; }
        if (S == 18) BAR();
#pragma unroll
        for (int nt = 0; nt < 4; ++nt) {
          acc[nt] = MFMA16(W[S & 1][nt], ha[S], acc[nt]);
          if (S + 2 < 28) W[S & 1][nt] = W2F(nt, S + 2);
          SB();
        }
      }
    }
    STAMP(22);
    BAR();
    *(LAS f4*)(sm + L_W2X + w * 2048 + lane * 16) = kh ? acc[0] : acc[2];
    *(LAS f4*)(sm + L_W2X + w * 2048 + 1024 + lane * 16) = kh ? acc[1] : acc[3];
    BAR();
    const f4 o0 = ldsf4(L_W2X + (w ^ 4) * 2048 + lane * 16), o1 = ldsf4(L_W2X + (w ^ 4) * 2048 + 1024 + lane * 16);
    const f4 t0 = (kh ? acc[2] : acc[0]) + o0, t1 = (kh ? acc[3] : acc[1]) + o1;
    const float gb[8] = {bflo(gbR[0]), bfhi(gbR[0]), bflo(gbR[1]), bfhi(gbR[1]), bflo(gbR[2]), bfhi(gbR[2]), bflo(gbR[3]), bfhi(gbR[3])};
    const float bb[8] = {bflo(b2R[0][0]), bfhi(b2R[0][0]), bflo(b2R[0][1]), bfhi(b2R[0][1]), bflo(b2R[1][0]), bfhi(b2R[1][0]), bflo(b2R[1][1]), bfhi(b2R[1][1])};
    float o[8];
#pragma unroll
    for (int r = 0; r < 8; ++r) o[r] = x1[r] + gb[r] * ((r < 4 ? t0[r & 3] : t1[r & 3]) + bb[r]);
    auto r0 = __builtin_amdgcn_permlane16_swap(pk(o[0], o[1]), pk(o[4], o[5]), false, false);
    auto r1 = __builtin_amdgcn_permlane16_swap(pk(o[2], o[3]), pk(o[6], o[7]), false, false);
    { const srd_t rO = __builtin_amdgcn_make_buffer_rsrc((void*)p.out, 0, SEQ * DM * 2, 0x00020000);
      __builtin_amdgcn_raw_buffer_store_b128(v4u{r0[0], r1[0], r0[1], r1[1]}, rO, (orow * DM + 64 * ct + 32 * kh + (g & 1) * 16 + (g >> 1) * 8) * 2, 0, 16); }
  }
  if (blockIdx.x == 0) while (__builtin_amdgcn_s_memrealtime() - pad_t0 < 4200ull) __builtin_amdgcn_s_sleep(1);
  STAMP(15);
  CSTAMP(41);
  STAMP_FLUSH(0);
}

extern "C" void kernel_launch(void* const* d_in, const int* in_sizes, int n_in, void* d_out, int out_size, void* d_ws, size_t ws_size, hipStream_t stream) {
  Params p;
  p.x = (const u16*)d_in[0]; p.wqkv = (const u16*)d_in[1]; p.wo = (const u16*)d_in[2];
  p.w1 = (const u16*)d_in[4]; p.b1 = (const u16*)d_in[5]; p.w2 = (const u16*)d_in[6]; p.b2 = (const u16*)d_in[7];
  p.c = (const u16*)d_in[8]; p.wmod = (const u16*)d_in[9]; p.out = (u16*)d_out; p.ws = (char*)d_ws;
  fused_block<<<256, 512, 0, stream>>>(p);
}
```

```cpp
#include <hip/hip_runtime.h>
#include <hip/hip_bf16.h>
#include <stdint.h>

typedef unsigned short u16;
typedef short v4s __attribute__((ext_vector_type(4)));
typedef short v8s __attribute__((ext_vector_type(8)));
typedef float f2 __attribute__((ext_vector_type(2)));
typedef float f4 __attribute__((ext_vector_type(4)));
typedef float f16v __attribute__((ext_vector_type(16)));
typedef unsigned v2u __attribute__((ext_vector_type(2)));
typedef unsigned v4u __attribute__((ext_vector_type(4)));
typedef __bf16 b2 __attribute__((ext_vector_type(2)));
#define LAS __attribute__((address_space(3)))
#define GAS __attribute__((address_space(1)))
#define DI __device__ __forceinline__
typedef __amdgpu_buffer_rsrc_t srd_t;

constexpr int SEQ = 2048, DM = 512, FF = 1792;
constexpr unsigned WS_EPOCH = 0;
constexpr unsigned WS_F0 = 4096;
constexpr unsigned WS_FP = WS_F0 + 8192;
constexpr unsigned WS_FM = WS_FP + 4 * 16 * 128;
constexpr unsigned WS_FQ = WS_FM + 2048;
constexpr unsigned WS_FW1 = WS_FQ + 2048;
constexpr unsigned WS_FWO = WS_FW1 + 2048;
constexpr unsigned WS_FW2 = WS_FWO + 1024;
static_assert(WS_FW2 + 4096 <= (64 << 10), "flags");
constexpr unsigned WS_WV = 64 << 10;
constexpr unsigned WS_MOD = 128 << 10;
constexpr unsigned WS_ST1 = 192 << 10;
constexpr unsigned WS_ST2A = 512 << 10;
constexpr unsigned WS_ST2B = 1024 << 10;
constexpr unsigned WS_HF = 2u << 20;
constexpr unsigned WS_KV = 4u << 20;
constexpr unsigned WS_OF = 8u << 20;
constexpr unsigned WS_X1F = 10u << 20;
constexpr unsigned WS_H1F = 12u << 20;
constexpr unsigned WS_TQ = 19u << 20;
constexpr unsigned WS_TW1 = WS_TQ + (1536u << 10);
constexpr unsigned WS_TWO = WS_TW1 + (1792u << 10);
constexpr unsigned WS_TW2 = WS_TWO + (512u << 10);
constexpr unsigned WS_STAMP = 25u << 20;
constexpr unsigned WS_BYTES = 27u << 20;
static_assert(WS_TW2 + (1792u << 10) <= WS_STAMP, "ws");
constexpr unsigned L_CA = 98304;
constexpr unsigned L_XS = 131072;
constexpr unsigned L_S1 = L_XS + 8192;
constexpr unsigned L_SO = L_S1 + 7168;
constexpr unsigned L_SC = L_SO + 12288;
constexpr unsigned L_CAP = L_SC + 2048;
constexpr unsigned L_CNT = L_CAP + 512;
static_assert(L_CNT + 16 <= 163840, "lds");
constexpr unsigned L_SQ = 0;
constexpr unsigned L_M2 = 8192;
constexpr unsigned L_WQKV = 0;
constexpr unsigned L_PARTU = 98304;
constexpr unsigned L_PARTV = 32768;
constexpr unsigned L_AK = 98304;
constexpr unsigned KPL = 6144;
constexpr unsigned L_AV = L_AK + 4 * KPL;
constexpr unsigned VPL = 6208, VHD = 12416;
constexpr unsigned L_PSA = L_AK + KPL, L_PSB = L_AK + 3 * KPL, L_PCB = L_AV + VPL, L_PGA = L_AV + VHD + VPL, L_PGB = L_PGA + 1024, L_B2C = L_PGA + 2048;
constexpr unsigned L_PSA2 = L_AK + 2 * KPL, L_PSB2 = L_AV, L_PCB2 = L_AV + VHD;
constexpr unsigned L_FIN = L_AK;
constexpr unsigned L_ONE = L_AV + 2 * VHD;
constexpr unsigned L_AQ = L_ONE + 6400;
constexpr unsigned L_PERS = L_AQ + 8192;
static_assert(L_PERS + 1024 <= 163840, "lds");
static_assert((L_AV % 256) == 0 && (L_ONE % 256) == 0, "bank plan");
constexpr unsigned L_WO = 0;
constexpr unsigned L_W1 = 65536;
constexpr unsigned L_W1S = 0;
constexpr unsigned L_XCH = 16384;
constexpr unsigned L_XF = 16384 + 12288;
constexpr unsigned L_W2X = 98304;

struct Params { const u16* x; const u16* wqkv; const u16* wo; const u16* w1; const u16* b1; const u16* w2; const u16* b2; const u16* c; const u16* wmod; u16* out; char* ws; };

DI float bflo(unsigned v) { return __uint_as_float(v << 16); }
DI float bfhi(unsigned v) { return __uint_as_float(v & 0xffff0000u); }
DI unsigned pk(float a, float b) { f2 v = {a, b}; b2 r = __builtin_convertvector(v, b2); return __builtin_bit_cast(unsigned, r); }
DI float rbf(float a) { return bflo(pk(a, 0.f)); }
DI v8s cat(v4s a, v4s b) { return __builtin_shufflevector(a, b, 0, 1, 2, 3, 4, 5, 6, 7); }
DI v4s trd(unsigned a) { return __builtin_amdgcn_ds_read_tr16_b64_v4i16((LAS v4s*)(uintptr_t)a); }
DI v8s asv8(v4u v) { return __builtin_bit_cast(v8s, v); }
DI v4u asv4u(v8s v) { return __builtin_bit_cast(v4u, v); }
DI v8s lds16(unsigned a) { return *(const LAS v8s*)(uintptr_t)a; }
DI f4 ldsf4(unsigned a) { return *(const LAS f4*)(uintptr_t)a; }
DI v2u lds8(unsigned a) { return *(const LAS v2u*)(uintptr_t)a; }
#define MFMA16(a, b, c) __builtin_amdgcn_mfma_f32_16x16x32_bf16((a), (b), (c), 0, 0, 0)
#define MFMA32(a, b, c) __builtin_amdgcn_mfma_f32_32x32x16_bf16((a), (b), (c), 0, 0, 0)
#define BAR() asm volatile("s_waitcnt lgkmcnt(0)\n\ts_barrier" ::: "memory")
#define VM0() asm volatile("s_waitcnt vmcnt(0)" ::: "memory")
#define LG0() asm volatile("s_waitcnt lgkmcnt(0)" ::: "memory")
#define CFENCE() asm volatile("" ::: "memory")
#define SB() __builtin_amdgcn_sched_barrier(0)
DI unsigned opq(unsigned v) { asm volatile("" : "+v"(v)); return v; }
DI void glds(srd_t r, unsigned voff, unsigned soff, unsigned ldsaddr) {
  asm volatile("s_mov_b32 m0, %0\n\ts_nop 0\n\tbuffer_load_dwordx4 %1, %2, %3 offen lds" ::"s"(ldsaddr), "v"(voff), "s"(r), "s"(soff) : "memory");
}
DI void glds1(srd_t r, unsigned voff, unsigned soff, unsigned ldsaddr) {
  asm volatile("s_mov_b32 m0, %0\n\ts_nop 0\n\tbuffer_load_dwordx4 %1, %2, %3 offen sc1 lds" ::"s"(ldsaddr), "v"(voff), "s"(r), "s"(soff) : "memory");
}
DI void glds4(srd_t r, unsigned voff, unsigned soff, unsigned ldsaddr) {
  asm volatile("s_mov_b32 m0, %0\n\ts_nop 0\n\tbuffer_load_dwordx4 %1, %2, %3 offen lds\n\tbuffer_load_dwordx4 %1, %2, %3 offen offset:1024 lds\n\t"
               "buffer_load_dwordx4 %1, %2, %3 offen offset:2048 lds\n\tbuffer_load_dwordx4 %1, %2, %3 offen offset:3072 lds" ::"s"(ldsaddr), "v"(voff), "s"(r), "s"(soff) : "memory");
}
DI void glds4x1(srd_t r, unsigned voff, unsigned soff, unsigned ldsaddr) {
  asm volatile("s_mov_b32 m0, %0\n\ts_nop 0\n\tbuffer_load_dwordx4 %1, %2, %3 offen sc1 lds\n\tbuffer_load_dwordx4 %1, %2, %3 offen offset:1024 sc1 lds\n\t"
               "buffer_load_dwordx4 %1, %2, %3 offen offset:2048 sc1 lds\n\tbuffer_load_dwordx4 %1, %2, %3 offen offset:3072 sc1 lds" ::"s"(ldsaddr), "v"(voff), "s"(r), "s"(soff) : "memory");
}
DI void glds2x1(srd_t r, unsigned voff, unsigned soff, unsigned ldsaddr) {
  asm volatile("s_mov_b32 m0, %0\n\ts_nop 0\n\tbuffer_load_dwordx4 %1, %2, %3 offen sc1 lds\n\tbuffer_load_dwordx4 %1, %2, %3 offen offset:1024 sc1 lds" ::"s"(ldsaddr), "v"(voff), "s"(r), "s"(soff) : "memory");
}
DI v4u ld16(srd_t r, unsigned voff, unsigned soff) { return __builtin_amdgcn_raw_buffer_load_b128(r, voff, soff, 16); }
DI v2u ld8(srd_t r, unsigned voff, unsigned soff) { return __builtin_amdgcn_raw_buffer_load_b64(r, voff, soff, 16); }
DI v4u ldp16(srd_t r, unsigned voff, unsigned soff) { return __builtin_amdgcn_raw_buffer_load_b128(r, voff, soff, 0); }
DI void stp16(srd_t r, v4u v, unsigned off) { __builtin_amdgcn_raw_buffer_store_b128(v, r, off, 0, 0); }
DI void st16(srd_t r, v4u v, unsigned off) { __builtin_amdgcn_raw_buffer_store_b128(v, r, off, 0, 16); }
DI void st8(srd_t r, v2u v, unsigned off) { __builtin_amdgcn_raw_buffer_store_b64(v, r, off, 0, 16); }
DI float sum16(float v) { auto r = __builtin_amdgcn_permlane16_swap(__float_as_uint(v), __float_as_uint(v), false, false); return __uint_as_float(r[0]) + __uint_as_float(r[1]); }
DI float sum32(float v) { auto r = __builtin_amdgcn_permlane32_swap(__float_as_uint(v), __float_as_uint(v), false, false); return __uint_as_float(r[0]) + __uint_as_float(r[1]); }
DI float max32(float v) { auto r = __builtin_amdgcn_permlane32_swap(__float_as_uint(v), __float_as_uint(v), false, false); return fmaxf(__uint_as_float(r[0]), __uint_as_float(r[1])); }
DI float sumg(float v) { return sum32(sum16(v)); }
#define RLX_AGENT __ATOMIC_RELAXED, __HIP_MEMORY_SCOPE_AGENT
typedef unsigned long long u64;
DI void arrive(char* ws, unsigned off, u64 tag) { (void)__hip_atomic_exchange((GAS u64*)(ws + off), tag, RLX_AGENT); }
DI void wait_words(char* ws, unsigned off, int n, int lane, u64 tag) {
  GAS u64* p = (GAS u64*)(ws + off) + (lane < n ? lane : 0);
  for (unsigned spins = 0; spins < (1u << 17); ++spins)
    if (__all(__hip_atomic_load(p, RLX_AGENT) == tag)) break;
  CFENCE();
}
template <int C> DI float dpp(float v) { return __uint_as_float(__builtin_amdgcn_update_dpp(0u, __float_as_uint(v), C, 0xf, 0xf, false)); }
DI float rowsum16(float v) { v += dpp<0x128>(v); v += dpp<0x124>(v); v += dpp<0x122>(v); v += dpp<0x121>(v); return v; }
#define PIN(x) asm volatile("" : "+v"(x))
#define STAMP(k)
#define STAMP_FLUSH(o)
#define CSTAMP(k)
DI float silu(float v) { return v * __builtin_amdgcn_rcpf(1.f + __builtin_amdgcn_exp2f(-1.4426950408889634f * v)); }

DI v8s trfragA(unsigned img, int half, int lane) {
  const int g = lane >> 4, q = (lane & 15) >> 2, pp = lane & 3;
  const unsigned a = img + (4 * g + q) * 64 + ((half ^ (g & 1)) * 32) + pp * 8;
  return cat(trd(a), trd(a + 1024));
}

template <bool FULL>
DI void attention(int t, int hd, int kt0, int lane, f16v mU, f16v mL, f16v& O, srd_t rWS, unsigned wo_src, unsigned w1_src, int w, const u16* xres, v2u& xr0, v2u& xr1, unsigned& stampv) {
  const int qr = lane & 31, h = lane >> 5, blk = (lane >> 4) & 1, q = (lane & 15) >> 2, pp = lane & 3;
  const v8s Qf = lds16(L_AQ + (hd * 2 + h) * 2048 + (32 * t + qr) * 16);
  const unsigned kb = opq(L_AK + (hd * 2 + h) * KPL + (32 * t + qr) * 16);
  const unsigned vb = opq((blk == 0 ? L_AV + hd * VHD + (pp >> 1) * VPL + (pp & 1) * 8 + (4 * h + q) * 16 : L_ONE + ((hd * 128 + (h ? 0 : 128)) & 255)) + t * 512);
  const f16v zero = {};
#define RIDE(n) do { if ((n) < 2) glds4x1(rWS, lane * 16, wo_src + (w * 2 + (n)) * 4096, L_WO + (w * 2 + (n)) * 4096); \
                     else glds4x1(rWS, lane * 16, w1_src + w * 4096, L_W1 + w * 4096); } while (0)
  if (FULL) {
    v8s Kf[9];
#pragma unroll
    for (int kt = 0; kt < 9; ++kt) Kf[kt] = lds16(kb + kt * 512);
    v8s V0 = cat(trd(vb), trd(vb + 128)), V1 = cat(trd(vb + 256), trd(vb + 384));
    f16v S[2];
    float m0 = -3e38f, m1 = -3e38f;
    S[0] = MFMA32(Kf[0], Qf, mL);
    SB();
#pragma unroll
    for (int kt = 0; kt < 9; ++kt) {
      if (kt < 8) S[(kt + 1) & 1] = MFMA32(Kf[kt + 1], Qf, kt + 1 == 8 ? mU : zero);
      SB();
#pragma unroll
      for (int r = 0; r < 16; r += 4) { m0 = fmaxf(fmaxf(m0, S[kt & 1][r]), S[kt & 1][r + 1]); m1 = fmaxf(fmaxf(m1, S[kt & 1][r + 2]), S[kt & 1][r + 3]); }
      SB();
    }
    const float m = max32(fmaxf(m0, m1));
    STAMP(23);
    f16v nm;
#pragma unroll
    for (int r = 0; r < 16; ++r) { nm[r] = -m; mU[r] -= m; mL[r] -= m; }
    O = zero;
    S[0] = MFMA32(Kf[0], Qf, mL);
    SB();
#pragma unroll
    for (int kt = 0; kt < 9; ++kt) {
      if (kt < 8) S[(kt + 1) & 1] = MFMA32(Kf[kt + 1], Qf, kt + 1 == 8 ? mU : nm);
      if (kt == 0) RIDE(0);
      if (kt == 3) RIDE(1);
      if (kt == 6) RIDE(2);
      SB();
      v4u P0, P1;
#pragma unroll
      for (int r = 0; r < 4; ++r) P0[r] = pk(__builtin_amdgcn_exp2f(S[kt & 1][2 * r]), __builtin_amdgcn_exp2f(S[kt & 1][2 * r + 1]));
      O = MFMA32(V0, asv8(P0), O);
      if (kt < 8) V0 = cat(trd(vb + (kt + 1) * 512), trd(vb + (kt + 1) * 512 + 128));
      if (kt == 2) xr0 = *(const v2u*)xres;
      if (kt == 5) xr1 = *(const v2u*)(xres + 16);
      SB();
#pragma unroll
      for (int r = 0; r < 4; ++r) P1[r] = pk(__builtin_amdgcn_exp2f(S[kt & 1][8 + 2 * r]), __builtin_amdgcn_exp2f(S[kt & 1][8 + 2 * r + 1]));
      O = MFMA32(V1, asv8(P1), O);
      if (kt < 8) V1 = cat(trd(vb + (kt + 1) * 512 + 256), trd(vb + (kt + 1) * 512 + 384));
      SB();
    }
  } else {
#pragma unroll
    for (int n = 0; n < 3; ++n) RIDE(n);
    xr0 = *(const v2u*)xres; xr1 = *(const v2u*)(xres + 16);
    float m = -3e38f;
#pragma unroll
    for (int kt = 0; kt < 9; ++kt) {
      if (kt >= kt0) {
        const f16v S = MFMA32(lds16(kb + kt * 512), Qf, kt == 8 ? mU : zero);
#pragma unroll
        for (int r = 0; r < 16; r += 2) m = fmaxf(fmaxf(m, S[r]), S[r + 1]);
      }
    }
    m = max32(m);
    f16v nm;
#pragma unroll
    for (int r = 0; r < 16; ++r) { nm[r] = -m; mU[r] -= m; }
    O = zero;
#pragma unroll
    for (int kt = 0; kt < 9; ++kt) {
      if (kt >= kt0) {
        const f16v S = MFMA32(lds16(kb + kt * 512), Qf, kt == 8 ? mU : nm);
        v4u P0, P1;
#pragma unroll
        for (int r = 0; r < 4; ++r) {
          P0[r] = pk(__builtin_amdgcn_exp2f(S[2 * r]), __builtin_amdgcn_exp2f(S[2 * r + 1]));
          P1[r] = pk(__builtin_amdgcn_exp2f(S[8 + 2 * r]), __builtin_amdgcn_exp2f(S[8 + 2 * r + 1]));
        }
        const v8s V0 = cat(trd(vb + kt * 512), trd(vb + kt * 512 + 128));
        const v8s V1 = cat(trd(vb + kt * 512 + 256), trd(vb + kt * 512 + 384));
        O = MFMA32(V0, asv8(P0), O);
        O = MFMA32(V1, asv8(P1), O);
      }
    }
  }
#undef RIDE
}

__global__ void __launch_bounds__(512, 2) fused_block(Params p) {
  __shared__ __attribute__((aligned(16))) char smem[163840];
  LAS char* sm = (LAS char*)smem;
  const int tid = threadIdx.x, lane = tid & 63;
  const int w = __builtin_amdgcn_readfirstlane(tid >> 6);
  const int b = blockIdx.x, ci = ((b & 7) >> 1) * 4 + ((b >> 3) & 3), cj = (b & 1) * 8 + (b >> 5);
  const int tok = lane & 15, g = lane >> 4, q4 = (lane & 15) >> 2, pp = lane & 3;
  const int hf = cj & 1, ct = cj >> 1, par = cj & 1;
  const int wq = w < 4 ? w : w - 1;
  unsigned stampv = 0;
  const srd_t rWS = __builtin_amdgcn_make_buffer_rsrc(p.ws, 0, WS_BYTES, 0x00020000);
  const int rA = lane >> 2, pA = lane & 3;
  const unsigned colA = (((pA >> 1) ^ ((rA >> 2) & 1)) * 32) + (pA & 1) * 16;
  const int rB = lane >> 3, pB = lane & 7;
  const unsigned colB = (((pB >> 1) ^ ((rB >> 1) & 3)) * 32) + (pB & 1) * 16;
  STAMP(0);
  CSTAMP(40);
  const unsigned long long pad_t0 = __builtin_amdgcn_s_memrealtime();

  const bool m2 = b < 160;
  const int msl = b % 40, mkq = b / 40;
  const srd_t rWmod = __builtin_amdgcn_make_buffer_rsrc((void*)p.wmod, 0, DM * 3072 * 2, 0x00020000);
  v4u b2v = {0, 0, 0, 0};
  if (w == 6 && lane < 8) b2v = *(const v4u*)(p.b2 + 64 * ct + lane * 8);
  v4u cr;
  v2u ep;
  if (w < 4) {
    __builtin_amdgcn_s_setprio(2);
    asm volatile("global_load_dwordx4 %0, %1, off" : "=v"(cr) : "v"(p.c + 128 * w + 8 * (lane & 15)) : "memory");
    asm volatile("buffer_load_dwordx2 %0, %1, %2, 0 offen sc1" : "=v"(ep) : "v"(WS_EPOCH), "s"(rWS) : "memory");
    const srd_t rX = __builtin_amdgcn_make_buffer_rsrc((void*)p.x, 0, SEQ * DM * 2, 0x00020000);
#pragma unroll
    for (int u = 0; u < 2; ++u) glds(rX, rA * 1024 + pA * 16, (128 * ci + 16 * (2 * w + u)) * 1024 + 64 * cj, L_XS + (2 * w + u) * 1024);
#pragma unroll
    for (int i0 = 0; i0 < 8; ++i0) { const int ii = (i0 + (ci & 3) * 2 + (cj & 1)) & 7; glds(rWmod, rA * 6144 + colA, (w * 8 + ii) * 16 * 6144 + (512 + 32 * cj) * 2, L_CA + (w * 8 + ii) * 1024); }
  } else if (w == 5) {
    __builtin_amdgcn_s_setprio(3);
    const srd_t r = __builtin_amdgcn_make_buffer_rsrc((void*)p.wqkv, 0, DM * 1536 * 2, 0x00020000);
#pragma unroll
    for (int e = 0; e < 6; ++e) glds(r, rA * 3072 + colA, (32 * ci + 16 * (e & 1)) * 3072 + ((e >> 1) * 512 + 32 * cj) * 2, L_SQ + e * 1024);
  }
  if (tid == 256) { *(LAS unsigned*)(uintptr_t)L_CNT = 0u; *(LAS unsigned*)(uintptr_t)(L_CNT + 4) = m2 ? 0u : 1u; *(LAS unsigned*)(uintptr_t)(L_CNT + 8) = 0u; *(LAS unsigned*)(uintptr_t)(L_CNT + 12) = 0u; }
  BAR();
#define READ_TAG() do { const v2u ep_ = ld8(rWS, WS_EPOCH, 0); TAG = (((u64)__builtin_amdgcn_readfirstlane(ep_[1]) << 32) | __builtin_amdgcn_readfirstlane(ep_[0])) + 1; } while (0)
  v8s rt[12];
  f16v mU, mL;
  u64 TAG;
  if (w < 4) {
    asm volatile("s_waitcnt vmcnt(10)" : "+v"(cr), "+v"(ep) :: "memory");
    const unsigned sc = L_SC + w * 256;
    if (lane < 16) *(LAS v4u*)(uintptr_t)(sc + lane * 16) = v4u{pk(silu(bflo(cr[0])), silu(bfhi(cr[0]))), pk(silu(bflo(cr[1])), silu(bfhi(cr[1]))), pk(silu(bflo(cr[2])), silu(bfhi(cr[2]))), pk(silu(bflo(cr[3])), silu(bfhi(cr[3])))};
    LG0();
    v8s sf[4];
#pragma unroll
    for (int s = 0; s < 4; ++s) { const v2u lo = lds8(sc + (32 * s + 4 * g) * 2), hi = lds8(sc + (32 * s + 16 + 4 * g) * 2); sf[s] = asv8(v4u{lo[0], lo[1], hi[0], hi[1]}); }
    TAG = (((u64)__builtin_amdgcn_readfirstlane(ep[1]) << 32) | __builtin_amdgcn_readfirstlane(ep[0])) + 1;
    asm volatile("s_waitcnt vmcnt(8)" ::: "memory");
    v2u xa[2][2];
#pragma unroll
    for (int u = 0; u < 2; ++u) {
      const unsigned a = L_XS + (2 * w + u) * 1024 + tok * 64 + g * 8; xa[u][0] = lds8(a); xa[u][1] = lds8(a + 32);
      const float x0 = bflo(xa[u][0][0]), x1 = bfhi(xa[u][0][0]), x2 = bflo(xa[u][0][1]), x3 = bfhi(xa[u][0][1]);
      const float x4 = bflo(xa[u][1][0]), x5 = bfhi(xa[u][1][0]), x6 = bflo(xa[u][1][1]), x7 = bfhi(xa[u][1][1]);
      const float s1 = sumg(((x0 + x1) + (x2 + x3)) + ((x4 + x5) + (x6 + x7)));
      const float s2 = sumg(((x0 * x0 + x1 * x1) + (x2 * x2 + x3 * x3)) + ((x4 * x4 + x5 * x5) + (x6 * x6 + x7 * x7)));
      if (g == 0) st8(rWS, v2u{__float_as_uint(s1), __float_as_uint(s2)}, WS_ST1 + ((ci * 16 + cj) * 128 + 16 * (2 * w + u) + tok) * 8);
    }
    asm volatile("s_waitcnt vmcnt(2)" ::: "memory");
    STAMP(1);
    f4 a0 = {0, 0, 0, 0}, a1 = {0, 0, 0, 0};
    {
      v8s W[4][2];
#pragma unroll
      for (int s = 0; s < 4; ++s) { W[s][0] = trfragA(L_CA + (4 * w + s) * 2048, 0, lane); W[s][1] = trfragA(L_CA + (4 * w + s) * 2048, 1, lane); }
#pragma unroll
      for (int s = 0; s < 4; ++s) { a0 = MFMA16(W[s][0], sf[s], a0); a1 = MFMA16(W[s][1], sf[s], a1); }
    }
    if (tok == 0) { *(LAS f4*)(uintptr_t)(L_CAP + (w * 32 + 4 * g) * 4) = a0; *(LAS f4*)(uintptr_t)(L_CAP + (w * 32 + 16 + 4 * g) * 4) = a1; }
    LG0();
    if (lane == 0) __hip_atomic_fetch_add((LAS unsigned*)(uintptr_t)L_CNT, 1u, __ATOMIC_RELAXED, __HIP_MEMORY_SCOPE_WORKGROUP);
    while (*(volatile LAS unsigned*)(uintptr_t)L_CNT < 4u) {}
    CFENCE();
    STAMP(19);
    a0 = ldsf4(L_CAP + 4 * g * 4); a1 = ldsf4(L_CAP + (16 + 4 * g) * 4);
#pragma unroll
    for (int e = 1; e < 4; ++e) { a0 += ldsf4(L_CAP + (e * 32 + 4 * g) * 4); a1 += ldsf4(L_CAP + (e * 32 + 16 + 4 * g) * 4); }
    float wv[8];
#pragma unroll
    for (int r = 0; r < 4; ++r) { wv[r] = rbf(1.f + rbf(a0[r])); wv[4 + r] = rbf(1.f + rbf(a1[r])); }
#pragma unroll
    for (int u = 0; u < 2; ++u) {
      const float x0 = bflo(xa[u][0][0]), x1 = bfhi(xa[u][0][0]), x2 = bflo(xa[u][0][1]), x3 = bfhi(xa[u][0][1]);
      const float x4 = bflo(xa[u][1][0]), x5 = bfhi(xa[u][1][0]), x6 = bflo(xa[u][1][1]), x7 = bfhi(xa[u][1][1]);
      const int T = 2 * w + u;
      st16(rWS, v4u{pk(x0 * wv[0], x1 * wv[1]), pk(x2 * wv[2], x3 * wv[3]), pk(x4 * wv[4], x5 * wv[5]), pk(x6 * wv[6], x7 * wv[7])},
           WS_HF + ((ci * 8 + T) * 16 + cj) * 1024 + lane * 16);
      if (u == 0 && w == 0 && tok == 0) {
        st8(rWS, v2u{pk(wv[0], wv[1]), pk(wv[2], wv[3])}, WS_WV + ci * 1024 + cj * 64 + g * 8);
        st8(rWS, v2u{pk(wv[4], wv[5]), pk(wv[6], wv[7])}, WS_WV + ci * 1024 + cj * 64 + 32 + g * 8);
      }
    }
    STAMP(21);
    VM0();
    if (lane == 0) { arrive(p.ws, WS_F0 + (ci * 64 + cj * 4 + w) * 8, TAG); __hip_atomic_fetch_add((LAS unsigned*)(uintptr_t)(L_CNT + 8), 1u, __ATOMIC_RELAXED, __HIP_MEMORY_SCOPE_WORKGROUP); }
    __builtin_amdgcn_s_setprio(0);
  } else if (w == 4) {
    if (m2) {
      const int mvec = msl >> 3, mcol = (mvec == 0 ? 0 : 512 + 512 * mvec) + (msl & 7) * 64;
      __builtin_amdgcn_s_sleep(8);
#pragma unroll
      for (int ii = 0; ii < 16; ++ii) glds(rWmod, rB * 6144 + colB, (128 * mkq + 8 * ii) * 6144 + mcol * 2, L_M2 + ii * 1024);
      const v4u cr = *(const v4u*)(p.c + 128 * mkq + 8 * (lane & 15));
      const unsigned sc = L_SC + 1024;
      if (lane < 16) *(LAS v4u*)(uintptr_t)(sc + lane * 16) = v4u{pk(silu(bflo(cr[0])), silu(bfhi(cr[0]))), pk(silu(bflo(cr[1])), silu(bfhi(cr[1]))), pk(silu(bflo(cr[2])), silu(bfhi(cr[2]))), pk(silu(bflo(cr[3])), silu(bfhi(cr[3])))};
      LG0();
      v8s sf[4];
#pragma unroll
      for (int s = 0; s < 4; ++s) { const v2u lo = lds8(sc + (32 * s + 4 * g) * 2), hi = lds8(sc + (32 * s + 16 + 4 * g) * 2); sf[s] = asv8(v4u{lo[0], lo[1], hi[0], hi[1]}); }
      VM0();
      f4 a[4];
#pragma unroll
      for (int nt = 0; nt < 4; ++nt) a[nt] = f4{0, 0, 0, 0};
      const int row = 4 * g + q4;
#pragma unroll
      for (int s = 0; s < 4; ++s) {
        v8s W[4];
#pragma unroll
        for (int nt = 0; nt < 4; ++nt) { const unsigned ad = L_M2 + s * 4096 + row * 128 + ((nt ^ ((row >> 1) & 3)) * 32) + pp * 8; W[nt] = cat(trd(ad), trd(ad + 2048)); }
#pragma unroll
        for (int nt = 0; nt < 4; ++nt) a[nt] = MFMA16(W[nt], sf[s], a[nt]);
      }
      if (tok == 0) {
#pragma unroll
        for (int nt = 0; nt < 4; ++nt)
          st16(rWS, v4u{__float_as_uint(a[nt][0]), __float_as_uint(a[nt][1]), __float_as_uint(a[nt][2]), __float_as_uint(a[nt][3])},
               WS_MOD + mkq * 10240 + mvec * 2048 + ((msl & 7) * 2 + (nt >> 1)) * 128 + g * 32 + (nt & 1) * 16);
      }
      LG0();
      if (lane == 0) *(volatile LAS unsigned*)(uintptr_t)(L_CNT + 4) = 1u;
      READ_TAG();
      VM0();
      if (lane == 0) arrive(p.ws, WS_FM + b * 8, TAG);
    } else READ_TAG();
    STAMP(1);
  } else {
    if (w == 5) {
      READ_TAG();
      VM0();
#pragma unroll
      for (int nt = 0; nt < 6; ++nt) st16(rWS, asv4u(trfragA(L_SQ + (nt >> 1) * 2048, nt & 1, lane)), WS_TQ + ((cj * 6 + nt) * 16 + ci) * 1024 + lane * 16);
      VM0();
      if (lane == 0) arrive(p.ws, WS_FQ + (cj * 16 + ci) * 8, TAG);
      __builtin_amdgcn_s_setprio(0);
    } else {
      READ_TAG();
      __builtin_amdgcn_s_sleep(16);
      if (w == 6) {
        const srd_t r1 = __builtin_amdgcn_make_buffer_rsrc((void*)p.w1, 0, DM * FF * 2, 0x00020000);
#pragma unroll
        for (int e = 0; e < 6; ++e) glds(r1, rA * (FF * 2) + colA, (32 * ci + 16 * (e & 1)) * (FF * 2) + (112 * cj + 16 * par + (e >> 1) * 32) * 2, L_S1 + e * 1024);
        glds(r1, (lane >> 1) * (FF * 2) + (lane & 1) * 16, 32 * ci * (FF * 2) + (112 * cj + (par ? 0 : 96)) * 2, L_S1 + 6144);
        VM0();
#pragma unroll
        for (int nt = 0; nt < 6; ++nt) rt[nt] = trfragA(L_S1 + (nt >> 1) * 2048, nt & 1, lane);
        { const unsigned a = L_S1 + 6144 + (4 * g + q4) * 32 + pp * 8; rt[6] = cat(trd(a), trd(a + 512)); }
      } else {
        const srd_t ro = __builtin_amdgcn_make_buffer_rsrc((void*)p.wo, 0, DM * DM * 2, 0x00020000);
#pragma unroll
        for (int e = 0; e < 4; ++e) glds(ro, rB * 1024 + colB, (32 * ((b & 127) >> 3) + 8 * e) * 1024 + (b & 7) * 128, L_SO + e * 1024);
        const srd_t r2 = __builtin_amdgcn_make_buffer_rsrc((void*)p.w2, 0, FF * DM * 2, 0x00020000);
        const int b2 = b < 224 ? b : 0;
#pragma unroll
        for (int e = 0; e < 8; ++e) { const int id = 2 * b2 + (e >> 2); glds(r2, rB * 1024 + colB, (32 * (id >> 3) + 8 * (e & 3)) * 1024 + (id & 7) * 128, L_SO + 4096 + e * 1024); }
        VM0();
        {
          const int row = 16 * (g >> 1) + 4 * (g & 1) + q4;
#pragma unroll
          for (int nt = 0; nt < 4; ++nt) { const unsigned a = L_SO + row * 128 + ((nt ^ ((row >> 1) & 3)) * 32) + pp * 8; rt[8 + nt] = cat(trd(a), trd(a + 1024)); }
        }
        const int row = 4 * g + q4;
#pragma unroll
        for (int e = 0; e < 8; ++e) { const int nt = e & 3; const unsigned a = L_SO + 4096 + (e >> 2) * 4096 + row * 128 + ((nt ^ ((row >> 1) & 3)) * 32) + pp * 8; rt[e] = cat(trd(a), trd(a + 2048)); }
      }
    }
    STAMP(1);
    if (w >= 6) {
      while (*(volatile LAS unsigned*)(uintptr_t)(L_CNT + 8) < 4u) __builtin_amdgcn_s_sleep(1);
      CFENCE();
      if (w == 6) {
#pragma unroll
        for (int e = 0; e < 7; ++e) st16(rWS, asv4u(rt[e]), WS_TW1 + ((cj * 7 + e) * 16 + ci) * 1024 + lane * 16);
      } else {
        if (b < 224) {
#pragma unroll
          for (int e = 0; e < 8; ++e) { const int id = 2 * b + (e >> 2); st16(rWS, asv4u(rt[e]), WS_TW2 + (((id & 7) * 56 + (id >> 3)) * 4 + (e & 3)) * 1024 + lane * 16); }
        }
        if (b < 128) {
#pragma unroll
          for (int e = 0; e < 4; ++e) st16(rWS, asv4u(rt[8 + e]), WS_TWO + (((b & 7) * 16 + (b >> 3)) * 4 + e) * 1024 + lane * 16);
        }
      }
    }
    wait_words(p.ws, WS_FQ + cj * 128, 16, lane, TAG);
    while (*(volatile LAS unsigned*)(uintptr_t)(L_CNT + 4) == 0u) __builtin_amdgcn_s_sleep(1);
    CFENCE();
    STAMP(19);
#pragma unroll
    for (int e = 0; e < 8; ++e) glds4x1(rWS, lane * 16, WS_TQ + cj * 98304 + ((w - 5) * 8 + ((e + 2 * (ci & 3)) & 7)) * 4096, L_WQKV + ((w - 5) * 8 + ((e + 2 * (ci & 3)) & 7)) * 4096);
  }
  {
    const int qr = lane & 31, h = lane >> 5;
#pragma unroll
    for (int r = 0; r < 16; ++r) { const int kr = (r & 3) + 8 * (r >> 2) + 4 * h; mU[r] = kr > qr ? -1e30f : 0.f; mL[r] = kr > qr ? 0.f : -1e30f; }
    asm volatile("" : "+v"(mU), "+v"(mL));
  }
  if (w == 0) {
    __builtin_amdgcn_s_setprio(3);
    wait_words(p.ws, WS_F0 + ci * 512, 64, lane, TAG);
    if (lane == 0) *(volatile LAS unsigned*)(uintptr_t)(L_CNT + 12) = 1u;
    __builtin_amdgcn_s_setprio(0);
  } else {
    while (*(volatile LAS unsigned*)(uintptr_t)(L_CNT + 12) == 0u) __builtin_amdgcn_s_sleep(1);
    CFENCE();
  }
  STAMP(2);
  v8s act[16];
  const unsigned hsrc = WS_HF + (ci * 8 + w) * 16 * 1024;
  v2u wl[2], wh[2];
#pragma unroll
  for (int u = 0; u < 2; ++u) { const unsigned o = (32 * (2 * w + u) + 4 * g) * 2; wl[u] = ld8(rWS, o, WS_WV + ci * 1024); wh[u] = ld8(rWS, o + 32, WS_WV + ci * 1024); }
#pragma unroll
  for (int s = 0; s < 5; ++s) act[s] = asv8(ld16(rWS, lane * 16, hsrc + s * 1024));
  if (w >= 5) asm volatile("s_waitcnt vmcnt(9)" ::: "memory");
  BAR();
  STAMP(3);
  if (lane == 0) {
    if (w == 6) arrive(p.ws, WS_FW1 + (cj * 16 + ci) * 8, TAG);
    if (w == 7 && b < 128) arrive(p.ws, WS_FWO + ((b & 7) * 16 + (b >> 3)) * 8, TAG);
    if (w == 7 && b < 224) { arrive(p.ws, WS_FW2 + (((2 * b) & 7) * 64 + ((2 * b) >> 3)) * 8, TAG); arrive(p.ws, WS_FW2 + (((2 * b + 1) & 7) * 64 + ((2 * b + 1) >> 3)) * 8, TAG); }
  }

  f4 accQ[2], accKV[4];
  float rstd, nmu;
  v4u gaR, cbR, sbR, gbR;
  v2u b2R[2];
  v4u mc0, md0;
  v4u mx[2];
  const int tt = w & 3, kh = w >> 2;
  const int ocol = 64 * ct + 32 * kh + 4 * g;
  {
    const unsigned bq = opq(L_WQKV + lane * 16), bv = opq(L_WQKV + 65536 + lane * 16);
#define QF(nt, s) lds16(((nt) < 4 ? bq : bv) + ((nt) & 3) * 16384 + (s) * 1024)
    {
      f4 au[6];
      v8s W[2][6];
      const unsigned c0 = opq(bq + w * 2048), d0 = opq(bv + w * 2048);
#pragma unroll
      for (int u = 0; u < 2; ++u)
#pragma unroll
        for (int nt = 0; nt < 6; ++nt) W[u][nt] = lds16((nt < 4 ? c0 : d0) + (nt & 3) * 16384 + u * 1024);
#pragma unroll
      for (int nt = 0; nt < 6; ++nt) au[nt] = f4{0, 0, 0, 0};
#pragma unroll
      for (int u = 0; u < 2; ++u) {
        const v8s a = asv8(v4u{wl[u][0], wl[u][1], wh[u][0], wh[u][1]});
#pragma unroll
        for (int nt = 0; nt < 6; ++nt) au[nt] = MFMA16(W[u][nt], a, au[nt]);
      }
      if (tok == 0) {
#pragma unroll
        for (int nt = 0; nt < 6; ++nt) *(LAS f4*)(sm + L_PARTU + (w * 96 + nt * 16 + 4 * g) * 4) = au[nt];
      }
    }
    BAR();
    if (tid >= 384 && tid < 480) {
      const int e0 = tid - 384;
      float s = 0;
#pragma unroll
      for (int e = 0; e < 8; ++e) s += *(const LAS float*)(sm + L_PARTU + (e * 96 + e0) * 4);
      *(LAS float*)(sm + L_PERS + e0 * 4) = s;
    }
    STAMP(16);
    v2u st[4];
    u64 fm0 = 0, fm1 = 0, fm2 = 0;
#pragma unroll
    for (int nt = 0; nt < 4; ++nt) accKV[nt] = f4{0, 0, 0, 0};
#pragma unroll
    for (int nt = 0; nt < 2; ++nt) accQ[nt] = f4{0, 0, 0, 0};
    {
      v8s W[6];
#pragma unroll
      for (int nt = 0; nt < 6; ++nt) W[nt] = QF(nt, 0);
      SB();
#pragma unroll
      for (int s = 0; s < 16; ++s) {
        if (s + 5 < 16) act[s + 5] = asv8(ld16(rWS, lane * 16, hsrc + (s + 5) * 1024));
        else if (s < 15) st[s - 11] = ld8(rWS, ((4 * g + s - 11) * 128 + 16 * w + tok) * 8, WS_ST1 + ci * 16 * 128 * 8);
        if (s == 12) { GAS u64* f = (GAS u64*)(p.ws + WS_FM); fm0 = __hip_atomic_load(f + lane, RLX_AGENT); fm1 = __hip_atomic_load(f + 64 + lane, RLX_AGENT); fm2 = __hip_atomic_load(f + 128 + (lane & 31), RLX_AGENT); }
#pragma unroll
        for (int nt = 0; nt < 6; ++nt) {
          if (nt < 2) accQ[nt] = MFMA16(W[nt], act[s], accQ[nt]); else accKV[nt - 2] = MFMA16(W[nt], act[s], accKV[nt - 2]);
          if (s + 1 < 16) W[nt] = QF(nt, s + 1);
          SB();
        }
      }
    }
    float s1 = 0, s2 = 0;
#pragma unroll
    for (int e = 0; e < 4; ++e) { s1 += __uint_as_float(st[e][0]); s2 += __uint_as_float(st[e][1]); }
    s1 = sumg(s1); s2 = sumg(s2);
    const float mu = s1 * (1.f / 512), var = fmaxf(s2 * (1.f / 512) - mu * mu, 0.f);
    rstd = __builtin_amdgcn_rsqf(var + 1e-5f); nmu = -rstd * mu;
    if (w == 6 && lane < 8) *(LAS v4u*)(sm + L_B2C + lane * 16) = b2v;
    PIN(rstd); PIN(nmu);
    {
          GAS u64* f = (GAS u64*)(p.ws + WS_FM);
          for (unsigned spins = 0; spins < (1u << 17) && !__all(fm0 == TAG && fm1 == TAG && fm2 == TAG); ++spins) {
            fm0 = __hip_atomic_load(f + lane, RLX_AGENT); fm1 = __hip_atomic_load(f + 64 + lane, RLX_AGENT); fm2 = __hip_atomic_load(f + 128 + (lane & 31), RLX_AGENT); }
          CFENCE();
          if (w < 6) {
            const int vec = w < 2 ? 0 : w < 4 ? 2 : 3, hq = w & 1;
            const unsigned dst = w == 0 ? L_PSA : w == 1 ? L_PSA2 : w == 2 ? L_PSB : w == 3 ? L_PSB2 : w == 4 ? L_PCB : L_PCB2;
            glds2x1(rWS, lane * 16, WS_MOD + (2 * hq) * 10240 + vec * 2048, dst);
            glds2x1(rWS, lane * 16, WS_MOD + (2 * hq + 1) * 10240 + vec * 2048, dst + 2048);
          } else glds1(rWS, (lane >> 4) * 10240 + (lane & 15) * 16, WS_MOD + (w == 6 ? 1 : 4) * 2048 + ct * 256, w == 6 ? L_PGA : L_PGB);
    }
    STAMP(4);
    BAR();
    {
      v4u kk, vv;
      unsigned pkd[4][2];
#pragma unroll
      for (int nt = 0; nt < 4; ++nt) {
        const f4 u = ldsf4(L_PERS + (32 + nt * 16 + 4 * g) * 4);
        pkd[nt][0] = pk(rstd * accKV[nt][0] + nmu * u[0], rstd * accKV[nt][1] + nmu * u[1]);
        pkd[nt][1] = pk(rstd * accKV[nt][2] + nmu * u[2], rstd * accKV[nt][3] + nmu * u[3]);
      }
      { auto r0 = __builtin_amdgcn_permlane16_swap(pkd[0][0], pkd[1][0], false, false); auto r1 = __builtin_amdgcn_permlane16_swap(pkd[0][1], pkd[1][1], false, false);
        kk = v4u{r0[0], r1[0], r0[1], r1[1]}; }
      { auto r0 = __builtin_amdgcn_permlane16_swap(pkd[2][0], pkd[3][0], false, false); auto r1 = __builtin_amdgcn_permlane16_swap(pkd[2][1], pkd[3][1], false, false);
        vv = v4u{r0[0], r1[0], r0[1], r1[1]}; }
      const int hd = g & 1, pl = g >> 1;
      st16(rWS, kk, WS_KV + (ci * 16 + cj) * 16384 + ((0 * 2 + hd) * 2 + pl) * 2048 + (16 * w + tok) * 16);
      st16(rWS, vv, WS_KV + (ci * 16 + cj) * 16384 + ((1 * 2 + hd) * 2 + pl) * 2048 + (16 * w + tok) * 16);
      *(LAS v4u*)(sm + L_AK + (hd * 2 + pl) * KPL + (256 + 16 * w + tok) * 16) = kk;
      *(LAS v4u*)(sm + L_AV + hd * VHD + pl * VPL + (256 + 16 * w + tok) * 16) = vv;
    }
    VM0();
    BAR();
    if (tid == 256) arrive(p.ws, WS_FP + (0 * 16 + ci) * 128 + cj * 8, TAG);
    STAMP(5);
    if (tid < 400) *(LAS v4u*)(sm + L_ONE + tid * 16) = v4u{0x3f803f80u, 0x3f803f80u, 0x3f803f80u, 0x3f803f80u};
    if (tid < 416) {
      const int seg = tid < 384 ? tid >> 7 : 3 + ((tid - 384) >> 4), e = tid < 384 ? tid & 127 : (tid - 384) & 15;
      f4 v;
      if (seg < 3) {
        const unsigned a0 = (seg == 0 ? L_PSA : seg == 1 ? L_PSB : L_PCB) + e * 16, a1 = (seg == 0 ? L_PSA2 : seg == 1 ? L_PSB2 : L_PCB2) + e * 16;
        v = (ldsf4(a0) + ldsf4(a0 + 2048)) + (ldsf4(a1) + ldsf4(a1 + 2048));
      } else {
        const unsigned a0 = (seg == 3 ? L_PGA : L_PGB) + e * 16;
        v = (ldsf4(a0) + ldsf4(a0 + 256)) + (ldsf4(a0 + 512) + ldsf4(a0 + 768));
      }
      *(LAS v2u*)(sm + L_FIN + (seg < 3 ? seg * 1024 : 3072 + (seg - 3) * 128) + e * 8) = v2u{pk(v[0], v[1]), pk(v[2], v[3])};
    }
    BAR();
    {
      f4 av[4];
      v8s W[2][4], a[2];
      const unsigned c0 = opq(bq + w * 2048), d0 = opq(bv + w * 2048);
#pragma unroll
      for (int u = 0; u < 2; ++u) {
        a[u] = lds16(L_FIN + (2 * w + u) * 64 + g * 16);
#pragma unroll
        for (int nt = 0; nt < 4; ++nt) W[u][nt] = lds16((nt < 2 ? c0 : d0) + (nt & 1) * 16384 + u * 1024);
      }
#pragma unroll
      for (int nt = 0; nt < 4; ++nt) av[nt] = f4{0, 0, 0, 0};
#pragma unroll
      for (int u = 0; u < 2; ++u)
#pragma unroll
        for (int nt = 0; nt < 4; ++nt) av[nt] = MFMA16(W[u][nt], a[u], av[nt]);
      if (tok == 0) {
#pragma unroll
        for (int nt = 0; nt < 4; ++nt) *(LAS f4*)(sm + L_PARTV + (w * 64 + nt * 16 + 4 * g) * 4) = av[nt];
      }
    }
    {
      gaR = asv4u(lds16(L_FIN + 3072 + kh * 64 + g * 16)); gbR = asv4u(lds16(L_FIN + 3200 + kh * 64 + g * 16));
      sbR = asv4u(lds16(L_FIN + 1024 + (2 * ct + kh) * 64 + g * 16)); cbR = asv4u(lds16(L_FIN + 2048 + (2 * ct + kh) * 64 + g * 16));
#pragma unroll
      for (int e = 0; e < 2; ++e) b2R[e] = lds8(L_B2C + (32 * kh + 4 * g + 16 * e) * 2);
      mc0 = asv4u(lds16(L_FIN + 2048 + 16 * lane)); md0 = asv4u(lds16(L_FIN + 1024 + 16 * lane));
#pragma unroll
      for (int u = 0; u < 2; ++u) mx[u] = asv4u(lds16(L_FIN + (tok == 1 ? 1024 : 2048) + (2 * w + u) * 64 + g * 16));
      BAR();
      if (tid < 64) {
        float s = 0;
#pragma unroll
        for (int e = 0; e < 8; ++e) s += *(const LAS float*)(sm + L_PARTV + (e * 64 + tid) * 4);
        *(LAS float*)(sm + L_PERS + (96 + tid) * 4) = s;
      }
    }
    if (ci < 2) {
      for (int e = tid; e < 8 * 256; e += 512) {
        const int pln = e >> 8, key = e & 255;
        if (ci == 0 || key < 128) {
          const unsigned a = pln < 4 ? L_AK + pln * KPL : L_AV + ((pln - 4) >> 1) * VHD + (pln & 1) * VPL;
          *(LAS v4u*)(sm + a + key * 16) = v4u{0, 0, 0, 0};
        }
      }
    }
    if (w == 4) {
      GAS u64* f0 = (GAS u64*)(p.ws + (lane == 0 && ci >= 1 ? WS_FP + (ci - 1) * 128 + cj * 8 : lane == 1 && ci >= 2 ? WS_FP + (ci - 2) * 128 + cj * 8
                                       : lane >= 16 && lane < 32 ? WS_FW1 + (cj * 16 + lane - 16) * 8 : WS_FWO + (ct * 16 + (lane & 15)) * 8));
      GAS u64* f1 = (GAS u64*)(p.ws + WS_FW2 + ct * 512) + (lane < 56 ? lane : 0);
      for (unsigned spins = 0; spins < (1u << 17); ++spins) {
        const u64 a = __hip_atomic_load(f0, RLX_AGENT), c = __hip_atomic_load(f1, RLX_AGENT);
        if (__all(a == TAG && c == TAG)) break;
      }
      CFENCE();
    }
    BAR();
    STAMP(6);
    {
      const unsigned dst = w < 4 ? L_AK + w * KPL : L_AV + ((w - 4) >> 1) * VHD + (w & 1) * VPL;
#pragma unroll
      for (int src = 0; src < 2; ++src)
        if (ci - 2 + src >= 0) glds2x1(rWS, lane * 16, WS_KV + ((ci - 2 + src) * 16 + cj) * 16384 + w * 2048, dst + src * 2048);
    }
    {
      const float qs = 0.25f * 1.4426950408889634f;
      unsigned pq[2][2];
#pragma unroll
      for (int nt = 0; nt < 2; ++nt) {
        const f4 u = ldsf4(L_PERS + (nt * 16 + 4 * g) * 4), v = ldsf4(L_PERS + (96 + nt * 16 + 4 * g) * 4);
        pq[nt][0] = pk(qs * (rstd * accQ[nt][0] + nmu * u[0] + v[0]), qs * (rstd * accQ[nt][1] + nmu * u[1] + v[1]));
        pq[nt][1] = pk(qs * (rstd * accQ[nt][2] + nmu * u[2] + v[2]), qs * (rstd * accQ[nt][3] + nmu * u[3] + v[3]));
      }
      auto r0 = __builtin_amdgcn_permlane16_swap(pq[0][0], pq[1][0], false, false); auto r1 = __builtin_amdgcn_permlane16_swap(pq[0][1], pq[1][1], false, false);
      *(LAS v4u*)(sm + L_AQ + ((g & 1) * 2 + (g >> 1)) * 2048 + (16 * w + tok) * 16) = v4u{r0[0], r1[0], r0[1], r1[1]};
    }
    VM0();
    BAR();
    STAMP(7);
  }

  const int orow = 128 * ci + 64 * hf + 16 * tt + tok;
  v2u xr0, xr1;
  {
    const int t = w & 3, hd = w >> 2;
    const int kt0 = ci >= 2 ? 0 : 8 - 4 * ci - t;
    f16v O;
    const u16* xres = p.x + (size_t)orow * DM + ocol;
    if (ci >= 2) attention<true>(t, hd, 0, lane, mU, mL, O, rWS, WS_TWO + ct * 65536, WS_TW1 + cj * 7 * 16384, w, xres, xr0, xr1, stampv);
    else attention<false>(t, hd, kt0, lane, mU, mL, O, rWS, WS_TWO + ct * 65536, WS_TW1 + cj * 7 * 16384, w, xres, xr0, xr1, stampv);
    const int h = lane >> 5;
    const float inv = __builtin_amdgcn_rcpf(O[8]);
    const f4 b0 = ldsf4(L_PERS + (96 + 32 + hd * 16 + 4 * h) * 4), b1 = ldsf4(L_PERS + (96 + 32 + hd * 16 + 8 + 4 * h) * 4);
    const v4u o = {pk(O[0] * inv + b0[0], O[1] * inv + b0[1]), pk(O[2] * inv + b0[2], O[3] * inv + b0[3]),
                   pk(O[4] * inv + b1[0], O[5] * inv + b1[1]), pk(O[6] * inv + b1[2], O[7] * inv + b1[3])};
    const int qr = lane & 31;
    st16(rWS, o, WS_OF + (ci * 8 * 16 + cj) * 1024 + ((2 * t + (qr >> 4)) * 16) * 1024 + ((2 * hd + h) * 16 + (qr & 15)) * 16);
  }
  STAMP(24);
  VM0();
  BAR();
  if (tid == 256) arrive(p.ws, WS_FP + (1 * 16 + ci) * 128 + cj * 8, TAG);
  STAMP(8);
  float b1v = 0.f;
  v8s aux2[2];
  float Cw = 0, Cw2 = 0, Cws = 0, Cs = 0, Cs2 = 0;
#define MODSUMS() do { \
      _Pragma("unroll") for (int r = 0; r < 4; ++r) { \
        const float w0 = rbf(1.f + bflo(mc0[r])), w1 = rbf(1.f + bfhi(mc0[r])), s0 = bflo(md0[r]), s1_ = bfhi(md0[r]); \
        Cw += w0 + w1; Cw2 += w0 * w0 + w1 * w1; Cws += w0 * s0 + w1 * s1_; Cs += s0 + s1_; Cs2 += s0 * s0 + s1_ * s1_; } \
      Cw = sumg(rowsum16(Cw)); Cw2 = sumg(rowsum16(Cw2)); Cws = sumg(rowsum16(Cws)); Cs = sumg(rowsum16(Cs)); Cs2 = sumg(rowsum16(Cs2)); \
      _Pragma("unroll") for (int u = 0; u < 2; ++u) { v4u t; \
        _Pragma("unroll") for (int d = 0; d < 4; ++d) t[d] = tok == 0 ? pk(1.f + bflo(mx[u][d]), 1.f + bfhi(mx[u][d])) : (tok == 1 ? mx[u][d] : 0x3f803f80u); \
        aux2[u] = asv8(t); } \
      asm volatile("" : "+v"(Cw), "+v"(Cw2), "+v"(Cws), "+v"(Cs), "+v"(Cs2), "+v"(aux2[0]), "+v"(aux2[1])); } while (0)
  if (w != 4) {
#pragma unroll
    for (int e = 0; e < 3; ++e) { const int gi = wq + 7 * e; if (gi < 16) glds4x1(rWS, lane * 16, WS_TW1 + (cj * 7 + 2) * 16384 + gi * 4096, L_W1 + 32768 + gi * 4096); }
  }
  MODSUMS();
  if (w == 4) wait_words(p.ws, WS_FP + (1 * 16 + ci) * 128, 16, lane, TAG);
  BAR();
  STAMP(9);

  float x1[8];
  {
    v8s oa[8];
    const unsigned osrc = WS_OF + ((ci * 8 + 4 * hf + tt) * 16 + 8 * kh) * 1024;
#pragma unroll
    for (int s = 0; s < 3; ++s) oa[s] = asv8(ld16(rWS, lane * 16, osrc + s * 1024));
    const unsigned bo = opq(L_WO + kh * 32768 + lane * 16);
    f4 acc[4];
#pragma unroll
    for (int nt = 0; nt < 4; ++nt) acc[nt] = f4{0, 0, 0, 0};
    {
      v8s W[2][4];
#pragma unroll
      for (int s = 0; s < 2; ++s)
#pragma unroll
        for (int nt = 0; nt < 4; ++nt) W[s][nt] = lds16(bo + (s * 4 + nt) * 1024);
      SB();
#pragma unroll
      for (int s = 3; s < 8; ++s) oa[s] = asv8(ld16(rWS, lane * 16, osrc + s * 1024));
      if (tid < 112) { const int nt = tid >> 4; b1v = bflo(p.b1[(nt == 6 ? 112 * cj + (par ? 0 : 96) : 112 * cj + 16 * par + nt * 16) + (tid & 15)]); }
      SB();
#pragma unroll
      for (int s = 0; s < 8; ++s)
#pragma unroll
        for (int nt = 0; nt < 4; ++nt) {
          acc[nt] = MFMA16(W[s & 1][nt], oa[s], acc[nt]);
          if (s + 2 < 8) W[s & 1][nt] = lds16(bo + ((s + 2) * 4 + nt) * 1024);
          SB();
        }
    }
    STAMP(26);
    VM0();
    BAR();
    *(LAS f4*)(sm + L_XCH + w * 2048 + lane * 16) = kh ? acc[0] : acc[2];
    *(LAS f4*)(sm + L_XCH + w * 2048 + 1024 + lane * 16) = kh ? acc[1] : acc[3];
    BAR();
    const f4 o0 = ldsf4(L_XCH + (w ^ 4) * 2048 + lane * 16), o1 = ldsf4(L_XCH + (w ^ 4) * 2048 + 1024 + lane * 16);
    const f4 t0 = (kh ? acc[2] : acc[0]) + o0, t1 = (kh ? acc[3] : acc[1]) + o1;
    const float xin[8] = {bflo(xr0[0]), bfhi(xr0[0]), bflo(xr0[1]), bfhi(xr0[1]), bflo(xr1[0]), bfhi(xr1[0]), bflo(xr1[1]), bfhi(xr1[1])};
    const float ga[8] = {bflo(gaR[0]), bfhi(gaR[0]), bflo(gaR[1]), bfhi(gaR[1]), bflo(gaR[2]), bfhi(gaR[2]), bflo(gaR[3]), bfhi(gaR[3])};
    const float cb[8] = {bflo(cbR[0]), bfhi(cbR[0]), bflo(cbR[1]), bfhi(cbR[1]), bflo(cbR[2]), bfhi(cbR[2]), bflo(cbR[3]), bfhi(cbR[3])};
    const float sv[8] = {bflo(sbR[0]), bfhi(sbR[0]), bflo(sbR[1]), bfhi(sbR[1]), bflo(sbR[2]), bfhi(sbR[2]), bflo(sbR[3]), bfhi(sbR[3])};
    float wv[8], xw[8];
#pragma unroll
    for (int r = 0; r < 8; ++r) { x1[r] = rbf(xin[r] + ga[r] * (r < 4 ? t0[r & 3] : t1[r & 3])); wv[r] = rbf(1.f + cb[r]); xw[r] = x1[r] * wv[r]; }
    st16(rWS, v4u{pk(xw[0], xw[1]), pk(xw[2], xw[3]), pk(xw[4], xw[5]), pk(xw[6], xw[7])}, WS_X1F + ((ci * 8 + 4 * hf + tt) * 16 + 2 * ct + kh) * 1024 + lane * 16);
    SB();
    float s1 = 0, s2 = 0, s3 = 0, s4 = 0, s5 = 0, s6 = 0;
#pragma unroll
    for (int r = 0; r < 8; ++r) { s1 += x1[r]; s2 += x1[r] * x1[r]; s3 += xw[r]; s4 += xw[r] * wv[r]; s5 += xw[r] * xw[r]; s6 += xw[r] * sv[r]; }
    s1 = sumg(s1); s2 = sumg(s2); s3 = sumg(s3); s4 = sumg(s4); s5 = sumg(s5); s6 = sumg(s6);
    const unsigned rec = (ci * 16 + 2 * ct + kh) * 128 + 64 * hf + 16 * tt + tok;
    if (g == 0) st16(rWS, v4u{__float_as_uint(s1), __float_as_uint(s2), __float_as_uint(s3), __float_as_uint(s4)}, WS_ST2A + rec * 16);
    if (g == 1) st8(rWS, v2u{__float_as_uint(s5), __float_as_uint(s6)}, WS_ST2B + rec * 8);
  }
  if (w < 4) {
    glds4x1(rWS, lane * 16, WS_TW1 + (cj * 7 + 6) * 16384 + w * 4096, L_W1S + w * 4096);
    glds4x1(rWS, lane * 16, WS_TW2 + ct * 229376 + 7 * 16384 + w * 4096, 49152 + w * 4096);
    asm volatile("s_waitcnt vmcnt(8)" ::: "memory");
  } else {
    glds4x1(rWS, lane * 16, WS_TW2 + ct * 229376 + (w - 4) * 4096, 32768 + (w - 4) * 4096);
    asm volatile("s_waitcnt vmcnt(4)" ::: "memory");
  }
  BAR();
  if (tid == 256) arrive(p.ws, WS_FP + (2 * 16 + ci) * 128 + cj * 8, TAG);
  STAMP(10);
  if (w == 4) asm volatile("s_waitcnt vmcnt(1)" ::: "memory"); else VM0();
  BAR();

  v4u stg[8];
  {
    const unsigned bP = opq(L_W1 + lane * 16), bR = opq(L_W1 + 65536 + lane * 16), bS = opq(L_W1S + lane * 16);
#define W1F(nt, s) lds16((nt) == 6 ? bS + (s) * 1024 : (nt) < 4 ? bP + (nt) * 16384 + (s) * 1024 : bR + ((nt) - 4) * 16384 + (s) * 1024)
    {
      f4 au[7];
#pragma unroll
      for (int nt = 0; nt < 7; ++nt) au[nt] = f4{0, 0, 0, 0};
      const unsigned c0 = opq(bP + w * 2048), d0 = opq(bR + w * 2048), e0 = opq(bS + w * 2048);
#pragma unroll
      for (int u = 0; u < 2; ++u) {
        v8s W[7];
#pragma unroll
        for (int nt = 0; nt < 7; ++nt) W[nt] = lds16(nt == 6 ? e0 + u * 1024 : nt < 4 ? c0 + nt * 16384 + u * 1024 : d0 + (nt - 4) * 16384 + u * 1024);
#pragma unroll
        for (int nt = 0; nt < 7; ++nt) au[nt] = MFMA16(W[nt], aux2[u], au[nt]);
      }
      if (tok < 3) {
#pragma unroll
        for (int nt = 0; nt < 7; ++nt) *(LAS f4*)(sm + L_XCH + ((w * 3 + tok) * 112 + nt * 16 + 4 * g) * 4) = au[nt];
      }
    }
    BAR();
    if (tid < 112) *(LAS float*)(sm + L_XF + (336 + tid) * 4) = b1v;
    {
      const int e0 = w < 4 ? tid : tid - 64;
      if (w != 4 && e0 < 336) {
        float s = 0;
#pragma unroll
        for (int e = 0; e < 8; ++e) s += *(const LAS float*)(sm + L_XCH + (e * 336 + e0) * 4);
        *(LAS float*)(sm + L_XF + e0 * 4) = s;
      }
    }
    if (w == 4) wait_words(p.ws, WS_FP + (2 * 16 + ci) * 128, 16, lane, TAG);
    BAR();
    STAMP(11);
    v8s act2[16];
    const unsigned asrc = WS_X1F + (ci * 8 + w) * 16 * 1024;
#pragma unroll
    for (int s = 0; s < 4; ++s) act2[s] = asv8(ld16(rWS, lane * 16, asrc + s * 1024));
    v4u sa[4]; v2u sb[4];
    f4 acc[7];
#pragma unroll
    for (int nt = 0; nt < 7; ++nt) acc[nt] = f4{0, 0, 0, 0};
    {
      v8s W[6];
#pragma unroll
      for (int nt = 0; nt < 6; ++nt) W[nt] = W1F(nt, 0);
      SB();
#pragma unroll
      for (int s = 0; s < 16; ++s) {
        if (s == 0) act2[4] = asv8(ld16(rWS, lane * 16, asrc + 4 * 1024));
        if (s + 5 < 16) act2[s + 5] = asv8(ld16(rWS, lane * 16, asrc + (s + 5) * 1024));
        else if (s < 15) { sa[s - 11] = ld16(rWS, ((4 * g + s - 11) * 128 + 16 * w + tok) * 16, WS_ST2A + ci * 16 * 128 * 16);
                           sb[s - 11] = ld8(rWS, ((4 * g + s - 11) * 128 + 16 * w + tok) * 8, WS_ST2B + ci * 16 * 128 * 8); }
#pragma unroll
        for (int nt = 0; nt < 6; ++nt) {
          acc[nt] = MFMA16(W[nt], act2[s], acc[nt]);
          if (s + 1 < 16) W[nt] = W1F(nt, s + 1);
          SB();
        }
      }
    }
    STAMP(18);
    CSTAMP(42);
    BAR();
    v8s WB[4];
#pragma unroll
    for (int s = 0; s < 4; ++s) WB[s] = W1F(6, s);
    float s1 = 0, s2 = 0, s3 = 0, s4 = 0, s5 = 0, s6 = 0;
#pragma unroll
    for (int e = 0; e < 4; ++e) { s1 += __uint_as_float(sa[e][0]); s2 += __uint_as_float(sa[e][1]); s3 += __uint_as_float(sa[e][2]); s4 += __uint_as_float(sa[e][3]); s5 += __uint_as_float(sb[e][0]); s6 += __uint_as_float(sb[e][1]); }
    s1 = sumg(s1); s2 = sumg(s2); s3 = sumg(s3); s4 = sumg(s4); s5 = sumg(s5); s6 = sumg(s6);
    const float mu1 = s1 * (1.f / 512), var1 = fmaxf(s2 * (1.f / 512) - mu1 * mu1, 0.f), r1 = __builtin_amdgcn_rsqf(var1 + 1e-5f);
    const float mu2 = (r1 * (s3 - mu1 * Cw) + Cs) * (1.f / 512);
    const float hh = (r1 * r1 * (s5 - 2.f * mu1 * s4 + mu1 * mu1 * Cw2) + 2.f * r1 * (s6 - mu1 * Cws) + Cs2) * (1.f / 512);
    const float r2 = __builtin_amdgcn_rsqf(fmaxf(hh - mu2 * mu2, 0.f) + 1e-5f);
    const float cA = r2 * r1, cB = -cA * mu1, cC = r2, cD = -r2 * mu2;
    const unsigned S0 = (7 * cj + par) >> 1;
    const unsigned hb = WS_H1F + (ci * 8 + w) * 56 * 1024;
    const unsigned xf = opq(L_XF + 16 * g);
#define BIAS4(nt) __builtin_elementwise_fma(f4{cB, cB, cB, cB}, ldsf4(xf + (nt) * 64), __builtin_elementwise_fma(f4{cC, cC, cC, cC}, ldsf4(xf + 448 + (nt) * 64), \
                    __builtin_elementwise_fma(f4{cD, cD, cD, cD}, ldsf4(xf + 896 + (nt) * 64), ldsf4(xf + 1344 + (nt) * 64))))
#define GELU4(o, a, bias) do { const f4 z_ = __builtin_elementwise_fma(f4{cA, cA, cA, cA}, a, bias); const f4 q_ = __builtin_elementwise_fma(z_ * z_, f4{-0.1029432397f, -0.1029432397f, -0.1029432397f, -0.1029432397f}, \
        f4{-2.3022081985f, -2.3022081985f, -2.3022081985f, -2.3022081985f}) * z_; f4 e_; \
        _Pragma("unroll") for (int r = 0; r < 4; ++r) e_[r] = __builtin_amdgcn_exp2f(q_[r]); \
        e_ += f4{1.f, 1.f, 1.f, 1.f}; \
        _Pragma("unroll") for (int r = 0; r < 4; ++r) e_[r] = __builtin_amdgcn_rcpf(e_[r]); \
        o = z_ * e_; } while (0)
    {
      unsigned hp[2][2];
      f4 bias = BIAS4(0);
      PIN(bias);
      SB();
#pragma unroll
      for (int nt = 0; nt < 6; ++nt) {
#pragma unroll
        for (int k = (nt * 16) / 6; k < ((nt + 1) * 16) / 6; ++k) { acc[6] = MFMA16(WB[k & 3], act2[k], acc[6]); if (k + 4 < 16) WB[k & 3] = W1F(6, k + 4); }
        if ((nt & 1) == 0) { const int gi = w * 3 + (nt >> 1), it = 1 + (gi >> 3), khh = (gi >> 2) & 1, pc4 = gi & 3;
          glds4x1(rWS, lane * 16, WS_TW2 + ct * 229376 + (khh * 7 + it) * 16384 + pc4 * 4096, 65536u + (it - 1) * 32768u + khh * 16384u + pc4 * 4096u); }
        if (nt >= 1 && nt < 5) {
#pragma unroll
          for (int e = 2 * (nt - 1); e < 2 * nt; ++e) { const int pc = w * 8 + e; const int chunk = (pc >> 4) == 0 ? 5 : ((pc >> 4) == 1 ? 12 : ((pc >> 4) == 2 ? 6 : 13));
            stg[e] = ld16(rWS, lane * 16, WS_TW2 + ct * 229376 + chunk * 16384 + (pc & 15) * 1024); }
        }
        f4 nb = bias;
        if (nt < 5) nb = BIAS4(nt + 1); else nb = BIAS4(6);
        f4 o;
        GELU4(o, acc[nt], bias);
        hp[nt & 1][0] = pk(o[0], o[1]); hp[nt & 1][1] = pk(o[2], o[3]);
        PIN(hp[nt & 1][0]); PIN(hp[nt & 1][1]); PIN(nb);
        bias = nb;
        if (nt & 1) st16(rWS, v4u{hp[0][0], hp[0][1], hp[1][0], hp[1][1]}, hb + (S0 + (nt >> 1)) * 1024 + lane * 16);
        SB();
      }
      STAMP(20);
      CSTAMP(43);
      f4 o;
      GELU4(o, acc[6], bias);
      const unsigned NT = 7 * cj + (par ? 0 : 6);
      st8(rWS, v2u{pk(o[0], o[1]), pk(o[2], o[3])}, hb + (NT >> 1) * 1024 + (NT & 1) * 512 + lane * 8);
    }
  }
  STAMP(25);
  VM0();
  BAR();
  if (tid == 256) arrive(p.ws, WS_FP + (3 * 16 + ci) * 128 + cj * 8, TAG);
  STAMP(13);
  if (w != 4) {
#pragma unroll
    for (int e = 0; e < 2; ++e) {
      const int gi = wq + 7 * e;
      if (gi < 8) glds4x1(rWS, lane * 16, WS_TW2 + ct * 229376 + ((gi >> 2) * 7 + 4) * 16384 + (gi & 3) * 4096, (gi >> 2) * 16384u + (gi & 3) * 4096u);
    }
  } else {
    if (b == 0) {
      GAS u64* f = (GAS u64*)(p.ws + WS_F0) + lane;
      for (unsigned spins = 0; spins < (1u << 17); ++spins) {
        bool ok = true;
#pragma unroll
        for (int i = 0; i < 16; ++i) ok &= __hip_atomic_load(f + i * 64, RLX_AGENT) == TAG;
        if (__all(ok)) break;
      }
      if (lane == 0) __hip_atomic_store((GAS u64*)(p.ws + WS_EPOCH), TAG, RLX_AGENT);
    }
    wait_words(p.ws, WS_FP + (3 * 16 + ci) * 128, 16, lane, TAG);
  }
  BAR();
  STAMP(14);

  {
    v8s ha[28];
    const unsigned hb = WS_H1F + ((ci * 8 + 4 * hf + tt) * 56 + 28 * kh) * 1024;
#define HLOAD(s) do { if ((s) % 7 == 3) { const v2u lo_ = ld8(rWS, lane * 8, hb + (s) * 1024), hi_ = ld8(rWS, lane * 8 + 512, hb + (s) * 1024); ha[s] = asv8(v4u{lo_[0], lo_[1], hi_[0], hi_[1]}); } \
                      else ha[s] = asv8(ld16(rWS, lane * 16, hb + (s) * 1024)); } while (0)
#pragma unroll
    for (int s = 0; s < 3; ++s) HLOAD(s);
    const unsigned bw = opq(kh * 16384 + lane * 16), bw2 = opq(bw + 65536), bw3 = opq(bw + 131072);
    f4 acc[4];
#pragma unroll
    for (int nt = 0; nt < 4; ++nt) acc[nt] = f4{0, 0, 0, 0};
#define W2F(nt, S) lds16((((S) >> 2) == 0 || ((S) >> 2) == 5 ? bw + 32768 : ((S) >> 2) == 1 || ((S) >> 2) == 6 ? bw2 : ((S) >> 2) == 2 ? bw2 + 32768 : ((S) >> 2) == 3 ? bw3 : bw) + (((S) & 3) * 4 + (nt)) * 1024)
    {
      v8s W[2][4];
#pragma unroll
      for (int S = 0; S < 2; ++S)
#pragma unroll
        for (int nt = 0; nt < 4; ++nt) W[S][nt] = W2F(nt, S);
      SB();
#pragma unroll
      for (int s = 3; s < 8; ++s) HLOAD(s);
      SB();
#pragma unroll
      for (int S = 0; S < 28; ++S) {
        if (S + 8 < 28) HLOAD(S + 8);
        if (S == 8) BAR();
        if (S >= 8 && S < 16) { const int e = S - 8, pc = w * 8 + e; *(LAS v4u*)(sm + 32768 + (pc >> 5) * 32768 + ((pc >> 4) & 1) * 16384 + (pc & 15) * 1024 + lane * 16) = stg[e]; }
        if (S == 18) BAR();
#pragma unroll
        for (int nt = 0; nt < 4; ++nt) {
          acc[nt] = MFMA16(W[S & 1][nt], ha[S], acc[nt]);
          if (S + 2 < 28) W[S & 1][nt] = W2F(nt, S + 2);
          SB();
        }
      }
    }
    STAMP(22);
    BAR();
    *(LAS f4*)(sm + L_W2X + w * 2048 + lane * 16) = kh ? acc[0] : acc[2];
    *(LAS f4*)(sm + L_W2X + w * 2048 + 1024 + lane * 16) = kh ? acc[1] : acc[3];
    BAR();
    const f4 o0 = ldsf4(L_W2X + (w ^ 4) * 2048 + lane * 16), o1 = ldsf4(L_W2X + (w ^ 4) * 2048 + 1024 + lane * 16);
    const f4 t0 = (kh ? acc[2] : acc[0]) + o0, t1 = (kh ? acc[3] : acc[1]) + o1;
    const float gb[8] = {bflo(gbR[0]), bfhi(gbR[0]), bflo(gbR[1]), bfhi(gbR[1]), bflo(gbR[2]), bfhi(gbR[2]), bflo(gbR[3]), bfhi(gbR[3])};
    const float bb[8] = {bflo(b2R[0][0]), bfhi(b2R[0][0]), bflo(b2R[0][1]), bfhi(b2R[0][1]), bflo(b2R[1][0]), bfhi(b2R[1][0]), bflo(b2R[1][1]), bfhi(b2R[1][1])};
    float o[8];
#pragma unroll
    for (int r = 0; r < 8; ++r) o[r] = x1[r] + gb[r] * ((r < 4 ? t0[r & 3] : t1[r & 3]) + bb[r]);
    auto r0 = __builtin_amdgcn_permlane16_swap(pk(o[0], o[1]), pk(o[4], o[5]), false, false);
    auto r1 = __builtin_amdgcn_permlane16_swap(pk(o[2], o[3]), pk(o[6], o[7]), false, false);
    { const srd_t rO = __builtin_amdgcn_make_buffer_rsrc((void*)p.out, 0, SEQ * DM * 2, 0x00020000);
      __builtin_amdgcn_raw_buffer_store_b128(v4u{r0[0], r1[0], r0[1], r1[1]}, rO, (orow * DM + 64 * ct + 32 * kh + (g & 1) * 16 + (g >> 1) * 8) * 2, 0, 16); }
  }
  { const srd_t rX2 = __builtin_amdgcn_make_buffer_rsrc(p.ws + (64u << 20), 0, 64u << 20, 0x00020000);
    #pragma unroll 1
    for (int e = 0; e < 16; ++e) __builtin_amdgcn_raw_buffer_store_b128(v4u{1u, 2u, 3u, (unsigned)e}, rX2, (blockIdx.x * 16 + e) * 8192 + tid * 16, 0, 16); }
  while (__builtin_amdgcn_s_memrealtime() - pad_t0 < 4200ull) __builtin_amdgcn_s_sleep(1);
  STAMP(15);
  CSTAMP(41);
  STAMP_FLUSH(0);
}

extern "C" void kernel_launch(void* const* d_in, const int* in_sizes, int n_in, void* d_out, int out_size, void* d_ws, size_t ws_size, hipStream_t stream) {
  Params p;
  p.x = (const u16*)d_in[0]; p.wqkv = (const u16*)d_in[1]; p.wo = (const u16*)d_in[2];
  p.w1 = (const u16*)d_in[4]; p.b1 = (const u16*)d_in[5]; p.w2 = (const u16*)d_in[6]; p.b2 = (const u16*)d_in[7];
  p.c = (const u16*)d_in[8]; p.wmod = (const u16*)d_in[9]; p.out = (u16*)d_out; p.ws = (char*)d_ws;
  fused_block<<<256, 512, 0, stream>>>(p);
}
```

```cpp
#include <hip/hip_runtime.h>
#include <hip/hip_bf16.h>
#include <stdint.h>

typedef unsigned short u16;
typedef short v4s __attribute__((ext_vector_type(4)));
typedef short v8s __attribute__((ext_vector_type(8)));
typedef float f2 __attribute__((ext_vector_type(2)));
typedef float f4 __attribute__((ext_vector_type(4)));
typedef float f16v __attribute__((ext_vector_type(16)));
typedef unsigned v2u __attribute__((ext_vector_type(2)));
typedef unsigned v4u __attribute__((ext_vector_type(4)));
typedef __bf16 b2 __attribute__((ext_vector_type(2)));
#define LAS __attribute__((address_space(3)))
#define GAS __attribute__((address_space(1)))
#define DI __device__ __forceinline__
typedef __amdgpu_buffer_rsrc_t srd_t;

constexpr int SEQ = 2048, DM = 512, FF = 1792;
constexpr unsigned WS_EPOCH = 0;
constexpr unsigned WS_F0 = 4096;
constexpr unsigned WS_FP = WS_F0 + 8192;
constexpr unsigned WS_FM = WS_FP + 4 * 16 * 128;
constexpr unsigned WS_FQ = WS_FM + 2048;
constexpr unsigned WS_FW1 = WS_FQ + 2048;
constexpr unsigned WS_FWO = WS_FW1 + 2048;
constexpr unsigned WS_FW2 = WS_FWO + 1024;
static_assert(WS_FW2 + 4096 <= (64 << 10), "flags");
constexpr unsigned WS_WV = 64 << 10;
constexpr unsigned WS_MOD = 128 << 10;
constexpr unsigned WS_ST1 = 192 << 10;
constexpr unsigned WS_ST2A = 512 << 10;
constexpr unsigned WS_ST2B = 1024 << 10;
constexpr unsigned WS_HF = 2u << 20;
constexpr unsigned WS_KV = 4u << 20;
constexpr unsigned WS_OF = 8u << 20;
constexpr unsigned WS_X1F = 10u << 20;
constexpr unsigned WS_H1F = 12u << 20;
constexpr unsigned WS_TQ = 19u << 20;
constexpr unsigned WS_TW1 = WS_TQ + (1536u << 10);
constexpr unsigned WS_TWO = WS_TW1 + (1792u << 10);
constexpr unsigned WS_TW2 = WS_TWO + (512u << 10);
constexpr unsigned WS_STAMP = 25u << 20;
constexpr unsigned WS_BYTES = 27u << 20;
static_assert(WS_TW2 + (1792u << 10) <= WS_STAMP, "ws");
constexpr unsigned L_CA = 98304;
constexpr unsigned L_XS = 131072;
constexpr unsigned L_S1 = L_XS + 8192;
constexpr unsigned L_SO = L_S1 + 7168;
constexpr unsigned L_SC = L_SO + 12288;
constexpr unsigned L_CAP = L_SC + 2048;
constexpr unsigned L_CNT = L_CAP + 512;
static_assert(L_CNT + 16 <= 163840, "lds");
constexpr unsigned L_SQ = 0;
constexpr unsigned L_M2 = 8192;
constexpr unsigned L_WQKV = 0;
constexpr unsigned L_PARTU = 98304;
constexpr unsigned L_PARTV = 32768;
constexpr unsigned L_AK = 98304;
constexpr unsigned KPL = 6144;
constexpr unsigned L_AV = L_AK + 4 * KPL;
constexpr unsigned VPL = 6208, VHD = 12416;
constexpr unsigned L_PSA = L_AK + KPL, L_PSB = L_AK + 3 * KPL, L_PCB = L_AV + VPL, L_PGA = L_AV + VHD + VPL, L_PGB = L_PGA + 1024, L_B2C = L_PGA + 2048;
constexpr unsigned L_PSA2 = L_AK + 2 * KPL, L_PSB2 = L_AV, L_PCB2 = L_AV + VHD;
constexpr unsigned L_FIN = L_AK;
constexpr unsigned L_ONE = L_AV + 2 * VHD;
constexpr unsigned L_AQ = L_ONE + 6400;
constexpr unsigned L_PERS = L_AQ + 8192;
static_assert(L_PERS + 1024 <= 163840, "lds");
static_assert((L_AV % 256) == 0 && (L_ONE % 256) == 0, "bank plan");
constexpr unsigned L_WO = 0;
constexpr unsigned L_W1 = 65536;
constexpr unsigned L_W1S = 0;
constexpr unsigned L_XCH = 16384;
constexpr unsigned L_XF = 16384 + 12288;
constexpr unsigned L_W2X = 98304;

struct Params { const u16* x; const u16* wqkv; const u16* wo; const u16* w1; const u16* b1; const u16* w2; const u16* b2; const u16* c; const u16* wmod; u16* out; char* ws; };

DI float bflo(unsigned v) { return __uint_as_float(v << 16); }
DI float bfhi(unsigned v) { return __uint_as_float(v & 0xffff0000u); }
DI unsigned pk(float a, float b) { f2 v = {a, b}; b2 r = __builtin_convertvector(v, b2); return __builtin_bit_cast(unsigned, r); }
DI float rbf(float a) { return bflo(pk(a, 0.f)); }
DI v8s cat(v4s a, v4s b) { return __builtin_shufflevector(a, b, 0, 1, 2, 3, 4, 5, 6, 7); }
DI v4s trd(unsigned a) { return __builtin_amdgcn_ds_read_tr16_b64_v4i16((LAS v4s*)(uintptr_t)a); }
DI v8s asv8(v4u v) { return __builtin_bit_cast(v8s, v); }
DI v4u asv4u(v8s v) { return __builtin_bit_cast(v4u, v); }
DI v8s lds16(unsigned a) { return *(const LAS v8s*)(uintptr_t)a; }
DI f4 ldsf4(unsigned a) { return *(const LAS f4*)(uintptr_t)a; }
DI v2u lds8(unsigned a) { return *(const LAS v2u*)(uintptr_t)a; }
#define MFMA16(a, b, c) __builtin_amdgcn_mfma_f32_16x16x32_bf16((a), (b), (c), 0, 0, 0)
#define MFMA32(a, b, c) __builtin_amdgcn_mfma_f32_32x32x16_bf16((a), (b), (c), 0, 0, 0)
#define BAR() asm volatile("s_waitcnt lgkmcnt(0)\n\ts_barrier" ::: "memory")
#define VM0() asm volatile("s_waitcnt vmcnt(0)" ::: "memory")
#define LG0() asm volatile("s_waitcnt lgkmcnt(0)" ::: "memory")
#define CFENCE() asm volatile("" ::: "memory")
#define SB() __builtin_amdgcn_sched_barrier(0)
DI unsigned opq(unsigned v) { asm volatile("" : "+v"(v)); return v; }
DI void glds(srd_t r, unsigned voff, unsigned soff, unsigned ldsaddr) {
  asm volatile("s_mov_b32 m0, %0\n\ts_nop 0\n\tbuffer_load_dwordx4 %1, %2, %3 offen lds" ::"s"(ldsaddr), "v"(voff), "s"(r), "s"(soff) : "memory");
}
DI void glds1(srd_t r, unsigned voff, unsigned soff, unsigned ldsaddr) {
  asm volatile("s_mov_b32 m0, %0\n\ts_nop 0\n\tbuffer_load_dwordx4 %1, %2, %3 offen sc1 lds" ::"s"(ldsaddr), "v"(voff), "s"(r), "s"(soff) : "memory");
}
DI void glds4(srd_t r, unsigned voff, unsigned soff, unsigned ldsaddr) {
  asm volatile("s_mov_b32 m0, %0\n\ts_nop 0\n\tbuffer_load_dwordx4 %1, %2, %3 offen lds\n\tbuffer_load_dwordx4 %1, %2, %3 offen offset:1024 lds\n\t"
               "buffer_load_dwordx4 %1, %2, %3 offen offset:2048 lds\n\tbuffer_load_dwordx4 %1, %2, %3 offen offset:3072 lds" ::"s"(ldsaddr), "v"(voff), "s"(r), "s"(soff) : "memory");
}
DI void glds4x1(srd_t r, unsigned voff, unsigned soff, unsigned ldsaddr) {
  asm volatile("s_mov_b32 m0, %0\n\ts_nop 0\n\tbuffer_load_dwordx4 %1, %2, %3 offen sc1 lds\n\tbuffer_load_dwordx4 %1, %2, %3 offen offset:1024 sc1 lds\n\t"
               "buffer_load_dwordx4 %1, %2, %3 offen offset:2048 sc1 lds\n\tbuffer_load_dwordx4 %1, %2, %3 offen offset:3072 sc1 lds" ::"s"(ldsaddr), "v"(voff), "s"(r), "s"(soff) : "memory");
}
DI void glds2x1(srd_t r, unsigned voff, unsigned soff, unsigned ldsaddr) {
  asm volatile("s_mov_b32 m0, %0\n\ts_nop 0\n\tbuffer_load_dwordx4 %1, %2, %3 offen sc1 lds\n\tbuffer_load_dwordx4 %1, %2, %3 offen offset:1024 sc1 lds" ::"s"(ldsaddr), "v"(voff), "s"(r), "s"(soff) : "memory");
}
DI v4u ld16(srd_t r, unsigned voff, unsigned soff) { return __builtin_amdgcn_raw_buffer_load_b128(r, voff, soff, 16); }
DI v2u ld8(srd_t r, unsigned voff, unsigned soff) { return __builtin_amdgcn_raw_buffer_load_b64(r, voff, soff, 16); }
DI v4u ldp16(srd_t r, unsigned voff, unsigned soff) { return __builtin_amdgcn_raw_buffer_load_b128(r, voff, soff, 0); }
DI void stp16(srd_t r, v4u v, unsigned off) { __builtin_amdgcn_raw_buffer_store_b128(v, r, off, 0, 0); }
DI void st16(srd_t r, v4u v, unsigned off) { __builtin_amdgcn_raw_buffer_store_b128(v, r, off, 0, 16); }
DI void st8(srd_t r, v2u v, unsigned off) { __builtin_amdgcn_raw_buffer_store_b64(v, r, off, 0, 16); }
DI float sum16(float v) { auto r = __builtin_amdgcn_permlane16_swap(__float_as_uint(v), __float_as_uint(v), false, false); return __uint_as_float(r[0]) + __uint_as_float(r[1]); }
DI float sum32(float v) { auto r = __builtin_amdgcn_permlane32_swap(__float_as_uint(v), __float_as_uint(v), false, false); return __uint_as_float(r[0]) + __uint_as_float(r[1]); }
DI float max32(float v) { auto r = __builtin_amdgcn_permlane32_swap(__float_as_uint(v), __float_as_uint(v), false, false); return fmaxf(__uint_as_float(r[0]), __uint_as_float(r[1])); }
DI float sumg(float v) { return sum32(sum16(v)); }
#define RLX_AGENT __ATOMIC_RELAXED, __HIP_MEMORY_SCOPE_AGENT
typedef unsigned long long u64;
DI void arrive(char* ws, unsigned off, u64 tag) { (void)__hip_atomic_exchange((GAS u64*)(ws + off), tag, RLX_AGENT); }
DI void wait_words(char* ws, unsigned off, int n, int lane, u64 tag) {
  GAS u64* p = (GAS u64*)(ws + off) + (lane < n ? lane : 0);
  for (unsigned spins = 0; spins < (1u << 17); ++spins)
    if (__all(__hip_atomic_load(p, RLX_AGENT) == tag)) break;
  CFENCE();
}
template <int C> DI float dpp(float v) { return __uint_as_float(__builtin_amdgcn_update_dpp(0u, __float_as_uint(v), C, 0xf, 0xf, false)); }
DI float rowsum16(float v) { v += dpp<0x128>(v); v += dpp<0x124>(v); v += dpp<0x122>(v); v += dpp<0x121>(v); return v; }
#define PIN(x) asm volatile("" : "+v"(x))
#define STAMP(k)
#define STAMP_FLUSH(o)
#define CSTAMP(k)
DI float silu(float v) { return v * __builtin_amdgcn_rcpf(1.f + __builtin_amdgcn_exp2f(-1.4426950408889634f * v)); }

DI v8s trfragA(unsigned img, int half, int lane) {
  const int g = lane >> 4, q = (lane & 15) >> 2, pp = lane & 3;
  const unsigned a = img + (4 * g + q) * 64 + ((half ^ (g & 1)) * 32) + pp * 8;
  return cat(trd(a), trd(a + 1024));
}

template <bool FULL>
DI void attention(int t, int hd, int kt0, int lane, f16v mU, f16v mL, f16v& O, srd_t rWS, unsigned wo_src, unsigned w1_src, int w, const u16* xres, v2u& xr0, v2u& xr1, unsigned& stampv) {
  const int qr = lane & 31, h = lane >> 5, blk = (lane >> 4) & 1, q = (lane & 15) >> 2, pp = lane & 3;
  const v8s Qf = lds16(L_AQ + (hd * 2 + h) * 2048 + (32 * t + qr) * 16);
  const unsigned kb = opq(L_AK + (hd * 2 + h) * KPL + (32 * t + qr) * 16);
  const unsigned vb = opq((blk == 0 ? L_AV + hd * VHD + (pp >> 1) * VPL + (pp & 1) * 8 + (4 * h + q) * 16 : L_ONE + ((hd * 128 + (h ? 0 : 128)) & 255)) + t * 512);
  const f16v zero = {};
#define RIDE(n) do { if ((n) < 2) glds4x1(rWS, lane * 16, wo_src + (w * 2 + (n)) * 4096, L_WO + (w * 2 + (n)) * 4096); \
                     else glds4x1(rWS, lane * 16, w1_src + w * 4096, L_W1 + w * 4096); } while (0)
  if (FULL) {
    v8s Kf[9];
#pragma unroll
    for (int kt = 0; kt < 9; ++kt) Kf[kt] = lds16(kb + kt * 512);
    v8s V0 = cat(trd(vb), trd(vb + 128)), V1 = cat(trd(vb + 256), trd(vb + 384));
    f16v S[2];
    float m0 = -3e38f, m1 = -3e38f;
    S[0] = MFMA32(Kf[0], Qf, mL);
    SB();
#pragma unroll
    for (int kt = 0; kt < 9; ++kt) {
      if (kt < 8) S[(kt + 1) & 1] = MFMA32(Kf[kt + 1], Qf, kt + 1 == 8 ? mU : zero);
      SB();
#pragma unroll
      for (int r = 0; r < 16; r += 4) { m0 = fmaxf(fmaxf(m0, S[kt & 1][r]), S[kt & 1][r + 1]); m1 = fmaxf(fmaxf(m1, S[kt & 1][r + 2]), S[kt & 1][r + 3]); }
      SB();
    }
    const float m = max32(fmaxf(m0, m1));
    STAMP(23);
    f16v nm;
#pragma unroll
    for (int r = 0; r < 16; ++r) { nm[r] = -m; mU[r] -= m; mL[r] -= m; }
    O = zero;
    S[0] = MFMA32(Kf[0], Qf, mL);
    SB();
#pragma unroll
    for (int kt = 0; kt < 9; ++kt) {
      if (kt < 8) S[(kt + 1) & 1] = MFMA32(Kf[kt + 1], Qf, kt + 1 == 8 ? mU : nm);
      if (kt == 0) RIDE(0);
      if (kt == 3) RIDE(1);
      if (kt == 6) RIDE(2);
      SB();
      v4u P0, P1;
#pragma unroll
      for (int r = 0; r < 4; ++r) P0[r] = pk(__builtin_amdgcn_exp2f(S[kt & 1][2 * r]), __builtin_amdgcn_exp2f(S[kt & 1][2 * r + 1]));
      O = MFMA32(V0, asv8(P0), O);
      if (kt < 8) V0 = cat(trd(vb + (kt + 1) * 512), trd(vb + (kt + 1) * 512 + 128));
      if (kt == 2) xr0 = *(const v2u*)xres;
      if (kt == 5) xr1 = *(const v2u*)(xres + 16);
      SB();
#pragma unroll
      for (int r = 0; r < 4; ++r) P1[r] = pk(__builtin_amdgcn_exp2f(S[kt & 1][8 + 2 * r]), __builtin_amdgcn_exp2f(S[kt & 1][8 + 2 * r + 1]));
      O = MFMA32(V1, asv8(P1), O);
      if (kt < 8) V1 = cat(trd(vb + (kt + 1) * 512 + 256), trd(vb + (kt + 1) * 512 + 384));
      SB();
    }
  } else {
#pragma unroll
    for (int n = 0; n < 3; ++n) RIDE(n);
    xr0 = *(const v2u*)xres; xr1 = *(const v2u*)(xres + 16);
    float m = -3e38f;
#pragma unroll
    for (int kt = 0; kt < 9; ++kt) {
      if (kt >= kt0) {
        const f16v S = MFMA32(lds16(kb + kt * 512), Qf, kt == 8 ? mU : zero);
#pragma unroll
        for (int r = 0; r < 16; r += 2) m = fmaxf(fmaxf(m, S[r]), S[r + 1]);
      }
    }
    m = max32(m);
    f16v nm;
#pragma unroll
    for (int r = 0; r < 16; ++r) { nm[r] = -m; mU[r] -= m; }
    O = zero;
#pragma unroll
    for (int kt = 0; kt < 9; ++kt) {
      if (kt >= kt0) {
        const f16v S = MFMA32(lds16(kb + kt * 512), Qf, kt == 8 ? mU : nm);
        v4u P0, P1;
#pragma unroll
        for (int r = 0; r < 4; ++r) {
          P0[r] = pk(__builtin_amdgcn_exp2f(S[2 * r]), __builtin_amdgcn_exp2f(S[2 * r + 1]));
          P1[r] = pk(__builtin_amdgcn_exp2f(S[8 + 2 * r]), __builtin_amdgcn_exp2f(S[8 + 2 * r + 1]));
        }
        const v8s V0 = cat(trd(vb + kt * 512), trd(vb + kt * 512 + 128));
        const v8s V1 = cat(trd(vb + kt * 512 + 256), trd(vb + kt * 512 + 384));
        O = MFMA32(V0, asv8(P0), O);
        O = MFMA32(V1, asv8(P1), O);
      }
    }
  }
#undef RIDE
}

__global__ void __launch_bounds__(512, 2) fused_block(Params p) {
  __shared__ __attribute__((aligned(16))) char smem[163840];
  LAS char* sm = (LAS char*)smem;
  const int tid = threadIdx.x, lane = tid & 63;
  const int w = __builtin_amdgcn_readfirstlane(tid >> 6);
  const int b = blockIdx.x, ci = ((b & 7) >> 1) * 4 + ((b >> 3) & 3), cj = (b & 1) * 8 + (b >> 5);
  const int tok = lane & 15, g = lane >> 4, q4 = (lane & 15) >> 2, pp = lane & 3;
  const int hf = cj & 1, ct = cj >> 1, par = cj & 1;
  const int wq = w < 4 ? w : w - 1;
  unsigned stampv = 0;
  const srd_t rWS = __builtin_amdgcn_make_buffer_rsrc(p.ws, 0, WS_BYTES, 0x00020000);
  const int rA = lane >> 2, pA = lane & 3;
  const unsigned colA = (((pA >> 1) ^ ((rA >> 2) & 1)) * 32) + (pA & 1) * 16;
  const int rB = lane >> 3, pB = lane & 7;
  const unsigned colB = (((pB >> 1) ^ ((rB >> 1) & 3)) * 32) + (pB & 1) * 16;
  STAMP(0);
  CSTAMP(40);

  const bool m2 = b < 160;
  const int msl = b % 40, mkq = b / 40;
  const srd_t rWmod = __builtin_amdgcn_make_buffer_rsrc((void*)p.wmod, 0, DM * 3072 * 2, 0x00020000);
  v4u b2v = {0, 0, 0, 0};
  if (w == 6 && lane < 8) b2v = *(const v4u*)(p.b2 + 64 * ct + lane * 8);
  v4u cr;
  v2u ep;
  if (w < 4) {
    __builtin_amdgcn_s_setprio(2);
    asm volatile("global_load_dwordx4 %0, %1, off" : "=v"(cr) : "v"(p.c + 128 * w + 8 * (lane & 15)) : "memory");
    asm volatile("buffer_load_dwordx2 %0, %1, %2, 0 offen sc1" : "=v"(ep) : "v"(WS_EPOCH), "s"(rWS) : "memory");
    const srd_t rX = __builtin_amdgcn_make_buffer_rsrc((void*)p.x, 0, SEQ * DM * 2, 0x00020000);
#pragma unroll
    for (int u = 0; u < 2; ++u) glds(rX, rA * 1024 + pA * 16, (128 * ci + 16 * (2 * w + u)) * 1024 + 64 * cj, L_XS + (2 * w + u) * 1024);
#pragma unroll
    for (int i0 = 0; i0 < 8; ++i0) { const int ii = (i0 + (ci & 3) * 2 + (cj & 1)) & 7; glds(rWmod, rA * 6144 + colA, (w * 8 + ii) * 16 * 6144 + (512 + 32 * cj) * 2, L_CA + (w * 8 + ii) * 1024); }
  } else if (w == 5) {
    __builtin_amdgcn_s_setprio(3);
    const srd_t r = __builtin_amdgcn_make_buffer_rsrc((void*)p.wqkv, 0, DM * 1536 * 2, 0x00020000);
#pragma unroll
    for (int e = 0; e < 6; ++e) glds(r, rA * 3072 + colA, (32 * ci + 16 * (e & 1)) * 3072 + ((e >> 1) * 512 + 32 * cj) * 2, L_SQ + e * 1024);
  }
  if (tid == 256) { *(LAS unsigned*)(uintptr_t)L_CNT = 0u; *(LAS unsigned*)(uintptr_t)(L_CNT + 4) = m2 ? 0u : 1u; *(LAS unsigned*)(uintptr_t)(L_CNT + 8) = 0u; *(LAS unsigned*)(uintptr_t)(L_CNT + 12) = 0u; }
  BAR();
#define READ_TAG() do { const v2u ep_ = ld8(rWS, WS_EPOCH, 0); TAG = (((u64)__builtin_amdgcn_readfirstlane(ep_[1]) << 32) | __builtin_amdgcn_readfirstlane(ep_[0])) + 1; } while (0)
  v8s rt[12];
  f16v mU, mL;
  u64 TAG;
  if (w < 4) {
    asm volatile("s_waitcnt vmcnt(10)" : "+v"(cr), "+v"(ep) :: "memory");
    const unsigned sc = L_SC + w * 256;
    if (lane < 16) *(LAS v4u*)(uintptr_t)(sc + lane * 16) = v4u{pk(silu(bflo(cr[0])), silu(bfhi(cr[0]))), pk(silu(bflo(cr[1])), silu(bfhi(cr[1]))), pk(silu(bflo(cr[2])), silu(bfhi(cr[2]))), pk(silu(bflo(cr[3])), silu(bfhi(cr[3])))};
    LG0();
    v8s sf[4];
#pragma unroll
    for (int s = 0; s < 4; ++s) { const v2u lo = lds8(sc + (32 * s + 4 * g) * 2), hi = lds8(sc + (32 * s + 16 + 4 * g) * 2); sf[s] = asv8(v4u{lo[0], lo[1], hi[0], hi[1]}); }
    TAG = (((u64)__builtin_amdgcn_readfirstlane(ep[1]) << 32) | __builtin_amdgcn_readfirstlane(ep[0])) + 1;
    asm volatile("s_waitcnt vmcnt(8)" ::: "memory");
    v2u xa[2][2];
#pragma unroll
    for (int u = 0; u < 2; ++u) {
      const unsigned a = L_XS + (2 * w + u) * 1024 + tok * 64 + g * 8; xa[u][0] = lds8(a); xa[u][1] = lds8(a + 32);
      const float x0 = bflo(xa[u][0][0]), x1 = bfhi(xa[u][0][0]), x2 = bflo(xa[u][0][1]), x3 = bfhi(xa[u][0][1]);
      const float x4 = bflo(xa[u][1][0]), x5 = bfhi(xa[u][1][0]), x6 = bflo(xa[u][1][1]), x7 = bfhi(xa[u][1][1]);
      const float s1 = sumg(((x0 + x1) + (x2 + x3)) + ((x4 + x5) + (x6 + x7)));
      const float s2 = sumg(((x0 * x0 + x1 * x1) + (x2 * x2 + x3 * x3)) + ((x4 * x4 + x5 * x5) + (x6 * x6 + x7 * x7)));
      if (g == 0) st8(rWS, v2u{__float_as_uint(s1), __float_as_uint(s2)}, WS_ST1 + ((ci * 16 + cj) * 128 + 16 * (2 * w + u) + tok) * 8);
    }
    asm volatile("s_waitcnt vmcnt(2)" ::: "memory");
    STAMP(1);
    f4 a0 = {0, 0, 0, 0}, a1 = {0, 0, 0, 0};
    {
      v8s W[4][2];
#pragma unroll
      for (int s = 0; s < 4; ++s) { W[s][0] = trfragA(L_CA + (4 * w + s) * 2048, 0, lane); W[s][1] = trfragA(L_CA + (4 * w + s) * 2048, 1, lane); }
#pragma unroll
      for (int s = 0; s < 4; ++s) { a0 = MFMA16(W[s][0], sf[s], a0); a1 = MFMA16(W[s][1], sf[s], a1); }
    }
    if (tok == 0) { *(LAS f4*)(uintptr_t)(L_CAP + (w * 32 + 4 * g) * 4) = a0; *(LAS f4*)(uintptr_t)(L_CAP + (w * 32 + 16 + 4 * g) * 4) = a1; }
    LG0();
    if (lane == 0) __hip_atomic_fetch_add((LAS unsigned*)(uintptr_t)L_CNT, 1u, __ATOMIC_RELAXED, __HIP_MEMORY_SCOPE_WORKGROUP);
    while (*(volatile LAS unsigned*)(uintptr_t)L_CNT < 4u) {}
    CFENCE();
    STAMP(19);
    a0 = ldsf4(L_CAP + 4 * g * 4); a1 = ldsf4(L_CAP + (16 + 4 * g) * 4);
#pragma unroll
    for (int e = 1; e < 4; ++e) { a0 += ldsf4(L_CAP + (e * 32 + 4 * g) * 4); a1 += ldsf4(L_CAP + (e * 32 + 16 + 4 * g) * 4); }
    float wv[8];
#pragma unroll
    for (int r = 0; r < 4; ++r) { wv[r] = rbf(1.f + rbf(a0[r])); wv[4 + r] = rbf(1.f + rbf(a1[r])); }
#pragma unroll
    for (int u = 0; u < 2; ++u) {
      const float x0 = bflo(xa[u][0][0]), x1 = bfhi(xa[u][0][0]), x2 = bflo(xa[u][0][1]), x3 = bfhi(xa[u][0][1]);
      const float x4 = bflo(xa[u][1][0]), x5 = bfhi(xa[u][1][0]), x6 = bflo(xa[u][1][1]), x7 = bfhi(xa[u][1][1]);
      const int T = 2 * w + u;
      st16(rWS, v4u{pk(x0 * wv[0], x1 * wv[1]), pk(x2 * wv[2], x3 * wv[3]), pk(x4 * wv[4], x5 * wv[5]), pk(x6 * wv[6], x7 * wv[7])},
           WS_HF + ((ci * 8 + T) * 16 + cj) * 1024 + lane * 16);
      if (u == 0 && w == 0 && tok == 0) {
        st8(rWS, v2u{pk(wv[0], wv[1]), pk(wv[2], wv[3])}, WS_WV + ci * 1024 + cj * 64 + g * 8);
        st8(rWS, v2u{pk(wv[4], wv[5]), pk(wv[6], wv[7])}, WS_WV + ci * 1024 + cj * 64 + 32 + g * 8);
      }
    }
    STAMP(21);
    VM0();
    if (lane == 0) { arrive(p.ws, WS_F0 + (ci * 64 + cj * 4 + w) * 8, TAG); __hip_atomic_fetch_add((LAS unsigned*)(uintptr_t)(L_CNT + 8), 1u, __ATOMIC_RELAXED, __HIP_MEMORY_SCOPE_WORKGROUP); }
    __builtin_amdgcn_s_setprio(0);
  } else if (w == 4) {
    if (m2) {
      const int mvec = msl >> 3, mcol = (mvec == 0 ? 0 : 512 + 512 * mvec) + (msl & 7) * 64;
      __builtin_amdgcn_s_sleep(8);
#pragma unroll
      for (int ii = 0; ii < 16; ++ii) glds(rWmod, rB * 6144 + colB, (128 * mkq + 8 * ii) * 6144 + mcol * 2, L_M2 + ii * 1024);
      const v4u cr = *(const v4u*)(p.c + 128 * mkq + 8 * (lane & 15));
      const unsigned sc = L_SC + 1024;
      if (lane < 16) *(LAS v4u*)(uintptr_t)(sc + lane * 16) = v4u{pk(silu(bflo(cr[0])), silu(bfhi(cr[0]))), pk(silu(bflo(cr[1])), silu(bfhi(cr[1]))), pk(silu(bflo(cr[2])), silu(bfhi(cr[2]))), pk(silu(bflo(cr[3])), silu(bfhi(cr[3])))};
      LG0();
      v8s sf[4];
#pragma unroll
      for (int s = 0; s < 4; ++s) { const v2u lo = lds8(sc + (32 * s + 4 * g) * 2), hi = lds8(sc + (32 * s + 16 + 4 * g) * 2); sf[s] = asv8(v4u{lo[0], lo[1], hi[0], hi[1]}); }
      VM0();
      f4 a[4];
#pragma unroll
      for (int nt = 0; nt < 4; ++nt) a[nt] = f4{0, 0, 0, 0};
      const int row = 4 * g + q4;
#pragma unroll
      for (int s = 0; s < 4; ++s) {
        v8s W[4];
#pragma unroll
        for (int nt = 0; nt < 4; ++nt) { const unsigned ad = L_M2 + s * 4096 + row * 128 + ((nt ^ ((row >> 1) & 3)) * 32) + pp * 8; W[nt] = cat(trd(ad), trd(ad + 2048)); }
#pragma unroll
        for (int nt = 0; nt < 4; ++nt) a[nt] = MFMA16(W[nt], sf[s], a[nt]);
      }
      if (tok == 0) {
#pragma unroll
        for (int nt = 0; nt < 4; ++nt)
          st16(rWS, v4u{__float_as_uint(a[nt][0]), __float_as_uint(a[nt][1]), __float_as_uint(a[nt][2]), __float_as_uint(a[nt][3])},
               WS_MOD + mkq * 10240 + mvec * 2048 + ((msl & 7) * 2 + (nt >> 1)) * 128 + g * 32 + (nt & 1) * 16);
      }
      LG0();
      if (lane == 0) *(volatile LAS unsigned*)(uintptr_t)(L_CNT + 4) = 1u;
      READ_TAG();
      VM0();
      if (lane == 0) arrive(p.ws, WS_FM + b * 8, TAG);
    } else READ_TAG();
    STAMP(1);
  } else {
    if (w == 5) {
      READ_TAG();
      VM0();
#pragma unroll
      for (int nt = 0; nt < 6; ++nt) st16(rWS, asv4u(trfragA(L_SQ + (nt >> 1) * 2048, nt & 1, lane)), WS_TQ + ((cj * 6 + nt) * 16 + ci) * 1024 + lane * 16);
      VM0();
      if (lane == 0) arrive(p.ws, WS_FQ + (cj * 16 + ci) * 8, TAG);
      __builtin_amdgcn_s_setprio(0);
    } else {
      READ_TAG();
      __builtin_amdgcn_s_sleep(16);
      if (w == 6) {
        const srd_t r1 = __builtin_amdgcn_make_buffer_rsrc((void*)p.w1, 0, DM * FF * 2, 0x00020000);
#pragma unroll
        for (int e = 0; e < 6; ++e) glds(r1, rA * (FF * 2) + colA, (32 * ci + 16 * (e & 1)) * (FF * 2) + (112 * cj + 16 * par + (e >> 1) * 32) * 2, L_S1 + e * 1024);
        glds(r1, (lane >> 1) * (FF * 2) + (lane & 1) * 16, 32 * ci * (FF * 2) + (112 * cj + (par ? 0 : 96)) * 2, L_S1 + 6144);
        VM0();
#pragma unroll
        for (int nt = 0; nt < 6; ++nt) rt[nt] = trfragA(L_S1 + (nt >> 1) * 2048, nt & 1, lane);
        { const unsigned a = L_S1 + 6144 + (4 * g + q4) * 32 + pp * 8; rt[6] = cat(trd(a), trd(a + 512)); }
      } else {
        const srd_t ro = __builtin_amdgcn_make_buffer_rsrc((void*)p.wo, 0, DM * DM * 2, 0x00020000);
#pragma unroll
        for (int e = 0; e < 4; ++e) glds(ro, rB * 1024 + colB, (32 * ((b & 127) >> 3) + 8 * e) * 1024 + (b & 7) * 128, L_SO + e * 1024);
        const srd_t r2 = __builtin_amdgcn_make_buffer_rsrc((void*)p.w2, 0, FF * DM * 2, 0x00020000);
        const int b2 = b < 224 ? b : 0;
#pragma unroll
        for (int e = 0; e < 8; ++e) { const int id = 2 * b2 + (e >> 2); glds(r2, rB * 1024 + colB, (32 * (id >> 3) + 8 * (e & 3)) * 1024 + (id & 7) * 128, L_SO + 4096 + e * 1024); }
        VM0();
        {
          const int row = 16 * (g >> 1) + 4 * (g & 1) + q4;
#pragma unroll
          for (int nt = 0; nt < 4; ++nt) { const unsigned a = L_SO + row * 128 + ((nt ^ ((row >> 1) & 3)) * 32) + pp * 8; rt[8 + nt] = cat(trd(a), trd(a + 1024)); }
        }
        const int row = 4 * g + q4;
#pragma unroll
        for (int e = 0; e < 8; ++e) { const int nt = e & 3; const unsigned a = L_SO + 4096 + (e >> 2) * 4096 + row * 128 + ((nt ^ ((row >> 1) & 3)) * 32) + pp * 8; rt[e] = cat(trd(a), trd(a + 2048)); }
      }
    }
    STAMP(1);
    if (w >= 6) {
      while (*(volatile LAS unsigned*)(uintptr_t)(L_CNT + 8) < 4u) __builtin_amdgcn_s_sleep(1);
      CFENCE();
      if (w == 6) {
#pragma unroll
        for (int e = 0; e < 7; ++e) st16(rWS, asv4u(rt[e]), WS_TW1 + ((cj * 7 + e) * 16 + ci) * 1024 + lane * 16);
      } else {
        if (b < 224) {
#pragma unroll
          for (int e = 0; e < 8; ++e) { const int id = 2 * b + (e >> 2); st16(rWS, asv4u(rt[e]), WS_TW2 + (((id & 7) * 56 + (id >> 3)) * 4 + (e & 3)) * 1024 + lane * 16); }
        }
        if (b < 128) {
#pragma unroll
          for (int e = 0; e < 4; ++e) st16(rWS, asv4u(rt[8 + e]), WS_TWO + (((b & 7) * 16 + (b >> 3)) * 4 + e) * 1024 + lane * 16);
        }
      }
    }
    wait_words(p.ws, WS_FQ + cj * 128, 16, lane, TAG);
    while (*(volatile LAS unsigned*)(uintptr_t)(L_CNT + 4) == 0u) __builtin_amdgcn_s_sleep(1);
    CFENCE();
    STAMP(19);
#pragma unroll
    for (int e = 0; e < 8; ++e) glds4x1(rWS, lane * 16, WS_TQ + cj * 98304 + ((w - 5) * 8 + ((e + 2 * (ci & 3)) & 7)) * 4096, L_WQKV + ((w - 5) * 8 + ((e + 2 * (ci & 3)) & 7)) * 4096);
  }
  {
    const int qr = lane & 31, h = lane >> 5;
#pragma unroll
    for (int r = 0; r < 16; ++r) { const int kr = (r & 3) + 8 * (r >> 2) + 4 * h; mU[r] = kr > qr ? -1e30f : 0.f; mL[r] = kr > qr ? 0.f : -1e30f; }
    asm volatile("" : "+v"(mU), "+v"(mL));
  }
  if (w == 0) {
    __builtin_amdgcn_s_setprio(3);
    wait_words(p.ws, WS_F0 + ci * 512, 64, lane, TAG);
    if (lane == 0) *(volatile LAS unsigned*)(uintptr_t)(L_CNT + 12) = 1u;
    __builtin_amdgcn_s_setprio(0);
  } else {
    while (*(volatile LAS unsigned*)(uintptr_t)(L_CNT + 12) == 0u) __builtin_amdgcn_s_sleep(1);
    CFENCE();
  }
  STAMP(2);
  v8s act[16];
  const unsigned hsrc = WS_HF + (ci * 8 + w) * 16 * 1024;
  v2u wl[2], wh[2];
#pragma unroll
  for (int u = 0; u < 2; ++u) { const unsigned o = (32 * (2 * w + u) + 4 * g) * 2; wl[u] = ld8(rWS, o, WS_WV + ci * 1024); wh[u] = ld8(rWS, o + 32, WS_WV + ci * 1024); }
#pragma unroll
  for (int s = 0; s < 5; ++s) act[s] = asv8(ld16(rWS, lane * 16, hsrc + s * 1024));
  if (w >= 5) asm volatile("s_waitcnt vmcnt(9)" ::: "memory");
  BAR();
  STAMP(3);
  if (lane == 0) {
    if (w == 6) arrive(p.ws, WS_FW1 + (cj * 16 + ci) * 8, TAG);
    if (w == 7 && b < 128) arrive(p.ws, WS_FWO + ((b & 7) * 16 + (b >> 3)) * 8, TAG);
    if (w == 7 && b < 224) { arrive(p.ws, WS_FW2 + (((2 * b) & 7) * 64 + ((2 * b) >> 3)) * 8, TAG); arrive(p.ws, WS_FW2 + (((2 * b + 1) & 7) * 64 + ((2 * b + 1) >> 3)) * 8, TAG); }
  }

  f4 accQ[2], accKV[4];
  float rstd, nmu;
  v4u gaR, cbR, sbR, gbR;
  v2u b2R[2];
  v4u mc0, md0;
  v4u mx[2];
  const int tt = w & 3, kh = w >> 2;
  const int ocol = 64 * ct + 32 * kh + 4 * g;
  {
    const unsigned bq = opq(L_WQKV + lane * 16), bv = opq(L_WQKV + 65536 + lane * 16);
#define QF(nt, s) lds16(((nt) < 4 ? bq : bv) + ((nt) & 3) * 16384 + (s) * 1024)
    {
      f4 au[6];
      v8s W[2][6];
      const unsigned c0 = opq(bq + w * 2048), d0 = opq(bv + w * 2048);
#pragma unroll
      for (int u = 0; u < 2; ++u)
#pragma unroll
        for (int nt = 0; nt < 6; ++nt) W[u][nt] = lds16((nt < 4 ? c0 : d0) + (nt & 3) * 16384 + u * 1024);
#pragma unroll
      for (int nt = 0; nt < 6; ++nt) au[nt] = f4{0, 0, 0, 0};
#pragma unroll
      for (int u = 0; u < 2; ++u) {
        const v8s a = asv8(v4u{wl[u][0], wl[u][1], wh[u][0], wh[u][1]});
#pragma unroll
        for (int nt = 0; nt < 6; ++nt) au[nt] = MFMA16(W[u][nt], a, au[nt]);
      }
      if (tok == 0) {
#pragma unroll
        for (int nt = 0; nt < 6; ++nt) *(LAS f4*)(sm + L_PARTU + (w * 96 + nt * 16 + 4 * g) * 4) = au[nt];
      }
    }
    BAR();
    if (tid >= 384 && tid < 480) {
      const int e0 = tid - 384;
      float s = 0;
#pragma unroll
      for (int e = 0; e < 8; ++e) s += *(const LAS float*)(sm + L_PARTU + (e * 96 + e0) * 4);
      *(LAS float*)(sm + L_PERS + e0 * 4) = s;
    }
    STAMP(16);
    v2u st[4];
    u64 fm0 = 0, fm1 = 0, fm2 = 0;
#pragma unroll
    for (int nt = 0; nt < 4; ++nt) accKV[nt] = f4{0, 0, 0, 0};
#pragma unroll
    for (int nt = 0; nt < 2; ++nt) accQ[nt] = f4{0, 0, 0, 0};
    {
      v8s W[6];
#pragma unroll
      for (int nt = 0; nt < 6; ++nt) W[nt] = QF(nt, 0);
      SB();
#pragma unroll
      for (int s = 0; s < 16; ++s) {
        if (s + 5 < 16) act[s + 5] = asv8(ld16(rWS, lane * 16, hsrc + (s + 5) * 1024));
        else if (s < 15) st[s - 11] = ld8(rWS, ((4 * g + s - 11) * 128 + 16 * w + tok) * 8, WS_ST1 + ci * 16 * 128 * 8);
        if (s == 12) { GAS u64* f = (GAS u64*)(p.ws + WS_FM); fm0 = __hip_atomic_load(f + lane, RLX_AGENT); fm1 = __hip_atomic_load(f + 64 + lane, RLX_AGENT); fm2 = __hip_atomic_load(f + 128 + (lane & 31), RLX_AGENT); }
#pragma unroll
        for (int nt = 0; nt < 6; ++nt) {
          if (nt < 2) accQ[nt] = MFMA16(W[nt], act[s], accQ[nt]); else accKV[nt - 2] = MFMA16(W[nt], act[s], accKV[nt - 2]);
          if (s + 1 < 16) W[nt] = QF(nt, s + 1);
          SB();
        }
      }
    }
    float s1 = 0, s2 = 0;
#pragma unroll
    for (int e = 0; e < 4; ++e) { s1 += __uint_as_float(st[e][0]); s2 += __uint_as_float(st[e][1]); }
    s1 = sumg(s1); s2 = sumg(s2);
    const float mu = s1 * (1.f / 512), var = fmaxf(s2 * (1.f / 512) - mu * mu, 0.f);
    rstd = __builtin_amdgcn_rsqf(var + 1e-5f); nmu = -rstd * mu;
    if (w == 6 && lane < 8) *(LAS v4u*)(sm + L_B2C + lane * 16) = b2v;
    PIN(rstd); PIN(nmu);
    {
          GAS u64* f = (GAS u64*)(p.ws + WS_FM);
          for (unsigned spins = 0; spins < (1u << 17) && !__all(fm0 == TAG && fm1 == TAG && fm2 == TAG); ++spins) {
            fm0 = __hip_atomic_load(f + lane, RLX_AGENT); fm1 = __hip_atomic_load(f + 64 + lane, RLX_AGENT); fm2 = __hip_atomic_load(f + 128 + (lane & 31), RLX_AGENT); }
          CFENCE();
          if (w < 6) {
            const int vec = w < 2 ? 0 : w < 4 ? 2 : 3, hq = w & 1;
            const unsigned dst = w == 0 ? L_PSA : w == 1 ? L_PSA2 : w == 2 ? L_PSB : w == 3 ? L_PSB2 : w == 4 ? L_PCB : L_PCB2;
            glds2x1(rWS, lane * 16, WS_MOD + (2 * hq) * 10240 + vec * 2048, dst);
            glds2x1(rWS, lane * 16, WS_MOD + (2 * hq + 1) * 10240 + vec * 2048, dst + 2048);
          } else glds1(rWS, (lane >> 4) * 10240 + (lane & 15) * 16, WS_MOD + (w == 6 ? 1 : 4) * 2048 + ct * 256, w == 6 ? L_PGA : L_PGB);
    }
    STAMP(4);
    BAR();
    {
      v4u kk, vv;
      unsigned pkd[4][2];
#pragma unroll
      for (int nt = 0; nt < 4; ++nt) {
        const f4 u = ldsf4(L_PERS + (32 + nt * 16 + 4 * g) * 4);
        pkd[nt][0] = pk(rstd * accKV[nt][0] + nmu * u[0], rstd * accKV[nt][1] + nmu * u[1]);
        pkd[nt][1] = pk(rstd * accKV[nt][2] + nmu * u[2], rstd * accKV[nt][3] + nmu * u[3]);
      }
      { auto r0 = __builtin_amdgcn_permlane16_swap(pkd[0][0], pkd[1][0], false, false); auto r1 = __builtin_amdgcn_permlane16_swap(pkd[0][1], pkd[1][1], false, false);
        kk = v4u{r0[0], r1[0], r0[1], r1[1]}; }
      { auto r0 = __builtin_amdgcn_permlane16_swap(pkd[2][0], pkd[3][0], false, false); auto r1 = __builtin_amdgcn_permlane16_swap(pkd[2][1], pkd[3][1], false, false);
        vv = v4u{r0[0], r1[0], r0[1], r1[1]}; }
      const int hd = g & 1, pl = g >> 1;
      st16(rWS, kk, WS_KV + (ci * 16 + cj) * 16384 + ((0 * 2 + hd) * 2 + pl) * 2048 + (16 * w + tok) * 16);
      st16(rWS, vv, WS_KV + (ci * 16 + cj) * 16384 + ((1 * 2 + hd) * 2 + pl) * 2048 + (16 * w + tok) * 16);
      *(LAS v4u*)(sm + L_AK + (hd * 2 + pl) * KPL + (256 + 16 * w + tok) * 16) = kk;
      *(LAS v4u*)(sm + L_AV + hd * VHD + pl * VPL + (256 + 16 * w + tok) * 16) = vv;
    }
    VM0();
    BAR();
    if (tid == 256) arrive(p.ws, WS_FP + (0 * 16 + ci) * 128 + cj * 8, TAG);
    STAMP(5);
    if (tid < 400) *(LAS v4u*)(sm + L_ONE + tid * 16) = v4u{0x3f803f80u, 0x3f803f80u, 0x3f803f80u, 0x3f803f80u};
    if (tid < 416) {
      const int seg = tid < 384 ? tid >> 7 : 3 + ((tid - 384) >> 4), e = tid < 384 ? tid & 127 : (tid - 384) & 15;
      f4 v;
      if (seg < 3) {
        const unsigned a0 = (seg == 0 ? L_PSA : seg == 1 ? L_PSB : L_PCB) + e * 16, a1 = (seg == 0 ? L_PSA2 : seg == 1 ? L_PSB2 : L_PCB2) + e * 16;
        v = (ldsf4(a0) + ldsf4(a0 + 2048)) + (ldsf4(a1) + ldsf4(a1 + 2048));
      } else {
        const unsigned a0 = (seg == 3 ? L_PGA : L_PGB) + e * 16;
        v = (ldsf4(a0) + ldsf4(a0 + 256)) + (ldsf4(a0 + 512) + ldsf4(a0 + 768));
      }
      *(LAS v2u*)(sm + L_FIN + (seg < 3 ? seg * 1024 : 3072 + (seg - 3) * 128) + e * 8) = v2u{pk(v[0], v[1]), pk(v[2], v[3])};
    }
    BAR();
    {
      f4 av[4];
      v8s W[2][4], a[2];
      const unsigned c0 = opq(bq + w * 2048), d0 = opq(bv + w * 2048);
#pragma unroll
      for (int u = 0; u < 2; ++u) {
        a[u] = lds16(L_FIN + (2 * w + u) * 64 + g * 16);
#pragma unroll
        for (int nt = 0; nt < 4; ++nt) W[u][nt] = lds16((nt < 2 ? c0 : d0) + (nt & 1) * 16384 + u * 1024);
      }
#pragma unroll
      for (int nt = 0; nt < 4; ++nt) av[nt] = f4{0, 0, 0, 0};
#pragma unroll
      for (int u = 0; u < 2; ++u)
#pragma unroll
        for (int nt = 0; nt < 4; ++nt) av[nt] = MFMA16(W[u][nt], a[u], av[nt]);
      if (tok == 0) {
#pragma unroll
        for (int nt = 0; nt < 4; ++nt) *(LAS f4*)(sm + L_PARTV + (w * 64 + nt * 16 + 4 * g) * 4) = av[nt];
      }
    }
    {
      gaR = asv4u(lds16(L_FIN + 3072 + kh * 64 + g * 16)); gbR = asv4u(lds16(L_FIN + 3200 + kh * 64 + g * 16));
      sbR = asv4u(lds16(L_FIN + 1024 + (2 * ct + kh) * 64 + g * 16)); cbR = asv4u(lds16(L_FIN + 2048 + (2 * ct + kh) * 64 + g * 16));
#pragma unroll
      for (int e = 0; e < 2; ++e) b2R[e] = lds8(L_B2C + (32 * kh + 4 * g + 16 * e) * 2);
      mc0 = asv4u(lds16(L_FIN + 2048 + 16 * lane)); md0 = asv4u(lds16(L_FIN + 1024 + 16 * lane));
#pragma unroll
      for (int u = 0; u < 2; ++u) mx[u] = asv4u(lds16(L_FIN + (tok == 1 ? 1024 : 2048) + (2 * w + u) * 64 + g * 16));
      BAR();
      if (tid < 64) {
        float s = 0;
#pragma unroll
        for (int e = 0; e < 8; ++e) s += *(const LAS float*)(sm + L_PARTV + (e * 64 + tid) * 4);
        *(LAS float*)(sm + L_PERS + (96 + tid) * 4) = s;
      }
    }
    if (ci < 2) {
      for (int e = tid; e < 8 * 256; e += 512) {
        const int pln = e >> 8, key = e & 255;
        if (ci == 0 || key < 128) {
          const unsigned a = pln < 4 ? L_AK + pln * KPL : L_AV + ((pln - 4) >> 1) * VHD + (pln & 1) * VPL;
          *(LAS v4u*)(sm + a + key * 16) = v4u{0, 0, 0, 0};
        }
      }
    }
    if (w == 4) {
      GAS u64* f0 = (GAS u64*)(p.ws + (lane == 0 && ci >= 1 ? WS_FP + (ci - 1) * 128 + cj * 8 : lane == 1 && ci >= 2 ? WS_FP + (ci - 2) * 128 + cj * 8
                                       : lane >= 16 && lane < 32 ? WS_FW1 + (cj * 16 + lane - 16) * 8 : WS_FWO + (ct * 16 + (lane & 15)) * 8));
      GAS u64* f1 = (GAS u64*)(p.ws + WS_FW2 + ct * 512) + (lane < 56 ? lane : 0);
      for (unsigned spins = 0; spins < (1u << 17); ++spins) {
        const u64 a = __hip_atomic_load(f0, RLX_AGENT), c = __hip_atomic_load(f1, RLX_AGENT);
        if (__all(a == TAG && c == TAG)) break;
      }
      CFENCE();
    }
    BAR();
    STAMP(6);
    {
      const unsigned dst = w < 4 ? L_AK + w * KPL : L_AV + ((w - 4) >> 1) * VHD + (w & 1) * VPL;
#pragma unroll
      for (int src = 0; src < 2; ++src)
        if (ci - 2 + src >= 0) glds2x1(rWS, lane * 16, WS_KV + ((ci - 2 + src) * 16 + cj) * 16384 + w * 2048, dst + src * 2048);
    }
    {
      const float qs = 0.25f * 1.4426950408889634f;
      unsigned pq[2][2];
#pragma unroll
      for (int nt = 0; nt < 2; ++nt) {
        const f4 u = ldsf4(L_PERS + (nt * 16 + 4 * g) * 4), v = ldsf4(L_PERS + (96 + nt * 16 + 4 * g) * 4);
        pq[nt][0] = pk(qs * (rstd * accQ[nt][0] + nmu * u[0] + v[0]), qs * (rstd * accQ[nt][1] + nmu * u[1] + v[1]));
        pq[nt][1] = pk(qs * (rstd * accQ[nt][2] + nmu * u[2] + v[2]), qs * (rstd * accQ[nt][3] + nmu * u[3] + v[3]));
      }
      auto r0 = __builtin_amdgcn_permlane16_swap(pq[0][0], pq[1][0], false, false); auto r1 = __builtin_amdgcn_permlane16_swap(pq[0][1], pq[1][1], false, false);
      *(LAS v4u*)(sm + L_AQ + ((g & 1) * 2 + (g >> 1)) * 2048 + (16 * w + tok) * 16) = v4u{r0[0], r1[0], r0[1], r1[1]};
    }
    VM0();
    BAR();
    STAMP(7);
  }

  const int orow = 128 * ci + 64 * hf + 16 * tt + tok;
  v2u xr0, xr1;
  {
    const int t = w & 3, hd = w >> 2;
    const int kt0 = ci >= 2 ? 0 : 8 - 4 * ci - t;
    f16v O;
    const u16* xres = p.x + (size_t)orow * DM + ocol;
    if (ci >= 2) attention<true>(t, hd, 0, lane, mU, mL, O, rWS, WS_TWO + ct * 65536, WS_TW1 + cj * 7 * 16384, w, xres, xr0, xr1, stampv);
    else attention<false>(t, hd, kt0, lane, mU, mL, O, rWS, WS_TWO + ct * 65536, WS_TW1 + cj * 7 * 16384, w, xres, xr0, xr1, stampv);
    const int h = lane >> 5;
    const float inv = __builtin_amdgcn_rcpf(O[8]);
    const f4 b0 = ldsf4(L_PERS + (96 + 32 + hd * 16 + 4 * h) * 4), b1 = ldsf4(L_PERS + (96 + 32 + hd * 16 + 8 + 4 * h) * 4);
    const v4u o = {pk(O[0] * inv + b0[0], O[1] * inv + b0[1]), pk(O[2] * inv + b0[2], O[3] * inv + b0[3]),
                   pk(O[4] * inv + b1[0], O[5] * inv + b1[1]), pk(O[6] * inv + b1[2], O[7] * inv + b1[3])};
    const int qr = lane & 31;
    st16(rWS, o, WS_OF + (ci * 8 * 16 + cj) * 1024 + ((2 * t + (qr >> 4)) * 16) * 1024 + ((2 * hd + h) * 16 + (qr & 15)) * 16);
  }
  STAMP(24);
  VM0();
  BAR();
  if (tid == 256) arrive(p.ws, WS_FP + (1 * 16 + ci) * 128 + cj * 8, TAG);
  STAMP(8);
  float b1v = 0.f;
  v8s aux2[2];
  float Cw = 0, Cw2 = 0, Cws = 0, Cs = 0, Cs2 = 0;
#define MODSUMS() do { \
      _Pragma("unroll") for (int r = 0; r < 4; ++r) { \
        const float w0 = rbf(1.f + bflo(mc0[r])), w1 = rbf(1.f + bfhi(mc0[r])), s0 = bflo(md0[r]), s1_ = bfhi(md0[r]); \
        Cw += w0 + w1; Cw2 += w0 * w0 + w1 * w1; Cws += w0 * s0 + w1 * s1_; Cs += s0 + s1_; Cs2 += s0 * s0 + s1_ * s1_; } \
      Cw = sumg(rowsum16(Cw)); Cw2 = sumg(rowsum16(Cw2)); Cws = sumg(rowsum16(Cws)); Cs = sumg(rowsum16(Cs)); Cs2 = sumg(rowsum16(Cs2)); \
      _Pragma("unroll") for (int u = 0; u < 2; ++u) { v4u t; \
        _Pragma("unroll") for (int d = 0; d < 4; ++d) t[d] = tok == 0 ? pk(1.f + bflo(mx[u][d]), 1.f + bfhi(mx[u][d])) : (tok == 1 ? mx[u][d] : 0x3f803f80u); \
        aux2[u] = asv8(t); } \
      asm volatile("" : "+v"(Cw), "+v"(Cw2), "+v"(Cws), "+v"(Cs), "+v"(Cs2), "+v"(aux2[0]), "+v"(aux2[1])); } while (0)
  if (w != 4) {
#pragma unroll
    for (int e = 0; e < 3; ++e) { const int gi = wq + 7 * e; if (gi < 16) glds4x1(rWS, lane * 16, WS_TW1 + (cj * 7 + 2) * 16384 + gi * 4096, L_W1 + 32768 + gi * 4096); }
  }
  MODSUMS();
  if (w == 4) wait_words(p.ws, WS_FP + (1 * 16 + ci) * 128, 16, lane, TAG);
  BAR();
  STAMP(9);

  float x1[8];
  {
    v8s oa[8];
    const unsigned osrc = WS_OF + ((ci * 8 + 4 * hf + tt) * 16 + 8 * kh) * 1024;
#pragma unroll
    for (int s = 0; s < 3; ++s) oa[s] = asv8(ld16(rWS, lane * 16, osrc + s * 1024));
    const unsigned bo = opq(L_WO + kh * 32768 + lane * 16);
    f4 acc[4];
#pragma unroll
    for (int nt = 0; nt < 4; ++nt) acc[nt] = f4{0, 0, 0, 0};
    {
      v8s W[2][4];
#pragma unroll
      for (int s = 0; s < 2; ++s)
#pragma unroll
        for (int nt = 0; nt < 4; ++nt) W[s][nt] = lds16(bo + (s * 4 + nt) * 1024);
      SB();
#pragma unroll
      for (int s = 3; s < 8; ++s) oa[s] = asv8(ld16(rWS, lane * 16, osrc + s * 1024));
      if (tid < 112) { const int nt = tid >> 4; b1v = bflo(p.b1[(nt == 6 ? 112 * cj + (par ? 0 : 96) : 112 * cj + 16 * par + nt * 16) + (tid & 15)]); }
      SB();
#pragma unroll
      for (int s = 0; s < 8; ++s)
#pragma unroll
        for (int nt = 0; nt < 4; ++nt) {
          acc[nt] = MFMA16(W[s & 1][nt], oa[s], acc[nt]);
          if (s + 2 < 8) W[s & 1][nt] = lds16(bo + ((s + 2) * 4 + nt) * 1024);
          SB();
        }
    }
    STAMP(26);
    VM0();
    BAR();
    *(LAS f4*)(sm + L_XCH + w * 2048 + lane * 16) = kh ? acc[0] : acc[2];
    *(LAS f4*)(sm + L_XCH + w * 2048 + 1024 + lane * 16) = kh ? acc[1] : acc[3];
    BAR();
    const f4 o0 = ldsf4(L_XCH + (w ^ 4) * 2048 + lane * 16), o1 = ldsf4(L_XCH + (w ^ 4) * 2048 + 1024 + lane * 16);
    const f4 t0 = (kh ? acc[2] : acc[0]) + o0, t1 = (kh ? acc[3] : acc[1]) + o1;
    const float xin[8] = {bflo(xr0[0]), bfhi(xr0[0]), bflo(xr0[1]), bfhi(xr0[1]), bflo(xr1[0]), bfhi(xr1[0]), bflo(xr1[1]), bfhi(xr1[1])};
    const float ga[8] = {bflo(gaR[0]), bfhi(gaR[0]), bflo(gaR[1]), bfhi(gaR[1]), bflo(gaR[2]), bfhi(gaR[2]), bflo(gaR[3]), bfhi(gaR[3])};
    const float cb[8] = {bflo(cbR[0]), bfhi(cbR[0]), bflo(cbR[1]), bfhi(cbR[1]), bflo(cbR[2]), bfhi(cbR[2]), bflo(cbR[3]), bfhi(cbR[3])};
    const float sv[8] = {bflo(sbR[0]), bfhi(sbR[0]), bflo(sbR[1]), bfhi(sbR[1]), bflo(sbR[2]), bfhi(sbR[2]), bflo(sbR[3]), bfhi(sbR[3])};
    float wv[8], xw[8];
#pragma unroll
    for (int r = 0; r < 8; ++r) { x1[r] = rbf(xin[r] + ga[r] * (r < 4 ? t0[r & 3] : t1[r & 3])); wv[r] = rbf(1.f + cb[r]); xw[r] = x1[r] * wv[r]; }
    st16(rWS, v4u{pk(xw[0], xw[1]), pk(xw[2], xw[3]), pk(xw[4], xw[5]), pk(xw[6], xw[7])}, WS_X1F + ((ci * 8 + 4 * hf + tt) * 16 + 2 * ct + kh) * 1024 + lane * 16);
    SB();
    float s1 = 0, s2 = 0, s3 = 0, s4 = 0, s5 = 0, s6 = 0;
#pragma unroll
    for (int r = 0; r < 8; ++r) { s1 += x1[r]; s2 += x1[r] * x1[r]; s3 += xw[r]; s4 += xw[r] * wv[r]; s5 += xw[r] * xw[r]; s6 += xw[r] * sv[r]; }
    s1 = sumg(s1); s2 = sumg(s2); s3 = sumg(s3); s4 = sumg(s4); s5 = sumg(s5); s6 = sumg(s6);
    const unsigned rec = (ci * 16 + 2 * ct + kh) * 128 + 64 * hf + 16 * tt + tok;
    if (g == 0) st16(rWS, v4u{__float_as_uint(s1), __float_as_uint(s2), __float_as_uint(s3), __float_as_uint(s4)}, WS_ST2A + rec * 16);
    if (g == 1) st8(rWS, v2u{__float_as_uint(s5), __float_as_uint(s6)}, WS_ST2B + rec * 8);
  }
  if (w < 4) {
    glds4x1(rWS, lane * 16, WS_TW1 + (cj * 7 + 6) * 16384 + w * 4096, L_W1S + w * 4096);
    glds4x1(rWS, lane * 16, WS_TW2 + ct * 229376 + 7 * 16384 + w * 4096, 49152 + w * 4096);
    asm volatile("s_waitcnt vmcnt(8)" ::: "memory");
  } else {
    glds4x1(rWS, lane * 16, WS_TW2 + ct * 229376 + (w - 4) * 4096, 32768 + (w - 4) * 4096);
    asm volatile("s_waitcnt vmcnt(4)" ::: "memory");
  }
  BAR();
  if (tid == 256) arrive(p.ws, WS_FP + (2 * 16 + ci) * 128 + cj * 8, TAG);
  STAMP(10);
  if (w == 4) asm volatile("s_waitcnt vmcnt(1)" ::: "memory"); else VM0();
  BAR();

  v4u stg[8];
  {
    const unsigned bP = opq(L_W1 + lane * 16), bR = opq(L_W1 + 65536 + lane * 16), bS = opq(L_W1S + lane * 16);
#define W1F(nt, s) lds16((nt) == 6 ? bS + (s) * 1024 : (nt) < 4 ? bP + (nt) * 16384 + (s) * 1024 : bR + ((nt) - 4) * 16384 + (s) * 1024)
    {
      f4 au[7];
#pragma unroll
      for (int nt = 0; nt < 7; ++nt) au[nt] = f4{0, 0, 0, 0};
      const unsigned c0 = opq(bP + w * 2048), d0 = opq(bR + w * 2048), e0 = opq(bS + w * 2048);
#pragma unroll
      for (int u = 0; u < 2; ++u) {
        v8s W[7];
#pragma unroll
        for (int nt = 0; nt < 7; ++nt) W[nt] = lds16(nt == 6 ? e0 + u * 1024 : nt < 4 ? c0 + nt * 16384 + u * 1024 : d0 + (nt - 4) * 16384 + u * 1024);
#pragma unroll
        for (int nt = 0; nt < 7; ++nt) au[nt] = MFMA16(W[nt], aux2[u], au[nt]);
      }
      if (tok < 3) {
#pragma unroll
        for (int nt = 0; nt < 7; ++nt) *(LAS f4*)(sm + L_XCH + ((w * 3 + tok) * 112 + nt * 16 + 4 * g) * 4) = au[nt];
      }
    }
    BAR();
    if (tid < 112) *(LAS float*)(sm + L_XF + (336 + tid) * 4) = b1v;
    {
      const int e0 = w < 4 ? tid : tid - 64;
      if (w != 4 && e0 < 336) {
        float s = 0;
#pragma unroll
        for (int e = 0; e < 8; ++e) s += *(const LAS float*)(sm + L_XCH + (e * 336 + e0) * 4);
        *(LAS float*)(sm + L_XF + e0 * 4) = s;
      }
    }
    if (w == 4) wait_words(p.ws, WS_FP + (2 * 16 + ci) * 128, 16, lane, TAG);
    BAR();
    STAMP(11);
    v8s act2[16];
    const unsigned asrc = WS_X1F + (ci * 8 + w) * 16 * 1024;
#pragma unroll
    for (int s = 0; s < 4; ++s) act2[s] = asv8(ld16(rWS, lane * 16, asrc + s * 1024));
    v4u sa[4]; v2u sb[4];
    f4 acc[7];
#pragma unroll
    for (int nt = 0; nt < 7; ++nt) acc[nt] = f4{0, 0, 0, 0};
    {
      v8s W[6];
#pragma unroll
      for (int nt = 0; nt < 6; ++nt) W[nt] = W1F(nt, 0);
      SB();
#pragma unroll
      for (int s = 0; s < 16; ++s) {
        if (s == 0) act2[4] = asv8(ld16(rWS, lane * 16, asrc + 4 * 1024));
        if (s + 5 < 16) act2[s + 5] = asv8(ld16(rWS, lane * 16, asrc + (s + 5) * 1024));
        else if (s < 15) { sa[s - 11] = ld16(rWS, ((4 * g + s - 11) * 128 + 16 * w + tok) * 16, WS_ST2A + ci * 16 * 128 * 16);
                           sb[s - 11] = ld8(rWS, ((4 * g + s - 11) * 128 + 16 * w + tok) * 8, WS_ST2B + ci * 16 * 128 * 8); }
#pragma unroll
        for (int nt = 0; nt < 6; ++nt) {
          acc[nt] = MFMA16(W[nt], act2[s], acc[nt]);
          if (s + 1 < 16) W[nt] = W1F(nt, s + 1);
          SB();
        }
      }
    }
    STAMP(18);
    CSTAMP(42);
    BAR();
    v8s WB[4];
#pragma unroll
    for (int s = 0; s < 4; ++s) WB[s] = W1F(6, s);
    float s1 = 0, s2 = 0, s3 = 0, s4 = 0, s5 = 0, s6 = 0;
#pragma unroll
    for (int e = 0; e < 4; ++e) { s1 += __uint_as_float(sa[e][0]); s2 += __uint_as_float(sa[e][1]); s3 += __uint_as_float(sa[e][2]); s4 += __uint_as_float(sa[e][3]); s5 += __uint_as_float(sb[e][0]); s6 += __uint_as_float(sb[e][1]); }
    s1 = sumg(s1); s2 = sumg(s2); s3 = sumg(s3); s4 = sumg(s4); s5 = sumg(s5); s6 = sumg(s6);
    const float mu1 = s1 * (1.f / 512), var1 = fmaxf(s2 * (1.f / 512) - mu1 * mu1, 0.f), r1 = __builtin_amdgcn_rsqf(var1 + 1e-5f);
    const float mu2 = (r1 * (s3 - mu1 * Cw) + Cs) * (1.f / 512);
    const float hh = (r1 * r1 * (s5 - 2.f * mu1 * s4 + mu1 * mu1 * Cw2) + 2.f * r1 * (s6 - mu1 * Cws) + Cs2) * (1.f / 512);
    const float r2 = __builtin_amdgcn_rsqf(fmaxf(hh - mu2 * mu2, 0.f) + 1e-5f);
    const float cA = r2 * r1, cB = -cA * mu1, cC = r2, cD = -r2 * mu2;
    const unsigned S0 = (7 * cj + par) >> 1;
    const unsigned hb = WS_H1F + (ci * 8 + w) * 56 * 1024;
    const unsigned xf = opq(L_XF + 16 * g);
#define BIAS4(nt) __builtin_elementwise_fma(f4{cB, cB, cB, cB}, ldsf4(xf + (nt) * 64), __builtin_elementwise_fma(f4{cC, cC, cC, cC}, ldsf4(xf + 448 + (nt) * 64), \
                    __builtin_elementwise_fma(f4{cD, cD, cD, cD}, ldsf4(xf + 896 + (nt) * 64), ldsf4(xf + 1344 + (nt) * 64))))
#define GELU4(o, a, bias) do { const f4 z_ = __builtin_elementwise_fma(f4{cA, cA, cA, cA}, a, bias); const f4 q_ = __builtin_elementwise_fma(z_ * z_, f4{-0.1029432397f, -0.1029432397f, -0.1029432397f, -0.1029432397f}, \
        f4{-2.3022081985f, -2.3022081985f, -2.3022081985f, -2.3022081985f}) * z_; f4 e_; \
        _Pragma("unroll") for (int r = 0; r < 4; ++r) e_[r] = __builtin_amdgcn_exp2f(q_[r]); \
        e_ += f4{1.f, 1.f, 1.f, 1.f}; \
        _Pragma("unroll") for (int r = 0; r < 4; ++r) e_[r] = __builtin_amdgcn_rcpf(e_[r]); \
        o = z_ * e_; } while (0)
    {
      unsigned hp[2][2];
      f4 bias = BIAS4(0);
      PIN(bias);
      SB();
#pragma unroll
      for (int nt = 0; nt < 6; ++nt) {
#pragma unroll
        for (int k = (nt * 16) / 6; k < ((nt + 1) * 16) / 6; ++k) { acc[6] = MFMA16(WB[k & 3], act2[k], acc[6]); if (k + 4 < 16) WB[k & 3] = W1F(6, k + 4); }
        if ((nt & 1) == 0) { const int gi = w * 3 + (nt >> 1), it = 1 + (gi >> 3), khh = (gi >> 2) & 1, pc4 = gi & 3;
          glds4x1(rWS, lane * 16, WS_TW2 + ct * 229376 + (khh * 7 + it) * 16384 + pc4 * 4096, 65536u + (it - 1) * 32768u + khh * 16384u + pc4 * 4096u); }
        if (nt >= 1 && nt < 5) {
#pragma unroll
          for (int e = 2 * (nt - 1); e < 2 * nt; ++e) { const int pc = w * 8 + e; const int chunk = (pc >> 4) == 0 ? 5 : ((pc >> 4) == 1 ? 12 : ((pc >> 4) == 2 ? 6 : 13));
            stg[e] = ld16(rWS, lane * 16, WS_TW2 + ct * 229376 + chunk * 16384 + (pc & 15) * 1024); }
        }
        f4 nb = bias;
        if (nt < 5) nb = BIAS4(nt + 1); else nb = BIAS4(6);
        f4 o;
        GELU4(o, acc[nt], bias);
        hp[nt & 1][0] = pk(o[0], o[1]); hp[nt & 1][1] = pk(o[2], o[3]);
        PIN(hp[nt & 1][0]); PIN(hp[nt & 1][1]); PIN(nb);
        bias = nb;
        if (nt & 1) st16(rWS, v4u{hp[0][0], hp[0][1], hp[1][0], hp[1][1]}, hb + (S0 + (nt >> 1)) * 1024 + lane * 16);
        SB();
      }
      STAMP(20);
      CSTAMP(43);
      f4 o;
      GELU4(o, acc[6], bias);
      const unsigned NT = 7 * cj + (par ? 0 : 6);
      st8(rWS, v2u{pk(o[0], o[1]), pk(o[2], o[3])}, hb + (NT >> 1) * 1024 + (NT & 1) * 512 + lane * 8);
    }
  }
  STAMP(25);
  VM0();
  BAR();
  if (tid == 256) arrive(p.ws, WS_FP + (3 * 16 + ci) * 128 + cj * 8, TAG);
  STAMP(13);
  if (w != 4) {
#pragma unroll
    for (int e = 0; e < 2; ++e) {
      const int gi = wq + 7 * e;
      if (gi < 8) glds4x1(rWS, lane * 16, WS_TW2 + ct * 229376 + ((gi >> 2) * 7 + 4) * 16384 + (gi & 3) * 4096, (gi >> 2) * 16384u + (gi & 3) * 4096u);
    }
  } else {
    if (b == 0) {
      GAS u64* f = (GAS u64*)(p.ws + WS_F0) + lane;
      for (unsigned spins = 0; spins < (1u << 17); ++spins) {
        bool ok = true;
#pragma unroll
        for (int i = 0; i < 16; ++i) ok &= __hip_atomic_load(f + i * 64, RLX_AGENT) == TAG;
        if (__all(ok)) break;
      }
      if (lane == 0) __hip_atomic_store((GAS u64*)(p.ws + WS_EPOCH), TAG, RLX_AGENT);
    }
    wait_words(p.ws, WS_FP + (3 * 16 + ci) * 128, 16, lane, TAG);
  }
  BAR();
  STAMP(14);

  {
    v8s ha[28];
    const unsigned hb = WS_H1F + ((ci * 8 + 4 * hf + tt) * 56 + 28 * kh) * 1024;
#define HLOAD(s) do { if ((s) % 7 == 3) { const v2u lo_ = ld8(rWS, lane * 8, hb + (s) * 1024), hi_ = ld8(rWS, lane * 8 + 512, hb + (s) * 1024); ha[s] = asv8(v4u{lo_[0], lo_[1], hi_[0], hi_[1]}); } \
                      else ha[s] = asv8(ld16(rWS, lane * 16, hb + (s) * 1024)); } while (0)
#pragma unroll
    for (int s = 0; s < 3; ++s) HLOAD(s);
    const unsigned bw = opq(kh * 16384 + lane * 16), bw2 = opq(bw + 65536), bw3 = opq(bw + 131072);
    f4 acc[4];
#pragma unroll
    for (int nt = 0; nt < 4; ++nt) acc[nt] = f4{0, 0, 0, 0};
#define W2F(nt, S) lds16((((S) >> 2) == 0 || ((S) >> 2) == 5 ? bw + 32768 : ((S) >> 2) == 1 || ((S) >> 2) == 6 ? bw2 : ((S) >> 2) == 2 ? bw2 + 32768 : ((S) >> 2) == 3 ? bw3 : bw) + (((S) & 3) * 4 + (nt)) * 1024)
    {
      v8s W[2][4];
#pragma unroll
      for (int S = 0; S < 2; ++S)
#pragma unroll
        for (int nt = 0; nt < 4; ++nt) W[S][nt] = W2F(nt, S);
      SB();
#pragma unroll
      for (int s = 3; s < 8; ++s) HLOAD(s);
      SB();
#pragma unroll
      for (int S = 0; S < 28; ++S) {
        if (S + 8 < 28) HLOAD(S + 8);
        if (S == 8) BAR();
        if (S >= 8 && S < 16) { const int e = S - 8, pc = w * 8 + e; *(LAS v4u*)(sm + 32768 + (pc >> 5) * 32768 + ((pc >> 4) & 1) * 16384 + (pc & 15) * 1024 + lane * 16) = stg[e]; }
        if (S == 18) BAR();
#pragma unroll
        for (int nt = 0; nt < 4; ++nt) {
          acc[nt] = MFMA16(W[S & 1][nt], ha[S], acc[nt]);
          if (S + 2 < 28) W[S & 1][nt] = W2F(nt, S + 2);
          SB();
        }
      }
    }
    STAMP(22);
    BAR();
    *(LAS f4*)(sm + L_W2X + w * 2048 + lane * 16) = kh ? acc[0] : acc[2];
    *(LAS f4*)(sm + L_W2X + w * 2048 + 1024 + lane * 16) = kh ? acc[1] : acc[3];
    BAR();
    const f4 o0 = ldsf4(L_W2X + (w ^ 4) * 2048 + lane * 16), o1 = ldsf4(L_W2X + (w ^ 4) * 2048 + 1024 + lane * 16);
    const f4 t0 = (kh ? acc[2] : acc[0]) + o0, t1 = (kh ? acc[3] : acc[1]) + o1;
    const float gb[8] = {bflo(gbR[0]), bfhi(gbR[0]), bflo(gbR[1]), bfhi(gbR[1]), bflo(gbR[2]), bfhi(gbR[2]), bflo(gbR[3]), bfhi(gbR[3])};
    const float bb[8] = {bflo(b2R[0][0]), bfhi(b2R[0][0]), bflo(b2R[0][1]), bfhi(b2R[0][1]), bflo(b2R[1][0]), bfhi(b2R[1][0]), bflo(b2R[1][1]), bfhi(b2R[1][1])};
    float o[8];
#pragma unroll
    for (int r = 0; r < 8; ++r) o[r] = x1[r] + gb[r] * ((r < 4 ? t0[r & 3] : t1[r & 3]) + bb[r]);
    auto r0 = __builtin_amdgcn_permlane16_swap(pk(o[0], o[1]), pk(o[4], o[5]), false, false);
    auto r1 = __builtin_amdgcn_permlane16_swap(pk(o[2], o[3]), pk(o[6], o[7]), false, false);
    const srd_t rO = __builtin_amdgcn_make_buffer_rsrc((void*)p.out, 0, SEQ * DM * 2, 0x00020000);
    st16(rO, v4u{r0[0], r1[0], r0[1], r1[1]}, (orow * DM + 64 * ct + 32 * kh + (g & 1) * 16 + (g >> 1) * 8) * 2);
  }
  STAMP(15);
  CSTAMP(41);
  STAMP_FLUSH(0);
}

extern "C" void kernel_launch(void* const* d_in, const int* in_sizes, int n_in, void* d_out, int out_size, void* d_ws, size_t ws_size, hipStream_t stream) {
  Params p;
  p.x = (const u16*)d_in[0]; p.wqkv = (const u16*)d_in[1]; p.wo = (const u16*)d_in[2];
  p.w1 = (const u16*)d_in[4]; p.b1 = (const u16*)d_in[5]; p.w2 = (const u16*)d_in[6]; p.b2 = (const u16*)d_in[7];
  p.c = (const u16*)d_in[8]; p.wmod = (const u16*)d_in[9]; p.out = (u16*)d_out; p.ws = (char*)d_ws;
  fused_block<<<256, 512, 0, stream>>>(p);
}
```
